```python
import torch

def _softmax(x, axis=-1):
    return torch.softmax(x, dim=axis)
def _gelu(x):
    return torch.nn.functional.gelu(x, approximate="tanh")
def _silu(x):
    return torch.nn.functional.silu(x)
def _sigmoid(x):
    return torch.sigmoid(x)
def _layernorm(x, axis=-1, eps=1e-5):
    m = x.mean(dim=axis, keepdim=True)
    v = x.var(dim=axis, keepdim=True, unbiased=False)
    return (x - m) / torch.sqrt(v + eps)
def _scatter_add(tgt, idx, src, axis=0):
    return tgt.clone().index_add_(axis, idx, src)
def _segment_sum(src, seg, n):
    out = torch.zeros((n,) + src.shape[1:], dtype=src.dtype)
    return out.index_add_(0, seg, src)
def _pad(x, pw):
    flat = tuple(v for pair in reversed(pw) for v in pair)
    return torch.nn.functional.pad(x, flat)

kernel_name = 'gated_mix__compressed_kv_cla__dense_activation_mish__prompt'

def setup_inputs():

    _w = (lambda a: (lambda t: t.to(torch.bfloat16) if t.is_floating_point() else t)(torch.as_tensor(a)))
    def _A_setup():
        B, H, Sq, Sk, D, M = 1, 32, 2048, 2048, 128, 512
        Dc = 4 * D
        return {
            "x": _w((lambda *s: torch.randn(*s, dtype=torch.bfloat16) * 0.05)(B, Sq, M)),
            "c_kv": _w((lambda *s: torch.randn(*s, dtype=torch.bfloat16) * 0.2)(B, Sk, Dc)),
            "w_q": _w((lambda *s: torch.randn(*s, dtype=torch.bfloat16) * 12.8)(M, H, D) * (M ** -0.5)),
            "w_uk": _w((lambda *s: torch.randn(*s, dtype=torch.bfloat16) * 12.8)(Dc, H, D) * (Dc ** -0.5)),
            "w_uv": _w((lambda *s: torch.randn(*s, dtype=torch.bfloat16) * 0.2)(Dc, H, D) * (Dc ** -0.5)),
            "w_o": _w((lambda *s: torch.randn(*s, dtype=torch.bfloat16) * 0.2)(H, D, M) * ((H * D) ** -0.5)),
        }
    _A_in = _A_setup()

    def _B_setup():
        B, S, D, F = 1, 2048, 512, 1792
        return {
            "x": _w((lambda *s: torch.randn(*s, dtype=torch.bfloat16) * 0.05)(B, S, D)),
            "w1": _w((lambda *s: torch.randn(*s, dtype=torch.bfloat16) * 6.4)(D, F) * (D ** -0.5)),
            "b1": _w((lambda *s: torch.randn(*s, dtype=torch.bfloat16) * 0.05)(F)),
            "w2": _w((lambda *s: torch.randn(*s, dtype=torch.bfloat16) * 0.05)(F, D) * (F ** -0.5)),
        }
    _B_in = _B_setup()

    return {
        "A_x": _A_in["x"],
        "A_c_kv": _A_in["c_kv"],
        "A_w_q": _A_in["w_q"],
        "A_w_uk": _A_in["w_uk"],
        "A_w_uv": _A_in["w_uv"],
        "A_w_o": _A_in["w_o"],
        "B_x": _B_in["x"],
        "B_w1": _B_in["w1"],
        "B_b1": _B_in["b1"],
        "B_w2": _B_in["w2"],
        "gate_w": _w((lambda *s: torch.randn(*s, dtype=torch.bfloat16) * 0.05)(512) * 16.0),
    }

def reference(A_x, A_c_kv, A_w_q, A_w_uk, A_w_uv, A_w_o, B_x, B_w1, B_b1, B_w2, gate_w):
    def _A_ref(x, c_kv, w_q, w_uk, w_uv, w_o):
        B, Sq, M = x.shape[0], x.shape[1], x.shape[2]
        Sk = c_kv.shape[1]
        H, D = w_q.shape[1], w_q.shape[2]
        q = torch.einsum("bqm,mhd->bhqd", x, w_q)
        k = torch.einsum("bkc,chd->bhkd", c_kv, w_uk)
        v = torch.einsum("bkc,chd->bhkd", c_kv, w_uv)
        scores = torch.einsum("bhqd,bhkd->bhqk", q, k) * (D ** -0.5)
        off = Sk - Sq
        qi = torch.arange(Sq)[:, None]
        kj = torch.arange(Sk)[None, :]
        cmask = kj <= (qi + off)
        scores = torch.where(cmask[None, None], scores, -1e9)
        p = _softmax(scores, axis=-1)
        ctx = torch.einsum("bhqk,bhkd->bhqd", p, v)
        return torch.einsum("bhqd,hdm->bqm", ctx, w_o)

    def _B_ref(x, w1, b1, w2):
        h = torch.einsum("bsd,df->bsf", x, w1) + b1
        sp = torch.log1p(torch.exp(-torch.abs(h))) + torch.where(h > 0, h, torch.zeros_like(h))
        act = h * torch.tanh(sp)
        return torch.einsum("bsf,fd->bsd", act, w2)

    x = A_x
    g = _sigmoid((x * gate_w).sum(-1, keepdims=True))
    a = _A_ref(x, A_c_kv, A_w_q, A_w_uk, A_w_uv, A_w_o)
    b = _B_ref(x, B_w1, B_b1, B_w2)
    return g * a + (1 - g) * b
```

```cpp
#include <hip/hip_runtime.h>
#include <hip/hip_bf16.h>
typedef float f32x4 __attribute__((ext_vector_type(4)));
typedef short bf16x8 __attribute__((ext_vector_type(8)));
typedef unsigned short u16;
constexpr int S=2048, DM=512, H=32, D=128, HD=H*D, F=1792;

__device__ __forceinline__ u16 f2bf(float f){ __bf16 b=(__bf16)f; return __builtin_bit_cast(u16,b);}
__device__ __forceinline__ float bf2f(u16 u){ return __uint_as_float(((unsigned)u)<<16);}

template<int MODE>
__global__ __launch_bounds__(256) void gemm_k(const u16* __restrict__ A, int lda, const u16* __restrict__ B, int N, int K,
                                              void* __restrict__ out, float alpha, const u16* __restrict__ bias,
                                              const float* __restrict__ prev, const float* __restrict__ g){
  __shared__ u16 As[128][40];
  __shared__ u16 Bs[128][40];
  int tid=threadIdx.x, lane=tid&63, w=tid>>6, wr=w>>1, wc=w&1, c=lane&15, gq=lane>>4;
  int bm=blockIdx.y*128, bn=blockIdx.x*128;
  f32x4 acc[4][4]={};
  for(int k0=0;k0<K;k0+=32){
    __syncthreads();
    for(int i=tid;i<128*4;i+=256){ int r=i>>2, ch=i&3; *(uint4*)&As[r][ch*8]=*(const uint4*)&A[(size_t)(bm+r)*lda+k0+ch*8]; }
    for(int i=tid;i<32*16;i+=256){ int kk=i>>4, ch=i&15; uint4 v=*(const uint4*)&B[(size_t)(k0+kk)*N+bn+ch*8]; const u16* p=(const u16*)&v;
      for(int j=0;j<8;j++) Bs[ch*8+j][kk]=p[j]; }
    __syncthreads();
    bf16x8 a[4],b[4];
    for(int m=0;m<4;m++) a[m]=*(const bf16x8*)&As[wr*64+m*16+c][gq*8];
    for(int n=0;n<4;n++) b[n]=*(const bf16x8*)&Bs[wc*64+n*16+c][gq*8];
    for(int m=0;m<4;m++) for(int n=0;n<4;n++) acc[m][n]=__builtin_amdgcn_mfma_f32_16x16x32_bf16(a[m],b[n],acc[m][n],0,0,0);
  }
  #pragma unroll
  for(int m=0;m<4;m++)
  #pragma unroll
  for(int n=0;n<4;n++)
  #pragma unroll
  for(int j=0;j<4;j++){
    int r=bm+wr*64+m*16+gq*4+j, cc=bn+wc*64+n*16+c; float v=acc[m][n][j];
    size_t o=(size_t)r*N+cc;
    if constexpr(MODE==0) ((u16*)out)[o]=f2bf(v*alpha);
    else if constexpr(MODE==1){ float h=v+bf2f(bias[cc]); float e=__expf(-fabsf(h)); float num=h>0.f?1.f+2.f*e:e*e+2.f*e; float den=h>0.f?num+2.f*e*e:num+2.f; ((u16*)out)[o]=f2bf(h*num/den); }
    else if constexpr(MODE==2) ((float*)out)[o]=v;
    else { float gg=g[r]; ((u16*)out)[o]=f2bf(gg*prev[o]+(1.f-gg)*v); }
  }
}

__global__ void gate_k(const u16* __restrict__ x, const u16* __restrict__ gw, float* __restrict__ g){
  int row=blockIdx.x*4+(threadIdx.x>>6), lane=threadIdx.x&63; float s=0;
  for(int i=lane;i<DM;i+=64) s+=bf2f(x[row*DM+i])*bf2f(gw[i]);
  for(int o=32;o;o>>=1) s+=__shfl_xor(s,o);
  if(lane==0) g[row]=1.f/(1.f+__expf(-s));
}

__global__ __launch_bounds__(256) void attn_k(const u16* __restrict__ Q, const u16* __restrict__ Kk, const u16* __restrict__ V, u16* __restrict__ ctx){
  int h=blockIdx.y, lane=threadIdx.x&63, w=threadIdx.x>>6, c=lane&15, gq=lane>>4;
  int q0=(31-blockIdx.x)*64+w*16, q=q0+c;
  bf16x8 qf[4];
  for(int s=0;s<4;s++) qf[s]=*(const bf16x8*)&Q[(size_t)q*HD+h*D+s*32+gq*8];
  f32x4 o[8]={}; float m=-1e30f,l=0;
  int nk=(q0+16+31)/32;
  for(int t=0;t<nk;t++){
    int k0=t*32; f32x4 s0={},s1={};
    for(int s=0;s<4;s++){
      bf16x8 ka=*(const bf16x8*)&Kk[(size_t)(k0+c)*HD+h*D+s*32+gq*8];
      bf16x8 kb=*(const bf16x8*)&Kk[(size_t)(k0+16+c)*HD+h*D+s*32+gq*8];
      s0=__builtin_amdgcn_mfma_f32_16x16x32_bf16(ka,qf[s],s0,0,0,0);
      s1=__builtin_amdgcn_mfma_f32_16x16x32_bf16(kb,qf[s],s1,0,0,0);
    }
    float p[8]; float mx=-1e30f;
    for(int i=0;i<4;i++){ int ka=k0+gq*4+i, kb=ka+16; p[i]=ka<=q?s0[i]:-INFINITY; p[4+i]=kb<=q?s1[i]:-INFINITY; mx=fmaxf(mx,fmaxf(p[i],p[4+i])); }
    mx=fmaxf(mx,__shfl_xor(mx,16)); mx=fmaxf(mx,__shfl_xor(mx,32));
    float mn=fmaxf(m,mx), al=exp2f(m-mn); m=mn; float ps=0;
    for(int i=0;i<8;i++){ p[i]=exp2f(p[i]-mn); ps+=p[i]; }
    ps+=__shfl_xor(ps,16); ps+=__shfl_xor(ps,32); l=l*al+ps;
    bf16x8 pf; for(int i=0;i<8;i++) pf[i]=(short)f2bf(p[i]);
    for(int db=0;db<8;db++){
      bf16x8 vf;
      for(int j=0;j<8;j++){ int key=k0+16*(j>>2)+4*gq+(j&3); vf[j]=(short)V[(size_t)key*HD+h*D+db*16+c]; }
      f32x4 t=o[db]; t[0]*=al;t[1]*=al;t[2]*=al;t[3]*=al;
      o[db]=__builtin_amdgcn_mfma_f32_16x16x32_bf16(vf,pf,t,0,0,0);
    }
  }
  float il=1.f/l;
  for(int db=0;db<8;db++) for(int i=0;i<4;i++) ctx[(size_t)q*HD+h*D+db*16+gq*4+i]=f2bf(o[db][i]*il);
}

extern "C" void kernel_launch(void* const* d_in, const int* in_sizes, int n_in, void* d_out, int out_size, void* d_ws, size_t ws_size, hipStream_t stream) {
  const u16* x=(const u16*)d_in[0]; const u16* ckv=(const u16*)d_in[1]; const u16* wq=(const u16*)d_in[2]; const u16* wuk=(const u16*)d_in[3];
  const u16* wuv=(const u16*)d_in[4]; const u16* wo=(const u16*)d_in[5]; const u16* bx=(const u16*)d_in[6]; const u16* w1=(const u16*)d_in[7];
  const u16* b1=(const u16*)d_in[8]; const u16* w2=(const u16*)d_in[9]; const u16* gw=(const u16*)d_in[10];
  char* ws=(char*)d_ws; const size_t SZ=(size_t)S*HD*2;
  u16* Q=(u16*)ws; u16* K=(u16*)(ws+SZ); u16* V=(u16*)(ws+2*SZ); u16* ctx=(u16*)(ws+3*SZ); u16* Hh=(u16*)(ws+4*SZ);
  float* a=(float*)(ws+5*SZ); float* g=(float*)(ws+6*SZ);
  const float qs=0.08838834764831845f*1.4426950408889634f;
  gemm_k<0><<<dim3(HD/128,S/128),256,0,stream>>>(x,DM,wq,HD,DM,Q,qs,nullptr,nullptr,nullptr);
  gemm_k<0><<<dim3(HD/128,S/128),256,0,stream>>>(ckv,DM,wuk,HD,DM,K,1.f,nullptr,nullptr,nullptr);
  gemm_k<0><<<dim3(HD/128,S/128),256,0,stream>>>(ckv,DM,wuv,HD,DM,V,1.f,nullptr,nullptr,nullptr);
  gemm_k<1><<<dim3(F/128,S/128),256,0,stream>>>(x,DM,w1,F,DM,Hh,1.f,b1,nullptr,nullptr);
  gate_k<<<S/4,256,0,stream>>>(x,gw,g);
  attn_k<<<dim3(32,H),256,0,stream>>>(Q,K,V,ctx);
  gemm_k<2><<<dim3(DM/128,S/128),256,0,stream>>>(ctx,HD,wo,DM,HD,a,1.f,nullptr,nullptr,nullptr);
  gemm_k<3><<<dim3(DM/128,S/128),256,0,stream>>>(Hh,F,w2,DM,F,d_out,1.f,nullptr,a,g);
}
```

```cpp
#include <hip/hip_runtime.h>
#include <hip/hip_bf16.h>
typedef float f32x4 __attribute__((ext_vector_type(4)));
typedef short bf16x8 __attribute__((ext_vector_type(8)));
typedef unsigned short u16;
constexpr int S=2048, DM=512, H=32, D=128, HD=H*D, F=1792;

__device__ __forceinline__ u16 f2bf(float f){ __bf16 b=(__bf16)f; return __builtin_bit_cast(u16,b);}
__device__ __forceinline__ float bf2f(u16 u){ return __uint_as_float(((unsigned)u)<<16);}

template<int MODE>
__global__ __launch_bounds__(256) void gemm_k(const u16* __restrict__ A, int lda, const u16* __restrict__ B, int N, int K,
                                              void* __restrict__ out, float alpha, const u16* __restrict__ bias,
                                              const float* __restrict__ prev, const float* __restrict__ g){
  __shared__ u16 As[128][40];
  __shared__ u16 Bs[128][40];
  int tid=threadIdx.x, lane=tid&63, w=tid>>6, wr=w>>1, wc=w&1, c=lane&15, gq=lane>>4;
  int bm=blockIdx.y*128, bn=blockIdx.x*128;
  f32x4 acc[4][4]={};
  for(int k0=0;k0<K;k0+=32){
    __syncthreads();
    for(int i=tid;i<128*4;i+=256){ int r=i>>2, ch=i&3; *(uint4*)&As[r][ch*8]=*(const uint4*)&A[(size_t)(bm+r)*lda+k0+ch*8]; }
    for(int i=tid;i<32*16;i+=256){ int kk=i>>4, ch=i&15; uint4 v=*(const uint4*)&B[(size_t)(k0+kk)*N+bn+ch*8]; const u16* p=(const u16*)&v;
      for(int j=0;j<8;j++) Bs[ch*8+j][kk]=p[j]; }
    __syncthreads();
    bf16x8 a[4],b[4];
    for(int m=0;m<4;m++) a[m]=*(const bf16x8*)&As[wr*64+m*16+c][gq*8];
    for(int n=0;n<4;n++) b[n]=*(const bf16x8*)&Bs[wc*64+n*16+c][gq*8];
    for(int m=0;m<4;m++) for(int n=0;n<4;n++) acc[m][n]=__builtin_amdgcn_mfma_f32_16x16x32_bf16(a[m],b[n],acc[m][n],0,0,0);
  }
  #pragma unroll
  for(int m=0;m<4;m++)
  #pragma unroll
  for(int n=0;n<4;n++)
  #pragma unroll
  for(int j=0;j<4;j++){
    int r=bm+wr*64+m*16+gq*4+j, cc=bn+wc*64+n*16+c; float v=acc[m][n][j];
    size_t o=(size_t)r*N+cc;
    if constexpr(MODE==0) ((u16*)out)[o]=f2bf(v*alpha);
    else if constexpr(MODE==1){ float h=v+bf2f(bias[cc]); float e=__expf(-fabsf(h)); float num=h>0.f?1.f+2.f*e:e*e+2.f*e; float den=h>0.f?num+2.f*e*e:num+2.f; ((u16*)out)[o]=f2bf(h*num/den); }
    else if constexpr(MODE==2) ((float*)out)[o]=v;
    else if constexpr(MODE==4){ int h=cc>>7,d=cc&127; int p=d>>5, jh=(d>>4)&1, gq2=(d>>2)&3, i=d&3; int s=2*p+(gq2>>1), hh=gq2&1;
        size_t a=((((size_t)(r>>5)*32+h)*8+s)*64+hh*32+(r&31))*8+4*jh+i; ((u16*)out)[a]=f2bf(v*alpha); }
    else { float gg=g[r]; ((u16*)out)[o]=f2bf(gg*prev[o]+(1.f-gg)*v); }
  }
}

__global__ __launch_bounds__(256,1) void k1_asm(const u16* __restrict__ x, const void* ckv, const void* wuk, const void* wuv, const void* w1, const void* b1,
    void* KVf, void* AF, const u16* __restrict__ gw, float* __restrict__ gate, unsigned* __restrict__ flags, unsigned long long* stamps){
  __shared__ __attribute__((aligned(16))) char lds[163840];
  unsigned ldsb=(unsigned)(size_t)lds;

  const int idx=blockIdx.x>>3;
  if(idx>=28){
    const int G=(blockIdx.x&7)*4+idx-28, lane=threadIdx.x&63, w=threadIdx.x>>6, row=64*G+16*w+(lane&15), q=lane>>4;
    if(threadIdx.x<8) flags[(G*8+threadIdx.x)*32]=0u;
    const uint4* xp=(const uint4*)(x+row*512+q*128); const uint4* gp=(const uint4*)(gw+q*128);
    float s=0.f;
    #pragma unroll 4
    for(int i=0;i<16;i++){ uint4 a=xp[i], b=gp[i];
      s+=__uint_as_float(a.x<<16)*__uint_as_float(b.x<<16)+__uint_as_float(a.x&0xffff0000u)*__uint_as_float(b.x&0xffff0000u);
      s+=__uint_as_float(a.y<<16)*__uint_as_float(b.y<<16)+__uint_as_float(a.y&0xffff0000u)*__uint_as_float(b.y&0xffff0000u);
      s+=__uint_as_float(a.z<<16)*__uint_as_float(b.z<<16)+__uint_as_float(a.z&0xffff0000u)*__uint_as_float(b.z&0xffff0000u);
      s+=__uint_as_float(a.w<<16)*__uint_as_float(b.w<<16)+__uint_as_float(a.w&0xffff0000u)*__uint_as_float(b.w&0xffff0000u); }
    s+=__shfl_xor(s,16); s+=__shfl_xor(s,32);
    if(q==0) gate[row]=1.f/(1.f+__expf(-s));
  }
  asm volatile(
OPQ
  :: "{s[8:9]}"(x), "{s[10:11]}"(ckv), "{s[12:13]}"(wuk), "{s[14:15]}"(wuv), "{s[16:17]}"(w1), "{s[18:19]}"(b1), "{s[20:21]}"(KVf), "{s[22:23]}"(AF),
     "{s24}"(blockIdx.x), "{s25}"(ldsb), "{v0}"(threadIdx.x)
  : OPQ);

}
__global__ __launch_bounds__(256,1) void attn_asm(const void* Qf, const void* KVf, void* Cf, unsigned long long* stamps){
  __shared__ __attribute__((aligned(16))) char lds[163840];
  unsigned ldsb=(unsigned)(size_t)lds; unsigned flag;

  asm volatile(
OPQ
  : "={s31}"(flag)
  : "{s[8:9]}"(Qf), "{s[10:11]}"(KVf), "{s[12:13]}"(Cf), "{s14}"(blockIdx.x), "{s15}"(ldsb), "{v0}"(threadIdx.x)
  : OPQ);

}
__global__ __launch_bounds__(256,1) void k3_asm(const void* AF, const void* wo, const void* w2, const void* gate, void* X, void* flags, void* out, unsigned long long* stamps){
  __shared__ __attribute__((aligned(16))) char lds[163840];
  unsigned ldsb=(unsigned)(size_t)lds;

  asm volatile(
OPQ
  :: "{s[8:9]}"(AF), "{s[10:11]}"(wo), "{s[12:13]}"(w2), "{s[14:15]}"(gate), "{s[16:17]}"(X), "{s[18:19]}"(flags), "{s[20:21]}"(out),
     "{s24}"(blockIdx.x), "{s25}"(ldsb), "{v0}"(threadIdx.x)
  : OPQ);

}
extern "C" void kernel_launch(void* const* d_in, const int* in_sizes, int n_in, void* d_out, int out_size, void* d_ws, size_t ws_size, hipStream_t stream) {
  const u16* x=(const u16*)d_in[0]; const u16* ckv=(const u16*)d_in[1]; const u16* wq=(const u16*)d_in[2]; const u16* wuk=(const u16*)d_in[3];
  const u16* wuv=(const u16*)d_in[4]; const u16* wo=(const u16*)d_in[5]; const u16* w1=(const u16*)d_in[7];
  const u16* b1=(const u16*)d_in[8]; const u16* w2=(const u16*)d_in[9]; const u16* gw=(const u16*)d_in[10];
  char* ws=(char*)d_ws; const size_t MB=1u<<20;
  u16* Q=(u16*)ws; void* KV=ws+16*MB; void* AF=ws+48*MB; void* X=ws+80*MB; unsigned* fl=(unsigned*)(ws+96*MB); float* g=(float*)(ws+97*MB);
  unsigned long long* st=(unsigned long long*)(ws+100*MB);
  const float qs=0.08838834764831845f*1.4426950408889634f;
  k1_asm<<<256,256,0,stream>>>(x,ckv,wuk,wuv,w1,b1,KV,AF,gw,g,fl,st);
  gemm_k<4><<<dim3(HD/128,S/128),256,0,stream>>>(x,DM,wq,HD,DM,Q,qs,nullptr,nullptr,nullptr);
  attn_asm<<<256,256,0,stream>>>(Q,KV,AF,st+65536);
  k3_asm<<<256,256,0,stream>>>(AF,wo,w2,g,X,fl,d_out,st+131072);
}
```

```cpp
#include <hip/hip_runtime.h>
#include <hip/hip_bf16.h>
typedef unsigned short u16;
constexpr size_t WS_KV=0, WS_AF=33554432, WS_FL=62914560, WS_G=63963136;
__device__ __forceinline__ float bf2f(unsigned u){ return __uint_as_float(u<<16); }
__device__ __forceinline__ u16 f2bf(float f){ __bf16 b=(__bf16)f; return __builtin_bit_cast(u16,b); }

typedef __attribute__((address_space(1))) u16 gu16;
typedef float f32x4 __attribute__((ext_vector_type(4)));
typedef unsigned u32x2 __attribute__((ext_vector_type(2)));
typedef short s16x8 __attribute__((ext_vector_type(8)));
__device__ __forceinline__ void attn_true_max_rows(const gu16* x, const gu16* wq, const gu16* KV, gu16* AF, u16* qs_lds, int h, int j, int w, int lane){
  const float qs=0.08838834764831845f*1.4426950408889634f;
  const gu16* kvh=KV+(size_t)h*524288;
  const int c=lane&15, g=lane>>4;
  #pragma unroll 1
  for(int grp=0;grp<4;grp++){
    const int row0=(grp<2? 128*j : 128*(15-j))+32*w+16*(grp&1), row=row0+c;
    #pragma unroll 1
    for(int db=0;db<8;db++){
      f32x4 acc={0.f,0.f,0.f,0.f};
      #pragma unroll 1
      for(int ks=0;ks<16;ks++){
        const s16x8 a=*(const __attribute__((address_space(1))) s16x8*)(x+(size_t)row*512+ks*32+g*8);
        s16x8 b;
        #pragma unroll
        for(int e=0;e<8;e++) b[e]=(short)wq[(size_t)(ks*32+g*8+e)*4096+h*128+db*16+c];
        acc=__builtin_amdgcn_mfma_f32_16x16x32_bf16(a,b,acc,0,0,0);
      }
      #pragma unroll
      for(int i=0;i<4;i++) qs_lds[(4*g+i)*128+db*16+c]=f2bf(acc[i]*qs);
    }
    __builtin_amdgcn_s_waitcnt(0xc07f);
    s16x8 qf[4];
    #pragma unroll
    for(int S=0;S<4;S++){
      #pragma unroll
      for(int e=0;e<8;e++) qf[S][e]=(short)qs_lds[c*128+32*S+16*(e>>2)+4*g+(e&3)]; }
    __builtin_amdgcn_s_waitcnt(0xc07f);
    f32x4 o[8];
    #pragma unroll
    for(int d=0;d<8;d++) o[d]=(f32x4){0.f,0.f,0.f,0.f};
    float m=-1e30f, l=0.f;
    const int nu=(row0+15)/32+1;
    #pragma unroll 1
    for(int u=0;u<nu;u++){
      f32x4 s0={0.f,0.f,0.f,0.f}, s1={0.f,0.f,0.f,0.f};
      #pragma unroll
      for(int S=0;S<4;S++){
        const gu16* kp=kvh+((size_t)(u*16+S)*64+lane)*8;
        s0=__builtin_amdgcn_mfma_f32_16x16x32_bf16(*(const __attribute__((address_space(1))) s16x8*)kp,qf[S],s0,0,0,0);
        s1=__builtin_amdgcn_mfma_f32_16x16x32_bf16(*(const __attribute__((address_space(1))) s16x8*)(kp+4*512),qf[S],s1,0,0,0); }
      float p[8], mx=-1e30f;
      #pragma unroll
      for(int i=0;i<4;i++){ const int ka=32*u+4*g+i; p[i]=ka<=row?s0[i]:-INFINITY; p[4+i]=ka+16<=row?s1[i]:-INFINITY; mx=fmaxf(mx,fmaxf(p[i],p[4+i])); }
      mx=fmaxf(mx,__shfl_xor(mx,16)); mx=fmaxf(mx,__shfl_xor(mx,32));
      const float mn=fmaxf(m,mx), al=exp2f(m-mn); m=mn; float ps=0.f; s16x8 pf;
      #pragma unroll
      for(int i=0;i<8;i++){ p[i]=exp2f(p[i]-mn); ps+=p[i]; pf[i]=(short)f2bf(p[i]); }
      ps+=__shfl_xor(ps,16); ps+=__shfl_xor(ps,32); l=l*al+ps;
      #pragma unroll
      for(int d=0;d<8;d++){
        const s16x8 vf=*(const __attribute__((address_space(1))) s16x8*)(kvh+((size_t)(u*16+8+d)*64+lane)*8);
        f32x4 t=o[d]; t[0]*=al; t[1]*=al; t[2]*=al; t[3]*=al;
        o[d]=__builtin_amdgcn_mfma_f32_16x16x32_bf16(vf,pf,t,0,0,0);
      }
    }
    const float il=1.f/l;
    #pragma unroll
    for(int d=0;d<8;d++){
      #pragma unroll
      for(int i=0;i<4;i++){
        gu16* pp=AF+((size_t)((row>>4)*184+4*h+(d>>1))*64+lane)*8+4*(d&1)+i;
        __hip_atomic_store(pp,f2bf(o[d][i]*il),__ATOMIC_RELAXED,__HIP_MEMORY_SCOPE_AGENT); }
    }
  }
}

__global__ __launch_bounds__(256,1) void gated_mix_fused(const u16* __restrict__ x, const u16* ckv, const u16* __restrict__ wq, const u16* wuk, const u16* wuv, const u16* wo,
    const u16* w1, const u16* b1, const u16* w2, const u16* __restrict__ gw, char* ws, u16* out){
  __shared__ __attribute__((aligned(16))) char lds[163840];
  const unsigned ldsb=(unsigned)(size_t)lds;
  unsigned flag; unsigned long long tag;
  unsigned long long px=(unsigned long long)x, pq=(unsigned long long)wq, po=(unsigned long long)wo, p2=(unsigned long long)w2, pw=(unsigned long long)ws, pout=(unsigned long long)out;
  unsigned blk=blockIdx.x, tid=threadIdx.x;
  asm volatile(
"v_lshrrev_b32 v48, 6, v0\n"
"s_and_b32 s35, s30, 7\n"
"s_lshr_b32 s36, s30, 3\n"
"v_readfirstlane_b32 s34, v48\n"
"v_and_b32 v48, 63, v0\n"
"v_lshlrev_b32 v14, 4, v48\n"
"v_and_b32 v15, 15, v48\n"
"v_lshrrev_b32 v16, 4, v48\n"
"s_lshr_b32 s39, s34, 1\n"
"s_and_b32 s40, s34, 1\n"
"s_lshl_b32 s44, s35, 2\n"
"s_lshr_b32 s45, s36, 3\n"
"s_add_u32 s37, s44, s45\n"
"s_and_b32 s38, s36, 7\n"
"s_add_u32 s64, s26, 0x3c00000\n"
"s_addc_u32 s65, s27, 0\n"
"s_and_b32 s65, s65, 0xffff\n"
"s_mov_b32 s66, 0xa2000\n"
"s_mov_b32 s67, 0x20000\n"
"s_mov_b32 s48, s10\n"
"s_and_b32 s49, s11, 0xffff\n"
"s_mov_b32 s50, 0x200000\n"
"s_mov_b32 s51, 0x20000\n"
"s_mov_b32 s52, s14\n"
"s_and_b32 s53, s15, 0xffff\n"
"s_mov_b32 s54, 0x400000\n"
"s_mov_b32 s55, 0x20000\n"
"s_mov_b32 s56, s16\n"
"s_and_b32 s57, s17, 0xffff\n"
"s_mov_b32 s58, 0x400000\n"
"s_mov_b32 s59, 0x20000\n"
"s_lshl_b32 s44, s38, 18\n"
"s_lshl_b32 s45, s34, 16\n"
"s_add_u32 s44, s44, s45\n"
"s_add_u32 s68, s44, 0\n"
"s_add_u32 s69, s44, 8192\n"
"s_add_u32 s70, s44, 16384\n"
"s_add_u32 s71, s44, 24576\n"
"s_add_u32 s72, s44, 32768\n"
"s_add_u32 s73, s44, 40960\n"
"s_add_u32 s74, s44, 49152\n"
"s_add_u32 s75, s44, 57344\n"
"s_lshl_b32 s82, s34, 13\n"
"s_lshl_b32 s44, s34, 17\n"
"s_lshl_b32 s45, s37, 8\n"
"s_add_u32 s44, s44, s45\n"
"s_add_u32 s76, s44, 0\n"
"s_add_u32 s77, s44, 32768\n"
"s_add_u32 s78, s44, 65536\n"
"s_add_u32 s79, s44, 98304\n"
"s_mul_i32 s83, s34, 4352\n"
"v_and_b32 v48, 63, v0\n"
"v_lshrrev_b32 v49, 3, v48\n"
"v_and_b32 v50, 7, v48\n"
"v_xor_b32 v50, v50, v16\n"
"v_lshlrev_b32 v50, 4, v50\n"
"v_lshl_add_u32 v11, v49, 10, v50\n"
"v_xor_b32 v50, 64, v50\n"
"v_lshl_add_u32 v12, v49, 10, v50\n"
"v_lshrrev_b32 v49, 1, v15\n"
"v_xor_b32 v49, v49, v16\n"
"v_lshlrev_b32 v49, 5, v49\n"
"v_and_b32 v50, 1, v15\n"
"v_lshl_add_u32 v49, v50, 4, v49\n"
"s_mov_b32 s44, 8192\n"
"v_mul_lo_u32 v50, s44, v16\n"
"v_add_u32 v13, v49, v50\n"
"s_add_u32 m0, s82, 0x0\n"
"s_nop 0\n"
"buffer_load_dwordx4 v11, s[48:51], s68 offen lds\n"
"s_add_u32 m0, s82, 0x400\n"
"s_nop 0\n"
"buffer_load_dwordx4 v12, s[48:51], s69 offen lds\n"
"s_add_u32 m0, s82, 0x800\n"
"s_nop 0\n"
"buffer_load_dwordx4 v11, s[48:51], s70 offen lds\n"
"s_add_u32 m0, s82, 0xc00\n"
"s_nop 0\n"
"buffer_load_dwordx4 v12, s[48:51], s71 offen lds\n"
"s_add_u32 m0, s82, 0x1000\n"
"s_nop 0\n"
"buffer_load_dwordx4 v11, s[48:51], s72 offen lds\n"
"s_add_u32 m0, s82, 0x1400\n"
"s_nop 0\n"
"buffer_load_dwordx4 v12, s[48:51], s73 offen lds\n"
"s_add_u32 m0, s82, 0x1800\n"
"s_nop 0\n"
"buffer_load_dwordx4 v11, s[48:51], s74 offen lds\n"
"s_add_u32 m0, s82, 0x1c00\n"
"s_nop 0\n"
"buffer_load_dwordx4 v12, s[48:51], s75 offen lds\n"
"s_add_u32 m0, s83, 0x8000\n"
"s_nop 0\n"
"buffer_load_dwordx4 v13, s[52:55], s76 offen lds\n"
"s_add_u32 m0, s83, 0x8400\n"
"s_nop 0\n"
"buffer_load_dwordx4 v13, s[52:55], s77 offen lds\n"
"s_add_u32 m0, s83, 0x8880\n"
"s_nop 0\n"
"buffer_load_dwordx4 v13, s[52:55], s78 offen lds\n"
"s_add_u32 m0, s83, 0x8c80\n"
"s_nop 0\n"
"buffer_load_dwordx4 v13, s[52:55], s79 offen lds\n"
"s_add_u32 m0, s83, 0xc400\n"
"s_nop 0\n"
"buffer_load_dwordx4 v13, s[56:59], s76 offen lds\n"
"s_add_u32 m0, s83, 0xc800\n"
"s_nop 0\n"
"buffer_load_dwordx4 v13, s[56:59], s77 offen lds\n"
"s_add_u32 m0, s83, 0xcc80\n"
"s_nop 0\n"
"buffer_load_dwordx4 v13, s[56:59], s78 offen lds\n"
"s_add_u32 m0, s83, 0xd080\n"
"s_nop 0\n"
"buffer_load_dwordx4 v13, s[56:59], s79 offen lds\n"
"v_add_u32 v11, 0x80, v11\n"
"v_add_u32 v12, 0x80, v12\n"
"v_add_u32 v13, 0x80000, v13\n"
"s_cmp_lt_u32 s37, 28\n"
"s_cbranch_scc1 LM_nogl\n"
"s_mov_b32 s88, s4\n"
"s_and_b32 s89, s5, 0xffff\n"
"s_mov_b32 s90, 0x400\n"
"s_mov_b32 s91, 0x20000\n"
"buffer_load_dwordx4 v[128:131], v14, s[88:91], 0 offen ;@cond\n"
"LM_nogl:\n"
"s_getpc_b64 s[60:61]\n"
"LM_pc:\n"
"s_and_b32 s61, s61, 0xffff\n"
"s_mov_b32 s62, LM_codeend-LM_pc\n"
"s_mov_b32 s63, 0x20000\n"
"s_lshl_b32 s44, s36, 2\n"
"s_add_u32 s44, s44, s34\n"
"s_lshl_b32 s44, s44, 10\n"
"v_and_b32 v57, 7, v0\n"
"v_lshlrev_b32 v57, 7, v57\n"
"buffer_load_dword v63, v57, s[60:63], s44 offen\n"
"v_and_b32 v58, 63, v0\n"
"s_cmp_lg_u32 s34, 0\n"
"s_cbranch_scc1 LM_pft0\n"
"s_mov_b32 s60, s14\n"
"s_and_b32 s61, s15, 0xffff\n"
"s_mov_b32 s62, 0x400000\n"
"s_mov_b32 s63, 0x20000\n"
"v_lshrrev_b32 v60, 1, v58\n"
"v_and_b32 v60, 7, v60\n"
"s_lshl_b32 s44, s38, 3\n"
"v_add_u32 v60, s44, v60\n"
"v_lshlrev_b32 v60, 13, v60\n"
"v_and_b32 v61, 1, v58\n"
"v_lshl_add_u32 v60, v61, 7, v60\n"
"s_lshl_b32 s44, s37, 8\n"
"s_add_u32 s44, s44, 1048576\n"
"v_add_u32 v42, s44, v60\n"
"s_mov_b32 s84, 524288\n"
"LM_pft0:\n"
"s_cmp_lg_u32 s34, 1\n"
"s_cbranch_scc1 LM_pft1\n"
"s_mov_b32 s60, s16\n"
"s_and_b32 s61, s17, 0xffff\n"
"s_mov_b32 s62, 0x400000\n"
"s_mov_b32 s63, 0x20000\n"
"v_lshrrev_b32 v60, 1, v58\n"
"v_and_b32 v60, 7, v60\n"
"s_lshl_b32 s44, s38, 3\n"
"v_add_u32 v60, s44, v60\n"
"v_lshlrev_b32 v60, 13, v60\n"
"v_and_b32 v61, 1, v58\n"
"v_lshl_add_u32 v60, v61, 7, v60\n"
"s_lshl_b32 s44, s37, 8\n"
"s_add_u32 s44, s44, 1048576\n"
"v_add_u32 v42, s44, v60\n"
"s_mov_b32 s84, 524288\n"
"LM_pft1:\n"
"s_cmp_lg_u32 s34, 2\n"
"s_cbranch_scc1 LM_pft2\n"
"s_mov_b32 s60, s10\n"
"s_and_b32 s61, s11, 0xffff\n"
"s_mov_b32 s62, 0x200000\n"
"s_mov_b32 s63, 0x20000\n"
"s_lshl_b32 s44, s38, 8\n"
"s_and_b32 s45, s37, 3\n"
"s_lshl_b32 s45, s45, 6\n"
"s_add_u32 s44, s44, s45\n"
"v_add_u32 v60, s44, v58\n"
"v_lshlrev_b32 v60, 10, v60\n"
"v_add_u32 v42, 0x100, v60\n"
"s_mov_b32 s84, 128\n"
"LM_pft2:\n"
"s_cmp_lg_u32 s34, 3\n"
"s_cbranch_scc1 LM_pft3\n"
"s_mov_b32 s60, s10\n"
"s_and_b32 s61, s11, 0xffff\n"
"s_mov_b32 s62, 0x200000\n"
"s_mov_b32 s63, 0x20000\n"
"s_lshl_b32 s44, s38, 8\n"
"s_and_b32 s45, s37, 3\n"
"s_lshl_b32 s45, s45, 6\n"
"s_add_u32 s44, s44, s45\n"
"v_add_u32 v60, s44, v58\n"
"v_lshlrev_b32 v60, 10, v60\n"
"v_add_u32 v42, 0x100, v60\n"
"s_mov_b32 s84, 128\n"
"LM_pft3:\n"
"v_mov_b32 v56, 0\n"
"buffer_load_dwordx2 v[46:47], v56, s[64:67], 0 offen sc1\n"
"s_mov_b32 s88, s22\n"
"s_and_b32 s89, s23, 0xffff\n"
"s_mov_b32 s90, 0xe00\n"
"s_mov_b32 s91, 0x20000\n"
"s_lshl_b32 s44, s37, 7\n"
"v_lshlrev_b32 v57, 3, v16\n"
"buffer_load_dwordx2 v[120:121], v57, s[88:91], s44 offen offset:0\n"
"buffer_load_dwordx2 v[122:123], v57, s[88:91], s44 offen offset:32\n"
"buffer_load_dwordx2 v[124:125], v57, s[88:91], s44 offen offset:64\n"
"buffer_load_dwordx2 v[126:127], v57, s[88:91], s44 offen offset:96\n"
"s_lshl_b32 s44, s39, 14\n"
"s_mul_i32 s45, s40, 17408\n"
"s_add_u32 s45, s45, 32768\n"
"v_lshrrev_b32 v48, 1, v15\n"
"v_xor_b32 v48, v48, v16\n"
"v_lshlrev_b32 v48, 4, v48\n"
"v_lshl_add_u32 v48, v15, 7, v48\n"
"v_add_u32 v1, s44, v48\n"
"v_xor_b32 v48, 64, v48\n"
"v_add_u32 v2, s44, v48\n"
"v_lshrrev_b32 v48, 2, v15\n"
"v_and_b32 v49, 3, v15\n"
"v_mul_u32_u24 v50, 0x880, v16\n"
"v_lshl_add_u32 v50, v48, 8, v50\n"
"v_lshl_add_u32 v50, v49, 3, v50\n"
"v_add_u32 v50, s45, v50\n"
"v_xor_b32 v51, 0, v48\n"
"v_lshl_add_u32 v3, v51, 5, v50\n"
"v_add_u32 v7, 0x2200, v3\n"
"v_xor_b32 v51, 1, v48\n"
"v_lshl_add_u32 v4, v51, 5, v50\n"
"v_add_u32 v8, 0x2200, v4\n"
"v_xor_b32 v51, 2, v48\n"
"v_lshl_add_u32 v5, v51, 5, v50\n"
"v_add_u32 v9, 0x2200, v5\n"
"v_xor_b32 v51, 3, v48\n"
"v_lshl_add_u32 v6, v51, 5, v50\n"
"v_add_u32 v10, 0x2200, v6\n"
"s_lshl_b32 s44, s34, 13\n"
"s_mov_b32 s45, 32768\n"
"v_lshrrev_b32 v48, 1, v15\n"
"v_xor_b32 v48, v48, v16\n"
"v_lshlrev_b32 v48, 4, v48\n"
"v_lshl_add_u32 v48, v15, 7, v48\n"
"v_add_u32 v18, s44, v48\n"
"v_xor_b32 v48, 64, v48\n"
"v_add_u32 v19, s44, v48\n"
"v_lshrrev_b32 v48, 2, v15\n"
"v_and_b32 v49, 3, v15\n"
"v_mul_u32_u24 v50, 0x880, v16\n"
"v_lshl_add_u32 v50, v48, 8, v50\n"
"v_lshl_add_u32 v50, v49, 3, v50\n"
"v_add_u32 v50, s45, v50\n"
"v_xor_b32 v51, 0, v48\n"
"v_lshl_add_u32 v20, v51, 5, v50\n"
"v_add_u32 v24, 0x2200, v20\n"
"v_xor_b32 v51, 1, v48\n"
"v_lshl_add_u32 v21, v51, 5, v50\n"
"v_add_u32 v25, 0x2200, v21\n"
"v_xor_b32 v51, 2, v48\n"
"v_lshl_add_u32 v22, v51, 5, v50\n"
"v_add_u32 v26, 0x2200, v22\n"
"v_xor_b32 v51, 3, v48\n"
"v_lshl_add_u32 v23, v51, 5, v50\n"
"v_add_u32 v27, 0x2200, v23\n"
"v_lshrrev_b32 v48, 2, v15\n"
"v_and_b32 v49, 3, v15\n"
"v_lshrrev_b32 v50, 1, v48\n"
"v_and_b32 v51, 1, v16\n"
"v_lshl_or_b32 v50, v51, 1, v50\n"
"v_lshlrev_b32 v51, 10, v16\n"
"v_lshl_add_u32 v51, v48, 7, v51\n"
"v_lshl_add_u32 v51, v49, 3, v51\n"
"v_add_u32 v51, 0xc400, v51\n"
"v_mov_b32 v52, 0\n"
"v_xor_b32 v52, v52, v50\n"
"v_lshl_add_u32 v28, v52, 5, v51\n"
"v_mov_b32 v52, 1\n"
"v_xor_b32 v52, v52, v50\n"
"v_lshl_add_u32 v29, v52, 5, v51\n"
"v_mov_b32 v52, 2\n"
"v_xor_b32 v52, v52, v50\n"
"v_lshl_add_u32 v30, v52, 5, v51\n"
"v_mov_b32 v52, 3\n"
"v_xor_b32 v52, v52, v50\n"
"v_lshl_add_u32 v31, v52, 5, v51\n"
"v_add_u32 v32, 0x1000, v28\n"
"v_add_u32 v33, 0x1000, v29\n"
"v_add_u32 v34, 0x1000, v30\n"
"v_add_u32 v35, 0x1000, v31\n"
"v_and_b32 v48, 63, v0\n"
"v_lshrrev_b32 v49, 3, v48\n"
"v_and_b32 v50, 7, v48\n"
"v_xor_b32 v50, v50, v16\n"
"v_lshlrev_b32 v50, 4, v50\n"
"v_lshl_add_u32 v36, v49, 10, v50\n"
"v_xor_b32 v50, 64, v50\n"
"v_lshl_add_u32 v37, v49, 10, v50\n"
"v_lshrrev_b32 v49, 1, v15\n"
"v_xor_b32 v49, v49, v16\n"
"v_lshlrev_b32 v49, 5, v49\n"
"v_and_b32 v50, 1, v15\n"
"v_lshl_add_u32 v49, v50, 4, v49\n"
"s_mov_b32 s44, 8192\n"
"v_mul_lo_u32 v50, s44, v16\n"
"v_add_u32 v38, v49, v50\n"
"v_and_b32 v48, 63, v0\n"
"v_lshrrev_b32 v49, 3, v48\n"
"v_and_b32 v50, 3, v49\n"
"v_lshrrev_b32 v51, 2, v49\n"
"v_lshrrev_b32 v52, 1, v50\n"
"v_and_b32 v53, 7, v48\n"
"v_lshrrev_b32 v54, 1, v53\n"
"v_xor_b32 v54, v54, v52\n"
"v_and_b32 v53, 1, v53\n"
"v_lshlrev_b32 v53, 4, v53\n"
"v_lshl_add_u32 v54, v54, 5, v53\n"
"v_xor_b32 v55, 64, v54\n"
"v_mul_u32_u24 v51, 4, v51\n"
"v_add_u32 v51, v51, v50\n"
"s_mov_b32 s44, 3584\n"
"v_mul_lo_u32 v51, s44, v51\n"
"v_add_u32 v39, v51, v54\n"
"v_add_u32 v40, v51, v55\n"
"s_cmp_lt_u32 s37, 28\n"
"s_cbranch_scc1 LM_nogv\n"
"v_and_b32 v48, 63, v0\n"
"v_lshrrev_b32 v48, 3, v48\n"
"v_lshlrev_b32 v39, 4, v48\n"
"v_mov_b32 v40, v39\n"
"LM_nogv:\n"
"s_add_u32 m0, s82, 0x10800\n"
"s_nop 0\n"
"buffer_load_dwordx4 v11, s[48:51], s68 offen lds\n"
"s_add_u32 m0, s82, 0x10c00\n"
"s_nop 0\n"
"buffer_load_dwordx4 v12, s[48:51], s69 offen lds\n"
"s_add_u32 m0, s82, 0x11000\n"
"s_nop 0\n"
"buffer_load_dwordx4 v11, s[48:51], s70 offen lds\n"
"s_add_u32 m0, s82, 0x11400\n"
"s_nop 0\n"
"buffer_load_dwordx4 v12, s[48:51], s71 offen lds\n"
"s_add_u32 m0, s82, 0x11800\n"
"s_nop 0\n"
"buffer_load_dwordx4 v11, s[48:51], s72 offen lds\n"
"s_add_u32 m0, s82, 0x11c00\n"
"s_nop 0\n"
"buffer_load_dwordx4 v12, s[48:51], s73 offen lds\n"
"s_add_u32 m0, s82, 0x12000\n"
"s_nop 0\n"
"buffer_load_dwordx4 v11, s[48:51], s74 offen lds\n"
"s_add_u32 m0, s82, 0x12400\n"
"s_nop 0\n"
"buffer_load_dwordx4 v12, s[48:51], s75 offen lds\n"
"s_add_u32 m0, s83, 0x18800\n"
"s_nop 0\n"
"buffer_load_dwordx4 v13, s[52:55], s76 offen lds\n"
"s_add_u32 m0, s83, 0x18c00\n"
"s_nop 0\n"
"buffer_load_dwordx4 v13, s[52:55], s77 offen lds\n"
"s_add_u32 m0, s83, 0x19080\n"
"s_nop 0\n"
"buffer_load_dwordx4 v13, s[52:55], s78 offen lds\n"
"s_add_u32 m0, s83, 0x19480\n"
"s_nop 0\n"
"buffer_load_dwordx4 v13, s[52:55], s79 offen lds\n"
"s_add_u32 m0, s83, 0x1cc00\n"
"s_nop 0\n"
"buffer_load_dwordx4 v13, s[56:59], s76 offen lds\n"
"s_add_u32 m0, s83, 0x1d000\n"
"s_nop 0\n"
"buffer_load_dwordx4 v13, s[56:59], s77 offen lds\n"
"s_add_u32 m0, s83, 0x1d480\n"
"s_nop 0\n"
"buffer_load_dwordx4 v13, s[56:59], s78 offen lds\n"
"s_add_u32 m0, s83, 0x1d880\n"
"s_nop 0\n"
"buffer_load_dwordx4 v13, s[56:59], s79 offen lds\n"
"v_add_u32 v11, 0x80, v11\n"
"v_add_u32 v12, 0x80, v12\n"
"v_add_u32 v13, 0x80000, v13\n"
"buffer_load_dword v17, v42, s[60:63], 0 offen\n"
"v_add_u32 v42, s84, v42\n"
"buffer_load_dword v41, v42, s[60:63], 0 offen\n"
"v_add_u32 v42, s84, v42\n"
"s_waitcnt vmcnt(18)\n"
"v_readfirstlane_b32 s42, v46\n"
"v_readfirstlane_b32 s43, v47\n"
"s_nop 0\n"
"s_add_u32 s42, s42, 1\n"
"s_addc_u32 s43, s43, 0\n"
"v_lshlrev_b32 v48, 16, v120\n"
"v_and_b32 v49, 0xffff0000, v120\n"
"v_lshlrev_b32 v50, 16, v121\n"
"v_and_b32 v51, 0xffff0000, v121\n"
"v_lshlrev_b32 v52, 16, v122\n"
"v_and_b32 v53, 0xffff0000, v122\n"
"v_lshlrev_b32 v54, 16, v123\n"
"v_and_b32 v55, 0xffff0000, v123\n"
"v_lshlrev_b32 v56, 16, v124\n"
"v_and_b32 v57, 0xffff0000, v124\n"
"v_lshlrev_b32 v58, 16, v125\n"
"v_and_b32 v59, 0xffff0000, v125\n"
"v_lshlrev_b32 v60, 16, v126\n"
"v_and_b32 v61, 0xffff0000, v126\n"
"v_lshlrev_b32 v62, 16, v127\n"
"v_and_b32 v63, 0xffff0000, v127\n"
"v_mov_b32 v64, v48\n"
"v_mov_b32 v65, v49\n"
"v_mov_b32 v66, v50\n"
"v_mov_b32 v67, v51\n"
"v_mov_b32 v68, v48\n"
"v_mov_b32 v69, v49\n"
"v_mov_b32 v70, v50\n"
"v_mov_b32 v71, v51\n"
"v_mov_b32 v72, v48\n"
"v_mov_b32 v73, v49\n"
"v_mov_b32 v74, v50\n"
"v_mov_b32 v75, v51\n"
"v_mov_b32 v76, v48\n"
"v_mov_b32 v77, v49\n"
"v_mov_b32 v78, v50\n"
"v_mov_b32 v79, v51\n"
"v_mov_b32 v80, v52\n"
"v_mov_b32 v81, v53\n"
"v_mov_b32 v82, v54\n"
"v_mov_b32 v83, v55\n"
"v_mov_b32 v84, v52\n"
"v_mov_b32 v85, v53\n"
"v_mov_b32 v86, v54\n"
"v_mov_b32 v87, v55\n"
"v_mov_b32 v88, v52\n"
"v_mov_b32 v89, v53\n"
"v_mov_b32 v90, v54\n"
"v_mov_b32 v91, v55\n"
"v_mov_b32 v92, v52\n"
"v_mov_b32 v93, v53\n"
"v_mov_b32 v94, v54\n"
"v_mov_b32 v95, v55\n"
"v_mov_b32 v96, v56\n"
"v_mov_b32 v97, v57\n"
"v_mov_b32 v98, v58\n"
"v_mov_b32 v99, v59\n"
"v_mov_b32 v100, v56\n"
"v_mov_b32 v101, v57\n"
"v_mov_b32 v102, v58\n"
"v_mov_b32 v103, v59\n"
"v_mov_b32 v104, v56\n"
"v_mov_b32 v105, v57\n"
"v_mov_b32 v106, v58\n"
"v_mov_b32 v107, v59\n"
"v_mov_b32 v108, v56\n"
"v_mov_b32 v109, v57\n"
"v_mov_b32 v110, v58\n"
"v_mov_b32 v111, v59\n"
"v_mov_b32 v112, v60\n"
"v_mov_b32 v113, v61\n"
"v_mov_b32 v114, v62\n"
"v_mov_b32 v115, v63\n"
"v_mov_b32 v116, v60\n"
"v_mov_b32 v117, v61\n"
"v_mov_b32 v118, v62\n"
"v_mov_b32 v119, v63\n"
"v_mov_b32 v120, v60\n"
"v_mov_b32 v121, v61\n"
"v_mov_b32 v122, v62\n"
"v_mov_b32 v123, v63\n"
"v_mov_b32 v124, v60\n"
"v_mov_b32 v125, v61\n"
"v_mov_b32 v126, v62\n"
"v_mov_b32 v127, v63\n"
"s_cmp_lt_u32 s37, 28\n"
"s_cbranch_scc1 LM_nogc\n"
"s_cmp_lg_u32 s34, 0\n"
"s_cbranch_scc1 LM_nogc\n"
"s_add_u32 s88, s26, 0x3e00000\n"
"s_addc_u32 s89, s27, 0\n"
"s_and_b32 s89, s89, 0xffff\n"
"s_mov_b32 s90, 0x40000\n"
"s_mov_b32 s91, 0x20000\n"
"s_lshl_b32 s44, s36, 13\n"
"v_lshlrev_b32 v60, 3, v14\n"
"v_mov_b32 v132, v128\n"
"v_lshrrev_b32 v133, 16, v128\n"
"v_mov_b32 v134, v129\n"
"v_lshrrev_b32 v135, 16, v129\n"
"v_mov_b32 v136, v130\n"
"v_lshrrev_b32 v137, 16, v130\n"
"v_mov_b32 v138, v131\n"
"v_lshrrev_b32 v139, 16, v131\n"
"buffer_store_dword v132, v60, s[88:91], s44 offen offset:0 ;@cond\n"
"buffer_store_dword v133, v60, s[88:91], s44 offen offset:16 ;@cond\n"
"buffer_store_dword v134, v60, s[88:91], s44 offen offset:32 ;@cond\n"
"buffer_store_dword v135, v60, s[88:91], s44 offen offset:48 ;@cond\n"
"buffer_store_dword v136, v60, s[88:91], s44 offen offset:64 ;@cond\n"
"buffer_store_dword v137, v60, s[88:91], s44 offen offset:80 ;@cond\n"
"buffer_store_dword v138, v60, s[88:91], s44 offen offset:96 ;@cond\n"
"buffer_store_dword v139, v60, s[88:91], s44 offen offset:112 ;@cond\n"
"LM_nogc:\n"
"v_and_b32 v62, 63, v0\n"
"v_lshlrev_b32 v62, 7, v62\n"
"s_barrier\n"
"s_cmp_eq_u32 s40, 1\n"
"s_cbranch_scc1 LM_vw\n"
"ds_read_b64_tr_b16 a[160:161], v3 offset:0\n"
"ds_read_b64_tr_b16 a[162:163], v3 offset:1024\n"
"ds_read_b128 a[128:131], v1 offset:0\n"
"ds_read_b128 a[132:135], v1 offset:2048\n"
"ds_read_b128 a[136:139], v1 offset:4096\n"
"ds_read_b128 a[140:143], v1 offset:6144\n"
"ds_read_b128 a[144:147], v1 offset:8192\n"
"ds_read_b128 a[148:151], v1 offset:10240\n"
"ds_read_b128 a[152:155], v1 offset:12288\n"
"ds_read_b128 a[156:159], v1 offset:14336\n"
"ds_read_b64_tr_b16 a[164:165], v4 offset:0\n"
"ds_read_b64_tr_b16 a[166:167], v4 offset:1024\n"
"ds_read_b64_tr_b16 a[168:169], v5 offset:0\n"
"ds_read_b64_tr_b16 a[170:171], v5 offset:1024\n"
"ds_read_b64_tr_b16 a[172:173], v6 offset:0\n"
"ds_read_b64_tr_b16 a[174:175], v6 offset:1024\n"
"ds_read_b64_tr_b16 a[176:177], v3 offset:128\n"
"ds_read_b64_tr_b16 a[178:179], v3 offset:1152\n"
"ds_read_b64_tr_b16 a[180:181], v4 offset:128\n"
"ds_read_b64_tr_b16 a[182:183], v4 offset:1152\n"
"ds_read_b64_tr_b16 a[184:185], v5 offset:128\n"
"ds_read_b64_tr_b16 a[186:187], v5 offset:1152\n"
"ds_read_b64_tr_b16 a[188:189], v6 offset:128\n"
"ds_read_b64_tr_b16 a[190:191], v6 offset:1152\n"
"s_waitcnt lgkmcnt(0)\n"
"v_mfma_f32_16x16x32_bf16 v[128:131], a[160:163], a[128:131], 0\n"
"ds_read_b64_tr_b16 a[224:225], v7 offset:0\n"
"v_add_u32 v1, 0x10800, v1\n"
"v_mfma_f32_16x16x32_bf16 v[132:135], a[160:163], a[132:135], 0\n"
"v_add_u32 v3, 0x10800, v3\n"
"v_add_u32 v4, 0x10800, v4\n"
"v_mfma_f32_16x16x32_bf16 v[136:139], a[160:163], a[136:139], 0\n"
"ds_read_b64_tr_b16 a[226:227], v7 offset:1024\n"
"v_add_u32 v5, 0x10800, v5\n"
"v_mfma_f32_16x16x32_bf16 v[140:143], a[160:163], a[140:143], 0\n"
"v_add_u32 v6, 0x10800, v6\n"
"v_mfma_f32_16x16x32_bf16 v[144:147], a[160:163], a[144:147], 0\n"
"ds_read_b128 a[192:195], v2 offset:0\n"
"v_mfma_f32_16x16x32_bf16 v[148:151], a[160:163], a[148:151], 0\n"
"v_mfma_f32_16x16x32_bf16 v[152:155], a[160:163], a[152:155], 0\n"
"ds_read_b128 a[196:199], v2 offset:2048\n"
"v_mfma_f32_16x16x32_bf16 v[156:159], a[160:163], a[156:159], 0\n"
"v_mfma_f32_16x16x32_bf16 v[160:163], a[164:167], a[128:131], 0\n"
"ds_read_b128 a[200:203], v2 offset:4096\n"
"v_mfma_f32_16x16x32_bf16 v[164:167], a[164:167], a[132:135], 0\n"
"v_mfma_f32_16x16x32_bf16 v[168:171], a[164:167], a[136:139], 0\n"
"ds_read_b128 a[204:207], v2 offset:6144\n"
"v_mfma_f32_16x16x32_bf16 v[172:175], a[164:167], a[140:143], 0\n"
"v_mfma_f32_16x16x32_bf16 v[176:179], a[164:167], a[144:147], 0\n"
"ds_read_b128 a[208:211], v2 offset:8192\n"
"v_mfma_f32_16x16x32_bf16 v[180:183], a[164:167], a[148:151], 0\n"
"v_mfma_f32_16x16x32_bf16 v[184:187], a[164:167], a[152:155], 0\n"
"ds_read_b128 a[212:215], v2 offset:10240\n"
"v_mfma_f32_16x16x32_bf16 v[188:191], a[164:167], a[156:159], 0\n"
"v_mfma_f32_16x16x32_bf16 v[192:195], a[168:171], a[128:131], 0\n"
"ds_read_b128 a[216:219], v2 offset:12288\n"
"v_mfma_f32_16x16x32_bf16 v[196:199], a[168:171], a[132:135], 0\n"
"v_mfma_f32_16x16x32_bf16 v[200:203], a[168:171], a[136:139], 0\n"
"ds_read_b128 a[220:223], v2 offset:14336\n"
"v_mfma_f32_16x16x32_bf16 v[204:207], a[168:171], a[140:143], 0\n"
"v_mfma_f32_16x16x32_bf16 v[208:211], a[168:171], a[144:147], 0\n"
"ds_read_b64_tr_b16 a[228:229], v8 offset:0\n"
"v_mfma_f32_16x16x32_bf16 v[212:215], a[168:171], a[148:151], 0\n"
"v_mfma_f32_16x16x32_bf16 v[216:219], a[168:171], a[152:155], 0\n"
"ds_read_b64_tr_b16 a[230:231], v8 offset:1024\n"
"v_mfma_f32_16x16x32_bf16 v[220:223], a[168:171], a[156:159], 0\n"
"v_mfma_f32_16x16x32_bf16 v[224:227], a[172:175], a[128:131], 0\n"
"ds_read_b64_tr_b16 a[232:233], v9 offset:0\n"
"v_mfma_f32_16x16x32_bf16 v[228:231], a[172:175], a[132:135], 0\n"
"v_mfma_f32_16x16x32_bf16 v[232:235], a[172:175], a[136:139], 0\n"
"ds_read_b64_tr_b16 a[234:235], v9 offset:1024\n"
"v_mfma_f32_16x16x32_bf16 v[236:239], a[172:175], a[140:143], 0\n"
"v_mfma_f32_16x16x32_bf16 v[240:243], a[172:175], a[144:147], 0\n"
"ds_read_b64_tr_b16 a[236:237], v10 offset:0\n"
"v_mfma_f32_16x16x32_bf16 v[244:247], a[172:175], a[148:151], 0\n"
"v_mfma_f32_16x16x32_bf16 v[248:251], a[172:175], a[152:155], 0\n"
"ds_read_b64_tr_b16 a[238:239], v10 offset:1024\n"
"v_mfma_f32_16x16x32_bf16 v[252:255], a[172:175], a[156:159], 0\n"
"v_mfma_f32_16x16x32_bf16 a[0:3], a[176:179], a[128:131], 0\n"
"ds_read_b64_tr_b16 a[240:241], v7 offset:128\n"
"v_mfma_f32_16x16x32_bf16 a[4:7], a[176:179], a[132:135], 0\n"
"v_mfma_f32_16x16x32_bf16 a[8:11], a[176:179], a[136:139], 0\n"
"ds_read_b64_tr_b16 a[242:243], v7 offset:1152\n"
"v_mfma_f32_16x16x32_bf16 a[12:15], a[176:179], a[140:143], 0\n"
"v_mfma_f32_16x16x32_bf16 a[16:19], a[176:179], a[144:147], 0\n"
"ds_read_b64_tr_b16 a[244:245], v8 offset:128\n"
"v_mfma_f32_16x16x32_bf16 a[20:23], a[176:179], a[148:151], 0\n"
"v_mfma_f32_16x16x32_bf16 a[24:27], a[176:179], a[152:155], 0\n"
"ds_read_b64_tr_b16 a[246:247], v8 offset:1152\n"
"v_mfma_f32_16x16x32_bf16 a[28:31], a[176:179], a[156:159], 0\n"
"v_mfma_f32_16x16x32_bf16 a[32:35], a[180:183], a[128:131], 0\n"
"ds_read_b64_tr_b16 a[248:249], v9 offset:128\n"
"v_mfma_f32_16x16x32_bf16 a[36:39], a[180:183], a[132:135], 0\n"
"v_mfma_f32_16x16x32_bf16 a[40:43], a[180:183], a[136:139], 0\n"
"ds_read_b64_tr_b16 a[250:251], v9 offset:1152\n"
"v_mfma_f32_16x16x32_bf16 a[44:47], a[180:183], a[140:143], 0\n"
"v_mfma_f32_16x16x32_bf16 a[48:51], a[180:183], a[144:147], 0\n"
"ds_read_b64_tr_b16 a[252:253], v10 offset:128\n"
"v_mfma_f32_16x16x32_bf16 a[52:55], a[180:183], a[148:151], 0\n"
"v_mfma_f32_16x16x32_bf16 a[56:59], a[180:183], a[152:155], 0\n"
"ds_read_b64_tr_b16 a[254:255], v10 offset:1152\n"
"v_mfma_f32_16x16x32_bf16 a[60:63], a[180:183], a[156:159], 0\n"
"v_mfma_f32_16x16x32_bf16 a[64:67], a[184:187], a[128:131], 0\n"
"v_mfma_f32_16x16x32_bf16 a[68:71], a[184:187], a[132:135], 0\n"
"v_mfma_f32_16x16x32_bf16 a[72:75], a[184:187], a[136:139], 0\n"
"v_mfma_f32_16x16x32_bf16 a[76:79], a[184:187], a[140:143], 0\n"
"v_mfma_f32_16x16x32_bf16 a[80:83], a[184:187], a[144:147], 0\n"
"v_mfma_f32_16x16x32_bf16 a[84:87], a[184:187], a[148:151], 0\n"
"v_mfma_f32_16x16x32_bf16 a[88:91], a[184:187], a[152:155], 0\n"
"v_mfma_f32_16x16x32_bf16 a[92:95], a[184:187], a[156:159], 0\n"
"v_mfma_f32_16x16x32_bf16 a[96:99], a[188:191], a[128:131], 0\n"
"v_mfma_f32_16x16x32_bf16 a[100:103], a[188:191], a[132:135], 0\n"
"v_mfma_f32_16x16x32_bf16 a[104:107], a[188:191], a[136:139], 0\n"
"v_mfma_f32_16x16x32_bf16 a[108:111], a[188:191], a[140:143], 0\n"
"v_mfma_f32_16x16x32_bf16 a[112:115], a[188:191], a[144:147], 0\n"
"v_mfma_f32_16x16x32_bf16 a[116:119], a[188:191], a[148:151], 0\n"
"v_mfma_f32_16x16x32_bf16 a[120:123], a[188:191], a[152:155], 0\n"
"v_mfma_f32_16x16x32_bf16 a[124:127], a[188:191], a[156:159], 0\n"
"s_waitcnt lgkmcnt(0)\n"
"v_mfma_f32_16x16x32_bf16 v[128:131], a[224:227], a[192:195], v[128:131]\n"
"s_waitcnt vmcnt(2)\n"
"s_barrier\n"
"v_mfma_f32_16x16x32_bf16 v[132:135], a[224:227], a[196:199], v[132:135]\n"
"s_add_u32 m0, s82, 0x0\n"
"ds_read_b64_tr_b16 a[160:161], v3 offset:0\n"
"v_mfma_f32_16x16x32_bf16 v[136:139], a[224:227], a[200:203], v[136:139]\n"
"buffer_load_dwordx4 v11, s[48:51], s68 offen lds\n"
"v_mfma_f32_16x16x32_bf16 v[140:143], a[224:227], a[204:207], v[140:143]\n"
"ds_read_b64_tr_b16 a[162:163], v3 offset:1024\n"
"v_add_u32 v2, 0x10800, v2\n"
"v_mfma_f32_16x16x32_bf16 v[144:147], a[224:227], a[208:211], v[144:147]\n"
"s_add_u32 m0, s82, 0x400\n"
"v_add_u32 v7, 0x10800, v7\n"
"v_mfma_f32_16x16x32_bf16 v[148:151], a[224:227], a[212:215], v[148:151]\n"
"ds_read_b128 a[128:131], v1 offset:0\n"
"v_add_u32 v8, 0x10800, v8\n"
"v_mfma_f32_16x16x32_bf16 v[152:155], a[224:227], a[216:219], v[152:155]\n"
"buffer_load_dwordx4 v12, s[48:51], s69 offen lds\n"
"v_mfma_f32_16x16x32_bf16 v[156:159], a[224:227], a[220:223], v[156:159]\n"
"ds_read_b128 a[132:135], v1 offset:2048\n"
"v_add_u32 v9, 0x10800, v9\n"
"v_mfma_f32_16x16x32_bf16 v[160:163], a[228:231], a[192:195], v[160:163]\n"
"s_add_u32 m0, s82, 0x800\n"
"v_add_u32 v10, 0x10800, v10\n"
"v_mfma_f32_16x16x32_bf16 v[164:167], a[228:231], a[196:199], v[164:167]\n"
"ds_read_b128 a[136:139], v1 offset:4096\n"
"v_mfma_f32_16x16x32_bf16 v[168:171], a[228:231], a[200:203], v[168:171]\n"
"buffer_load_dwordx4 v11, s[48:51], s70 offen lds\n"
"v_mfma_f32_16x16x32_bf16 v[172:175], a[228:231], a[204:207], v[172:175]\n"
"s_add_u32 m0, s82, 0xc00\n"
"ds_read_b128 a[140:143], v1 offset:6144\n"
"v_mfma_f32_16x16x32_bf16 v[176:179], a[228:231], a[208:211], v[176:179]\n"
"v_mfma_f32_16x16x32_bf16 v[180:183], a[228:231], a[212:215], v[180:183]\n"
"buffer_load_dwordx4 v12, s[48:51], s71 offen lds\n"
"v_mfma_f32_16x16x32_bf16 v[184:187], a[228:231], a[216:219], v[184:187]\n"
"ds_read_b128 a[144:147], v1 offset:8192\n"
"v_mfma_f32_16x16x32_bf16 v[188:191], a[228:231], a[220:223], v[188:191]\n"
"s_add_u32 m0, s82, 0x1000\n"
"ds_read_b128 a[148:151], v1 offset:10240\n"
"v_mfma_f32_16x16x32_bf16 v[192:195], a[232:235], a[192:195], v[192:195]\n"
"v_mfma_f32_16x16x32_bf16 v[196:199], a[232:235], a[196:199], v[196:199]\n"
"buffer_load_dwordx4 v11, s[48:51], s72 offen lds\n"
"v_mfma_f32_16x16x32_bf16 v[200:203], a[232:235], a[200:203], v[200:203]\n"
"ds_read_b128 a[152:155], v1 offset:12288\n"
"v_mfma_f32_16x16x32_bf16 v[204:207], a[232:235], a[204:207], v[204:207]\n"
"s_add_u32 m0, s82, 0x1400\n"
"ds_read_b128 a[156:159], v1 offset:14336\n"
"v_mfma_f32_16x16x32_bf16 v[208:211], a[232:235], a[208:211], v[208:211]\n"
"v_mfma_f32_16x16x32_bf16 v[212:215], a[232:235], a[212:215], v[212:215]\n"
"buffer_load_dwordx4 v12, s[48:51], s73 offen lds\n"
"v_mfma_f32_16x16x32_bf16 v[216:219], a[232:235], a[216:219], v[216:219]\n"
"s_add_u32 m0, s82, 0x1800\n"
"ds_read_b64_tr_b16 a[164:165], v4 offset:0\n"
"v_mfma_f32_16x16x32_bf16 v[220:223], a[232:235], a[220:223], v[220:223]\n"
"ds_read_b64_tr_b16 a[166:167], v4 offset:1024\n"
"v_mfma_f32_16x16x32_bf16 v[224:227], a[236:239], a[192:195], v[224:227]\n"
"buffer_load_dwordx4 v11, s[48:51], s74 offen lds\n"
"v_mfma_f32_16x16x32_bf16 v[228:231], a[236:239], a[196:199], v[228:231]\n"
"ds_read_b64_tr_b16 a[168:169], v5 offset:0\n"
"v_mfma_f32_16x16x32_bf16 v[232:235], a[236:239], a[200:203], v[232:235]\n"
"s_add_u32 m0, s82, 0x1c00\n"
"v_mfma_f32_16x16x32_bf16 v[236:239], a[236:239], a[204:207], v[236:239]\n"
"ds_read_b64_tr_b16 a[170:171], v5 offset:1024\n"
"v_mfma_f32_16x16x32_bf16 v[240:243], a[236:239], a[208:211], v[240:243]\n"
"buffer_load_dwordx4 v12, s[48:51], s75 offen lds\n"
"v_mfma_f32_16x16x32_bf16 v[244:247], a[236:239], a[212:215], v[244:247]\n"
"ds_read_b64_tr_b16 a[172:173], v6 offset:0\n"
"v_mfma_f32_16x16x32_bf16 v[248:251], a[236:239], a[216:219], v[248:251]\n"
"s_add_u32 m0, s83, 0x8000\n"
"v_mfma_f32_16x16x32_bf16 v[252:255], a[236:239], a[220:223], v[252:255]\n"
"buffer_load_dwordx4 v13, s[52:55], s76 offen lds\n"
"v_mfma_f32_16x16x32_bf16 a[0:3], a[240:243], a[192:195], a[0:3]\n"
"ds_read_b64_tr_b16 a[174:175], v6 offset:1024\n"
"v_mfma_f32_16x16x32_bf16 a[4:7], a[240:243], a[196:199], a[4:7]\n"
"s_add_u32 m0, s83, 0x8400\n"
"ds_read_b64_tr_b16 a[176:177], v3 offset:128\n"
"v_mfma_f32_16x16x32_bf16 a[8:11], a[240:243], a[200:203], a[8:11]\n"
"v_mfma_f32_16x16x32_bf16 a[12:15], a[240:243], a[204:207], a[12:15]\n"
"buffer_load_dwordx4 v13, s[52:55], s77 offen lds\n"
"v_mfma_f32_16x16x32_bf16 a[16:19], a[240:243], a[208:211], a[16:19]\n"
"ds_read_b64_tr_b16 a[178:179], v3 offset:1152\n"
"v_mfma_f32_16x16x32_bf16 a[20:23], a[240:243], a[212:215], a[20:23]\n"
"s_add_u32 m0, s83, 0x8880\n"
"ds_read_b64_tr_b16 a[180:181], v4 offset:128\n"
"v_mfma_f32_16x16x32_bf16 a[24:27], a[240:243], a[216:219], a[24:27]\n"
"v_mfma_f32_16x16x32_bf16 a[28:31], a[240:243], a[220:223], a[28:31]\n"
"buffer_load_dwordx4 v13, s[52:55], s78 offen lds\n"
"v_mfma_f32_16x16x32_bf16 a[32:35], a[244:247], a[192:195], a[32:35]\n"
"ds_read_b64_tr_b16 a[182:183], v4 offset:1152\n"
"v_mfma_f32_16x16x32_bf16 a[36:39], a[244:247], a[196:199], a[36:39]\n"
"s_add_u32 m0, s83, 0x8c80\n"
"ds_read_b64_tr_b16 a[184:185], v5 offset:128\n"
"v_mfma_f32_16x16x32_bf16 a[40:43], a[244:247], a[200:203], a[40:43]\n"
"buffer_load_dwordx4 v13, s[52:55], s79 offen lds\n"
"v_mfma_f32_16x16x32_bf16 a[44:47], a[244:247], a[204:207], a[44:47]\n"
"ds_read_b64_tr_b16 a[186:187], v5 offset:1152\n"
"v_mfma_f32_16x16x32_bf16 a[48:51], a[244:247], a[208:211], a[48:51]\n"
"s_add_u32 m0, s83, 0xc400\n"
"v_mfma_f32_16x16x32_bf16 a[52:55], a[244:247], a[212:215], a[52:55]\n"
"ds_read_b64_tr_b16 a[188:189], v6 offset:128\n"
"v_mfma_f32_16x16x32_bf16 a[56:59], a[244:247], a[216:219], a[56:59]\n"
"buffer_load_dwordx4 v13, s[56:59], s76 offen lds\n"
"v_mfma_f32_16x16x32_bf16 a[60:63], a[244:247], a[220:223], a[60:63]\n"
"ds_read_b64_tr_b16 a[190:191], v6 offset:1152\n"
"v_mfma_f32_16x16x32_bf16 a[64:67], a[248:251], a[192:195], a[64:67]\n"
"s_add_u32 m0, s83, 0xc800\n"
"v_mfma_f32_16x16x32_bf16 a[68:71], a[248:251], a[196:199], a[68:71]\n"
"v_mfma_f32_16x16x32_bf16 a[72:75], a[248:251], a[200:203], a[72:75]\n"
"buffer_load_dwordx4 v13, s[56:59], s77 offen lds\n"
"v_mfma_f32_16x16x32_bf16 a[76:79], a[248:251], a[204:207], a[76:79]\n"
"s_add_u32 m0, s83, 0xcc80\n"
"v_mfma_f32_16x16x32_bf16 a[80:83], a[248:251], a[208:211], a[80:83]\n"
"v_mfma_f32_16x16x32_bf16 a[84:87], a[248:251], a[212:215], a[84:87]\n"
"buffer_load_dwordx4 v13, s[56:59], s78 offen lds\n"
"v_mfma_f32_16x16x32_bf16 a[88:91], a[248:251], a[216:219], a[88:91]\n"
"v_mfma_f32_16x16x32_bf16 a[92:95], a[248:251], a[220:223], a[92:95]\n"
"s_add_u32 m0, s83, 0xd080\n"
"v_mfma_f32_16x16x32_bf16 a[96:99], a[252:255], a[192:195], a[96:99]\n"
"v_mfma_f32_16x16x32_bf16 a[100:103], a[252:255], a[196:199], a[100:103]\n"
"buffer_load_dwordx4 v13, s[56:59], s79 offen lds\n"
"v_mfma_f32_16x16x32_bf16 a[104:107], a[252:255], a[200:203], a[104:107]\n"
"v_mfma_f32_16x16x32_bf16 a[108:111], a[252:255], a[204:207], a[108:111]\n"
"buffer_load_dword v43, v42, s[60:63], 0 offen\n"
"v_mfma_f32_16x16x32_bf16 a[112:115], a[252:255], a[208:211], a[112:115]\n"
"v_add_u32 v11, 0x80, v11\n"
"v_add_u32 v12, 0x80, v12\n"
"v_add_u32 v13, 0x80000, v13\n"
"v_add_u32 v42, s84, v42\n"
"v_mfma_f32_16x16x32_bf16 a[116:119], a[252:255], a[212:215], a[116:119]\n"
"v_mfma_f32_16x16x32_bf16 a[120:123], a[252:255], a[216:219], a[120:123]\n"
"v_mfma_f32_16x16x32_bf16 a[124:127], a[252:255], a[220:223], a[124:127]\n"
"s_waitcnt lgkmcnt(0)\n"
"v_mfma_f32_16x16x32_bf16 v[128:131], a[160:163], a[128:131], v[128:131]\n"
"ds_read_b64_tr_b16 a[224:225], v7 offset:0\n"
"v_add_u32 v1, 0xfffef800, v1\n"
"v_mfma_f32_16x16x32_bf16 v[132:135], a[160:163], a[132:135], v[132:135]\n"
"v_add_u32 v3, 0xfffef800, v3\n"
"v_add_u32 v4, 0xfffef800, v4\n"
"v_mfma_f32_16x16x32_bf16 v[136:139], a[160:163], a[136:139], v[136:139]\n"
"s_mov_b32 s90, 0x400000\n"
"ds_read_b64_tr_b16 a[226:227], v7 offset:1024\n"
"v_mfma_f32_16x16x32_bf16 v[140:143], a[160:163], a[140:143], v[140:143]\n"
"v_add_u32 v5, 0xfffef800, v5\n"
"v_add_u32 v6, 0xfffef800, v6\n"
"v_mfma_f32_16x16x32_bf16 v[144:147], a[160:163], a[144:147], v[144:147]\n"
"ds_read_b128 a[192:195], v2 offset:0\n"
"v_mfma_f32_16x16x32_bf16 v[148:151], a[160:163], a[148:151], v[148:151]\n"
"s_mov_b32 s46, 16384\n"
"v_mfma_f32_16x16x32_bf16 v[152:155], a[160:163], a[152:155], v[152:155]\n"
"ds_read_b128 a[196:199], v2 offset:2048\n"
"v_mfma_f32_16x16x32_bf16 v[156:159], a[160:163], a[156:159], v[156:159]\n"
"v_mfma_f32_16x16x32_bf16 v[160:163], a[164:167], a[128:131], v[160:163]\n"
"s_cmp_eq_u32 s34, 0\n"
"ds_read_b128 a[200:203], v2 offset:4096\n"
"v_mfma_f32_16x16x32_bf16 v[164:167], a[164:167], a[132:135], v[164:167]\n"
"v_mfma_f32_16x16x32_bf16 v[168:171], a[164:167], a[136:139], v[168:171]\n"
"ds_read_b128 a[204:207], v2 offset:6144\n"
"v_mfma_f32_16x16x32_bf16 v[172:175], a[164:167], a[140:143], v[172:175]\n"
"s_cselect_b32 s88, s8, s12\n"
"v_mfma_f32_16x16x32_bf16 v[176:179], a[164:167], a[144:147], v[176:179]\n"
"ds_read_b128 a[208:211], v2 offset:8192\n"
"v_mfma_f32_16x16x32_bf16 v[180:183], a[164:167], a[148:151], v[180:183]\n"
"v_mfma_f32_16x16x32_bf16 v[184:187], a[164:167], a[152:155], v[184:187]\n"
"s_cselect_b32 s89, s9, s13\n"
"ds_read_b128 a[212:215], v2 offset:10240\n"
"v_mfma_f32_16x16x32_bf16 v[188:191], a[164:167], a[156:159], v[188:191]\n"
"v_mfma_f32_16x16x32_bf16 v[192:195], a[168:171], a[128:131], v[192:195]\n"
"ds_read_b128 a[216:219], v2 offset:12288\n"
"v_mfma_f32_16x16x32_bf16 v[196:199], a[168:171], a[132:135], v[196:199]\n"
"s_cselect_b32 s90, 0x200000, s90\n"
"v_mfma_f32_16x16x32_bf16 v[200:203], a[168:171], a[136:139], v[200:203]\n"
"ds_read_b128 a[220:223], v2 offset:14336\n"
"v_mfma_f32_16x16x32_bf16 v[204:207], a[168:171], a[140:143], v[204:207]\n"
"v_mfma_f32_16x16x32_bf16 v[208:211], a[168:171], a[144:147], v[208:211]\n"
"s_cselect_b32 s46, 8192, s46\n"
"ds_read_b64_tr_b16 a[228:229], v8 offset:0\n"
"v_mfma_f32_16x16x32_bf16 v[212:215], a[168:171], a[148:151], v[212:215]\n"
"v_mfma_f32_16x16x32_bf16 v[216:219], a[168:171], a[152:155], v[216:219]\n"
"ds_read_b64_tr_b16 a[230:231], v8 offset:1024\n"
"v_mfma_f32_16x16x32_bf16 v[220:223], a[168:171], a[156:159], v[220:223]\n"
"s_cmp_eq_u32 s34, 3\n"
"v_mfma_f32_16x16x32_bf16 v[224:227], a[172:175], a[128:131], v[224:227]\n"
"ds_read_b64_tr_b16 a[232:233], v9 offset:0\n"
"v_mfma_f32_16x16x32_bf16 v[228:231], a[172:175], a[132:135], v[228:231]\n"
"v_mfma_f32_16x16x32_bf16 v[232:235], a[172:175], a[136:139], v[232:235]\n"
"s_cselect_b32 s88, s20, s88\n"
"ds_read_b64_tr_b16 a[234:235], v9 offset:1024\n"
"v_mfma_f32_16x16x32_bf16 v[236:239], a[172:175], a[140:143], v[236:239]\n"
"v_mfma_f32_16x16x32_bf16 v[240:243], a[172:175], a[144:147], v[240:243]\n"
"ds_read_b64_tr_b16 a[236:237], v10 offset:0\n"
"v_mfma_f32_16x16x32_bf16 v[244:247], a[172:175], a[148:151], v[244:247]\n"
"s_cselect_b32 s89, s21, s89\n"
"v_mfma_f32_16x16x32_bf16 v[248:251], a[172:175], a[152:155], v[248:251]\n"
"ds_read_b64_tr_b16 a[238:239], v10 offset:1024\n"
"v_mfma_f32_16x16x32_bf16 v[252:255], a[172:175], a[156:159], v[252:255]\n"
"v_mfma_f32_16x16x32_bf16 a[0:3], a[176:179], a[128:131], a[0:3]\n"
"ds_read_b64_tr_b16 a[240:241], v7 offset:128\n"
"v_mfma_f32_16x16x32_bf16 a[4:7], a[176:179], a[132:135], a[4:7]\n"
"s_cselect_b32 s90, 0x1c0000, s90\n"
"v_mfma_f32_16x16x32_bf16 a[8:11], a[176:179], a[136:139], a[8:11]\n"
"ds_read_b64_tr_b16 a[242:243], v7 offset:1152\n"
"v_mfma_f32_16x16x32_bf16 a[12:15], a[176:179], a[140:143], a[12:15]\n"
"v_mfma_f32_16x16x32_bf16 a[16:19], a[176:179], a[144:147], a[16:19]\n"
"s_cselect_b32 s46, 7168, s46\n"
"ds_read_b64_tr_b16 a[244:245], v8 offset:128\n"
"v_mfma_f32_16x16x32_bf16 a[20:23], a[176:179], a[148:151], a[20:23]\n"
"v_mfma_f32_16x16x32_bf16 a[24:27], a[176:179], a[152:155], a[24:27]\n"
"ds_read_b64_tr_b16 a[246:247], v8 offset:1152\n"
"v_mfma_f32_16x16x32_bf16 a[28:31], a[176:179], a[156:159], a[28:31]\n"
"s_and_b32 s89, s89, 0xffff\n"
"v_mfma_f32_16x16x32_bf16 a[32:35], a[180:183], a[128:131], a[32:35]\n"
"ds_read_b64_tr_b16 a[248:249], v9 offset:128\n"
"v_mfma_f32_16x16x32_bf16 a[36:39], a[180:183], a[132:135], a[36:39]\n"
"v_mfma_f32_16x16x32_bf16 a[40:43], a[180:183], a[136:139], a[40:43]\n"
"s_mov_b32 s91, 0x20000\n"
"ds_read_b64_tr_b16 a[250:251], v9 offset:1152\n"
"v_mfma_f32_16x16x32_bf16 a[44:47], a[180:183], a[140:143], a[44:47]\n"
"v_mfma_f32_16x16x32_bf16 a[48:51], a[180:183], a[144:147], a[48:51]\n"
"ds_read_b64_tr_b16 a[252:253], v10 offset:128\n"
"v_mfma_f32_16x16x32_bf16 a[52:55], a[180:183], a[148:151], a[52:55]\n"
"s_mul_i32 s46, s30, s46\n"
"v_mfma_f32_16x16x32_bf16 a[56:59], a[180:183], a[152:155], a[56:59]\n"
"ds_read_b64_tr_b16 a[254:255], v10 offset:1152\n"
"v_mfma_f32_16x16x32_bf16 a[60:63], a[180:183], a[156:159], a[60:63]\n"
"v_mfma_f32_16x16x32_bf16 a[64:67], a[184:187], a[128:131], a[64:67]\n"
"s_cmp_eq_u32 s34, 2\n"
"v_mfma_f32_16x16x32_bf16 a[68:71], a[184:187], a[132:135], a[68:71]\n"
"v_mfma_f32_16x16x32_bf16 a[72:75], a[184:187], a[136:139], a[72:75]\n"
"v_mfma_f32_16x16x32_bf16 a[76:79], a[184:187], a[140:143], a[76:79]\n"
"s_cselect_b32 s45, 8192, 0\n"
"v_mfma_f32_16x16x32_bf16 a[80:83], a[184:187], a[144:147], a[80:83]\n"
"v_mfma_f32_16x16x32_bf16 a[84:87], a[184:187], a[148:151], a[84:87]\n"
"v_mfma_f32_16x16x32_bf16 a[88:91], a[184:187], a[152:155], a[88:91]\n"
"s_add_u32 s46, s46, s45\n"
"v_mfma_f32_16x16x32_bf16 a[92:95], a[184:187], a[156:159], a[92:95]\n"
"v_mfma_f32_16x16x32_bf16 a[96:99], a[188:191], a[128:131], a[96:99]\n"
"v_mfma_f32_16x16x32_bf16 a[100:103], a[188:191], a[132:135], a[100:103]\n"
"buffer_load_dword v63, v62, s[88:91], s46 offen\n"
"v_mfma_f32_16x16x32_bf16 a[104:107], a[188:191], a[136:139], a[104:107]\n"
"v_mfma_f32_16x16x32_bf16 a[108:111], a[188:191], a[140:143], a[108:111]\n"
"v_mfma_f32_16x16x32_bf16 a[112:115], a[188:191], a[144:147], a[112:115]\n"
"v_mfma_f32_16x16x32_bf16 a[116:119], a[188:191], a[148:151], a[116:119]\n"
"v_mfma_f32_16x16x32_bf16 a[120:123], a[188:191], a[152:155], a[120:123]\n"
"v_mfma_f32_16x16x32_bf16 a[124:127], a[188:191], a[156:159], a[124:127]\n"
"s_waitcnt lgkmcnt(0)\n"
"v_mfma_f32_16x16x32_bf16 v[128:131], a[224:227], a[192:195], v[128:131]\n"
"s_waitcnt vmcnt(2)\n"
"s_barrier\n"
"v_mfma_f32_16x16x32_bf16 v[132:135], a[224:227], a[196:199], v[132:135]\n"
"s_add_u32 m0, s82, 0x10800\n"
"ds_read_b64_tr_b16 a[160:161], v3 offset:0\n"
"v_mfma_f32_16x16x32_bf16 v[136:139], a[224:227], a[200:203], v[136:139]\n"
"buffer_load_dwordx4 v11, s[48:51], s68 offen lds\n"
"v_mfma_f32_16x16x32_bf16 v[140:143], a[224:227], a[204:207], v[140:143]\n"
"ds_read_b64_tr_b16 a[162:163], v3 offset:1024\n"
"v_add_u32 v2, 0xfffef800, v2\n"
"v_mfma_f32_16x16x32_bf16 v[144:147], a[224:227], a[208:211], v[144:147]\n"
"s_add_u32 m0, s82, 0x10c00\n"
"v_add_u32 v7, 0xfffef800, v7\n"
"v_mfma_f32_16x16x32_bf16 v[148:151], a[224:227], a[212:215], v[148:151]\n"
"ds_read_b128 a[128:131], v1 offset:0\n"
"v_add_u32 v8, 0xfffef800, v8\n"
"v_mfma_f32_16x16x32_bf16 v[152:155], a[224:227], a[216:219], v[152:155]\n"
"buffer_load_dwordx4 v12, s[48:51], s69 offen lds\n"
"v_mfma_f32_16x16x32_bf16 v[156:159], a[224:227], a[220:223], v[156:159]\n"
"ds_read_b128 a[132:135], v1 offset:2048\n"
"v_add_u32 v9, 0xfffef800, v9\n"
"v_mfma_f32_16x16x32_bf16 v[160:163], a[228:231], a[192:195], v[160:163]\n"
"s_add_u32 m0, s82, 0x11000\n"
"v_add_u32 v10, 0xfffef800, v10\n"
"v_mfma_f32_16x16x32_bf16 v[164:167], a[228:231], a[196:199], v[164:167]\n"
"ds_read_b128 a[136:139], v1 offset:4096\n"
"v_mfma_f32_16x16x32_bf16 v[168:171], a[228:231], a[200:203], v[168:171]\n"
"buffer_load_dwordx4 v11, s[48:51], s70 offen lds\n"
"v_mfma_f32_16x16x32_bf16 v[172:175], a[228:231], a[204:207], v[172:175]\n"
"s_add_u32 m0, s82, 0x11400\n"
"ds_read_b128 a[140:143], v1 offset:6144\n"
"v_mfma_f32_16x16x32_bf16 v[176:179], a[228:231], a[208:211], v[176:179]\n"
"v_mfma_f32_16x16x32_bf16 v[180:183], a[228:231], a[212:215], v[180:183]\n"
"buffer_load_dwordx4 v12, s[48:51], s71 offen lds\n"
"v_mfma_f32_16x16x32_bf16 v[184:187], a[228:231], a[216:219], v[184:187]\n"
"ds_read_b128 a[144:147], v1 offset:8192\n"
"v_mfma_f32_16x16x32_bf16 v[188:191], a[228:231], a[220:223], v[188:191]\n"
"s_add_u32 m0, s82, 0x11800\n"
"ds_read_b128 a[148:151], v1 offset:10240\n"
"v_mfma_f32_16x16x32_bf16 v[192:195], a[232:235], a[192:195], v[192:195]\n"
"v_mfma_f32_16x16x32_bf16 v[196:199], a[232:235], a[196:199], v[196:199]\n"
"buffer_load_dwordx4 v11, s[48:51], s72 offen lds\n"
"v_mfma_f32_16x16x32_bf16 v[200:203], a[232:235], a[200:203], v[200:203]\n"
"ds_read_b128 a[152:155], v1 offset:12288\n"
"v_mfma_f32_16x16x32_bf16 v[204:207], a[232:235], a[204:207], v[204:207]\n"
"s_add_u32 m0, s82, 0x11c00\n"
"ds_read_b128 a[156:159], v1 offset:14336\n"
"v_mfma_f32_16x16x32_bf16 v[208:211], a[232:235], a[208:211], v[208:211]\n"
"v_mfma_f32_16x16x32_bf16 v[212:215], a[232:235], a[212:215], v[212:215]\n"
"buffer_load_dwordx4 v12, s[48:51], s73 offen lds\n"
"v_mfma_f32_16x16x32_bf16 v[216:219], a[232:235], a[216:219], v[216:219]\n"
"s_add_u32 m0, s82, 0x12000\n"
"ds_read_b64_tr_b16 a[164:165], v4 offset:0\n"
"v_mfma_f32_16x16x32_bf16 v[220:223], a[232:235], a[220:223], v[220:223]\n"
"ds_read_b64_tr_b16 a[166:167], v4 offset:1024\n"
"v_mfma_f32_16x16x32_bf16 v[224:227], a[236:239], a[192:195], v[224:227]\n"
"buffer_load_dwordx4 v11, s[48:51], s74 offen lds\n"
"v_mfma_f32_16x16x32_bf16 v[228:231], a[236:239], a[196:199], v[228:231]\n"
"ds_read_b64_tr_b16 a[168:169], v5 offset:0\n"
"v_mfma_f32_16x16x32_bf16 v[232:235], a[236:239], a[200:203], v[232:235]\n"
"s_add_u32 m0, s82, 0x12400\n"
"v_mfma_f32_16x16x32_bf16 v[236:239], a[236:239], a[204:207], v[236:239]\n"
"ds_read_b64_tr_b16 a[170:171], v5 offset:1024\n"
"v_mfma_f32_16x16x32_bf16 v[240:243], a[236:239], a[208:211], v[240:243]\n"
"buffer_load_dwordx4 v12, s[48:51], s75 offen lds\n"
"v_mfma_f32_16x16x32_bf16 v[244:247], a[236:239], a[212:215], v[244:247]\n"
"ds_read_b64_tr_b16 a[172:173], v6 offset:0\n"
"v_mfma_f32_16x16x32_bf16 v[248:251], a[236:239], a[216:219], v[248:251]\n"
"s_add_u32 m0, s83, 0x18800\n"
"v_mfma_f32_16x16x32_bf16 v[252:255], a[236:239], a[220:223], v[252:255]\n"
"buffer_load_dwordx4 v13, s[52:55], s76 offen lds\n"
"v_mfma_f32_16x16x32_bf16 a[0:3], a[240:243], a[192:195], a[0:3]\n"
"ds_read_b64_tr_b16 a[174:175], v6 offset:1024\n"
"v_mfma_f32_16x16x32_bf16 a[4:7], a[240:243], a[196:199], a[4:7]\n"
"s_add_u32 m0, s83, 0x18c00\n"
"ds_read_b64_tr_b16 a[176:177], v3 offset:128\n"
"v_mfma_f32_16x16x32_bf16 a[8:11], a[240:243], a[200:203], a[8:11]\n"
"v_mfma_f32_16x16x32_bf16 a[12:15], a[240:243], a[204:207], a[12:15]\n"
"buffer_load_dwordx4 v13, s[52:55], s77 offen lds\n"
"v_mfma_f32_16x16x32_bf16 a[16:19], a[240:243], a[208:211], a[16:19]\n"
"ds_read_b64_tr_b16 a[178:179], v3 offset:1152\n"
"v_mfma_f32_16x16x32_bf16 a[20:23], a[240:243], a[212:215], a[20:23]\n"
"s_add_u32 m0, s83, 0x19080\n"
"ds_read_b64_tr_b16 a[180:181], v4 offset:128\n"
"v_mfma_f32_16x16x32_bf16 a[24:27], a[240:243], a[216:219], a[24:27]\n"
"v_mfma_f32_16x16x32_bf16 a[28:31], a[240:243], a[220:223], a[28:31]\n"
"buffer_load_dwordx4 v13, s[52:55], s78 offen lds\n"
"v_mfma_f32_16x16x32_bf16 a[32:35], a[244:247], a[192:195], a[32:35]\n"
"ds_read_b64_tr_b16 a[182:183], v4 offset:1152\n"
"v_mfma_f32_16x16x32_bf16 a[36:39], a[244:247], a[196:199], a[36:39]\n"
"s_add_u32 m0, s83, 0x19480\n"
"ds_read_b64_tr_b16 a[184:185], v5 offset:128\n"
"v_mfma_f32_16x16x32_bf16 a[40:43], a[244:247], a[200:203], a[40:43]\n"
"buffer_load_dwordx4 v13, s[52:55], s79 offen lds\n"
"v_mfma_f32_16x16x32_bf16 a[44:47], a[244:247], a[204:207], a[44:47]\n"
"ds_read_b64_tr_b16 a[186:187], v5 offset:1152\n"
"v_mfma_f32_16x16x32_bf16 a[48:51], a[244:247], a[208:211], a[48:51]\n"
"s_add_u32 m0, s83, 0x1cc00\n"
"v_mfma_f32_16x16x32_bf16 a[52:55], a[244:247], a[212:215], a[52:55]\n"
"ds_read_b64_tr_b16 a[188:189], v6 offset:128\n"
"v_mfma_f32_16x16x32_bf16 a[56:59], a[244:247], a[216:219], a[56:59]\n"
"buffer_load_dwordx4 v13, s[56:59], s76 offen lds\n"
"v_mfma_f32_16x16x32_bf16 a[60:63], a[244:247], a[220:223], a[60:63]\n"
"ds_read_b64_tr_b16 a[190:191], v6 offset:1152\n"
"v_mfma_f32_16x16x32_bf16 a[64:67], a[248:251], a[192:195], a[64:67]\n"
"s_add_u32 m0, s83, 0x1d000\n"
"v_mfma_f32_16x16x32_bf16 a[68:71], a[248:251], a[196:199], a[68:71]\n"
"v_mfma_f32_16x16x32_bf16 a[72:75], a[248:251], a[200:203], a[72:75]\n"
"buffer_load_dwordx4 v13, s[56:59], s77 offen lds\n"
"v_mfma_f32_16x16x32_bf16 a[76:79], a[248:251], a[204:207], a[76:79]\n"
"s_add_u32 m0, s83, 0x1d480\n"
"v_mfma_f32_16x16x32_bf16 a[80:83], a[248:251], a[208:211], a[80:83]\n"
"v_mfma_f32_16x16x32_bf16 a[84:87], a[248:251], a[212:215], a[84:87]\n"
"buffer_load_dwordx4 v13, s[56:59], s78 offen lds\n"
"v_mfma_f32_16x16x32_bf16 a[88:91], a[248:251], a[216:219], a[88:91]\n"
"v_mfma_f32_16x16x32_bf16 a[92:95], a[248:251], a[220:223], a[92:95]\n"
"s_add_u32 m0, s83, 0x1d880\n"
"v_mfma_f32_16x16x32_bf16 a[96:99], a[252:255], a[192:195], a[96:99]\n"
"v_mfma_f32_16x16x32_bf16 a[100:103], a[252:255], a[196:199], a[100:103]\n"
"buffer_load_dwordx4 v13, s[56:59], s79 offen lds\n"
"v_mfma_f32_16x16x32_bf16 a[104:107], a[252:255], a[200:203], a[104:107]\n"
"v_mfma_f32_16x16x32_bf16 a[108:111], a[252:255], a[204:207], a[108:111]\n"
"buffer_load_dword v17, v42, s[60:63], 0 offen\n"
"v_mfma_f32_16x16x32_bf16 a[112:115], a[252:255], a[208:211], a[112:115]\n"
"v_add_u32 v11, 0x80, v11\n"
"v_add_u32 v12, 0x80, v12\n"
"v_add_u32 v13, 0x80000, v13\n"
"v_add_u32 v42, s84, v42\n"
"v_mfma_f32_16x16x32_bf16 a[116:119], a[252:255], a[212:215], a[116:119]\n"
"v_mfma_f32_16x16x32_bf16 a[120:123], a[252:255], a[216:219], a[120:123]\n"
"v_mfma_f32_16x16x32_bf16 a[124:127], a[252:255], a[220:223], a[124:127]\n"
"s_mov_b32 s85, -2\n"
"LM_kloop:\n"
"s_waitcnt lgkmcnt(0)\n"
"v_mfma_f32_16x16x32_bf16 v[128:131], a[160:163], a[128:131], v[128:131]\n"
"ds_read_b64_tr_b16 a[224:225], v7 offset:0\n"
"v_add_u32 v1, 0x10800, v1\n"
"v_mfma_f32_16x16x32_bf16 v[132:135], a[160:163], a[132:135], v[132:135]\n"
"v_add_u32 v3, 0x10800, v3\n"
"v_add_u32 v4, 0x10800, v4\n"
"v_mfma_f32_16x16x32_bf16 v[136:139], a[160:163], a[136:139], v[136:139]\n"
"ds_read_b64_tr_b16 a[226:227], v7 offset:1024\n"
"v_add_u32 v5, 0x10800, v5\n"
"v_mfma_f32_16x16x32_bf16 v[140:143], a[160:163], a[140:143], v[140:143]\n"
"v_add_u32 v6, 0x10800, v6\n"
"v_mfma_f32_16x16x32_bf16 v[144:147], a[160:163], a[144:147], v[144:147]\n"
"ds_read_b128 a[192:195], v2 offset:0\n"
"v_mfma_f32_16x16x32_bf16 v[148:151], a[160:163], a[148:151], v[148:151]\n"
"v_mfma_f32_16x16x32_bf16 v[152:155], a[160:163], a[152:155], v[152:155]\n"
"ds_read_b128 a[196:199], v2 offset:2048\n"
"v_mfma_f32_16x16x32_bf16 v[156:159], a[160:163], a[156:159], v[156:159]\n"
"v_mfma_f32_16x16x32_bf16 v[160:163], a[164:167], a[128:131], v[160:163]\n"
"ds_read_b128 a[200:203], v2 offset:4096\n"
"v_mfma_f32_16x16x32_bf16 v[164:167], a[164:167], a[132:135], v[164:167]\n"
"v_mfma_f32_16x16x32_bf16 v[168:171], a[164:167], a[136:139], v[168:171]\n"
"ds_read_b128 a[204:207], v2 offset:6144\n"
"v_mfma_f32_16x16x32_bf16 v[172:175], a[164:167], a[140:143], v[172:175]\n"
"v_mfma_f32_16x16x32_bf16 v[176:179], a[164:167], a[144:147], v[176:179]\n"
"ds_read_b128 a[208:211], v2 offset:8192\n"
"v_mfma_f32_16x16x32_bf16 v[180:183], a[164:167], a[148:151], v[180:183]\n"
"v_mfma_f32_16x16x32_bf16 v[184:187], a[164:167], a[152:155], v[184:187]\n"
"ds_read_b128 a[212:215], v2 offset:10240\n"
"v_mfma_f32_16x16x32_bf16 v[188:191], a[164:167], a[156:159], v[188:191]\n"
"v_mfma_f32_16x16x32_bf16 v[192:195], a[168:171], a[128:131], v[192:195]\n"
"ds_read_b128 a[216:219], v2 offset:12288\n"
"v_mfma_f32_16x16x32_bf16 v[196:199], a[168:171], a[132:135], v[196:199]\n"
"v_mfma_f32_16x16x32_bf16 v[200:203], a[168:171], a[136:139], v[200:203]\n"
"ds_read_b128 a[220:223], v2 offset:14336\n"
"v_mfma_f32_16x16x32_bf16 v[204:207], a[168:171], a[140:143], v[204:207]\n"
"v_mfma_f32_16x16x32_bf16 v[208:211], a[168:171], a[144:147], v[208:211]\n"
"ds_read_b64_tr_b16 a[228:229], v8 offset:0\n"
"v_mfma_f32_16x16x32_bf16 v[212:215], a[168:171], a[148:151], v[212:215]\n"
"v_mfma_f32_16x16x32_bf16 v[216:219], a[168:171], a[152:155], v[216:219]\n"
"ds_read_b64_tr_b16 a[230:231], v8 offset:1024\n"
"v_mfma_f32_16x16x32_bf16 v[220:223], a[168:171], a[156:159], v[220:223]\n"
"v_mfma_f32_16x16x32_bf16 v[224:227], a[172:175], a[128:131], v[224:227]\n"
"ds_read_b64_tr_b16 a[232:233], v9 offset:0\n"
"v_mfma_f32_16x16x32_bf16 v[228:231], a[172:175], a[132:135], v[228:231]\n"
"v_mfma_f32_16x16x32_bf16 v[232:235], a[172:175], a[136:139], v[232:235]\n"
"ds_read_b64_tr_b16 a[234:235], v9 offset:1024\n"
"v_mfma_f32_16x16x32_bf16 v[236:239], a[172:175], a[140:143], v[236:239]\n"
"v_mfma_f32_16x16x32_bf16 v[240:243], a[172:175], a[144:147], v[240:243]\n"
"ds_read_b64_tr_b16 a[236:237], v10 offset:0\n"
"v_mfma_f32_16x16x32_bf16 v[244:247], a[172:175], a[148:151], v[244:247]\n"
"v_mfma_f32_16x16x32_bf16 v[248:251], a[172:175], a[152:155], v[248:251]\n"
"ds_read_b64_tr_b16 a[238:239], v10 offset:1024\n"
"v_mfma_f32_16x16x32_bf16 v[252:255], a[172:175], a[156:159], v[252:255]\n"
"v_mfma_f32_16x16x32_bf16 a[0:3], a[176:179], a[128:131], a[0:3]\n"
"ds_read_b64_tr_b16 a[240:241], v7 offset:128\n"
"v_mfma_f32_16x16x32_bf16 a[4:7], a[176:179], a[132:135], a[4:7]\n"
"v_mfma_f32_16x16x32_bf16 a[8:11], a[176:179], a[136:139], a[8:11]\n"
"ds_read_b64_tr_b16 a[242:243], v7 offset:1152\n"
"v_mfma_f32_16x16x32_bf16 a[12:15], a[176:179], a[140:143], a[12:15]\n"
"v_mfma_f32_16x16x32_bf16 a[16:19], a[176:179], a[144:147], a[16:19]\n"
"ds_read_b64_tr_b16 a[244:245], v8 offset:128\n"
"v_mfma_f32_16x16x32_bf16 a[20:23], a[176:179], a[148:151], a[20:23]\n"
"v_mfma_f32_16x16x32_bf16 a[24:27], a[176:179], a[152:155], a[24:27]\n"
"ds_read_b64_tr_b16 a[246:247], v8 offset:1152\n"
"v_mfma_f32_16x16x32_bf16 a[28:31], a[176:179], a[156:159], a[28:31]\n"
"v_mfma_f32_16x16x32_bf16 a[32:35], a[180:183], a[128:131], a[32:35]\n"
"ds_read_b64_tr_b16 a[248:249], v9 offset:128\n"
"v_mfma_f32_16x16x32_bf16 a[36:39], a[180:183], a[132:135], a[36:39]\n"
"v_mfma_f32_16x16x32_bf16 a[40:43], a[180:183], a[136:139], a[40:43]\n"
"ds_read_b64_tr_b16 a[250:251], v9 offset:1152\n"
"v_mfma_f32_16x16x32_bf16 a[44:47], a[180:183], a[140:143], a[44:47]\n"
"v_mfma_f32_16x16x32_bf16 a[48:51], a[180:183], a[144:147], a[48:51]\n"
"ds_read_b64_tr_b16 a[252:253], v10 offset:128\n"
"v_mfma_f32_16x16x32_bf16 a[52:55], a[180:183], a[148:151], a[52:55]\n"
"v_mfma_f32_16x16x32_bf16 a[56:59], a[180:183], a[152:155], a[56:59]\n"
"ds_read_b64_tr_b16 a[254:255], v10 offset:1152\n"
"v_mfma_f32_16x16x32_bf16 a[60:63], a[180:183], a[156:159], a[60:63]\n"
"v_mfma_f32_16x16x32_bf16 a[64:67], a[184:187], a[128:131], a[64:67]\n"
"v_mfma_f32_16x16x32_bf16 a[68:71], a[184:187], a[132:135], a[68:71]\n"
"v_mfma_f32_16x16x32_bf16 a[72:75], a[184:187], a[136:139], a[72:75]\n"
"v_mfma_f32_16x16x32_bf16 a[76:79], a[184:187], a[140:143], a[76:79]\n"
"v_mfma_f32_16x16x32_bf16 a[80:83], a[184:187], a[144:147], a[80:83]\n"
"v_mfma_f32_16x16x32_bf16 a[84:87], a[184:187], a[148:151], a[84:87]\n"
"v_mfma_f32_16x16x32_bf16 a[88:91], a[184:187], a[152:155], a[88:91]\n"
"v_mfma_f32_16x16x32_bf16 a[92:95], a[184:187], a[156:159], a[92:95]\n"
"v_mfma_f32_16x16x32_bf16 a[96:99], a[188:191], a[128:131], a[96:99]\n"
"v_mfma_f32_16x16x32_bf16 a[100:103], a[188:191], a[132:135], a[100:103]\n"
"v_mfma_f32_16x16x32_bf16 a[104:107], a[188:191], a[136:139], a[104:107]\n"
"v_mfma_f32_16x16x32_bf16 a[108:111], a[188:191], a[140:143], a[108:111]\n"
"v_mfma_f32_16x16x32_bf16 a[112:115], a[188:191], a[144:147], a[112:115]\n"
"v_mfma_f32_16x16x32_bf16 a[116:119], a[188:191], a[148:151], a[116:119]\n"
"v_mfma_f32_16x16x32_bf16 a[120:123], a[188:191], a[152:155], a[120:123]\n"
"v_mfma_f32_16x16x32_bf16 a[124:127], a[188:191], a[156:159], a[124:127]\n"
"s_waitcnt lgkmcnt(0)\n"
"v_mfma_f32_16x16x32_bf16 v[128:131], a[224:227], a[192:195], v[128:131]\n"
"s_waitcnt vmcnt(1)\n"
"s_barrier\n"
"v_mfma_f32_16x16x32_bf16 v[132:135], a[224:227], a[196:199], v[132:135]\n"
"s_add_u32 m0, s82, 0x0\n"
"ds_read_b64_tr_b16 a[160:161], v3 offset:0\n"
"v_mfma_f32_16x16x32_bf16 v[136:139], a[224:227], a[200:203], v[136:139]\n"
"buffer_load_dwordx4 v11, s[48:51], s68 offen lds\n"
"v_mfma_f32_16x16x32_bf16 v[140:143], a[224:227], a[204:207], v[140:143]\n"
"ds_read_b64_tr_b16 a[162:163], v3 offset:1024\n"
"v_add_u32 v2, 0x10800, v2\n"
"v_mfma_f32_16x16x32_bf16 v[144:147], a[224:227], a[208:211], v[144:147]\n"
"s_add_u32 m0, s82, 0x400\n"
"v_add_u32 v7, 0x10800, v7\n"
"v_mfma_f32_16x16x32_bf16 v[148:151], a[224:227], a[212:215], v[148:151]\n"
"ds_read_b128 a[128:131], v1 offset:0\n"
"v_add_u32 v8, 0x10800, v8\n"
"v_mfma_f32_16x16x32_bf16 v[152:155], a[224:227], a[216:219], v[152:155]\n"
"buffer_load_dwordx4 v12, s[48:51], s69 offen lds\n"
"v_mfma_f32_16x16x32_bf16 v[156:159], a[224:227], a[220:223], v[156:159]\n"
"ds_read_b128 a[132:135], v1 offset:2048\n"
"v_add_u32 v9, 0x10800, v9\n"
"v_mfma_f32_16x16x32_bf16 v[160:163], a[228:231], a[192:195], v[160:163]\n"
"s_add_u32 m0, s82, 0x800\n"
"v_add_u32 v10, 0x10800, v10\n"
"v_mfma_f32_16x16x32_bf16 v[164:167], a[228:231], a[196:199], v[164:167]\n"
"ds_read_b128 a[136:139], v1 offset:4096\n"
"v_mfma_f32_16x16x32_bf16 v[168:171], a[228:231], a[200:203], v[168:171]\n"
"buffer_load_dwordx4 v11, s[48:51], s70 offen lds\n"
"v_mfma_f32_16x16x32_bf16 v[172:175], a[228:231], a[204:207], v[172:175]\n"
"s_add_u32 m0, s82, 0xc00\n"
"ds_read_b128 a[140:143], v1 offset:6144\n"
"v_mfma_f32_16x16x32_bf16 v[176:179], a[228:231], a[208:211], v[176:179]\n"
"v_mfma_f32_16x16x32_bf16 v[180:183], a[228:231], a[212:215], v[180:183]\n"
"buffer_load_dwordx4 v12, s[48:51], s71 offen lds\n"
"v_mfma_f32_16x16x32_bf16 v[184:187], a[228:231], a[216:219], v[184:187]\n"
"ds_read_b128 a[144:147], v1 offset:8192\n"
"v_mfma_f32_16x16x32_bf16 v[188:191], a[228:231], a[220:223], v[188:191]\n"
"s_add_u32 m0, s82, 0x1000\n"
"ds_read_b128 a[148:151], v1 offset:10240\n"
"v_mfma_f32_16x16x32_bf16 v[192:195], a[232:235], a[192:195], v[192:195]\n"
"v_mfma_f32_16x16x32_bf16 v[196:199], a[232:235], a[196:199], v[196:199]\n"
"buffer_load_dwordx4 v11, s[48:51], s72 offen lds\n"
"v_mfma_f32_16x16x32_bf16 v[200:203], a[232:235], a[200:203], v[200:203]\n"
"ds_read_b128 a[152:155], v1 offset:12288\n"
"v_mfma_f32_16x16x32_bf16 v[204:207], a[232:235], a[204:207], v[204:207]\n"
"s_add_u32 m0, s82, 0x1400\n"
"ds_read_b128 a[156:159], v1 offset:14336\n"
"v_mfma_f32_16x16x32_bf16 v[208:211], a[232:235], a[208:211], v[208:211]\n"
"v_mfma_f32_16x16x32_bf16 v[212:215], a[232:235], a[212:215], v[212:215]\n"
"buffer_load_dwordx4 v12, s[48:51], s73 offen lds\n"
"v_mfma_f32_16x16x32_bf16 v[216:219], a[232:235], a[216:219], v[216:219]\n"
"s_add_u32 m0, s82, 0x1800\n"
"ds_read_b64_tr_b16 a[164:165], v4 offset:0\n"
"v_mfma_f32_16x16x32_bf16 v[220:223], a[232:235], a[220:223], v[220:223]\n"
"ds_read_b64_tr_b16 a[166:167], v4 offset:1024\n"
"v_mfma_f32_16x16x32_bf16 v[224:227], a[236:239], a[192:195], v[224:227]\n"
"buffer_load_dwordx4 v11, s[48:51], s74 offen lds\n"
"v_mfma_f32_16x16x32_bf16 v[228:231], a[236:239], a[196:199], v[228:231]\n"
"ds_read_b64_tr_b16 a[168:169], v5 offset:0\n"
"v_mfma_f32_16x16x32_bf16 v[232:235], a[236:239], a[200:203], v[232:235]\n"
"s_add_u32 m0, s82, 0x1c00\n"
"v_mfma_f32_16x16x32_bf16 v[236:239], a[236:239], a[204:207], v[236:239]\n"
"ds_read_b64_tr_b16 a[170:171], v5 offset:1024\n"
"v_mfma_f32_16x16x32_bf16 v[240:243], a[236:239], a[208:211], v[240:243]\n"
"buffer_load_dwordx4 v12, s[48:51], s75 offen lds\n"
"v_mfma_f32_16x16x32_bf16 v[244:247], a[236:239], a[212:215], v[244:247]\n"
"ds_read_b64_tr_b16 a[172:173], v6 offset:0\n"
"v_mfma_f32_16x16x32_bf16 v[248:251], a[236:239], a[216:219], v[248:251]\n"
"s_add_u32 m0, s83, 0x8000\n"
"v_mfma_f32_16x16x32_bf16 v[252:255], a[236:239], a[220:223], v[252:255]\n"
"buffer_load_dwordx4 v13, s[52:55], s76 offen lds\n"
"v_mfma_f32_16x16x32_bf16 a[0:3], a[240:243], a[192:195], a[0:3]\n"
"ds_read_b64_tr_b16 a[174:175], v6 offset:1024\n"
"v_mfma_f32_16x16x32_bf16 a[4:7], a[240:243], a[196:199], a[4:7]\n"
"s_add_u32 m0, s83, 0x8400\n"
"ds_read_b64_tr_b16 a[176:177], v3 offset:128\n"
"v_mfma_f32_16x16x32_bf16 a[8:11], a[240:243], a[200:203], a[8:11]\n"
"v_mfma_f32_16x16x32_bf16 a[12:15], a[240:243], a[204:207], a[12:15]\n"
"buffer_load_dwordx4 v13, s[52:55], s77 offen lds\n"
"v_mfma_f32_16x16x32_bf16 a[16:19], a[240:243], a[208:211], a[16:19]\n"
"ds_read_b64_tr_b16 a[178:179], v3 offset:1152\n"
"v_mfma_f32_16x16x32_bf16 a[20:23], a[240:243], a[212:215], a[20:23]\n"
"s_add_u32 m0, s83, 0x8880\n"
"ds_read_b64_tr_b16 a[180:181], v4 offset:128\n"
"v_mfma_f32_16x16x32_bf16 a[24:27], a[240:243], a[216:219], a[24:27]\n"
"v_mfma_f32_16x16x32_bf16 a[28:31], a[240:243], a[220:223], a[28:31]\n"
"buffer_load_dwordx4 v13, s[52:55], s78 offen lds\n"
"v_mfma_f32_16x16x32_bf16 a[32:35], a[244:247], a[192:195], a[32:35]\n"
"ds_read_b64_tr_b16 a[182:183], v4 offset:1152\n"
"v_mfma_f32_16x16x32_bf16 a[36:39], a[244:247], a[196:199], a[36:39]\n"
"s_add_u32 m0, s83, 0x8c80\n"
"ds_read_b64_tr_b16 a[184:185], v5 offset:128\n"
"v_mfma_f32_16x16x32_bf16 a[40:43], a[244:247], a[200:203], a[40:43]\n"
"buffer_load_dwordx4 v13, s[52:55], s79 offen lds\n"
"v_mfma_f32_16x16x32_bf16 a[44:47], a[244:247], a[204:207], a[44:47]\n"
"ds_read_b64_tr_b16 a[186:187], v5 offset:1152\n"
"v_mfma_f32_16x16x32_bf16 a[48:51], a[244:247], a[208:211], a[48:51]\n"
"s_add_u32 m0, s83, 0xc400\n"
"v_mfma_f32_16x16x32_bf16 a[52:55], a[244:247], a[212:215], a[52:55]\n"
"ds_read_b64_tr_b16 a[188:189], v6 offset:128\n"
"v_mfma_f32_16x16x32_bf16 a[56:59], a[244:247], a[216:219], a[56:59]\n"
"buffer_load_dwordx4 v13, s[56:59], s76 offen lds\n"
"v_mfma_f32_16x16x32_bf16 a[60:63], a[244:247], a[220:223], a[60:63]\n"
"ds_read_b64_tr_b16 a[190:191], v6 offset:1152\n"
"v_mfma_f32_16x16x32_bf16 a[64:67], a[248:251], a[192:195], a[64:67]\n"
"s_add_u32 m0, s83, 0xc800\n"
"v_mfma_f32_16x16x32_bf16 a[68:71], a[248:251], a[196:199], a[68:71]\n"
"v_mfma_f32_16x16x32_bf16 a[72:75], a[248:251], a[200:203], a[72:75]\n"
"buffer_load_dwordx4 v13, s[56:59], s77 offen lds\n"
"v_mfma_f32_16x16x32_bf16 a[76:79], a[248:251], a[204:207], a[76:79]\n"
"s_add_u32 m0, s83, 0xcc80\n"
"v_mfma_f32_16x16x32_bf16 a[80:83], a[248:251], a[208:211], a[80:83]\n"
"v_mfma_f32_16x16x32_bf16 a[84:87], a[248:251], a[212:215], a[84:87]\n"
"buffer_load_dwordx4 v13, s[56:59], s78 offen lds\n"
"v_mfma_f32_16x16x32_bf16 a[88:91], a[248:251], a[216:219], a[88:91]\n"
"v_mfma_f32_16x16x32_bf16 a[92:95], a[248:251], a[220:223], a[92:95]\n"
"s_add_u32 m0, s83, 0xd080\n"
"v_mfma_f32_16x16x32_bf16 a[96:99], a[252:255], a[192:195], a[96:99]\n"
"v_mfma_f32_16x16x32_bf16 a[100:103], a[252:255], a[196:199], a[100:103]\n"
"buffer_load_dwordx4 v13, s[56:59], s79 offen lds\n"
"v_mfma_f32_16x16x32_bf16 a[104:107], a[252:255], a[200:203], a[104:107]\n"
"v_mfma_f32_16x16x32_bf16 a[108:111], a[252:255], a[204:207], a[108:111]\n"
"buffer_load_dword v43, v42, s[60:63], 0 offen\n"
"v_mfma_f32_16x16x32_bf16 a[112:115], a[252:255], a[208:211], a[112:115]\n"
"v_add_u32 v11, 0x80, v11\n"
"v_add_u32 v12, 0x80, v12\n"
"v_add_u32 v13, 0x80000, v13\n"
"v_add_u32 v42, s84, v42\n"
"v_mfma_f32_16x16x32_bf16 a[116:119], a[252:255], a[212:215], a[116:119]\n"
"v_mfma_f32_16x16x32_bf16 a[120:123], a[252:255], a[216:219], a[120:123]\n"
"v_mfma_f32_16x16x32_bf16 a[124:127], a[252:255], a[220:223], a[124:127]\n"
"s_waitcnt lgkmcnt(0)\n"
"v_mfma_f32_16x16x32_bf16 v[128:131], a[160:163], a[128:131], v[128:131]\n"
"ds_read_b64_tr_b16 a[224:225], v7 offset:0\n"
"v_add_u32 v1, 0xfffef800, v1\n"
"v_mfma_f32_16x16x32_bf16 v[132:135], a[160:163], a[132:135], v[132:135]\n"
"v_add_u32 v3, 0xfffef800, v3\n"
"v_add_u32 v4, 0xfffef800, v4\n"
"v_mfma_f32_16x16x32_bf16 v[136:139], a[160:163], a[136:139], v[136:139]\n"
"ds_read_b64_tr_b16 a[226:227], v7 offset:1024\n"
"v_add_u32 v5, 0xfffef800, v5\n"
"v_mfma_f32_16x16x32_bf16 v[140:143], a[160:163], a[140:143], v[140:143]\n"
"v_add_u32 v6, 0xfffef800, v6\n"
"v_mfma_f32_16x16x32_bf16 v[144:147], a[160:163], a[144:147], v[144:147]\n"
"ds_read_b128 a[192:195], v2 offset:0\n"
"v_mfma_f32_16x16x32_bf16 v[148:151], a[160:163], a[148:151], v[148:151]\n"
"v_mfma_f32_16x16x32_bf16 v[152:155], a[160:163], a[152:155], v[152:155]\n"
"ds_read_b128 a[196:199], v2 offset:2048\n"
"v_mfma_f32_16x16x32_bf16 v[156:159], a[160:163], a[156:159], v[156:159]\n"
"v_mfma_f32_16x16x32_bf16 v[160:163], a[164:167], a[128:131], v[160:163]\n"
"ds_read_b128 a[200:203], v2 offset:4096\n"
"v_mfma_f32_16x16x32_bf16 v[164:167], a[164:167], a[132:135], v[164:167]\n"
"v_mfma_f32_16x16x32_bf16 v[168:171], a[164:167], a[136:139], v[168:171]\n"
"ds_read_b128 a[204:207], v2 offset:6144\n"
"v_mfma_f32_16x16x32_bf16 v[172:175], a[164:167], a[140:143], v[172:175]\n"
"v_mfma_f32_16x16x32_bf16 v[176:179], a[164:167], a[144:147], v[176:179]\n"
"ds_read_b128 a[208:211], v2 offset:8192\n"
"v_mfma_f32_16x16x32_bf16 v[180:183], a[164:167], a[148:151], v[180:183]\n"
"v_mfma_f32_16x16x32_bf16 v[184:187], a[164:167], a[152:155], v[184:187]\n"
"ds_read_b128 a[212:215], v2 offset:10240\n"
"v_mfma_f32_16x16x32_bf16 v[188:191], a[164:167], a[156:159], v[188:191]\n"
"v_mfma_f32_16x16x32_bf16 v[192:195], a[168:171], a[128:131], v[192:195]\n"
"ds_read_b128 a[216:219], v2 offset:12288\n"
"v_mfma_f32_16x16x32_bf16 v[196:199], a[168:171], a[132:135], v[196:199]\n"
"v_mfma_f32_16x16x32_bf16 v[200:203], a[168:171], a[136:139], v[200:203]\n"
"ds_read_b128 a[220:223], v2 offset:14336\n"
"v_mfma_f32_16x16x32_bf16 v[204:207], a[168:171], a[140:143], v[204:207]\n"
"v_mfma_f32_16x16x32_bf16 v[208:211], a[168:171], a[144:147], v[208:211]\n"
"ds_read_b64_tr_b16 a[228:229], v8 offset:0\n"
"v_mfma_f32_16x16x32_bf16 v[212:215], a[168:171], a[148:151], v[212:215]\n"
"v_mfma_f32_16x16x32_bf16 v[216:219], a[168:171], a[152:155], v[216:219]\n"
"ds_read_b64_tr_b16 a[230:231], v8 offset:1024\n"
"v_mfma_f32_16x16x32_bf16 v[220:223], a[168:171], a[156:159], v[220:223]\n"
"v_mfma_f32_16x16x32_bf16 v[224:227], a[172:175], a[128:131], v[224:227]\n"
"ds_read_b64_tr_b16 a[232:233], v9 offset:0\n"
"v_mfma_f32_16x16x32_bf16 v[228:231], a[172:175], a[132:135], v[228:231]\n"
"v_mfma_f32_16x16x32_bf16 v[232:235], a[172:175], a[136:139], v[232:235]\n"
"ds_read_b64_tr_b16 a[234:235], v9 offset:1024\n"
"v_mfma_f32_16x16x32_bf16 v[236:239], a[172:175], a[140:143], v[236:239]\n"
"v_mfma_f32_16x16x32_bf16 v[240:243], a[172:175], a[144:147], v[240:243]\n"
"ds_read_b64_tr_b16 a[236:237], v10 offset:0\n"
"v_mfma_f32_16x16x32_bf16 v[244:247], a[172:175], a[148:151], v[244:247]\n"
"v_mfma_f32_16x16x32_bf16 v[248:251], a[172:175], a[152:155], v[248:251]\n"
"ds_read_b64_tr_b16 a[238:239], v10 offset:1024\n"
"v_mfma_f32_16x16x32_bf16 v[252:255], a[172:175], a[156:159], v[252:255]\n"
"v_mfma_f32_16x16x32_bf16 a[0:3], a[176:179], a[128:131], a[0:3]\n"
"ds_read_b64_tr_b16 a[240:241], v7 offset:128\n"
"v_mfma_f32_16x16x32_bf16 a[4:7], a[176:179], a[132:135], a[4:7]\n"
"v_mfma_f32_16x16x32_bf16 a[8:11], a[176:179], a[136:139], a[8:11]\n"
"ds_read_b64_tr_b16 a[242:243], v7 offset:1152\n"
"v_mfma_f32_16x16x32_bf16 a[12:15], a[176:179], a[140:143], a[12:15]\n"
"v_mfma_f32_16x16x32_bf16 a[16:19], a[176:179], a[144:147], a[16:19]\n"
"ds_read_b64_tr_b16 a[244:245], v8 offset:128\n"
"v_mfma_f32_16x16x32_bf16 a[20:23], a[176:179], a[148:151], a[20:23]\n"
"v_mfma_f32_16x16x32_bf16 a[24:27], a[176:179], a[152:155], a[24:27]\n"
"ds_read_b64_tr_b16 a[246:247], v8 offset:1152\n"
"v_mfma_f32_16x16x32_bf16 a[28:31], a[176:179], a[156:159], a[28:31]\n"
"v_mfma_f32_16x16x32_bf16 a[32:35], a[180:183], a[128:131], a[32:35]\n"
"ds_read_b64_tr_b16 a[248:249], v9 offset:128\n"
"v_mfma_f32_16x16x32_bf16 a[36:39], a[180:183], a[132:135], a[36:39]\n"
"v_mfma_f32_16x16x32_bf16 a[40:43], a[180:183], a[136:139], a[40:43]\n"
"ds_read_b64_tr_b16 a[250:251], v9 offset:1152\n"
"v_mfma_f32_16x16x32_bf16 a[44:47], a[180:183], a[140:143], a[44:47]\n"
"v_mfma_f32_16x16x32_bf16 a[48:51], a[180:183], a[144:147], a[48:51]\n"
"ds_read_b64_tr_b16 a[252:253], v10 offset:128\n"
"v_mfma_f32_16x16x32_bf16 a[52:55], a[180:183], a[148:151], a[52:55]\n"
"v_mfma_f32_16x16x32_bf16 a[56:59], a[180:183], a[152:155], a[56:59]\n"
"ds_read_b64_tr_b16 a[254:255], v10 offset:1152\n"
"v_mfma_f32_16x16x32_bf16 a[60:63], a[180:183], a[156:159], a[60:63]\n"
"v_mfma_f32_16x16x32_bf16 a[64:67], a[184:187], a[128:131], a[64:67]\n"
"v_mfma_f32_16x16x32_bf16 a[68:71], a[184:187], a[132:135], a[68:71]\n"
"v_mfma_f32_16x16x32_bf16 a[72:75], a[184:187], a[136:139], a[72:75]\n"
"v_mfma_f32_16x16x32_bf16 a[76:79], a[184:187], a[140:143], a[76:79]\n"
"v_mfma_f32_16x16x32_bf16 a[80:83], a[184:187], a[144:147], a[80:83]\n"
"v_mfma_f32_16x16x32_bf16 a[84:87], a[184:187], a[148:151], a[84:87]\n"
"v_mfma_f32_16x16x32_bf16 a[88:91], a[184:187], a[152:155], a[88:91]\n"
"v_mfma_f32_16x16x32_bf16 a[92:95], a[184:187], a[156:159], a[92:95]\n"
"v_mfma_f32_16x16x32_bf16 a[96:99], a[188:191], a[128:131], a[96:99]\n"
"v_mfma_f32_16x16x32_bf16 a[100:103], a[188:191], a[132:135], a[100:103]\n"
"v_mfma_f32_16x16x32_bf16 a[104:107], a[188:191], a[136:139], a[104:107]\n"
"v_mfma_f32_16x16x32_bf16 a[108:111], a[188:191], a[140:143], a[108:111]\n"
"v_mfma_f32_16x16x32_bf16 a[112:115], a[188:191], a[144:147], a[112:115]\n"
"v_mfma_f32_16x16x32_bf16 a[116:119], a[188:191], a[148:151], a[116:119]\n"
"v_mfma_f32_16x16x32_bf16 a[120:123], a[188:191], a[152:155], a[120:123]\n"
"v_mfma_f32_16x16x32_bf16 a[124:127], a[188:191], a[156:159], a[124:127]\n"
"s_waitcnt lgkmcnt(0)\n"
"v_mfma_f32_16x16x32_bf16 v[128:131], a[224:227], a[192:195], v[128:131]\n"
"s_waitcnt vmcnt(1)\n"
"s_barrier\n"
"v_mfma_f32_16x16x32_bf16 v[132:135], a[224:227], a[196:199], v[132:135]\n"
"s_add_u32 m0, s82, 0x10800\n"
"ds_read_b64_tr_b16 a[160:161], v3 offset:0\n"
"v_mfma_f32_16x16x32_bf16 v[136:139], a[224:227], a[200:203], v[136:139]\n"
"buffer_load_dwordx4 v11, s[48:51], s68 offen lds\n"
"v_mfma_f32_16x16x32_bf16 v[140:143], a[224:227], a[204:207], v[140:143]\n"
"ds_read_b64_tr_b16 a[162:163], v3 offset:1024\n"
"v_add_u32 v2, 0xfffef800, v2\n"
"v_mfma_f32_16x16x32_bf16 v[144:147], a[224:227], a[208:211], v[144:147]\n"
"s_add_u32 m0, s82, 0x10c00\n"
"v_add_u32 v7, 0xfffef800, v7\n"
"v_mfma_f32_16x16x32_bf16 v[148:151], a[224:227], a[212:215], v[148:151]\n"
"ds_read_b128 a[128:131], v1 offset:0\n"
"v_add_u32 v8, 0xfffef800, v8\n"
"v_mfma_f32_16x16x32_bf16 v[152:155], a[224:227], a[216:219], v[152:155]\n"
"buffer_load_dwordx4 v12, s[48:51], s69 offen lds\n"
"v_mfma_f32_16x16x32_bf16 v[156:159], a[224:227], a[220:223], v[156:159]\n"
"ds_read_b128 a[132:135], v1 offset:2048\n"
"v_add_u32 v9, 0xfffef800, v9\n"
"v_mfma_f32_16x16x32_bf16 v[160:163], a[228:231], a[192:195], v[160:163]\n"
"s_add_u32 m0, s82, 0x11000\n"
"v_add_u32 v10, 0xfffef800, v10\n"
"v_mfma_f32_16x16x32_bf16 v[164:167], a[228:231], a[196:199], v[164:167]\n"
"ds_read_b128 a[136:139], v1 offset:4096\n"
"v_mfma_f32_16x16x32_bf16 v[168:171], a[228:231], a[200:203], v[168:171]\n"
"buffer_load_dwordx4 v11, s[48:51], s70 offen lds\n"
"v_mfma_f32_16x16x32_bf16 v[172:175], a[228:231], a[204:207], v[172:175]\n"
"s_add_u32 m0, s82, 0x11400\n"
"ds_read_b128 a[140:143], v1 offset:6144\n"
"v_mfma_f32_16x16x32_bf16 v[176:179], a[228:231], a[208:211], v[176:179]\n"
"v_mfma_f32_16x16x32_bf16 v[180:183], a[228:231], a[212:215], v[180:183]\n"
"buffer_load_dwordx4 v12, s[48:51], s71 offen lds\n"
"v_mfma_f32_16x16x32_bf16 v[184:187], a[228:231], a[216:219], v[184:187]\n"
"ds_read_b128 a[144:147], v1 offset:8192\n"
"v_mfma_f32_16x16x32_bf16 v[188:191], a[228:231], a[220:223], v[188:191]\n"
"s_add_u32 m0, s82, 0x11800\n"
"ds_read_b128 a[148:151], v1 offset:10240\n"
"v_mfma_f32_16x16x32_bf16 v[192:195], a[232:235], a[192:195], v[192:195]\n"
"v_mfma_f32_16x16x32_bf16 v[196:199], a[232:235], a[196:199], v[196:199]\n"
"buffer_load_dwordx4 v11, s[48:51], s72 offen lds\n"
"v_mfma_f32_16x16x32_bf16 v[200:203], a[232:235], a[200:203], v[200:203]\n"
"ds_read_b128 a[152:155], v1 offset:12288\n"
"v_mfma_f32_16x16x32_bf16 v[204:207], a[232:235], a[204:207], v[204:207]\n"
"s_add_u32 m0, s82, 0x11c00\n"
"ds_read_b128 a[156:159], v1 offset:14336\n"
"v_mfma_f32_16x16x32_bf16 v[208:211], a[232:235], a[208:211], v[208:211]\n"
"v_mfma_f32_16x16x32_bf16 v[212:215], a[232:235], a[212:215], v[212:215]\n"
"buffer_load_dwordx4 v12, s[48:51], s73 offen lds\n"
"v_mfma_f32_16x16x32_bf16 v[216:219], a[232:235], a[216:219], v[216:219]\n"
"s_add_u32 m0, s82, 0x12000\n"
"ds_read_b64_tr_b16 a[164:165], v4 offset:0\n"
"v_mfma_f32_16x16x32_bf16 v[220:223], a[232:235], a[220:223], v[220:223]\n"
"ds_read_b64_tr_b16 a[166:167], v4 offset:1024\n"
"v_mfma_f32_16x16x32_bf16 v[224:227], a[236:239], a[192:195], v[224:227]\n"
"buffer_load_dwordx4 v11, s[48:51], s74 offen lds\n"
"v_mfma_f32_16x16x32_bf16 v[228:231], a[236:239], a[196:199], v[228:231]\n"
"ds_read_b64_tr_b16 a[168:169], v5 offset:0\n"
"v_mfma_f32_16x16x32_bf16 v[232:235], a[236:239], a[200:203], v[232:235]\n"
"s_add_u32 m0, s82, 0x12400\n"
"v_mfma_f32_16x16x32_bf16 v[236:239], a[236:239], a[204:207], v[236:239]\n"
"ds_read_b64_tr_b16 a[170:171], v5 offset:1024\n"
"v_mfma_f32_16x16x32_bf16 v[240:243], a[236:239], a[208:211], v[240:243]\n"
"buffer_load_dwordx4 v12, s[48:51], s75 offen lds\n"
"v_mfma_f32_16x16x32_bf16 v[244:247], a[236:239], a[212:215], v[244:247]\n"
"ds_read_b64_tr_b16 a[172:173], v6 offset:0\n"
"v_mfma_f32_16x16x32_bf16 v[248:251], a[236:239], a[216:219], v[248:251]\n"
"s_add_u32 m0, s83, 0x18800\n"
"v_mfma_f32_16x16x32_bf16 v[252:255], a[236:239], a[220:223], v[252:255]\n"
"buffer_load_dwordx4 v13, s[52:55], s76 offen lds\n"
"v_mfma_f32_16x16x32_bf16 a[0:3], a[240:243], a[192:195], a[0:3]\n"
"ds_read_b64_tr_b16 a[174:175], v6 offset:1024\n"
"v_mfma_f32_16x16x32_bf16 a[4:7], a[240:243], a[196:199], a[4:7]\n"
"s_add_u32 m0, s83, 0x18c00\n"
"ds_read_b64_tr_b16 a[176:177], v3 offset:128\n"
"v_mfma_f32_16x16x32_bf16 a[8:11], a[240:243], a[200:203], a[8:11]\n"
"v_mfma_f32_16x16x32_bf16 a[12:15], a[240:243], a[204:207], a[12:15]\n"
"buffer_load_dwordx4 v13, s[52:55], s77 offen lds\n"
"v_mfma_f32_16x16x32_bf16 a[16:19], a[240:243], a[208:211], a[16:19]\n"
"ds_read_b64_tr_b16 a[178:179], v3 offset:1152\n"
"v_mfma_f32_16x16x32_bf16 a[20:23], a[240:243], a[212:215], a[20:23]\n"
"s_add_u32 m0, s83, 0x19080\n"
"ds_read_b64_tr_b16 a[180:181], v4 offset:128\n"
"v_mfma_f32_16x16x32_bf16 a[24:27], a[240:243], a[216:219], a[24:27]\n"
"v_mfma_f32_16x16x32_bf16 a[28:31], a[240:243], a[220:223], a[28:31]\n"
"buffer_load_dwordx4 v13, s[52:55], s78 offen lds\n"
"v_mfma_f32_16x16x32_bf16 a[32:35], a[244:247], a[192:195], a[32:35]\n"
"ds_read_b64_tr_b16 a[182:183], v4 offset:1152\n"
"v_mfma_f32_16x16x32_bf16 a[36:39], a[244:247], a[196:199], a[36:39]\n"
"s_add_u32 m0, s83, 0x19480\n"
"ds_read_b64_tr_b16 a[184:185], v5 offset:128\n"
"v_mfma_f32_16x16x32_bf16 a[40:43], a[244:247], a[200:203], a[40:43]\n"
"buffer_load_dwordx4 v13, s[52:55], s79 offen lds\n"
"v_mfma_f32_16x16x32_bf16 a[44:47], a[244:247], a[204:207], a[44:47]\n"
"ds_read_b64_tr_b16 a[186:187], v5 offset:1152\n"
"v_mfma_f32_16x16x32_bf16 a[48:51], a[244:247], a[208:211], a[48:51]\n"
"s_add_u32 m0, s83, 0x1cc00\n"
"v_mfma_f32_16x16x32_bf16 a[52:55], a[244:247], a[212:215], a[52:55]\n"
"ds_read_b64_tr_b16 a[188:189], v6 offset:128\n"
"v_mfma_f32_16x16x32_bf16 a[56:59], a[244:247], a[216:219], a[56:59]\n"
"buffer_load_dwordx4 v13, s[56:59], s76 offen lds\n"
"v_mfma_f32_16x16x32_bf16 a[60:63], a[244:247], a[220:223], a[60:63]\n"
"ds_read_b64_tr_b16 a[190:191], v6 offset:1152\n"
"v_mfma_f32_16x16x32_bf16 a[64:67], a[248:251], a[192:195], a[64:67]\n"
"s_add_u32 m0, s83, 0x1d000\n"
"v_mfma_f32_16x16x32_bf16 a[68:71], a[248:251], a[196:199], a[68:71]\n"
"v_mfma_f32_16x16x32_bf16 a[72:75], a[248:251], a[200:203], a[72:75]\n"
"buffer_load_dwordx4 v13, s[56:59], s77 offen lds\n"
"v_mfma_f32_16x16x32_bf16 a[76:79], a[248:251], a[204:207], a[76:79]\n"
"s_add_u32 m0, s83, 0x1d480\n"
"v_mfma_f32_16x16x32_bf16 a[80:83], a[248:251], a[208:211], a[80:83]\n"
"v_mfma_f32_16x16x32_bf16 a[84:87], a[248:251], a[212:215], a[84:87]\n"
"buffer_load_dwordx4 v13, s[56:59], s78 offen lds\n"
"v_mfma_f32_16x16x32_bf16 a[88:91], a[248:251], a[216:219], a[88:91]\n"
"v_mfma_f32_16x16x32_bf16 a[92:95], a[248:251], a[220:223], a[92:95]\n"
"s_add_u32 m0, s83, 0x1d880\n"
"v_mfma_f32_16x16x32_bf16 a[96:99], a[252:255], a[192:195], a[96:99]\n"
"v_mfma_f32_16x16x32_bf16 a[100:103], a[252:255], a[196:199], a[100:103]\n"
"buffer_load_dwordx4 v13, s[56:59], s79 offen lds\n"
"v_mfma_f32_16x16x32_bf16 a[104:107], a[252:255], a[200:203], a[104:107]\n"
"v_mfma_f32_16x16x32_bf16 a[108:111], a[252:255], a[204:207], a[108:111]\n"
"buffer_load_dword v17, v42, s[60:63], 0 offen\n"
"v_mfma_f32_16x16x32_bf16 a[112:115], a[252:255], a[208:211], a[112:115]\n"
"v_add_u32 v11, 0x80, v11\n"
"v_add_u32 v12, 0x80, v12\n"
"v_add_u32 v13, 0x80000, v13\n"
"v_add_u32 v42, s84, v42\n"
"v_mfma_f32_16x16x32_bf16 a[116:119], a[252:255], a[212:215], a[116:119]\n"
"v_mfma_f32_16x16x32_bf16 a[120:123], a[252:255], a[216:219], a[120:123]\n"
"s_add_u32 s85, s85, 1\n"
"v_mfma_f32_16x16x32_bf16 a[124:127], a[252:255], a[220:223], a[124:127]\n"
"s_cbranch_scc0 LM_kloop\n"
"s_waitcnt lgkmcnt(0)\n"
"v_mfma_f32_16x16x32_bf16 v[128:131], a[160:163], a[128:131], v[128:131]\n"
"s_mov_b32 s48, s8\n"
"ds_read_b64_tr_b16 a[224:225], v7 offset:0\n"
"v_mfma_f32_16x16x32_bf16 v[132:135], a[160:163], a[132:135], v[132:135]\n"
"s_and_b32 s49, s9, 0xffff\n"
"v_add_u32 v1, 0x10800, v1\n"
"v_mfma_f32_16x16x32_bf16 v[136:139], a[160:163], a[136:139], v[136:139]\n"
"s_mov_b32 s50, 0x200000\n"
"ds_read_b64_tr_b16 a[226:227], v7 offset:1024\n"
"v_mfma_f32_16x16x32_bf16 v[140:143], a[160:163], a[140:143], v[140:143]\n"
"v_add_u32 v3, 0x10800, v3\n"
"v_add_u32 v4, 0x10800, v4\n"
"v_mfma_f32_16x16x32_bf16 v[144:147], a[160:163], a[144:147], v[144:147]\n"
"s_mov_b32 s51, 0x20000\n"
"ds_read_b128 a[192:195], v2 offset:0\n"
"v_mfma_f32_16x16x32_bf16 v[148:151], a[160:163], a[148:151], v[148:151]\n"
"s_mov_b32 s52, s12\n"
"v_add_u32 v5, 0x10800, v5\n"
"v_mfma_f32_16x16x32_bf16 v[152:155], a[160:163], a[152:155], v[152:155]\n"
"ds_read_b128 a[196:199], v2 offset:2048\n"
"v_add_u32 v6, 0x10800, v6\n"
"v_mfma_f32_16x16x32_bf16 v[156:159], a[160:163], a[156:159], v[156:159]\n"
"s_and_b32 s53, s13, 0xffff\n"
"v_mfma_f32_16x16x32_bf16 v[160:163], a[164:167], a[128:131], v[160:163]\n"
"s_mov_b32 s54, 0x400000\n"
"ds_read_b128 a[200:203], v2 offset:4096\n"
"v_mfma_f32_16x16x32_bf16 v[164:167], a[164:167], a[132:135], v[164:167]\n"
"v_mfma_f32_16x16x32_bf16 v[168:171], a[164:167], a[136:139], v[168:171]\n"
"s_mov_b32 s55, 0x20000\n"
"ds_read_b128 a[204:207], v2 offset:6144\n"
"v_mfma_f32_16x16x32_bf16 v[172:175], a[164:167], a[140:143], v[172:175]\n"
"s_add_u32 s44, s26, 0x3e00000\n"
"v_mfma_f32_16x16x32_bf16 v[176:179], a[164:167], a[144:147], v[176:179]\n"
"s_addc_u32 s45, s27, 0\n"
"ds_read_b128 a[208:211], v2 offset:8192\n"
"v_mfma_f32_16x16x32_bf16 v[180:183], a[164:167], a[148:151], v[180:183]\n"
"v_mfma_f32_16x16x32_bf16 v[184:187], a[164:167], a[152:155], v[184:187]\n"
"s_cmp_ge_u32 s37, 28\n"
"ds_read_b128 a[212:215], v2 offset:10240\n"
"v_mfma_f32_16x16x32_bf16 v[188:191], a[164:167], a[156:159], v[188:191]\n"
"s_cselect_b32 s56, s44, s20\n"
"v_mfma_f32_16x16x32_bf16 v[192:195], a[168:171], a[128:131], v[192:195]\n"
"ds_read_b128 a[216:219], v2 offset:12288\n"
"v_mfma_f32_16x16x32_bf16 v[196:199], a[168:171], a[132:135], v[196:199]\n"
"s_cselect_b32 s57, s45, s21\n"
"v_mfma_f32_16x16x32_bf16 v[200:203], a[168:171], a[136:139], v[200:203]\n"
"s_mov_b32 s58, 0x1c0000\n"
"ds_read_b128 a[220:223], v2 offset:14336\n"
"v_mfma_f32_16x16x32_bf16 v[204:207], a[168:171], a[140:143], v[204:207]\n"
"v_mfma_f32_16x16x32_bf16 v[208:211], a[168:171], a[144:147], v[208:211]\n"
"s_cselect_b32 s58, 0x40000, s58\n"
"ds_read_b64_tr_b16 a[228:229], v8 offset:0\n"
"v_mfma_f32_16x16x32_bf16 v[212:215], a[168:171], a[148:151], v[212:215]\n"
"s_mov_b32 s91, 0x38000\n"
"v_mfma_f32_16x16x32_bf16 v[216:219], a[168:171], a[152:155], v[216:219]\n"
"s_cselect_b32 s91, 1024, s91\n"
"ds_read_b64_tr_b16 a[230:231], v8 offset:1024\n"
"v_mfma_f32_16x16x32_bf16 v[220:223], a[168:171], a[156:159], v[220:223]\n"
"v_mfma_f32_16x16x32_bf16 v[224:227], a[172:175], a[128:131], v[224:227]\n"
"s_and_b32 s57, s57, 0xffff\n"
"ds_read_b64_tr_b16 a[232:233], v9 offset:0\n"
"v_mfma_f32_16x16x32_bf16 v[228:231], a[172:175], a[132:135], v[228:231]\n"
"s_mov_b32 s59, 0x20000\n"
"v_mfma_f32_16x16x32_bf16 v[232:235], a[172:175], a[136:139], v[232:235]\n"
"ds_read_b64_tr_b16 a[234:235], v9 offset:1024\n"
"v_mfma_f32_16x16x32_bf16 v[236:239], a[172:175], a[140:143], v[236:239]\n"
"s_lshl_b32 s44, s38, 17\n"
"v_mfma_f32_16x16x32_bf16 v[240:243], a[172:175], a[144:147], v[240:243]\n"
"s_lshl_b32 s45, s34, 15\n"
"ds_read_b64_tr_b16 a[236:237], v10 offset:0\n"
"v_mfma_f32_16x16x32_bf16 v[244:247], a[172:175], a[148:151], v[244:247]\n"
"v_mfma_f32_16x16x32_bf16 v[248:251], a[172:175], a[152:155], v[248:251]\n"
"s_add_u32 s46, s44, s45\n"
"ds_read_b64_tr_b16 a[238:239], v10 offset:1024\n"
"v_mfma_f32_16x16x32_bf16 v[252:255], a[172:175], a[156:159], v[252:255]\n"
"s_add_u32 s68, s46, 0\n"
"v_mfma_f32_16x16x32_bf16 a[0:3], a[176:179], a[128:131], a[0:3]\n"
"s_add_u32 s69, s46, 8192\n"
"ds_read_b64_tr_b16 a[240:241], v7 offset:128\n"
"v_mfma_f32_16x16x32_bf16 a[4:7], a[176:179], a[132:135], a[4:7]\n"
"v_mfma_f32_16x16x32_bf16 a[8:11], a[176:179], a[136:139], a[8:11]\n"
"s_add_u32 s70, s46, 16384\n"
"ds_read_b64_tr_b16 a[242:243], v7 offset:1152\n"
"v_mfma_f32_16x16x32_bf16 a[12:15], a[176:179], a[140:143], a[12:15]\n"
"s_add_u32 s71, s46, 24576\n"
"v_mfma_f32_16x16x32_bf16 a[16:19], a[176:179], a[144:147], a[16:19]\n"
"ds_read_b64_tr_b16 a[244:245], v8 offset:128\n"
"v_mfma_f32_16x16x32_bf16 a[20:23], a[176:179], a[148:151], a[20:23]\n"
"s_sub_u32 s46, 1966080, s44\n"
"v_mfma_f32_16x16x32_bf16 a[24:27], a[176:179], a[152:155], a[24:27]\n"
"s_add_u32 s46, s46, s45\n"
"ds_read_b64_tr_b16 a[246:247], v8 offset:1152\n"
"v_mfma_f32_16x16x32_bf16 a[28:31], a[176:179], a[156:159], a[28:31]\n"
"v_mfma_f32_16x16x32_bf16 a[32:35], a[180:183], a[128:131], a[32:35]\n"
"s_add_u32 s72, s46, 0\n"
"ds_read_b64_tr_b16 a[248:249], v9 offset:128\n"
"v_mfma_f32_16x16x32_bf16 a[36:39], a[180:183], a[132:135], a[36:39]\n"
"s_add_u32 s73, s46, 8192\n"
"v_mfma_f32_16x16x32_bf16 a[40:43], a[180:183], a[136:139], a[40:43]\n"
"s_add_u32 s74, s46, 16384\n"
"ds_read_b64_tr_b16 a[250:251], v9 offset:1152\n"
"v_mfma_f32_16x16x32_bf16 a[44:47], a[180:183], a[140:143], a[44:47]\n"
"v_mfma_f32_16x16x32_bf16 a[48:51], a[180:183], a[144:147], a[48:51]\n"
"s_add_u32 s75, s46, 24576\n"
"ds_read_b64_tr_b16 a[252:253], v10 offset:128\n"
"v_mfma_f32_16x16x32_bf16 a[52:55], a[180:183], a[148:151], a[52:55]\n"
"s_mul_i32 s44, s34, 57344\n"
"v_mfma_f32_16x16x32_bf16 a[56:59], a[180:183], a[152:155], a[56:59]\n"
"ds_read_b64_tr_b16 a[254:255], v10 offset:1152\n"
"v_mfma_f32_16x16x32_bf16 a[60:63], a[180:183], a[156:159], a[60:63]\n"
"s_lshl_b32 s45, s37, 7\n"
"v_mfma_f32_16x16x32_bf16 a[64:67], a[184:187], a[128:131], a[64:67]\n"
"s_add_u32 s80, s44, s45\n"
"v_mfma_f32_16x16x32_bf16 a[68:71], a[184:187], a[132:135], a[68:71]\n"
"v_mfma_f32_16x16x32_bf16 a[72:75], a[184:187], a[136:139], a[72:75]\n"
"s_add_u32 s81, s80, 28672\n"
"v_mfma_f32_16x16x32_bf16 a[76:79], a[184:187], a[140:143], a[76:79]\n"
"s_lshl_b32 s44, s34, 8\n"
"v_mfma_f32_16x16x32_bf16 a[80:83], a[184:187], a[144:147], a[80:83]\n"
"s_lshl_b32 s45, s36, 13\n"
"v_mfma_f32_16x16x32_bf16 a[84:87], a[184:187], a[148:151], a[84:87]\n"
"v_mfma_f32_16x16x32_bf16 a[88:91], a[184:187], a[152:155], a[88:91]\n"
"s_add_u32 s44, s44, s45\n"
"v_mfma_f32_16x16x32_bf16 a[92:95], a[184:187], a[156:159], a[92:95]\n"
"s_add_u32 s45, s44, 128\n"
"v_mfma_f32_16x16x32_bf16 a[96:99], a[188:191], a[128:131], a[96:99]\n"
"v_mfma_f32_16x16x32_bf16 a[100:103], a[188:191], a[132:135], a[100:103]\n"
"s_cmp_ge_u32 s37, 28\n"
"v_mfma_f32_16x16x32_bf16 a[104:107], a[188:191], a[136:139], a[104:107]\n"
"s_cselect_b32 s80, s44, s80\n"
"v_mfma_f32_16x16x32_bf16 a[108:111], a[188:191], a[140:143], a[108:111]\n"
"v_mfma_f32_16x16x32_bf16 a[112:115], a[188:191], a[144:147], a[112:115]\n"
"s_cselect_b32 s81, s45, s81\n"
"v_mfma_f32_16x16x32_bf16 a[116:119], a[188:191], a[148:151], a[116:119]\n"
"s_lshl_b32 s84, s34, 11\n"
"v_mfma_f32_16x16x32_bf16 a[120:123], a[188:191], a[152:155], a[120:123]\n"
"v_mfma_f32_16x16x32_bf16 a[124:127], a[188:191], a[156:159], a[124:127]\n"
"s_waitcnt lgkmcnt(0)\n"
"v_mfma_f32_16x16x32_bf16 v[128:131], a[224:227], a[192:195], v[128:131]\n"
"s_waitcnt vmcnt(0)\n"
"s_barrier\n"
"v_mfma_f32_16x16x32_bf16 v[132:135], a[224:227], a[196:199], v[132:135]\n"
"s_add_u32 m0, s82, 0x0\n"
"ds_read_b64_tr_b16 a[160:161], v3 offset:0\n"
"v_mfma_f32_16x16x32_bf16 v[136:139], a[224:227], a[200:203], v[136:139]\n"
"v_add_u32 v2, 0x10800, v2\n"
"v_add_u32 v7, 0x10800, v7\n"
"v_mfma_f32_16x16x32_bf16 v[140:143], a[224:227], a[204:207], v[140:143]\n"
"buffer_load_dwordx4 v36, s[48:51], s68 offen lds ;@Q0\n"
"v_mfma_f32_16x16x32_bf16 v[144:147], a[224:227], a[208:211], v[144:147]\n"
"ds_read_b64_tr_b16 a[162:163], v3 offset:1024\n"
"v_add_u32 v8, 0x10800, v8\n"
"v_mfma_f32_16x16x32_bf16 v[148:151], a[224:227], a[212:215], v[148:151]\n"
"s_add_u32 m0, s82, 0x400\n"
"ds_read_b128 a[128:131], v1 offset:0\n"
"v_mfma_f32_16x16x32_bf16 v[152:155], a[224:227], a[216:219], v[152:155]\n"
"v_add_u32 v9, 0x10800, v9\n"
"v_add_u32 v10, 0x10800, v10\n"
"v_mfma_f32_16x16x32_bf16 v[156:159], a[224:227], a[220:223], v[156:159]\n"
"buffer_load_dwordx4 v37, s[48:51], s69 offen lds ;@Q0\n"
"v_mfma_f32_16x16x32_bf16 v[160:163], a[228:231], a[192:195], v[160:163]\n"
"ds_read_b128 a[132:135], v1 offset:2048\n"
"v_mfma_f32_16x16x32_bf16 v[164:167], a[228:231], a[196:199], v[164:167]\n"
"s_add_u32 m0, s82, 0x800\n"
"ds_read_b128 a[136:139], v1 offset:4096\n"
"v_mfma_f32_16x16x32_bf16 v[168:171], a[228:231], a[200:203], v[168:171]\n"
"v_mfma_f32_16x16x32_bf16 v[172:175], a[228:231], a[204:207], v[172:175]\n"
"buffer_load_dwordx4 v36, s[48:51], s70 offen lds ;@Q0\n"
"v_mfma_f32_16x16x32_bf16 v[176:179], a[228:231], a[208:211], v[176:179]\n"
"ds_read_b128 a[140:143], v1 offset:6144\n"
"v_mfma_f32_16x16x32_bf16 v[180:183], a[228:231], a[212:215], v[180:183]\n"
"s_add_u32 m0, s82, 0xc00\n"
"ds_read_b128 a[144:147], v1 offset:8192\n"
"v_mfma_f32_16x16x32_bf16 v[184:187], a[228:231], a[216:219], v[184:187]\n"
"v_mfma_f32_16x16x32_bf16 v[188:191], a[228:231], a[220:223], v[188:191]\n"
"ds_read_b128 a[148:151], v1 offset:10240\n"
"v_mfma_f32_16x16x32_bf16 v[192:195], a[232:235], a[192:195], v[192:195]\n"
"buffer_load_dwordx4 v37, s[48:51], s71 offen lds ;@Q0\n"
"v_mfma_f32_16x16x32_bf16 v[196:199], a[232:235], a[196:199], v[196:199]\n"
"ds_read_b128 a[152:155], v1 offset:12288\n"
"v_mfma_f32_16x16x32_bf16 v[200:203], a[232:235], a[200:203], v[200:203]\n"
"s_add_u32 m0, s82, 0x1000\n"
"v_mfma_f32_16x16x32_bf16 v[204:207], a[232:235], a[204:207], v[204:207]\n"
"ds_read_b128 a[156:159], v1 offset:14336\n"
"v_mfma_f32_16x16x32_bf16 v[208:211], a[232:235], a[208:211], v[208:211]\n"
"buffer_load_dwordx4 v36, s[48:51], s72 offen lds ;@Q0\n"
"v_mfma_f32_16x16x32_bf16 v[212:215], a[232:235], a[212:215], v[212:215]\n"
"ds_read_b64_tr_b16 a[164:165], v4 offset:0\n"
"v_mfma_f32_16x16x32_bf16 v[216:219], a[232:235], a[216:219], v[216:219]\n"
"s_add_u32 m0, s82, 0x1400\n"
"v_mfma_f32_16x16x32_bf16 v[220:223], a[232:235], a[220:223], v[220:223]\n"
"ds_read_b64_tr_b16 a[166:167], v4 offset:1024\n"
"v_mfma_f32_16x16x32_bf16 v[224:227], a[236:239], a[192:195], v[224:227]\n"
"buffer_load_dwordx4 v37, s[48:51], s73 offen lds ;@Q0\n"
"v_mfma_f32_16x16x32_bf16 v[228:231], a[236:239], a[196:199], v[228:231]\n"
"ds_read_b64_tr_b16 a[168:169], v5 offset:0\n"
"v_mfma_f32_16x16x32_bf16 v[232:235], a[236:239], a[200:203], v[232:235]\n"
"s_add_u32 m0, s82, 0x1800\n"
"v_mfma_f32_16x16x32_bf16 v[236:239], a[236:239], a[204:207], v[236:239]\n"
"ds_read_b64_tr_b16 a[170:171], v5 offset:1024\n"
"v_mfma_f32_16x16x32_bf16 v[240:243], a[236:239], a[208:211], v[240:243]\n"
"buffer_load_dwordx4 v36, s[48:51], s74 offen lds ;@Q0\n"
"v_mfma_f32_16x16x32_bf16 v[244:247], a[236:239], a[212:215], v[244:247]\n"
"ds_read_b64_tr_b16 a[172:173], v6 offset:0\n"
"v_mfma_f32_16x16x32_bf16 v[248:251], a[236:239], a[216:219], v[248:251]\n"
"v_mfma_f32_16x16x32_bf16 v[252:255], a[236:239], a[220:223], v[252:255]\n"
"s_add_u32 m0, s82, 0x1c00\n"
"ds_read_b64_tr_b16 a[174:175], v6 offset:1024\n"
"v_mfma_f32_16x16x32_bf16 a[0:3], a[240:243], a[192:195], a[0:3]\n"
"v_mfma_f32_16x16x32_bf16 a[4:7], a[240:243], a[196:199], a[4:7]\n"
"buffer_load_dwordx4 v37, s[48:51], s75 offen lds ;@Q0\n"
"v_mfma_f32_16x16x32_bf16 a[8:11], a[240:243], a[200:203], a[8:11]\n"
"ds_read_b64_tr_b16 a[176:177], v3 offset:128\n"
"v_mfma_f32_16x16x32_bf16 a[12:15], a[240:243], a[204:207], a[12:15]\n"
"s_add_u32 m0, s83, 0x8000\n"
"ds_read_b64_tr_b16 a[178:179], v3 offset:1152\n"
"v_mfma_f32_16x16x32_bf16 a[16:19], a[240:243], a[208:211], a[16:19]\n"
"v_mfma_f32_16x16x32_bf16 a[20:23], a[240:243], a[212:215], a[20:23]\n"
"buffer_load_dwordx4 v38, s[52:55], s76 offen lds ;@Q0\n"
"v_mfma_f32_16x16x32_bf16 a[24:27], a[240:243], a[216:219], a[24:27]\n"
"ds_read_b64_tr_b16 a[180:181], v4 offset:128\n"
"v_mfma_f32_16x16x32_bf16 a[28:31], a[240:243], a[220:223], a[28:31]\n"
"s_add_u32 m0, s83, 0x8400\n"
"ds_read_b64_tr_b16 a[182:183], v4 offset:1152\n"
"v_mfma_f32_16x16x32_bf16 a[32:35], a[244:247], a[192:195], a[32:35]\n"
"v_mfma_f32_16x16x32_bf16 a[36:39], a[244:247], a[196:199], a[36:39]\n"
"buffer_load_dwordx4 v38, s[52:55], s77 offen lds ;@Q0\n"
"v_mfma_f32_16x16x32_bf16 a[40:43], a[244:247], a[200:203], a[40:43]\n"
"ds_read_b64_tr_b16 a[184:185], v5 offset:128\n"
"v_mfma_f32_16x16x32_bf16 a[44:47], a[244:247], a[204:207], a[44:47]\n"
"s_add_u32 m0, s83, 0x8880\n"
"ds_read_b64_tr_b16 a[186:187], v5 offset:1152\n"
"v_mfma_f32_16x16x32_bf16 a[48:51], a[244:247], a[208:211], a[48:51]\n"
"v_mfma_f32_16x16x32_bf16 a[52:55], a[244:247], a[212:215], a[52:55]\n"
"ds_read_b64_tr_b16 a[188:189], v6 offset:128\n"
"v_mfma_f32_16x16x32_bf16 a[56:59], a[244:247], a[216:219], a[56:59]\n"
"buffer_load_dwordx4 v38, s[52:55], s78 offen lds ;@Q0\n"
"v_mfma_f32_16x16x32_bf16 a[60:63], a[244:247], a[220:223], a[60:63]\n"
"ds_read_b64_tr_b16 a[190:191], v6 offset:1152\n"
"v_mfma_f32_16x16x32_bf16 a[64:67], a[248:251], a[192:195], a[64:67]\n"
"s_add_u32 m0, s83, 0x8c80\n"
"v_mfma_f32_16x16x32_bf16 a[68:71], a[248:251], a[196:199], a[68:71]\n"
"v_mfma_f32_16x16x32_bf16 a[72:75], a[248:251], a[200:203], a[72:75]\n"
"buffer_load_dwordx4 v38, s[52:55], s79 offen lds ;@Q0\n"
"v_mfma_f32_16x16x32_bf16 a[76:79], a[248:251], a[204:207], a[76:79]\n"
"v_mfma_f32_16x16x32_bf16 a[80:83], a[248:251], a[208:211], a[80:83]\n"
"s_add_u32 m0, s84, 0xc400\n"
"v_mfma_f32_16x16x32_bf16 a[84:87], a[248:251], a[212:215], a[84:87]\n"
"v_mfma_f32_16x16x32_bf16 a[88:91], a[248:251], a[216:219], a[88:91]\n"
"buffer_load_dwordx4 v39, s[56:59], s80 offen lds ;@Q0\n"
"v_mfma_f32_16x16x32_bf16 a[92:95], a[248:251], a[220:223], a[92:95]\n"
"v_mfma_f32_16x16x32_bf16 a[96:99], a[252:255], a[192:195], a[96:99]\n"
"s_add_u32 m0, s84, 0xc800\n"
"v_mfma_f32_16x16x32_bf16 a[100:103], a[252:255], a[196:199], a[100:103]\n"
"v_mfma_f32_16x16x32_bf16 a[104:107], a[252:255], a[200:203], a[104:107]\n"
"buffer_load_dwordx4 v40, s[56:59], s81 offen lds ;@Q0\n"
"v_mfma_f32_16x16x32_bf16 a[108:111], a[252:255], a[204:207], a[108:111]\n"
"v_add_u32 v36, 0x80, v36\n"
"v_add_u32 v37, 0x80, v37\n"
"v_mfma_f32_16x16x32_bf16 a[112:115], a[252:255], a[208:211], a[112:115]\n"
"v_add_u32 v38, 0x80000, v38\n"
"v_add_u32 v39, s91, v39\n"
"v_mfma_f32_16x16x32_bf16 a[116:119], a[252:255], a[212:215], a[116:119]\n"
"v_add_u32 v40, s91, v40\n"
"v_mfma_f32_16x16x32_bf16 a[120:123], a[252:255], a[216:219], a[120:123]\n"
"v_mfma_f32_16x16x32_bf16 a[124:127], a[252:255], a[220:223], a[124:127]\n"
"s_waitcnt lgkmcnt(0)\n"
"v_mfma_f32_16x16x32_bf16 v[128:131], a[160:163], a[128:131], v[128:131]\n"
"ds_read_b64_tr_b16 a[224:225], v7 offset:0\n"
"v_add_u32 v1, 0xfffef800, v1\n"
"v_mfma_f32_16x16x32_bf16 v[132:135], a[160:163], a[132:135], v[132:135]\n"
"v_add_u32 v3, 0xfffef800, v3\n"
"v_add_u32 v4, 0xfffef800, v4\n"
"v_mfma_f32_16x16x32_bf16 v[136:139], a[160:163], a[136:139], v[136:139]\n"
"ds_read_b64_tr_b16 a[226:227], v7 offset:1024\n"
"v_add_u32 v5, 0xfffef800, v5\n"
"v_mfma_f32_16x16x32_bf16 v[140:143], a[160:163], a[140:143], v[140:143]\n"
"v_add_u32 v6, 0xfffef800, v6\n"
"v_mfma_f32_16x16x32_bf16 v[144:147], a[160:163], a[144:147], v[144:147]\n"
"ds_read_b128 a[192:195], v2 offset:0\n"
"v_mfma_f32_16x16x32_bf16 v[148:151], a[160:163], a[148:151], v[148:151]\n"
"v_mfma_f32_16x16x32_bf16 v[152:155], a[160:163], a[152:155], v[152:155]\n"
"ds_read_b128 a[196:199], v2 offset:2048\n"
"v_mfma_f32_16x16x32_bf16 v[156:159], a[160:163], a[156:159], v[156:159]\n"
"v_mfma_f32_16x16x32_bf16 v[160:163], a[164:167], a[128:131], v[160:163]\n"
"ds_read_b128 a[200:203], v2 offset:4096\n"
"v_mfma_f32_16x16x32_bf16 v[164:167], a[164:167], a[132:135], v[164:167]\n"
"v_mfma_f32_16x16x32_bf16 v[168:171], a[164:167], a[136:139], v[168:171]\n"
"ds_read_b128 a[204:207], v2 offset:6144\n"
"v_mfma_f32_16x16x32_bf16 v[172:175], a[164:167], a[140:143], v[172:175]\n"
"v_mfma_f32_16x16x32_bf16 v[176:179], a[164:167], a[144:147], v[176:179]\n"
"ds_read_b128 a[208:211], v2 offset:8192\n"
"v_mfma_f32_16x16x32_bf16 v[180:183], a[164:167], a[148:151], v[180:183]\n"
"v_mfma_f32_16x16x32_bf16 v[184:187], a[164:167], a[152:155], v[184:187]\n"
"ds_read_b128 a[212:215], v2 offset:10240\n"
"v_mfma_f32_16x16x32_bf16 v[188:191], a[164:167], a[156:159], v[188:191]\n"
"v_mfma_f32_16x16x32_bf16 v[192:195], a[168:171], a[128:131], v[192:195]\n"
"ds_read_b128 a[216:219], v2 offset:12288\n"
"v_mfma_f32_16x16x32_bf16 v[196:199], a[168:171], a[132:135], v[196:199]\n"
"v_mfma_f32_16x16x32_bf16 v[200:203], a[168:171], a[136:139], v[200:203]\n"
"ds_read_b128 a[220:223], v2 offset:14336\n"
"v_mfma_f32_16x16x32_bf16 v[204:207], a[168:171], a[140:143], v[204:207]\n"
"v_mfma_f32_16x16x32_bf16 v[208:211], a[168:171], a[144:147], v[208:211]\n"
"ds_read_b64_tr_b16 a[228:229], v8 offset:0\n"
"v_mfma_f32_16x16x32_bf16 v[212:215], a[168:171], a[148:151], v[212:215]\n"
"v_mfma_f32_16x16x32_bf16 v[216:219], a[168:171], a[152:155], v[216:219]\n"
"ds_read_b64_tr_b16 a[230:231], v8 offset:1024\n"
"v_mfma_f32_16x16x32_bf16 v[220:223], a[168:171], a[156:159], v[220:223]\n"
"v_mfma_f32_16x16x32_bf16 v[224:227], a[172:175], a[128:131], v[224:227]\n"
"ds_read_b64_tr_b16 a[232:233], v9 offset:0\n"
"v_mfma_f32_16x16x32_bf16 v[228:231], a[172:175], a[132:135], v[228:231]\n"
"v_mfma_f32_16x16x32_bf16 v[232:235], a[172:175], a[136:139], v[232:235]\n"
"ds_read_b64_tr_b16 a[234:235], v9 offset:1024\n"
"v_mfma_f32_16x16x32_bf16 v[236:239], a[172:175], a[140:143], v[236:239]\n"
"v_mfma_f32_16x16x32_bf16 v[240:243], a[172:175], a[144:147], v[240:243]\n"
"ds_read_b64_tr_b16 a[236:237], v10 offset:0\n"
"v_mfma_f32_16x16x32_bf16 v[244:247], a[172:175], a[148:151], v[244:247]\n"
"v_mfma_f32_16x16x32_bf16 v[248:251], a[172:175], a[152:155], v[248:251]\n"
"ds_read_b64_tr_b16 a[238:239], v10 offset:1024\n"
"v_mfma_f32_16x16x32_bf16 v[252:255], a[172:175], a[156:159], v[252:255]\n"
"v_mfma_f32_16x16x32_bf16 a[0:3], a[176:179], a[128:131], a[0:3]\n"
"ds_read_b64_tr_b16 a[240:241], v7 offset:128\n"
"v_mfma_f32_16x16x32_bf16 a[4:7], a[176:179], a[132:135], a[4:7]\n"
"v_mfma_f32_16x16x32_bf16 a[8:11], a[176:179], a[136:139], a[8:11]\n"
"ds_read_b64_tr_b16 a[242:243], v7 offset:1152\n"
"v_mfma_f32_16x16x32_bf16 a[12:15], a[176:179], a[140:143], a[12:15]\n"
"v_mfma_f32_16x16x32_bf16 a[16:19], a[176:179], a[144:147], a[16:19]\n"
"ds_read_b64_tr_b16 a[244:245], v8 offset:128\n"
"v_mfma_f32_16x16x32_bf16 a[20:23], a[176:179], a[148:151], a[20:23]\n"
"v_mfma_f32_16x16x32_bf16 a[24:27], a[176:179], a[152:155], a[24:27]\n"
"ds_read_b64_tr_b16 a[246:247], v8 offset:1152\n"
"v_mfma_f32_16x16x32_bf16 a[28:31], a[176:179], a[156:159], a[28:31]\n"
"v_mfma_f32_16x16x32_bf16 a[32:35], a[180:183], a[128:131], a[32:35]\n"
"ds_read_b64_tr_b16 a[248:249], v9 offset:128\n"
"v_mfma_f32_16x16x32_bf16 a[36:39], a[180:183], a[132:135], a[36:39]\n"
"v_mfma_f32_16x16x32_bf16 a[40:43], a[180:183], a[136:139], a[40:43]\n"
"ds_read_b64_tr_b16 a[250:251], v9 offset:1152\n"
"v_mfma_f32_16x16x32_bf16 a[44:47], a[180:183], a[140:143], a[44:47]\n"
"v_mfma_f32_16x16x32_bf16 a[48:51], a[180:183], a[144:147], a[48:51]\n"
"ds_read_b64_tr_b16 a[252:253], v10 offset:128\n"
"v_mfma_f32_16x16x32_bf16 a[52:55], a[180:183], a[148:151], a[52:55]\n"
"v_mfma_f32_16x16x32_bf16 a[56:59], a[180:183], a[152:155], a[56:59]\n"
"ds_read_b64_tr_b16 a[254:255], v10 offset:1152\n"
"v_mfma_f32_16x16x32_bf16 a[60:63], a[180:183], a[156:159], a[60:63]\n"
"v_mfma_f32_16x16x32_bf16 a[64:67], a[184:187], a[128:131], a[64:67]\n"
"v_mfma_f32_16x16x32_bf16 a[68:71], a[184:187], a[132:135], a[68:71]\n"
"v_mfma_f32_16x16x32_bf16 a[72:75], a[184:187], a[136:139], a[72:75]\n"
"v_mfma_f32_16x16x32_bf16 a[76:79], a[184:187], a[140:143], a[76:79]\n"
"v_mfma_f32_16x16x32_bf16 a[80:83], a[184:187], a[144:147], a[80:83]\n"
"v_mfma_f32_16x16x32_bf16 a[84:87], a[184:187], a[148:151], a[84:87]\n"
"v_mfma_f32_16x16x32_bf16 a[88:91], a[184:187], a[152:155], a[88:91]\n"
"v_mfma_f32_16x16x32_bf16 a[92:95], a[184:187], a[156:159], a[92:95]\n"
"v_mfma_f32_16x16x32_bf16 a[96:99], a[188:191], a[128:131], a[96:99]\n"
"v_mfma_f32_16x16x32_bf16 a[100:103], a[188:191], a[132:135], a[100:103]\n"
"v_mfma_f32_16x16x32_bf16 a[104:107], a[188:191], a[136:139], a[104:107]\n"
"v_mfma_f32_16x16x32_bf16 a[108:111], a[188:191], a[140:143], a[108:111]\n"
"v_mfma_f32_16x16x32_bf16 a[112:115], a[188:191], a[144:147], a[112:115]\n"
"v_mfma_f32_16x16x32_bf16 a[116:119], a[188:191], a[148:151], a[116:119]\n"
"v_mfma_f32_16x16x32_bf16 a[120:123], a[188:191], a[152:155], a[120:123]\n"
"v_mfma_f32_16x16x32_bf16 a[124:127], a[188:191], a[156:159], a[124:127]\n"
"s_waitcnt lgkmcnt(0)\n"
"v_mfma_f32_16x16x32_bf16 v[128:131], a[224:227], a[192:195], v[128:131]\n"
"s_barrier\n"
"v_mfma_f32_16x16x32_bf16 v[132:135], a[224:227], a[196:199], v[132:135]\n"
"s_add_u32 m0, s82, 0xe400\n"
"v_mfma_f32_16x16x32_bf16 v[136:139], a[224:227], a[200:203], v[136:139]\n"
"v_mfma_f32_16x16x32_bf16 v[140:143], a[224:227], a[204:207], v[140:143]\n"
"buffer_load_dwordx4 v36, s[48:51], s68 offen lds ;@Q1\n"
"v_mfma_f32_16x16x32_bf16 v[144:147], a[224:227], a[208:211], v[144:147]\n"
"v_mfma_f32_16x16x32_bf16 v[148:151], a[224:227], a[212:215], v[148:151]\n"
"s_add_u32 m0, s82, 0xe800\n"
"v_mfma_f32_16x16x32_bf16 v[152:155], a[224:227], a[216:219], v[152:155]\n"
"v_mfma_f32_16x16x32_bf16 v[156:159], a[224:227], a[220:223], v[156:159]\n"
"buffer_load_dwordx4 v37, s[48:51], s69 offen lds ;@Q1\n"
"v_mfma_f32_16x16x32_bf16 v[160:163], a[228:231], a[192:195], v[160:163]\n"
"v_mfma_f32_16x16x32_bf16 v[164:167], a[228:231], a[196:199], v[164:167]\n"
"s_add_u32 m0, s82, 0xec00\n"
"v_mfma_f32_16x16x32_bf16 v[168:171], a[228:231], a[200:203], v[168:171]\n"
"v_mfma_f32_16x16x32_bf16 v[172:175], a[228:231], a[204:207], v[172:175]\n"
"buffer_load_dwordx4 v36, s[48:51], s70 offen lds ;@Q1\n"
"v_mfma_f32_16x16x32_bf16 v[176:179], a[228:231], a[208:211], v[176:179]\n"
"v_mfma_f32_16x16x32_bf16 v[180:183], a[228:231], a[212:215], v[180:183]\n"
"s_add_u32 m0, s82, 0xf000\n"
"v_mfma_f32_16x16x32_bf16 v[184:187], a[228:231], a[216:219], v[184:187]\n"
"v_mfma_f32_16x16x32_bf16 v[188:191], a[228:231], a[220:223], v[188:191]\n"
"v_mfma_f32_16x16x32_bf16 v[192:195], a[232:235], a[192:195], v[192:195]\n"
"buffer_load_dwordx4 v37, s[48:51], s71 offen lds ;@Q1\n"
"v_mfma_f32_16x16x32_bf16 v[196:199], a[232:235], a[196:199], v[196:199]\n"
"v_mfma_f32_16x16x32_bf16 v[200:203], a[232:235], a[200:203], v[200:203]\n"
"s_add_u32 m0, s82, 0xf400\n"
"v_mfma_f32_16x16x32_bf16 v[204:207], a[232:235], a[204:207], v[204:207]\n"
"v_mfma_f32_16x16x32_bf16 v[208:211], a[232:235], a[208:211], v[208:211]\n"
"buffer_load_dwordx4 v36, s[48:51], s72 offen lds ;@Q1\n"
"v_mfma_f32_16x16x32_bf16 v[212:215], a[232:235], a[212:215], v[212:215]\n"
"v_mfma_f32_16x16x32_bf16 v[216:219], a[232:235], a[216:219], v[216:219]\n"
"s_add_u32 m0, s82, 0xf800\n"
"v_mfma_f32_16x16x32_bf16 v[220:223], a[232:235], a[220:223], v[220:223]\n"
"v_mfma_f32_16x16x32_bf16 v[224:227], a[236:239], a[192:195], v[224:227]\n"
"buffer_load_dwordx4 v37, s[48:51], s73 offen lds ;@Q1\n"
"v_mfma_f32_16x16x32_bf16 v[228:231], a[236:239], a[196:199], v[228:231]\n"
"v_mfma_f32_16x16x32_bf16 v[232:235], a[236:239], a[200:203], v[232:235]\n"
"s_add_u32 m0, s82, 0xfc00\n"
"v_mfma_f32_16x16x32_bf16 v[236:239], a[236:239], a[204:207], v[236:239]\n"
"v_mfma_f32_16x16x32_bf16 v[240:243], a[236:239], a[208:211], v[240:243]\n"
"buffer_load_dwordx4 v36, s[48:51], s74 offen lds ;@Q1\n"
"v_mfma_f32_16x16x32_bf16 v[244:247], a[236:239], a[212:215], v[244:247]\n"
"v_mfma_f32_16x16x32_bf16 v[248:251], a[236:239], a[216:219], v[248:251]\n"
"v_mfma_f32_16x16x32_bf16 v[252:255], a[236:239], a[220:223], v[252:255]\n"
"s_add_u32 m0, s82, 0x10000\n"
"v_mfma_f32_16x16x32_bf16 a[0:3], a[240:243], a[192:195], a[0:3]\n"
"v_mfma_f32_16x16x32_bf16 a[4:7], a[240:243], a[196:199], a[4:7]\n"
"buffer_load_dwordx4 v37, s[48:51], s75 offen lds ;@Q1\n"
"v_mfma_f32_16x16x32_bf16 a[8:11], a[240:243], a[200:203], a[8:11]\n"
"v_mfma_f32_16x16x32_bf16 a[12:15], a[240:243], a[204:207], a[12:15]\n"
"s_add_u32 m0, s83, 0x16400\n"
"v_mfma_f32_16x16x32_bf16 a[16:19], a[240:243], a[208:211], a[16:19]\n"
"v_mfma_f32_16x16x32_bf16 a[20:23], a[240:243], a[212:215], a[20:23]\n"
"buffer_load_dwordx4 v38, s[52:55], s76 offen lds ;@Q1\n"
"v_mfma_f32_16x16x32_bf16 a[24:27], a[240:243], a[216:219], a[24:27]\n"
"v_mfma_f32_16x16x32_bf16 a[28:31], a[240:243], a[220:223], a[28:31]\n"
"s_add_u32 m0, s83, 0x16800\n"
"v_mfma_f32_16x16x32_bf16 a[32:35], a[244:247], a[192:195], a[32:35]\n"
"v_mfma_f32_16x16x32_bf16 a[36:39], a[244:247], a[196:199], a[36:39]\n"
"buffer_load_dwordx4 v38, s[52:55], s77 offen lds ;@Q1\n"
"v_mfma_f32_16x16x32_bf16 a[40:43], a[244:247], a[200:203], a[40:43]\n"
"v_mfma_f32_16x16x32_bf16 a[44:47], a[244:247], a[204:207], a[44:47]\n"
"s_add_u32 m0, s83, 0x16c80\n"
"v_mfma_f32_16x16x32_bf16 a[48:51], a[244:247], a[208:211], a[48:51]\n"
"v_mfma_f32_16x16x32_bf16 a[52:55], a[244:247], a[212:215], a[52:55]\n"
"v_mfma_f32_16x16x32_bf16 a[56:59], a[244:247], a[216:219], a[56:59]\n"
"buffer_load_dwordx4 v38, s[52:55], s78 offen lds ;@Q1\n"
"v_mfma_f32_16x16x32_bf16 a[60:63], a[244:247], a[220:223], a[60:63]\n"
"v_mfma_f32_16x16x32_bf16 a[64:67], a[248:251], a[192:195], a[64:67]\n"
"s_add_u32 m0, s83, 0x17080\n"
"v_mfma_f32_16x16x32_bf16 a[68:71], a[248:251], a[196:199], a[68:71]\n"
"v_mfma_f32_16x16x32_bf16 a[72:75], a[248:251], a[200:203], a[72:75]\n"
"buffer_load_dwordx4 v38, s[52:55], s79 offen lds ;@Q1\n"
"v_mfma_f32_16x16x32_bf16 a[76:79], a[248:251], a[204:207], a[76:79]\n"
"v_mfma_f32_16x16x32_bf16 a[80:83], a[248:251], a[208:211], a[80:83]\n"
"s_add_u32 m0, s84, 0x1a800\n"
"v_mfma_f32_16x16x32_bf16 a[84:87], a[248:251], a[212:215], a[84:87]\n"
"v_mfma_f32_16x16x32_bf16 a[88:91], a[248:251], a[216:219], a[88:91]\n"
"buffer_load_dwordx4 v39, s[56:59], s80 offen lds ;@Q1\n"
"v_mfma_f32_16x16x32_bf16 a[92:95], a[248:251], a[220:223], a[92:95]\n"
"v_mfma_f32_16x16x32_bf16 a[96:99], a[252:255], a[192:195], a[96:99]\n"
"s_add_u32 m0, s84, 0x1ac00\n"
"v_mfma_f32_16x16x32_bf16 a[100:103], a[252:255], a[196:199], a[100:103]\n"
"v_mfma_f32_16x16x32_bf16 a[104:107], a[252:255], a[200:203], a[104:107]\n"
"buffer_load_dwordx4 v40, s[56:59], s81 offen lds ;@Q1\n"
"v_mfma_f32_16x16x32_bf16 a[108:111], a[252:255], a[204:207], a[108:111]\n"
"v_add_u32 v36, 0x80, v36\n"
"v_add_u32 v37, 0x80, v37\n"
"v_mfma_f32_16x16x32_bf16 a[112:115], a[252:255], a[208:211], a[112:115]\n"
"v_add_u32 v38, 0x80000, v38\n"
"v_add_u32 v39, s91, v39\n"
"v_mfma_f32_16x16x32_bf16 a[116:119], a[252:255], a[212:215], a[116:119]\n"
"v_add_u32 v40, s91, v40\n"
"v_mfma_f32_16x16x32_bf16 a[120:123], a[252:255], a[216:219], a[120:123]\n"
"v_mfma_f32_16x16x32_bf16 a[124:127], a[252:255], a[220:223], a[124:127]\n"
"s_lshl_b32 s44, s37, 20\n"
"s_add_u32 s60, s26, s44\n"
"s_addc_u32 s61, s27, 0\n"
"s_and_b32 s61, s61, 0xffff\n"
"s_mov_b32 s62, 0x100000\n"
"s_mov_b32 s63, 0x20000\n"
"s_lshl_b32 s44, s38, 17\n"
"s_lshl_b32 s45, s39, 16\n"
"s_add_u32 s46, s44, s45\n"
"v_mov_b32 v17, v14\n"
"v_add_u32 v41, 0x1000, v14\n"
"s_add_u32 s86, s46, 0\n"
"s_add_u32 s87, s46, 2048\n"
"s_add_u32 s88, s46, 1024\n"
"s_add_u32 s89, s46, 3072\n"
"v_accvgpr_read_b32 v48, a0\n"
"v_accvgpr_read_b32 v49, a1\n"
"v_accvgpr_read_b32 v50, a2\n"
"v_accvgpr_read_b32 v51, a3\n"
"v_accvgpr_read_b32 v52, a32\n"
"v_accvgpr_read_b32 v53, a33\n"
"v_accvgpr_read_b32 v54, a34\n"
"v_accvgpr_read_b32 v55, a35\n"
"v_cvt_pk_bf16_f32 v128, v128, v129\n"
"v_cvt_pk_bf16_f32 v129, v130, v131\n"
"v_cvt_pk_bf16_f32 v130, v160, v161\n"
"v_cvt_pk_bf16_f32 v131, v162, v163\n"
"v_cvt_pk_bf16_f32 v160, v48, v49\n"
"v_cvt_pk_bf16_f32 v161, v50, v51\n"
"v_cvt_pk_bf16_f32 v162, v52, v53\n"
"v_cvt_pk_bf16_f32 v163, v54, v55\n"
"v_accvgpr_read_b32 v56, a64\n"
"v_accvgpr_read_b32 v57, a65\n"
"v_accvgpr_read_b32 v58, a66\n"
"v_accvgpr_read_b32 v59, a67\n"
"v_accvgpr_read_b32 v60, a96\n"
"v_accvgpr_read_b32 v61, a97\n"
"v_accvgpr_read_b32 v62, a98\n"
"v_accvgpr_read_b32 v63, a99\n"
"v_cvt_pk_bf16_f32 v192, v192, v193\n"
"v_cvt_pk_bf16_f32 v193, v194, v195\n"
"v_cvt_pk_bf16_f32 v194, v224, v225\n"
"v_cvt_pk_bf16_f32 v195, v226, v227\n"
"v_cvt_pk_bf16_f32 v224, v56, v57\n"
"v_cvt_pk_bf16_f32 v225, v58, v59\n"
"v_cvt_pk_bf16_f32 v226, v60, v61\n"
"v_cvt_pk_bf16_f32 v227, v62, v63\n"
"v_accvgpr_read_b32 v48, a4\n"
"v_accvgpr_read_b32 v49, a5\n"
"v_accvgpr_read_b32 v50, a6\n"
"v_accvgpr_read_b32 v51, a7\n"
"v_accvgpr_read_b32 v52, a36\n"
"v_accvgpr_read_b32 v53, a37\n"
"v_accvgpr_read_b32 v54, a38\n"
"v_accvgpr_read_b32 v55, a39\n"
"v_cvt_pk_bf16_f32 v132, v132, v133\n"
"v_cvt_pk_bf16_f32 v133, v134, v135\n"
"v_cvt_pk_bf16_f32 v134, v164, v165\n"
"v_cvt_pk_bf16_f32 v135, v166, v167\n"
"v_cvt_pk_bf16_f32 v164, v48, v49\n"
"v_cvt_pk_bf16_f32 v165, v50, v51\n"
"v_cvt_pk_bf16_f32 v166, v52, v53\n"
"v_cvt_pk_bf16_f32 v167, v54, v55\n"
"v_accvgpr_read_b32 v56, a68\n"
"v_accvgpr_read_b32 v57, a69\n"
"v_accvgpr_read_b32 v58, a70\n"
"v_accvgpr_read_b32 v59, a71\n"
"v_accvgpr_read_b32 v60, a100\n"
"v_accvgpr_read_b32 v61, a101\n"
"v_accvgpr_read_b32 v62, a102\n"
"v_accvgpr_read_b32 v63, a103\n"
"v_cvt_pk_bf16_f32 v196, v196, v197\n"
"v_cvt_pk_bf16_f32 v197, v198, v199\n"
"v_cvt_pk_bf16_f32 v198, v228, v229\n"
"v_cvt_pk_bf16_f32 v199, v230, v231\n"
"v_cvt_pk_bf16_f32 v228, v56, v57\n"
"v_cvt_pk_bf16_f32 v229, v58, v59\n"
"v_cvt_pk_bf16_f32 v230, v60, v61\n"
"v_cvt_pk_bf16_f32 v231, v62, v63\n"
"v_accvgpr_read_b32 v48, a8\n"
"v_accvgpr_read_b32 v49, a9\n"
"v_accvgpr_read_b32 v50, a10\n"
"v_accvgpr_read_b32 v51, a11\n"
"v_accvgpr_read_b32 v52, a40\n"
"v_accvgpr_read_b32 v53, a41\n"
"v_accvgpr_read_b32 v54, a42\n"
"v_accvgpr_read_b32 v55, a43\n"
"v_cvt_pk_bf16_f32 v136, v136, v137\n"
"v_cvt_pk_bf16_f32 v137, v138, v139\n"
"v_cvt_pk_bf16_f32 v138, v168, v169\n"
"v_cvt_pk_bf16_f32 v139, v170, v171\n"
"v_cvt_pk_bf16_f32 v168, v48, v49\n"
"v_cvt_pk_bf16_f32 v169, v50, v51\n"
"v_cvt_pk_bf16_f32 v170, v52, v53\n"
"v_cvt_pk_bf16_f32 v171, v54, v55\n"
"v_accvgpr_read_b32 v56, a72\n"
"v_accvgpr_read_b32 v57, a73\n"
"v_accvgpr_read_b32 v58, a74\n"
"v_accvgpr_read_b32 v59, a75\n"
"v_accvgpr_read_b32 v60, a104\n"
"v_accvgpr_read_b32 v61, a105\n"
"v_accvgpr_read_b32 v62, a106\n"
"v_accvgpr_read_b32 v63, a107\n"
"v_cvt_pk_bf16_f32 v200, v200, v201\n"
"v_cvt_pk_bf16_f32 v201, v202, v203\n"
"v_cvt_pk_bf16_f32 v202, v232, v233\n"
"v_cvt_pk_bf16_f32 v203, v234, v235\n"
"v_cvt_pk_bf16_f32 v232, v56, v57\n"
"v_cvt_pk_bf16_f32 v233, v58, v59\n"
"v_cvt_pk_bf16_f32 v234, v60, v61\n"
"v_cvt_pk_bf16_f32 v235, v62, v63\n"
"v_accvgpr_read_b32 v48, a12\n"
"v_accvgpr_read_b32 v49, a13\n"
"v_accvgpr_read_b32 v50, a14\n"
"v_accvgpr_read_b32 v51, a15\n"
"v_accvgpr_read_b32 v52, a44\n"
"v_accvgpr_read_b32 v53, a45\n"
"v_accvgpr_read_b32 v54, a46\n"
"v_accvgpr_read_b32 v55, a47\n"
"v_cvt_pk_bf16_f32 v140, v140, v141\n"
"v_cvt_pk_bf16_f32 v141, v142, v143\n"
"v_cvt_pk_bf16_f32 v142, v172, v173\n"
"v_cvt_pk_bf16_f32 v143, v174, v175\n"
"v_cvt_pk_bf16_f32 v172, v48, v49\n"
"v_cvt_pk_bf16_f32 v173, v50, v51\n"
"v_cvt_pk_bf16_f32 v174, v52, v53\n"
"v_cvt_pk_bf16_f32 v175, v54, v55\n"
"v_accvgpr_read_b32 v56, a76\n"
"v_accvgpr_read_b32 v57, a77\n"
"v_accvgpr_read_b32 v58, a78\n"
"v_accvgpr_read_b32 v59, a79\n"
"v_accvgpr_read_b32 v60, a108\n"
"v_accvgpr_read_b32 v61, a109\n"
"v_accvgpr_read_b32 v62, a110\n"
"v_accvgpr_read_b32 v63, a111\n"
"v_cvt_pk_bf16_f32 v204, v204, v205\n"
"v_cvt_pk_bf16_f32 v205, v206, v207\n"
"v_cvt_pk_bf16_f32 v206, v236, v237\n"
"v_cvt_pk_bf16_f32 v207, v238, v239\n"
"v_cvt_pk_bf16_f32 v236, v56, v57\n"
"v_cvt_pk_bf16_f32 v237, v58, v59\n"
"v_cvt_pk_bf16_f32 v238, v60, v61\n"
"v_cvt_pk_bf16_f32 v239, v62, v63\n"
"v_accvgpr_read_b32 v48, a16\n"
"v_accvgpr_read_b32 v49, a17\n"
"v_accvgpr_read_b32 v50, a18\n"
"v_accvgpr_read_b32 v51, a19\n"
"v_accvgpr_read_b32 v52, a48\n"
"v_accvgpr_read_b32 v53, a49\n"
"v_accvgpr_read_b32 v54, a50\n"
"v_accvgpr_read_b32 v55, a51\n"
"v_cvt_pk_bf16_f32 v144, v144, v145\n"
"v_cvt_pk_bf16_f32 v145, v146, v147\n"
"v_cvt_pk_bf16_f32 v146, v176, v177\n"
"v_cvt_pk_bf16_f32 v147, v178, v179\n"
"v_cvt_pk_bf16_f32 v176, v48, v49\n"
"v_cvt_pk_bf16_f32 v177, v50, v51\n"
"v_cvt_pk_bf16_f32 v178, v52, v53\n"
"v_cvt_pk_bf16_f32 v179, v54, v55\n"
"v_accvgpr_read_b32 v56, a80\n"
"v_accvgpr_read_b32 v57, a81\n"
"v_accvgpr_read_b32 v58, a82\n"
"v_accvgpr_read_b32 v59, a83\n"
"v_accvgpr_read_b32 v60, a112\n"
"v_accvgpr_read_b32 v61, a113\n"
"v_accvgpr_read_b32 v62, a114\n"
"v_accvgpr_read_b32 v63, a115\n"
"v_cvt_pk_bf16_f32 v208, v208, v209\n"
"v_cvt_pk_bf16_f32 v209, v210, v211\n"
"v_cvt_pk_bf16_f32 v210, v240, v241\n"
"v_cvt_pk_bf16_f32 v211, v242, v243\n"
"v_cvt_pk_bf16_f32 v240, v56, v57\n"
"v_cvt_pk_bf16_f32 v241, v58, v59\n"
"v_cvt_pk_bf16_f32 v242, v60, v61\n"
"v_cvt_pk_bf16_f32 v243, v62, v63\n"
"v_accvgpr_read_b32 v48, a20\n"
"v_accvgpr_read_b32 v49, a21\n"
"v_accvgpr_read_b32 v50, a22\n"
"v_accvgpr_read_b32 v51, a23\n"
"v_accvgpr_read_b32 v52, a52\n"
"v_accvgpr_read_b32 v53, a53\n"
"v_accvgpr_read_b32 v54, a54\n"
"v_accvgpr_read_b32 v55, a55\n"
"v_cvt_pk_bf16_f32 v148, v148, v149\n"
"v_cvt_pk_bf16_f32 v149, v150, v151\n"
"v_cvt_pk_bf16_f32 v150, v180, v181\n"
"v_cvt_pk_bf16_f32 v151, v182, v183\n"
"v_cvt_pk_bf16_f32 v180, v48, v49\n"
"v_cvt_pk_bf16_f32 v181, v50, v51\n"
"v_cvt_pk_bf16_f32 v182, v52, v53\n"
"v_cvt_pk_bf16_f32 v183, v54, v55\n"
"v_accvgpr_read_b32 v56, a84\n"
"v_accvgpr_read_b32 v57, a85\n"
"v_accvgpr_read_b32 v58, a86\n"
"v_accvgpr_read_b32 v59, a87\n"
"v_accvgpr_read_b32 v60, a116\n"
"v_accvgpr_read_b32 v61, a117\n"
"v_accvgpr_read_b32 v62, a118\n"
"v_accvgpr_read_b32 v63, a119\n"
"v_cvt_pk_bf16_f32 v212, v212, v213\n"
"v_cvt_pk_bf16_f32 v213, v214, v215\n"
"v_cvt_pk_bf16_f32 v214, v244, v245\n"
"v_cvt_pk_bf16_f32 v215, v246, v247\n"
"v_cvt_pk_bf16_f32 v244, v56, v57\n"
"v_cvt_pk_bf16_f32 v245, v58, v59\n"
"v_cvt_pk_bf16_f32 v246, v60, v61\n"
"v_cvt_pk_bf16_f32 v247, v62, v63\n"
"v_accvgpr_read_b32 v48, a24\n"
"v_accvgpr_read_b32 v49, a25\n"
"v_accvgpr_read_b32 v50, a26\n"
"v_accvgpr_read_b32 v51, a27\n"
"v_accvgpr_read_b32 v52, a56\n"
"v_accvgpr_read_b32 v53, a57\n"
"v_accvgpr_read_b32 v54, a58\n"
"v_accvgpr_read_b32 v55, a59\n"
"v_cvt_pk_bf16_f32 v152, v152, v153\n"
"v_cvt_pk_bf16_f32 v153, v154, v155\n"
"v_cvt_pk_bf16_f32 v154, v184, v185\n"
"v_cvt_pk_bf16_f32 v155, v186, v187\n"
"v_cvt_pk_bf16_f32 v184, v48, v49\n"
"v_cvt_pk_bf16_f32 v185, v50, v51\n"
"v_cvt_pk_bf16_f32 v186, v52, v53\n"
"v_cvt_pk_bf16_f32 v187, v54, v55\n"
"v_accvgpr_read_b32 v56, a88\n"
"v_accvgpr_read_b32 v57, a89\n"
"v_accvgpr_read_b32 v58, a90\n"
"v_accvgpr_read_b32 v59, a91\n"
"v_accvgpr_read_b32 v60, a120\n"
"v_accvgpr_read_b32 v61, a121\n"
"v_accvgpr_read_b32 v62, a122\n"
"v_accvgpr_read_b32 v63, a123\n"
"v_cvt_pk_bf16_f32 v216, v216, v217\n"
"v_cvt_pk_bf16_f32 v217, v218, v219\n"
"v_cvt_pk_bf16_f32 v218, v248, v249\n"
"v_cvt_pk_bf16_f32 v219, v250, v251\n"
"v_cvt_pk_bf16_f32 v248, v56, v57\n"
"v_cvt_pk_bf16_f32 v249, v58, v59\n"
"v_cvt_pk_bf16_f32 v250, v60, v61\n"
"v_cvt_pk_bf16_f32 v251, v62, v63\n"
"v_accvgpr_read_b32 v48, a28\n"
"v_accvgpr_read_b32 v49, a29\n"
"v_accvgpr_read_b32 v50, a30\n"
"v_accvgpr_read_b32 v51, a31\n"
"v_accvgpr_read_b32 v52, a60\n"
"v_accvgpr_read_b32 v53, a61\n"
"v_accvgpr_read_b32 v54, a62\n"
"v_accvgpr_read_b32 v55, a63\n"
"v_cvt_pk_bf16_f32 v156, v156, v157\n"
"v_cvt_pk_bf16_f32 v157, v158, v159\n"
"v_cvt_pk_bf16_f32 v158, v188, v189\n"
"v_cvt_pk_bf16_f32 v159, v190, v191\n"
"v_cvt_pk_bf16_f32 v188, v48, v49\n"
"v_cvt_pk_bf16_f32 v189, v50, v51\n"
"v_cvt_pk_bf16_f32 v190, v52, v53\n"
"v_cvt_pk_bf16_f32 v191, v54, v55\n"
"v_accvgpr_read_b32 v56, a92\n"
"v_accvgpr_read_b32 v57, a93\n"
"v_accvgpr_read_b32 v58, a94\n"
"v_accvgpr_read_b32 v59, a95\n"
"v_accvgpr_read_b32 v60, a124\n"
"v_accvgpr_read_b32 v61, a125\n"
"v_accvgpr_read_b32 v62, a126\n"
"v_accvgpr_read_b32 v63, a127\n"
"v_cvt_pk_bf16_f32 v220, v220, v221\n"
"v_cvt_pk_bf16_f32 v221, v222, v223\n"
"v_cvt_pk_bf16_f32 v222, v252, v253\n"
"v_cvt_pk_bf16_f32 v223, v254, v255\n"
"v_cvt_pk_bf16_f32 v252, v56, v57\n"
"v_cvt_pk_bf16_f32 v253, v58, v59\n"
"v_cvt_pk_bf16_f32 v254, v60, v61\n"
"v_cvt_pk_bf16_f32 v255, v62, v63\n"
"s_branch LM_q\n"
"LM_vw:\n"
"ds_read_b64_tr_b16 a[160:161], v3 offset:0\n"
"ds_read_b64_tr_b16 a[162:163], v3 offset:1024\n"
"ds_read_b128 a[128:131], v1 offset:0\n"
"ds_read_b128 a[132:135], v1 offset:2048\n"
"ds_read_b128 a[136:139], v1 offset:4096\n"
"ds_read_b128 a[140:143], v1 offset:6144\n"
"ds_read_b128 a[144:147], v1 offset:8192\n"
"ds_read_b128 a[148:151], v1 offset:10240\n"
"ds_read_b128 a[152:155], v1 offset:12288\n"
"ds_read_b128 a[156:159], v1 offset:14336\n"
"ds_read_b64_tr_b16 a[164:165], v4 offset:0\n"
"ds_read_b64_tr_b16 a[166:167], v4 offset:1024\n"
"ds_read_b64_tr_b16 a[168:169], v5 offset:0\n"
"ds_read_b64_tr_b16 a[170:171], v5 offset:1024\n"
"ds_read_b64_tr_b16 a[172:173], v6 offset:0\n"
"ds_read_b64_tr_b16 a[174:175], v6 offset:1024\n"
"ds_read_b64_tr_b16 a[176:177], v3 offset:128\n"
"ds_read_b64_tr_b16 a[178:179], v3 offset:1152\n"
"ds_read_b64_tr_b16 a[180:181], v4 offset:128\n"
"ds_read_b64_tr_b16 a[182:183], v4 offset:1152\n"
"ds_read_b64_tr_b16 a[184:185], v5 offset:128\n"
"ds_read_b64_tr_b16 a[186:187], v5 offset:1152\n"
"ds_read_b64_tr_b16 a[188:189], v6 offset:128\n"
"ds_read_b64_tr_b16 a[190:191], v6 offset:1152\n"
"s_waitcnt lgkmcnt(0)\n"
"v_mfma_f32_16x16x32_bf16 v[128:131], a[128:131], a[160:163], 0\n"
"ds_read_b64_tr_b16 a[224:225], v7 offset:0\n"
"v_add_u32 v1, 0x10800, v1\n"
"v_mfma_f32_16x16x32_bf16 v[132:135], a[132:135], a[160:163], 0\n"
"v_add_u32 v3, 0x10800, v3\n"
"v_add_u32 v4, 0x10800, v4\n"
"v_mfma_f32_16x16x32_bf16 v[136:139], a[136:139], a[160:163], 0\n"
"ds_read_b64_tr_b16 a[226:227], v7 offset:1024\n"
"v_add_u32 v5, 0x10800, v5\n"
"v_mfma_f32_16x16x32_bf16 v[140:143], a[140:143], a[160:163], 0\n"
"v_add_u32 v6, 0x10800, v6\n"
"v_mfma_f32_16x16x32_bf16 v[144:147], a[144:147], a[160:163], 0\n"
"ds_read_b128 a[192:195], v2 offset:0\n"
"v_mfma_f32_16x16x32_bf16 v[148:151], a[148:151], a[160:163], 0\n"
"v_mfma_f32_16x16x32_bf16 v[152:155], a[152:155], a[160:163], 0\n"
"ds_read_b128 a[196:199], v2 offset:2048\n"
"v_mfma_f32_16x16x32_bf16 v[156:159], a[156:159], a[160:163], 0\n"
"v_mfma_f32_16x16x32_bf16 v[160:163], a[128:131], a[164:167], 0\n"
"ds_read_b128 a[200:203], v2 offset:4096\n"
"v_mfma_f32_16x16x32_bf16 v[164:167], a[132:135], a[164:167], 0\n"
"v_mfma_f32_16x16x32_bf16 v[168:171], a[136:139], a[164:167], 0\n"
"ds_read_b128 a[204:207], v2 offset:6144\n"
"v_mfma_f32_16x16x32_bf16 v[172:175], a[140:143], a[164:167], 0\n"
"v_mfma_f32_16x16x32_bf16 v[176:179], a[144:147], a[164:167], 0\n"
"ds_read_b128 a[208:211], v2 offset:8192\n"
"v_mfma_f32_16x16x32_bf16 v[180:183], a[148:151], a[164:167], 0\n"
"v_mfma_f32_16x16x32_bf16 v[184:187], a[152:155], a[164:167], 0\n"
"ds_read_b128 a[212:215], v2 offset:10240\n"
"v_mfma_f32_16x16x32_bf16 v[188:191], a[156:159], a[164:167], 0\n"
"v_mfma_f32_16x16x32_bf16 v[192:195], a[128:131], a[168:171], 0\n"
"ds_read_b128 a[216:219], v2 offset:12288\n"
"v_mfma_f32_16x16x32_bf16 v[196:199], a[132:135], a[168:171], 0\n"
"v_mfma_f32_16x16x32_bf16 v[200:203], a[136:139], a[168:171], 0\n"
"ds_read_b128 a[220:223], v2 offset:14336\n"
"v_mfma_f32_16x16x32_bf16 v[204:207], a[140:143], a[168:171], 0\n"
"v_mfma_f32_16x16x32_bf16 v[208:211], a[144:147], a[168:171], 0\n"
"ds_read_b64_tr_b16 a[228:229], v8 offset:0\n"
"v_mfma_f32_16x16x32_bf16 v[212:215], a[148:151], a[168:171], 0\n"
"v_mfma_f32_16x16x32_bf16 v[216:219], a[152:155], a[168:171], 0\n"
"ds_read_b64_tr_b16 a[230:231], v8 offset:1024\n"
"v_mfma_f32_16x16x32_bf16 v[220:223], a[156:159], a[168:171], 0\n"
"v_mfma_f32_16x16x32_bf16 v[224:227], a[128:131], a[172:175], 0\n"
"ds_read_b64_tr_b16 a[232:233], v9 offset:0\n"
"v_mfma_f32_16x16x32_bf16 v[228:231], a[132:135], a[172:175], 0\n"
"v_mfma_f32_16x16x32_bf16 v[232:235], a[136:139], a[172:175], 0\n"
"ds_read_b64_tr_b16 a[234:235], v9 offset:1024\n"
"v_mfma_f32_16x16x32_bf16 v[236:239], a[140:143], a[172:175], 0\n"
"v_mfma_f32_16x16x32_bf16 v[240:243], a[144:147], a[172:175], 0\n"
"ds_read_b64_tr_b16 a[236:237], v10 offset:0\n"
"v_mfma_f32_16x16x32_bf16 v[244:247], a[148:151], a[172:175], 0\n"
"v_mfma_f32_16x16x32_bf16 v[248:251], a[152:155], a[172:175], 0\n"
"ds_read_b64_tr_b16 a[238:239], v10 offset:1024\n"
"v_mfma_f32_16x16x32_bf16 v[252:255], a[156:159], a[172:175], 0\n"
"v_mfma_f32_16x16x32_bf16 a[0:3], a[128:131], a[176:179], 0\n"
"ds_read_b64_tr_b16 a[240:241], v7 offset:128\n"
"v_mfma_f32_16x16x32_bf16 a[4:7], a[132:135], a[176:179], 0\n"
"v_mfma_f32_16x16x32_bf16 a[8:11], a[136:139], a[176:179], 0\n"
"ds_read_b64_tr_b16 a[242:243], v7 offset:1152\n"
"v_mfma_f32_16x16x32_bf16 a[12:15], a[140:143], a[176:179], 0\n"
"v_mfma_f32_16x16x32_bf16 a[16:19], a[144:147], a[176:179], 0\n"
"ds_read_b64_tr_b16 a[244:245], v8 offset:128\n"
"v_mfma_f32_16x16x32_bf16 a[20:23], a[148:151], a[176:179], 0\n"
"v_mfma_f32_16x16x32_bf16 a[24:27], a[152:155], a[176:179], 0\n"
"ds_read_b64_tr_b16 a[246:247], v8 offset:1152\n"
"v_mfma_f32_16x16x32_bf16 a[28:31], a[156:159], a[176:179], 0\n"
"v_mfma_f32_16x16x32_bf16 a[32:35], a[128:131], a[180:183], 0\n"
"ds_read_b64_tr_b16 a[248:249], v9 offset:128\n"
"v_mfma_f32_16x16x32_bf16 a[36:39], a[132:135], a[180:183], 0\n"
"v_mfma_f32_16x16x32_bf16 a[40:43], a[136:139], a[180:183], 0\n"
"ds_read_b64_tr_b16 a[250:251], v9 offset:1152\n"
"v_mfma_f32_16x16x32_bf16 a[44:47], a[140:143], a[180:183], 0\n"
"v_mfma_f32_16x16x32_bf16 a[48:51], a[144:147], a[180:183], 0\n"
"ds_read_b64_tr_b16 a[252:253], v10 offset:128\n"
"v_mfma_f32_16x16x32_bf16 a[52:55], a[148:151], a[180:183], 0\n"
"v_mfma_f32_16x16x32_bf16 a[56:59], a[152:155], a[180:183], 0\n"
"ds_read_b64_tr_b16 a[254:255], v10 offset:1152\n"
"v_mfma_f32_16x16x32_bf16 a[60:63], a[156:159], a[180:183], 0\n"
"v_mfma_f32_16x16x32_bf16 a[64:67], a[128:131], a[184:187], 0\n"
"v_mfma_f32_16x16x32_bf16 a[68:71], a[132:135], a[184:187], 0\n"
"v_mfma_f32_16x16x32_bf16 a[72:75], a[136:139], a[184:187], 0\n"
"v_mfma_f32_16x16x32_bf16 a[76:79], a[140:143], a[184:187], 0\n"
"v_mfma_f32_16x16x32_bf16 a[80:83], a[144:147], a[184:187], 0\n"
"v_mfma_f32_16x16x32_bf16 a[84:87], a[148:151], a[184:187], 0\n"
"v_mfma_f32_16x16x32_bf16 a[88:91], a[152:155], a[184:187], 0\n"
"v_mfma_f32_16x16x32_bf16 a[92:95], a[156:159], a[184:187], 0\n"
"v_mfma_f32_16x16x32_bf16 a[96:99], a[128:131], a[188:191], 0\n"
"v_mfma_f32_16x16x32_bf16 a[100:103], a[132:135], a[188:191], 0\n"
"v_mfma_f32_16x16x32_bf16 a[104:107], a[136:139], a[188:191], 0\n"
"v_mfma_f32_16x16x32_bf16 a[108:111], a[140:143], a[188:191], 0\n"
"v_mfma_f32_16x16x32_bf16 a[112:115], a[144:147], a[188:191], 0\n"
"v_mfma_f32_16x16x32_bf16 a[116:119], a[148:151], a[188:191], 0\n"
"v_mfma_f32_16x16x32_bf16 a[120:123], a[152:155], a[188:191], 0\n"
"v_mfma_f32_16x16x32_bf16 a[124:127], a[156:159], a[188:191], 0\n"
"s_waitcnt lgkmcnt(0)\n"
"v_mfma_f32_16x16x32_bf16 v[128:131], a[192:195], a[224:227], v[128:131]\n"
"s_waitcnt vmcnt(2)\n"
"s_barrier\n"
"v_mfma_f32_16x16x32_bf16 v[132:135], a[196:199], a[224:227], v[132:135]\n"
"s_add_u32 m0, s82, 0x0\n"
"ds_read_b64_tr_b16 a[160:161], v3 offset:0\n"
"v_mfma_f32_16x16x32_bf16 v[136:139], a[200:203], a[224:227], v[136:139]\n"
"buffer_load_dwordx4 v11, s[48:51], s68 offen lds\n"
"v_mfma_f32_16x16x32_bf16 v[140:143], a[204:207], a[224:227], v[140:143]\n"
"ds_read_b64_tr_b16 a[162:163], v3 offset:1024\n"
"v_add_u32 v2, 0x10800, v2\n"
"v_mfma_f32_16x16x32_bf16 v[144:147], a[208:211], a[224:227], v[144:147]\n"
"s_add_u32 m0, s82, 0x400\n"
"v_add_u32 v7, 0x10800, v7\n"
"v_mfma_f32_16x16x32_bf16 v[148:151], a[212:215], a[224:227], v[148:151]\n"
"ds_read_b128 a[128:131], v1 offset:0\n"
"v_add_u32 v8, 0x10800, v8\n"
"v_mfma_f32_16x16x32_bf16 v[152:155], a[216:219], a[224:227], v[152:155]\n"
"buffer_load_dwordx4 v12, s[48:51], s69 offen lds\n"
"v_mfma_f32_16x16x32_bf16 v[156:159], a[220:223], a[224:227], v[156:159]\n"
"ds_read_b128 a[132:135], v1 offset:2048\n"
"v_add_u32 v9, 0x10800, v9\n"
"v_mfma_f32_16x16x32_bf16 v[160:163], a[192:195], a[228:231], v[160:163]\n"
"s_add_u32 m0, s82, 0x800\n"
"v_add_u32 v10, 0x10800, v10\n"
"v_mfma_f32_16x16x32_bf16 v[164:167], a[196:199], a[228:231], v[164:167]\n"
"ds_read_b128 a[136:139], v1 offset:4096\n"
"v_mfma_f32_16x16x32_bf16 v[168:171], a[200:203], a[228:231], v[168:171]\n"
"buffer_load_dwordx4 v11, s[48:51], s70 offen lds\n"
"v_mfma_f32_16x16x32_bf16 v[172:175], a[204:207], a[228:231], v[172:175]\n"
"s_add_u32 m0, s82, 0xc00\n"
"ds_read_b128 a[140:143], v1 offset:6144\n"
"v_mfma_f32_16x16x32_bf16 v[176:179], a[208:211], a[228:231], v[176:179]\n"
"v_mfma_f32_16x16x32_bf16 v[180:183], a[212:215], a[228:231], v[180:183]\n"
"buffer_load_dwordx4 v12, s[48:51], s71 offen lds\n"
"v_mfma_f32_16x16x32_bf16 v[184:187], a[216:219], a[228:231], v[184:187]\n"
"ds_read_b128 a[144:147], v1 offset:8192\n"
"v_mfma_f32_16x16x32_bf16 v[188:191], a[220:223], a[228:231], v[188:191]\n"
"s_add_u32 m0, s82, 0x1000\n"
"ds_read_b128 a[148:151], v1 offset:10240\n"
"v_mfma_f32_16x16x32_bf16 v[192:195], a[192:195], a[232:235], v[192:195]\n"
"v_mfma_f32_16x16x32_bf16 v[196:199], a[196:199], a[232:235], v[196:199]\n"
"buffer_load_dwordx4 v11, s[48:51], s72 offen lds\n"
"v_mfma_f32_16x16x32_bf16 v[200:203], a[200:203], a[232:235], v[200:203]\n"
"ds_read_b128 a[152:155], v1 offset:12288\n"
"v_mfma_f32_16x16x32_bf16 v[204:207], a[204:207], a[232:235], v[204:207]\n"
"s_add_u32 m0, s82, 0x1400\n"
"ds_read_b128 a[156:159], v1 offset:14336\n"
"v_mfma_f32_16x16x32_bf16 v[208:211], a[208:211], a[232:235], v[208:211]\n"
"v_mfma_f32_16x16x32_bf16 v[212:215], a[212:215], a[232:235], v[212:215]\n"
"buffer_load_dwordx4 v12, s[48:51], s73 offen lds\n"
"v_mfma_f32_16x16x32_bf16 v[216:219], a[216:219], a[232:235], v[216:219]\n"
"s_add_u32 m0, s82, 0x1800\n"
"ds_read_b64_tr_b16 a[164:165], v4 offset:0\n"
"v_mfma_f32_16x16x32_bf16 v[220:223], a[220:223], a[232:235], v[220:223]\n"
"ds_read_b64_tr_b16 a[166:167], v4 offset:1024\n"
"v_mfma_f32_16x16x32_bf16 v[224:227], a[192:195], a[236:239], v[224:227]\n"
"buffer_load_dwordx4 v11, s[48:51], s74 offen lds\n"
"v_mfma_f32_16x16x32_bf16 v[228:231], a[196:199], a[236:239], v[228:231]\n"
"ds_read_b64_tr_b16 a[168:169], v5 offset:0\n"
"v_mfma_f32_16x16x32_bf16 v[232:235], a[200:203], a[236:239], v[232:235]\n"
"s_add_u32 m0, s82, 0x1c00\n"
"v_mfma_f32_16x16x32_bf16 v[236:239], a[204:207], a[236:239], v[236:239]\n"
"ds_read_b64_tr_b16 a[170:171], v5 offset:1024\n"
"v_mfma_f32_16x16x32_bf16 v[240:243], a[208:211], a[236:239], v[240:243]\n"
"buffer_load_dwordx4 v12, s[48:51], s75 offen lds\n"
"v_mfma_f32_16x16x32_bf16 v[244:247], a[212:215], a[236:239], v[244:247]\n"
"ds_read_b64_tr_b16 a[172:173], v6 offset:0\n"
"v_mfma_f32_16x16x32_bf16 v[248:251], a[216:219], a[236:239], v[248:251]\n"
"s_add_u32 m0, s83, 0x8000\n"
"v_mfma_f32_16x16x32_bf16 v[252:255], a[220:223], a[236:239], v[252:255]\n"
"buffer_load_dwordx4 v13, s[52:55], s76 offen lds\n"
"v_mfma_f32_16x16x32_bf16 a[0:3], a[192:195], a[240:243], a[0:3]\n"
"ds_read_b64_tr_b16 a[174:175], v6 offset:1024\n"
"v_mfma_f32_16x16x32_bf16 a[4:7], a[196:199], a[240:243], a[4:7]\n"
"s_add_u32 m0, s83, 0x8400\n"
"ds_read_b64_tr_b16 a[176:177], v3 offset:128\n"
"v_mfma_f32_16x16x32_bf16 a[8:11], a[200:203], a[240:243], a[8:11]\n"
"v_mfma_f32_16x16x32_bf16 a[12:15], a[204:207], a[240:243], a[12:15]\n"
"buffer_load_dwordx4 v13, s[52:55], s77 offen lds\n"
"v_mfma_f32_16x16x32_bf16 a[16:19], a[208:211], a[240:243], a[16:19]\n"
"ds_read_b64_tr_b16 a[178:179], v3 offset:1152\n"
"v_mfma_f32_16x16x32_bf16 a[20:23], a[212:215], a[240:243], a[20:23]\n"
"s_add_u32 m0, s83, 0x8880\n"
"ds_read_b64_tr_b16 a[180:181], v4 offset:128\n"
"v_mfma_f32_16x16x32_bf16 a[24:27], a[216:219], a[240:243], a[24:27]\n"
"v_mfma_f32_16x16x32_bf16 a[28:31], a[220:223], a[240:243], a[28:31]\n"
"buffer_load_dwordx4 v13, s[52:55], s78 offen lds\n"
"v_mfma_f32_16x16x32_bf16 a[32:35], a[192:195], a[244:247], a[32:35]\n"
"ds_read_b64_tr_b16 a[182:183], v4 offset:1152\n"
"v_mfma_f32_16x16x32_bf16 a[36:39], a[196:199], a[244:247], a[36:39]\n"
"s_add_u32 m0, s83, 0x8c80\n"
"ds_read_b64_tr_b16 a[184:185], v5 offset:128\n"
"v_mfma_f32_16x16x32_bf16 a[40:43], a[200:203], a[244:247], a[40:43]\n"
"buffer_load_dwordx4 v13, s[52:55], s79 offen lds\n"
"v_mfma_f32_16x16x32_bf16 a[44:47], a[204:207], a[244:247], a[44:47]\n"
"ds_read_b64_tr_b16 a[186:187], v5 offset:1152\n"
"v_mfma_f32_16x16x32_bf16 a[48:51], a[208:211], a[244:247], a[48:51]\n"
"s_add_u32 m0, s83, 0xc400\n"
"v_mfma_f32_16x16x32_bf16 a[52:55], a[212:215], a[244:247], a[52:55]\n"
"ds_read_b64_tr_b16 a[188:189], v6 offset:128\n"
"v_mfma_f32_16x16x32_bf16 a[56:59], a[216:219], a[244:247], a[56:59]\n"
"buffer_load_dwordx4 v13, s[56:59], s76 offen lds\n"
"v_mfma_f32_16x16x32_bf16 a[60:63], a[220:223], a[244:247], a[60:63]\n"
"ds_read_b64_tr_b16 a[190:191], v6 offset:1152\n"
"v_mfma_f32_16x16x32_bf16 a[64:67], a[192:195], a[248:251], a[64:67]\n"
"s_add_u32 m0, s83, 0xc800\n"
"v_mfma_f32_16x16x32_bf16 a[68:71], a[196:199], a[248:251], a[68:71]\n"
"v_mfma_f32_16x16x32_bf16 a[72:75], a[200:203], a[248:251], a[72:75]\n"
"buffer_load_dwordx4 v13, s[56:59], s77 offen lds\n"
"v_mfma_f32_16x16x32_bf16 a[76:79], a[204:207], a[248:251], a[76:79]\n"
"s_add_u32 m0, s83, 0xcc80\n"
"v_mfma_f32_16x16x32_bf16 a[80:83], a[208:211], a[248:251], a[80:83]\n"
"v_mfma_f32_16x16x32_bf16 a[84:87], a[212:215], a[248:251], a[84:87]\n"
"buffer_load_dwordx4 v13, s[56:59], s78 offen lds\n"
"v_mfma_f32_16x16x32_bf16 a[88:91], a[216:219], a[248:251], a[88:91]\n"
"v_mfma_f32_16x16x32_bf16 a[92:95], a[220:223], a[248:251], a[92:95]\n"
"s_add_u32 m0, s83, 0xd080\n"
"v_mfma_f32_16x16x32_bf16 a[96:99], a[192:195], a[252:255], a[96:99]\n"
"v_mfma_f32_16x16x32_bf16 a[100:103], a[196:199], a[252:255], a[100:103]\n"
"buffer_load_dwordx4 v13, s[56:59], s79 offen lds\n"
"v_mfma_f32_16x16x32_bf16 a[104:107], a[200:203], a[252:255], a[104:107]\n"
"v_mfma_f32_16x16x32_bf16 a[108:111], a[204:207], a[252:255], a[108:111]\n"
"buffer_load_dword v43, v42, s[60:63], 0 offen\n"
"v_mfma_f32_16x16x32_bf16 a[112:115], a[208:211], a[252:255], a[112:115]\n"
"v_add_u32 v11, 0x80, v11\n"
"v_add_u32 v12, 0x80, v12\n"
"v_add_u32 v13, 0x80000, v13\n"
"v_add_u32 v42, s84, v42\n"
"v_mfma_f32_16x16x32_bf16 a[116:119], a[212:215], a[252:255], a[116:119]\n"
"v_mfma_f32_16x16x32_bf16 a[120:123], a[216:219], a[252:255], a[120:123]\n"
"v_mfma_f32_16x16x32_bf16 a[124:127], a[220:223], a[252:255], a[124:127]\n"
"s_waitcnt lgkmcnt(0)\n"
"v_mfma_f32_16x16x32_bf16 v[128:131], a[128:131], a[160:163], v[128:131]\n"
"ds_read_b64_tr_b16 a[224:225], v7 offset:0\n"
"v_add_u32 v1, 0xfffef800, v1\n"
"v_mfma_f32_16x16x32_bf16 v[132:135], a[132:135], a[160:163], v[132:135]\n"
"v_add_u32 v3, 0xfffef800, v3\n"
"v_add_u32 v4, 0xfffef800, v4\n"
"v_mfma_f32_16x16x32_bf16 v[136:139], a[136:139], a[160:163], v[136:139]\n"
"s_mov_b32 s90, 0x400000\n"
"ds_read_b64_tr_b16 a[226:227], v7 offset:1024\n"
"v_mfma_f32_16x16x32_bf16 v[140:143], a[140:143], a[160:163], v[140:143]\n"
"v_add_u32 v5, 0xfffef800, v5\n"
"v_add_u32 v6, 0xfffef800, v6\n"
"v_mfma_f32_16x16x32_bf16 v[144:147], a[144:147], a[160:163], v[144:147]\n"
"ds_read_b128 a[192:195], v2 offset:0\n"
"v_mfma_f32_16x16x32_bf16 v[148:151], a[148:151], a[160:163], v[148:151]\n"
"s_mov_b32 s46, 16384\n"
"v_mfma_f32_16x16x32_bf16 v[152:155], a[152:155], a[160:163], v[152:155]\n"
"ds_read_b128 a[196:199], v2 offset:2048\n"
"v_mfma_f32_16x16x32_bf16 v[156:159], a[156:159], a[160:163], v[156:159]\n"
"v_mfma_f32_16x16x32_bf16 v[160:163], a[128:131], a[164:167], v[160:163]\n"
"s_cmp_eq_u32 s34, 0\n"
"ds_read_b128 a[200:203], v2 offset:4096\n"
"v_mfma_f32_16x16x32_bf16 v[164:167], a[132:135], a[164:167], v[164:167]\n"
"v_mfma_f32_16x16x32_bf16 v[168:171], a[136:139], a[164:167], v[168:171]\n"
"ds_read_b128 a[204:207], v2 offset:6144\n"
"v_mfma_f32_16x16x32_bf16 v[172:175], a[140:143], a[164:167], v[172:175]\n"
"s_cselect_b32 s88, s8, s12\n"
"v_mfma_f32_16x16x32_bf16 v[176:179], a[144:147], a[164:167], v[176:179]\n"
"ds_read_b128 a[208:211], v2 offset:8192\n"
"v_mfma_f32_16x16x32_bf16 v[180:183], a[148:151], a[164:167], v[180:183]\n"
"v_mfma_f32_16x16x32_bf16 v[184:187], a[152:155], a[164:167], v[184:187]\n"
"s_cselect_b32 s89, s9, s13\n"
"ds_read_b128 a[212:215], v2 offset:10240\n"
"v_mfma_f32_16x16x32_bf16 v[188:191], a[156:159], a[164:167], v[188:191]\n"
"v_mfma_f32_16x16x32_bf16 v[192:195], a[128:131], a[168:171], v[192:195]\n"
"ds_read_b128 a[216:219], v2 offset:12288\n"
"v_mfma_f32_16x16x32_bf16 v[196:199], a[132:135], a[168:171], v[196:199]\n"
"s_cselect_b32 s90, 0x200000, s90\n"
"v_mfma_f32_16x16x32_bf16 v[200:203], a[136:139], a[168:171], v[200:203]\n"
"ds_read_b128 a[220:223], v2 offset:14336\n"
"v_mfma_f32_16x16x32_bf16 v[204:207], a[140:143], a[168:171], v[204:207]\n"
"v_mfma_f32_16x16x32_bf16 v[208:211], a[144:147], a[168:171], v[208:211]\n"
"s_cselect_b32 s46, 8192, s46\n"
"ds_read_b64_tr_b16 a[228:229], v8 offset:0\n"
"v_mfma_f32_16x16x32_bf16 v[212:215], a[148:151], a[168:171], v[212:215]\n"
"v_mfma_f32_16x16x32_bf16 v[216:219], a[152:155], a[168:171], v[216:219]\n"
"ds_read_b64_tr_b16 a[230:231], v8 offset:1024\n"
"v_mfma_f32_16x16x32_bf16 v[220:223], a[156:159], a[168:171], v[220:223]\n"
"s_cmp_eq_u32 s34, 3\n"
"v_mfma_f32_16x16x32_bf16 v[224:227], a[128:131], a[172:175], v[224:227]\n"
"ds_read_b64_tr_b16 a[232:233], v9 offset:0\n"
"v_mfma_f32_16x16x32_bf16 v[228:231], a[132:135], a[172:175], v[228:231]\n"
"v_mfma_f32_16x16x32_bf16 v[232:235], a[136:139], a[172:175], v[232:235]\n"
"s_cselect_b32 s88, s20, s88\n"
"ds_read_b64_tr_b16 a[234:235], v9 offset:1024\n"
"v_mfma_f32_16x16x32_bf16 v[236:239], a[140:143], a[172:175], v[236:239]\n"
"v_mfma_f32_16x16x32_bf16 v[240:243], a[144:147], a[172:175], v[240:243]\n"
"ds_read_b64_tr_b16 a[236:237], v10 offset:0\n"
"v_mfma_f32_16x16x32_bf16 v[244:247], a[148:151], a[172:175], v[244:247]\n"
"s_cselect_b32 s89, s21, s89\n"
"v_mfma_f32_16x16x32_bf16 v[248:251], a[152:155], a[172:175], v[248:251]\n"
"ds_read_b64_tr_b16 a[238:239], v10 offset:1024\n"
"v_mfma_f32_16x16x32_bf16 v[252:255], a[156:159], a[172:175], v[252:255]\n"
"v_mfma_f32_16x16x32_bf16 a[0:3], a[128:131], a[176:179], a[0:3]\n"
"ds_read_b64_tr_b16 a[240:241], v7 offset:128\n"
"v_mfma_f32_16x16x32_bf16 a[4:7], a[132:135], a[176:179], a[4:7]\n"
"s_cselect_b32 s90, 0x1c0000, s90\n"
"v_mfma_f32_16x16x32_bf16 a[8:11], a[136:139], a[176:179], a[8:11]\n"
"ds_read_b64_tr_b16 a[242:243], v7 offset:1152\n"
"v_mfma_f32_16x16x32_bf16 a[12:15], a[140:143], a[176:179], a[12:15]\n"
"v_mfma_f32_16x16x32_bf16 a[16:19], a[144:147], a[176:179], a[16:19]\n"
"s_cselect_b32 s46, 7168, s46\n"
"ds_read_b64_tr_b16 a[244:245], v8 offset:128\n"
"v_mfma_f32_16x16x32_bf16 a[20:23], a[148:151], a[176:179], a[20:23]\n"
"v_mfma_f32_16x16x32_bf16 a[24:27], a[152:155], a[176:179], a[24:27]\n"
"ds_read_b64_tr_b16 a[246:247], v8 offset:1152\n"
"v_mfma_f32_16x16x32_bf16 a[28:31], a[156:159], a[176:179], a[28:31]\n"
"s_and_b32 s89, s89, 0xffff\n"
"v_mfma_f32_16x16x32_bf16 a[32:35], a[128:131], a[180:183], a[32:35]\n"
"ds_read_b64_tr_b16 a[248:249], v9 offset:128\n"
"v_mfma_f32_16x16x32_bf16 a[36:39], a[132:135], a[180:183], a[36:39]\n"
"v_mfma_f32_16x16x32_bf16 a[40:43], a[136:139], a[180:183], a[40:43]\n"
"s_mov_b32 s91, 0x20000\n"
"ds_read_b64_tr_b16 a[250:251], v9 offset:1152\n"
"v_mfma_f32_16x16x32_bf16 a[44:47], a[140:143], a[180:183], a[44:47]\n"
"v_mfma_f32_16x16x32_bf16 a[48:51], a[144:147], a[180:183], a[48:51]\n"
"ds_read_b64_tr_b16 a[252:253], v10 offset:128\n"
"v_mfma_f32_16x16x32_bf16 a[52:55], a[148:151], a[180:183], a[52:55]\n"
"s_mul_i32 s46, s30, s46\n"
"v_mfma_f32_16x16x32_bf16 a[56:59], a[152:155], a[180:183], a[56:59]\n"
"ds_read_b64_tr_b16 a[254:255], v10 offset:1152\n"
"v_mfma_f32_16x16x32_bf16 a[60:63], a[156:159], a[180:183], a[60:63]\n"
"v_mfma_f32_16x16x32_bf16 a[64:67], a[128:131], a[184:187], a[64:67]\n"
"s_cmp_eq_u32 s34, 2\n"
"v_mfma_f32_16x16x32_bf16 a[68:71], a[132:135], a[184:187], a[68:71]\n"
"v_mfma_f32_16x16x32_bf16 a[72:75], a[136:139], a[184:187], a[72:75]\n"
"v_mfma_f32_16x16x32_bf16 a[76:79], a[140:143], a[184:187], a[76:79]\n"
"s_cselect_b32 s45, 8192, 0\n"
"v_mfma_f32_16x16x32_bf16 a[80:83], a[144:147], a[184:187], a[80:83]\n"
"v_mfma_f32_16x16x32_bf16 a[84:87], a[148:151], a[184:187], a[84:87]\n"
"v_mfma_f32_16x16x32_bf16 a[88:91], a[152:155], a[184:187], a[88:91]\n"
"s_add_u32 s46, s46, s45\n"
"v_mfma_f32_16x16x32_bf16 a[92:95], a[156:159], a[184:187], a[92:95]\n"
"v_mfma_f32_16x16x32_bf16 a[96:99], a[128:131], a[188:191], a[96:99]\n"
"v_mfma_f32_16x16x32_bf16 a[100:103], a[132:135], a[188:191], a[100:103]\n"
"buffer_load_dword v63, v62, s[88:91], s46 offen\n"
"v_mfma_f32_16x16x32_bf16 a[104:107], a[136:139], a[188:191], a[104:107]\n"
"v_mfma_f32_16x16x32_bf16 a[108:111], a[140:143], a[188:191], a[108:111]\n"
"v_mfma_f32_16x16x32_bf16 a[112:115], a[144:147], a[188:191], a[112:115]\n"
"v_mfma_f32_16x16x32_bf16 a[116:119], a[148:151], a[188:191], a[116:119]\n"
"v_mfma_f32_16x16x32_bf16 a[120:123], a[152:155], a[188:191], a[120:123]\n"
"v_mfma_f32_16x16x32_bf16 a[124:127], a[156:159], a[188:191], a[124:127]\n"
"s_waitcnt lgkmcnt(0)\n"
"v_mfma_f32_16x16x32_bf16 v[128:131], a[192:195], a[224:227], v[128:131]\n"
"s_waitcnt vmcnt(2)\n"
"s_barrier\n"
"v_mfma_f32_16x16x32_bf16 v[132:135], a[196:199], a[224:227], v[132:135]\n"
"s_add_u32 m0, s82, 0x10800\n"
"ds_read_b64_tr_b16 a[160:161], v3 offset:0\n"
"v_mfma_f32_16x16x32_bf16 v[136:139], a[200:203], a[224:227], v[136:139]\n"
"buffer_load_dwordx4 v11, s[48:51], s68 offen lds\n"
"v_mfma_f32_16x16x32_bf16 v[140:143], a[204:207], a[224:227], v[140:143]\n"
"ds_read_b64_tr_b16 a[162:163], v3 offset:1024\n"
"v_add_u32 v2, 0xfffef800, v2\n"
"v_mfma_f32_16x16x32_bf16 v[144:147], a[208:211], a[224:227], v[144:147]\n"
"s_add_u32 m0, s82, 0x10c00\n"
"v_add_u32 v7, 0xfffef800, v7\n"
"v_mfma_f32_16x16x32_bf16 v[148:151], a[212:215], a[224:227], v[148:151]\n"
"ds_read_b128 a[128:131], v1 offset:0\n"
"v_add_u32 v8, 0xfffef800, v8\n"
"v_mfma_f32_16x16x32_bf16 v[152:155], a[216:219], a[224:227], v[152:155]\n"
"buffer_load_dwordx4 v12, s[48:51], s69 offen lds\n"
"v_mfma_f32_16x16x32_bf16 v[156:159], a[220:223], a[224:227], v[156:159]\n"
"ds_read_b128 a[132:135], v1 offset:2048\n"
"v_add_u32 v9, 0xfffef800, v9\n"
"v_mfma_f32_16x16x32_bf16 v[160:163], a[192:195], a[228:231], v[160:163]\n"
"s_add_u32 m0, s82, 0x11000\n"
"v_add_u32 v10, 0xfffef800, v10\n"
"v_mfma_f32_16x16x32_bf16 v[164:167], a[196:199], a[228:231], v[164:167]\n"
"ds_read_b128 a[136:139], v1 offset:4096\n"
"v_mfma_f32_16x16x32_bf16 v[168:171], a[200:203], a[228:231], v[168:171]\n"
"buffer_load_dwordx4 v11, s[48:51], s70 offen lds\n"
"v_mfma_f32_16x16x32_bf16 v[172:175], a[204:207], a[228:231], v[172:175]\n"
"s_add_u32 m0, s82, 0x11400\n"
"ds_read_b128 a[140:143], v1 offset:6144\n"
"v_mfma_f32_16x16x32_bf16 v[176:179], a[208:211], a[228:231], v[176:179]\n"
"v_mfma_f32_16x16x32_bf16 v[180:183], a[212:215], a[228:231], v[180:183]\n"
"buffer_load_dwordx4 v12, s[48:51], s71 offen lds\n"
"v_mfma_f32_16x16x32_bf16 v[184:187], a[216:219], a[228:231], v[184:187]\n"
"ds_read_b128 a[144:147], v1 offset:8192\n"
"v_mfma_f32_16x16x32_bf16 v[188:191], a[220:223], a[228:231], v[188:191]\n"
"s_add_u32 m0, s82, 0x11800\n"
"ds_read_b128 a[148:151], v1 offset:10240\n"
"v_mfma_f32_16x16x32_bf16 v[192:195], a[192:195], a[232:235], v[192:195]\n"
"v_mfma_f32_16x16x32_bf16 v[196:199], a[196:199], a[232:235], v[196:199]\n"
"buffer_load_dwordx4 v11, s[48:51], s72 offen lds\n"
"v_mfma_f32_16x16x32_bf16 v[200:203], a[200:203], a[232:235], v[200:203]\n"
"ds_read_b128 a[152:155], v1 offset:12288\n"
"v_mfma_f32_16x16x32_bf16 v[204:207], a[204:207], a[232:235], v[204:207]\n"
"s_add_u32 m0, s82, 0x11c00\n"
"ds_read_b128 a[156:159], v1 offset:14336\n"
"v_mfma_f32_16x16x32_bf16 v[208:211], a[208:211], a[232:235], v[208:211]\n"
"v_mfma_f32_16x16x32_bf16 v[212:215], a[212:215], a[232:235], v[212:215]\n"
"buffer_load_dwordx4 v12, s[48:51], s73 offen lds\n"
"v_mfma_f32_16x16x32_bf16 v[216:219], a[216:219], a[232:235], v[216:219]\n"
"s_add_u32 m0, s82, 0x12000\n"
"ds_read_b64_tr_b16 a[164:165], v4 offset:0\n"
"v_mfma_f32_16x16x32_bf16 v[220:223], a[220:223], a[232:235], v[220:223]\n"
"ds_read_b64_tr_b16 a[166:167], v4 offset:1024\n"
"v_mfma_f32_16x16x32_bf16 v[224:227], a[192:195], a[236:239], v[224:227]\n"
"buffer_load_dwordx4 v11, s[48:51], s74 offen lds\n"
"v_mfma_f32_16x16x32_bf16 v[228:231], a[196:199], a[236:239], v[228:231]\n"
"ds_read_b64_tr_b16 a[168:169], v5 offset:0\n"
"v_mfma_f32_16x16x32_bf16 v[232:235], a[200:203], a[236:239], v[232:235]\n"
"s_add_u32 m0, s82, 0x12400\n"
"v_mfma_f32_16x16x32_bf16 v[236:239], a[204:207], a[236:239], v[236:239]\n"
"ds_read_b64_tr_b16 a[170:171], v5 offset:1024\n"
"v_mfma_f32_16x16x32_bf16 v[240:243], a[208:211], a[236:239], v[240:243]\n"
"buffer_load_dwordx4 v12, s[48:51], s75 offen lds\n"
"v_mfma_f32_16x16x32_bf16 v[244:247], a[212:215], a[236:239], v[244:247]\n"
"ds_read_b64_tr_b16 a[172:173], v6 offset:0\n"
"v_mfma_f32_16x16x32_bf16 v[248:251], a[216:219], a[236:239], v[248:251]\n"
"s_add_u32 m0, s83, 0x18800\n"
"v_mfma_f32_16x16x32_bf16 v[252:255], a[220:223], a[236:239], v[252:255]\n"
"buffer_load_dwordx4 v13, s[52:55], s76 offen lds\n"
"v_mfma_f32_16x16x32_bf16 a[0:3], a[192:195], a[240:243], a[0:3]\n"
"ds_read_b64_tr_b16 a[174:175], v6 offset:1024\n"
"v_mfma_f32_16x16x32_bf16 a[4:7], a[196:199], a[240:243], a[4:7]\n"
"s_add_u32 m0, s83, 0x18c00\n"
"ds_read_b64_tr_b16 a[176:177], v3 offset:128\n"
"v_mfma_f32_16x16x32_bf16 a[8:11], a[200:203], a[240:243], a[8:11]\n"
"v_mfma_f32_16x16x32_bf16 a[12:15], a[204:207], a[240:243], a[12:15]\n"
"buffer_load_dwordx4 v13, s[52:55], s77 offen lds\n"
"v_mfma_f32_16x16x32_bf16 a[16:19], a[208:211], a[240:243], a[16:19]\n"
"ds_read_b64_tr_b16 a[178:179], v3 offset:1152\n"
"v_mfma_f32_16x16x32_bf16 a[20:23], a[212:215], a[240:243], a[20:23]\n"
"s_add_u32 m0, s83, 0x19080\n"
"ds_read_b64_tr_b16 a[180:181], v4 offset:128\n"
"v_mfma_f32_16x16x32_bf16 a[24:27], a[216:219], a[240:243], a[24:27]\n"
"v_mfma_f32_16x16x32_bf16 a[28:31], a[220:223], a[240:243], a[28:31]\n"
"buffer_load_dwordx4 v13, s[52:55], s78 offen lds\n"
"v_mfma_f32_16x16x32_bf16 a[32:35], a[192:195], a[244:247], a[32:35]\n"
"ds_read_b64_tr_b16 a[182:183], v4 offset:1152\n"
"v_mfma_f32_16x16x32_bf16 a[36:39], a[196:199], a[244:247], a[36:39]\n"
"s_add_u32 m0, s83, 0x19480\n"
"ds_read_b64_tr_b16 a[184:185], v5 offset:128\n"
"v_mfma_f32_16x16x32_bf16 a[40:43], a[200:203], a[244:247], a[40:43]\n"
"buffer_load_dwordx4 v13, s[52:55], s79 offen lds\n"
"v_mfma_f32_16x16x32_bf16 a[44:47], a[204:207], a[244:247], a[44:47]\n"
"ds_read_b64_tr_b16 a[186:187], v5 offset:1152\n"
"v_mfma_f32_16x16x32_bf16 a[48:51], a[208:211], a[244:247], a[48:51]\n"
"s_add_u32 m0, s83, 0x1cc00\n"
"v_mfma_f32_16x16x32_bf16 a[52:55], a[212:215], a[244:247], a[52:55]\n"
"ds_read_b64_tr_b16 a[188:189], v6 offset:128\n"
"v_mfma_f32_16x16x32_bf16 a[56:59], a[216:219], a[244:247], a[56:59]\n"
"buffer_load_dwordx4 v13, s[56:59], s76 offen lds\n"
"v_mfma_f32_16x16x32_bf16 a[60:63], a[220:223], a[244:247], a[60:63]\n"
"ds_read_b64_tr_b16 a[190:191], v6 offset:1152\n"
"v_mfma_f32_16x16x32_bf16 a[64:67], a[192:195], a[248:251], a[64:67]\n"
"s_add_u32 m0, s83, 0x1d000\n"
"v_mfma_f32_16x16x32_bf16 a[68:71], a[196:199], a[248:251], a[68:71]\n"
"v_mfma_f32_16x16x32_bf16 a[72:75], a[200:203], a[248:251], a[72:75]\n"
"buffer_load_dwordx4 v13, s[56:59], s77 offen lds\n"
"v_mfma_f32_16x16x32_bf16 a[76:79], a[204:207], a[248:251], a[76:79]\n"
"s_add_u32 m0, s83, 0x1d480\n"
"v_mfma_f32_16x16x32_bf16 a[80:83], a[208:211], a[248:251], a[80:83]\n"
"v_mfma_f32_16x16x32_bf16 a[84:87], a[212:215], a[248:251], a[84:87]\n"
"buffer_load_dwordx4 v13, s[56:59], s78 offen lds\n"
"v_mfma_f32_16x16x32_bf16 a[88:91], a[216:219], a[248:251], a[88:91]\n"
"v_mfma_f32_16x16x32_bf16 a[92:95], a[220:223], a[248:251], a[92:95]\n"
"s_add_u32 m0, s83, 0x1d880\n"
"v_mfma_f32_16x16x32_bf16 a[96:99], a[192:195], a[252:255], a[96:99]\n"
"v_mfma_f32_16x16x32_bf16 a[100:103], a[196:199], a[252:255], a[100:103]\n"
"buffer_load_dwordx4 v13, s[56:59], s79 offen lds\n"
"v_mfma_f32_16x16x32_bf16 a[104:107], a[200:203], a[252:255], a[104:107]\n"
"v_mfma_f32_16x16x32_bf16 a[108:111], a[204:207], a[252:255], a[108:111]\n"
"buffer_load_dword v17, v42, s[60:63], 0 offen\n"
"v_mfma_f32_16x16x32_bf16 a[112:115], a[208:211], a[252:255], a[112:115]\n"
"v_add_u32 v11, 0x80, v11\n"
"v_add_u32 v12, 0x80, v12\n"
"v_add_u32 v13, 0x80000, v13\n"
"v_add_u32 v42, s84, v42\n"
"v_mfma_f32_16x16x32_bf16 a[116:119], a[212:215], a[252:255], a[116:119]\n"
"v_mfma_f32_16x16x32_bf16 a[120:123], a[216:219], a[252:255], a[120:123]\n"
"v_mfma_f32_16x16x32_bf16 a[124:127], a[220:223], a[252:255], a[124:127]\n"
"s_mov_b32 s85, -2\n"
"LM_vloop:\n"
"s_waitcnt lgkmcnt(0)\n"
"v_mfma_f32_16x16x32_bf16 v[128:131], a[128:131], a[160:163], v[128:131]\n"
"ds_read_b64_tr_b16 a[224:225], v7 offset:0\n"
"v_add_u32 v1, 0x10800, v1\n"
"v_mfma_f32_16x16x32_bf16 v[132:135], a[132:135], a[160:163], v[132:135]\n"
"v_add_u32 v3, 0x10800, v3\n"
"v_add_u32 v4, 0x10800, v4\n"
"v_mfma_f32_16x16x32_bf16 v[136:139], a[136:139], a[160:163], v[136:139]\n"
"ds_read_b64_tr_b16 a[226:227], v7 offset:1024\n"
"v_add_u32 v5, 0x10800, v5\n"
"v_mfma_f32_16x16x32_bf16 v[140:143], a[140:143], a[160:163], v[140:143]\n"
"v_add_u32 v6, 0x10800, v6\n"
"v_mfma_f32_16x16x32_bf16 v[144:147], a[144:147], a[160:163], v[144:147]\n"
"ds_read_b128 a[192:195], v2 offset:0\n"
"v_mfma_f32_16x16x32_bf16 v[148:151], a[148:151], a[160:163], v[148:151]\n"
"v_mfma_f32_16x16x32_bf16 v[152:155], a[152:155], a[160:163], v[152:155]\n"
"ds_read_b128 a[196:199], v2 offset:2048\n"
"v_mfma_f32_16x16x32_bf16 v[156:159], a[156:159], a[160:163], v[156:159]\n"
"v_mfma_f32_16x16x32_bf16 v[160:163], a[128:131], a[164:167], v[160:163]\n"
"ds_read_b128 a[200:203], v2 offset:4096\n"
"v_mfma_f32_16x16x32_bf16 v[164:167], a[132:135], a[164:167], v[164:167]\n"
"v_mfma_f32_16x16x32_bf16 v[168:171], a[136:139], a[164:167], v[168:171]\n"
"ds_read_b128 a[204:207], v2 offset:6144\n"
"v_mfma_f32_16x16x32_bf16 v[172:175], a[140:143], a[164:167], v[172:175]\n"
"v_mfma_f32_16x16x32_bf16 v[176:179], a[144:147], a[164:167], v[176:179]\n"
"ds_read_b128 a[208:211], v2 offset:8192\n"
"v_mfma_f32_16x16x32_bf16 v[180:183], a[148:151], a[164:167], v[180:183]\n"
"v_mfma_f32_16x16x32_bf16 v[184:187], a[152:155], a[164:167], v[184:187]\n"
"ds_read_b128 a[212:215], v2 offset:10240\n"
"v_mfma_f32_16x16x32_bf16 v[188:191], a[156:159], a[164:167], v[188:191]\n"
"v_mfma_f32_16x16x32_bf16 v[192:195], a[128:131], a[168:171], v[192:195]\n"
"ds_read_b128 a[216:219], v2 offset:12288\n"
"v_mfma_f32_16x16x32_bf16 v[196:199], a[132:135], a[168:171], v[196:199]\n"
"v_mfma_f32_16x16x32_bf16 v[200:203], a[136:139], a[168:171], v[200:203]\n"
"ds_read_b128 a[220:223], v2 offset:14336\n"
"v_mfma_f32_16x16x32_bf16 v[204:207], a[140:143], a[168:171], v[204:207]\n"
"v_mfma_f32_16x16x32_bf16 v[208:211], a[144:147], a[168:171], v[208:211]\n"
"ds_read_b64_tr_b16 a[228:229], v8 offset:0\n"
"v_mfma_f32_16x16x32_bf16 v[212:215], a[148:151], a[168:171], v[212:215]\n"
"v_mfma_f32_16x16x32_bf16 v[216:219], a[152:155], a[168:171], v[216:219]\n"
"ds_read_b64_tr_b16 a[230:231], v8 offset:1024\n"
"v_mfma_f32_16x16x32_bf16 v[220:223], a[156:159], a[168:171], v[220:223]\n"
"v_mfma_f32_16x16x32_bf16 v[224:227], a[128:131], a[172:175], v[224:227]\n"
"ds_read_b64_tr_b16 a[232:233], v9 offset:0\n"
"v_mfma_f32_16x16x32_bf16 v[228:231], a[132:135], a[172:175], v[228:231]\n"
"v_mfma_f32_16x16x32_bf16 v[232:235], a[136:139], a[172:175], v[232:235]\n"
"ds_read_b64_tr_b16 a[234:235], v9 offset:1024\n"
"v_mfma_f32_16x16x32_bf16 v[236:239], a[140:143], a[172:175], v[236:239]\n"
"v_mfma_f32_16x16x32_bf16 v[240:243], a[144:147], a[172:175], v[240:243]\n"
"ds_read_b64_tr_b16 a[236:237], v10 offset:0\n"
"v_mfma_f32_16x16x32_bf16 v[244:247], a[148:151], a[172:175], v[244:247]\n"
"v_mfma_f32_16x16x32_bf16 v[248:251], a[152:155], a[172:175], v[248:251]\n"
"ds_read_b64_tr_b16 a[238:239], v10 offset:1024\n"
"v_mfma_f32_16x16x32_bf16 v[252:255], a[156:159], a[172:175], v[252:255]\n"
"v_mfma_f32_16x16x32_bf16 a[0:3], a[128:131], a[176:179], a[0:3]\n"
"ds_read_b64_tr_b16 a[240:241], v7 offset:128\n"
"v_mfma_f32_16x16x32_bf16 a[4:7], a[132:135], a[176:179], a[4:7]\n"
"v_mfma_f32_16x16x32_bf16 a[8:11], a[136:139], a[176:179], a[8:11]\n"
"ds_read_b64_tr_b16 a[242:243], v7 offset:1152\n"
"v_mfma_f32_16x16x32_bf16 a[12:15], a[140:143], a[176:179], a[12:15]\n"
"v_mfma_f32_16x16x32_bf16 a[16:19], a[144:147], a[176:179], a[16:19]\n"
"ds_read_b64_tr_b16 a[244:245], v8 offset:128\n"
"v_mfma_f32_16x16x32_bf16 a[20:23], a[148:151], a[176:179], a[20:23]\n"
"v_mfma_f32_16x16x32_bf16 a[24:27], a[152:155], a[176:179], a[24:27]\n"
"ds_read_b64_tr_b16 a[246:247], v8 offset:1152\n"
"v_mfma_f32_16x16x32_bf16 a[28:31], a[156:159], a[176:179], a[28:31]\n"
"v_mfma_f32_16x16x32_bf16 a[32:35], a[128:131], a[180:183], a[32:35]\n"
"ds_read_b64_tr_b16 a[248:249], v9 offset:128\n"
"v_mfma_f32_16x16x32_bf16 a[36:39], a[132:135], a[180:183], a[36:39]\n"
"v_mfma_f32_16x16x32_bf16 a[40:43], a[136:139], a[180:183], a[40:43]\n"
"ds_read_b64_tr_b16 a[250:251], v9 offset:1152\n"
"v_mfma_f32_16x16x32_bf16 a[44:47], a[140:143], a[180:183], a[44:47]\n"
"v_mfma_f32_16x16x32_bf16 a[48:51], a[144:147], a[180:183], a[48:51]\n"
"ds_read_b64_tr_b16 a[252:253], v10 offset:128\n"
"v_mfma_f32_16x16x32_bf16 a[52:55], a[148:151], a[180:183], a[52:55]\n"
"v_mfma_f32_16x16x32_bf16 a[56:59], a[152:155], a[180:183], a[56:59]\n"
"ds_read_b64_tr_b16 a[254:255], v10 offset:1152\n"
"v_mfma_f32_16x16x32_bf16 a[60:63], a[156:159], a[180:183], a[60:63]\n"
"v_mfma_f32_16x16x32_bf16 a[64:67], a[128:131], a[184:187], a[64:67]\n"
"v_mfma_f32_16x16x32_bf16 a[68:71], a[132:135], a[184:187], a[68:71]\n"
"v_mfma_f32_16x16x32_bf16 a[72:75], a[136:139], a[184:187], a[72:75]\n"
"v_mfma_f32_16x16x32_bf16 a[76:79], a[140:143], a[184:187], a[76:79]\n"
"v_mfma_f32_16x16x32_bf16 a[80:83], a[144:147], a[184:187], a[80:83]\n"
"v_mfma_f32_16x16x32_bf16 a[84:87], a[148:151], a[184:187], a[84:87]\n"
"v_mfma_f32_16x16x32_bf16 a[88:91], a[152:155], a[184:187], a[88:91]\n"
"v_mfma_f32_16x16x32_bf16 a[92:95], a[156:159], a[184:187], a[92:95]\n"
"v_mfma_f32_16x16x32_bf16 a[96:99], a[128:131], a[188:191], a[96:99]\n"
"v_mfma_f32_16x16x32_bf16 a[100:103], a[132:135], a[188:191], a[100:103]\n"
"v_mfma_f32_16x16x32_bf16 a[104:107], a[136:139], a[188:191], a[104:107]\n"
"v_mfma_f32_16x16x32_bf16 a[108:111], a[140:143], a[188:191], a[108:111]\n"
"v_mfma_f32_16x16x32_bf16 a[112:115], a[144:147], a[188:191], a[112:115]\n"
"v_mfma_f32_16x16x32_bf16 a[116:119], a[148:151], a[188:191], a[116:119]\n"
"v_mfma_f32_16x16x32_bf16 a[120:123], a[152:155], a[188:191], a[120:123]\n"
"v_mfma_f32_16x16x32_bf16 a[124:127], a[156:159], a[188:191], a[124:127]\n"
"s_waitcnt lgkmcnt(0)\n"
"v_mfma_f32_16x16x32_bf16 v[128:131], a[192:195], a[224:227], v[128:131]\n"
"s_waitcnt vmcnt(1)\n"
"s_barrier\n"
"v_mfma_f32_16x16x32_bf16 v[132:135], a[196:199], a[224:227], v[132:135]\n"
"s_add_u32 m0, s82, 0x0\n"
"ds_read_b64_tr_b16 a[160:161], v3 offset:0\n"
"v_mfma_f32_16x16x32_bf16 v[136:139], a[200:203], a[224:227], v[136:139]\n"
"buffer_load_dwordx4 v11, s[48:51], s68 offen lds\n"
"v_mfma_f32_16x16x32_bf16 v[140:143], a[204:207], a[224:227], v[140:143]\n"
"ds_read_b64_tr_b16 a[162:163], v3 offset:1024\n"
"v_add_u32 v2, 0x10800, v2\n"
"v_mfma_f32_16x16x32_bf16 v[144:147], a[208:211], a[224:227], v[144:147]\n"
"s_add_u32 m0, s82, 0x400\n"
"v_add_u32 v7, 0x10800, v7\n"
"v_mfma_f32_16x16x32_bf16 v[148:151], a[212:215], a[224:227], v[148:151]\n"
"ds_read_b128 a[128:131], v1 offset:0\n"
"v_add_u32 v8, 0x10800, v8\n"
"v_mfma_f32_16x16x32_bf16 v[152:155], a[216:219], a[224:227], v[152:155]\n"
"buffer_load_dwordx4 v12, s[48:51], s69 offen lds\n"
"v_mfma_f32_16x16x32_bf16 v[156:159], a[220:223], a[224:227], v[156:159]\n"
"ds_read_b128 a[132:135], v1 offset:2048\n"
"v_add_u32 v9, 0x10800, v9\n"
"v_mfma_f32_16x16x32_bf16 v[160:163], a[192:195], a[228:231], v[160:163]\n"
"s_add_u32 m0, s82, 0x800\n"
"v_add_u32 v10, 0x10800, v10\n"
"v_mfma_f32_16x16x32_bf16 v[164:167], a[196:199], a[228:231], v[164:167]\n"
"ds_read_b128 a[136:139], v1 offset:4096\n"
"v_mfma_f32_16x16x32_bf16 v[168:171], a[200:203], a[228:231], v[168:171]\n"
"buffer_load_dwordx4 v11, s[48:51], s70 offen lds\n"
"v_mfma_f32_16x16x32_bf16 v[172:175], a[204:207], a[228:231], v[172:175]\n"
"s_add_u32 m0, s82, 0xc00\n"
"ds_read_b128 a[140:143], v1 offset:6144\n"
"v_mfma_f32_16x16x32_bf16 v[176:179], a[208:211], a[228:231], v[176:179]\n"
"v_mfma_f32_16x16x32_bf16 v[180:183], a[212:215], a[228:231], v[180:183]\n"
"buffer_load_dwordx4 v12, s[48:51], s71 offen lds\n"
"v_mfma_f32_16x16x32_bf16 v[184:187], a[216:219], a[228:231], v[184:187]\n"
"ds_read_b128 a[144:147], v1 offset:8192\n"
"v_mfma_f32_16x16x32_bf16 v[188:191], a[220:223], a[228:231], v[188:191]\n"
"s_add_u32 m0, s82, 0x1000\n"
"ds_read_b128 a[148:151], v1 offset:10240\n"
"v_mfma_f32_16x16x32_bf16 v[192:195], a[192:195], a[232:235], v[192:195]\n"
"v_mfma_f32_16x16x32_bf16 v[196:199], a[196:199], a[232:235], v[196:199]\n"
"buffer_load_dwordx4 v11, s[48:51], s72 offen lds\n"
"v_mfma_f32_16x16x32_bf16 v[200:203], a[200:203], a[232:235], v[200:203]\n"
"ds_read_b128 a[152:155], v1 offset:12288\n"
"v_mfma_f32_16x16x32_bf16 v[204:207], a[204:207], a[232:235], v[204:207]\n"
"s_add_u32 m0, s82, 0x1400\n"
"ds_read_b128 a[156:159], v1 offset:14336\n"
"v_mfma_f32_16x16x32_bf16 v[208:211], a[208:211], a[232:235], v[208:211]\n"
"v_mfma_f32_16x16x32_bf16 v[212:215], a[212:215], a[232:235], v[212:215]\n"
"buffer_load_dwordx4 v12, s[48:51], s73 offen lds\n"
"v_mfma_f32_16x16x32_bf16 v[216:219], a[216:219], a[232:235], v[216:219]\n"
"s_add_u32 m0, s82, 0x1800\n"
"ds_read_b64_tr_b16 a[164:165], v4 offset:0\n"
"v_mfma_f32_16x16x32_bf16 v[220:223], a[220:223], a[232:235], v[220:223]\n"
"ds_read_b64_tr_b16 a[166:167], v4 offset:1024\n"
"v_mfma_f32_16x16x32_bf16 v[224:227], a[192:195], a[236:239], v[224:227]\n"
"buffer_load_dwordx4 v11, s[48:51], s74 offen lds\n"
"v_mfma_f32_16x16x32_bf16 v[228:231], a[196:199], a[236:239], v[228:231]\n"
"ds_read_b64_tr_b16 a[168:169], v5 offset:0\n"
"v_mfma_f32_16x16x32_bf16 v[232:235], a[200:203], a[236:239], v[232:235]\n"
"s_add_u32 m0, s82, 0x1c00\n"
"v_mfma_f32_16x16x32_bf16 v[236:239], a[204:207], a[236:239], v[236:239]\n"
"ds_read_b64_tr_b16 a[170:171], v5 offset:1024\n"
"v_mfma_f32_16x16x32_bf16 v[240:243], a[208:211], a[236:239], v[240:243]\n"
"buffer_load_dwordx4 v12, s[48:51], s75 offen lds\n"
"v_mfma_f32_16x16x32_bf16 v[244:247], a[212:215], a[236:239], v[244:247]\n"
"ds_read_b64_tr_b16 a[172:173], v6 offset:0\n"
"v_mfma_f32_16x16x32_bf16 v[248:251], a[216:219], a[236:239], v[248:251]\n"
"s_add_u32 m0, s83, 0x8000\n"
"v_mfma_f32_16x16x32_bf16 v[252:255], a[220:223], a[236:239], v[252:255]\n"
"buffer_load_dwordx4 v13, s[52:55], s76 offen lds\n"
"v_mfma_f32_16x16x32_bf16 a[0:3], a[192:195], a[240:243], a[0:3]\n"
"ds_read_b64_tr_b16 a[174:175], v6 offset:1024\n"
"v_mfma_f32_16x16x32_bf16 a[4:7], a[196:199], a[240:243], a[4:7]\n"
"s_add_u32 m0, s83, 0x8400\n"
"ds_read_b64_tr_b16 a[176:177], v3 offset:128\n"
"v_mfma_f32_16x16x32_bf16 a[8:11], a[200:203], a[240:243], a[8:11]\n"
"v_mfma_f32_16x16x32_bf16 a[12:15], a[204:207], a[240:243], a[12:15]\n"
"buffer_load_dwordx4 v13, s[52:55], s77 offen lds\n"
"v_mfma_f32_16x16x32_bf16 a[16:19], a[208:211], a[240:243], a[16:19]\n"
"ds_read_b64_tr_b16 a[178:179], v3 offset:1152\n"
"v_mfma_f32_16x16x32_bf16 a[20:23], a[212:215], a[240:243], a[20:23]\n"
"s_add_u32 m0, s83, 0x8880\n"
"ds_read_b64_tr_b16 a[180:181], v4 offset:128\n"
"v_mfma_f32_16x16x32_bf16 a[24:27], a[216:219], a[240:243], a[24:27]\n"
"v_mfma_f32_16x16x32_bf16 a[28:31], a[220:223], a[240:243], a[28:31]\n"
"buffer_load_dwordx4 v13, s[52:55], s78 offen lds\n"
"v_mfma_f32_16x16x32_bf16 a[32:35], a[192:195], a[244:247], a[32:35]\n"
"ds_read_b64_tr_b16 a[182:183], v4 offset:1152\n"
"v_mfma_f32_16x16x32_bf16 a[36:39], a[196:199], a[244:247], a[36:39]\n"
"s_add_u32 m0, s83, 0x8c80\n"
"ds_read_b64_tr_b16 a[184:185], v5 offset:128\n"
"v_mfma_f32_16x16x32_bf16 a[40:43], a[200:203], a[244:247], a[40:43]\n"
"buffer_load_dwordx4 v13, s[52:55], s79 offen lds\n"
"v_mfma_f32_16x16x32_bf16 a[44:47], a[204:207], a[244:247], a[44:47]\n"
"ds_read_b64_tr_b16 a[186:187], v5 offset:1152\n"
"v_mfma_f32_16x16x32_bf16 a[48:51], a[208:211], a[244:247], a[48:51]\n"
"s_add_u32 m0, s83, 0xc400\n"
"v_mfma_f32_16x16x32_bf16 a[52:55], a[212:215], a[244:247], a[52:55]\n"
"ds_read_b64_tr_b16 a[188:189], v6 offset:128\n"
"v_mfma_f32_16x16x32_bf16 a[56:59], a[216:219], a[244:247], a[56:59]\n"
"buffer_load_dwordx4 v13, s[56:59], s76 offen lds\n"
"v_mfma_f32_16x16x32_bf16 a[60:63], a[220:223], a[244:247], a[60:63]\n"
"ds_read_b64_tr_b16 a[190:191], v6 offset:1152\n"
"v_mfma_f32_16x16x32_bf16 a[64:67], a[192:195], a[248:251], a[64:67]\n"
"s_add_u32 m0, s83, 0xc800\n"
"v_mfma_f32_16x16x32_bf16 a[68:71], a[196:199], a[248:251], a[68:71]\n"
"v_mfma_f32_16x16x32_bf16 a[72:75], a[200:203], a[248:251], a[72:75]\n"
"buffer_load_dwordx4 v13, s[56:59], s77 offen lds\n"
"v_mfma_f32_16x16x32_bf16 a[76:79], a[204:207], a[248:251], a[76:79]\n"
"s_add_u32 m0, s83, 0xcc80\n"
"v_mfma_f32_16x16x32_bf16 a[80:83], a[208:211], a[248:251], a[80:83]\n"
"v_mfma_f32_16x16x32_bf16 a[84:87], a[212:215], a[248:251], a[84:87]\n"
"buffer_load_dwordx4 v13, s[56:59], s78 offen lds\n"
"v_mfma_f32_16x16x32_bf16 a[88:91], a[216:219], a[248:251], a[88:91]\n"
"v_mfma_f32_16x16x32_bf16 a[92:95], a[220:223], a[248:251], a[92:95]\n"
"s_add_u32 m0, s83, 0xd080\n"
"v_mfma_f32_16x16x32_bf16 a[96:99], a[192:195], a[252:255], a[96:99]\n"
"v_mfma_f32_16x16x32_bf16 a[100:103], a[196:199], a[252:255], a[100:103]\n"
"buffer_load_dwordx4 v13, s[56:59], s79 offen lds\n"
"v_mfma_f32_16x16x32_bf16 a[104:107], a[200:203], a[252:255], a[104:107]\n"
"v_mfma_f32_16x16x32_bf16 a[108:111], a[204:207], a[252:255], a[108:111]\n"
"buffer_load_dword v43, v42, s[60:63], 0 offen\n"
"v_mfma_f32_16x16x32_bf16 a[112:115], a[208:211], a[252:255], a[112:115]\n"
"v_add_u32 v11, 0x80, v11\n"
"v_add_u32 v12, 0x80, v12\n"
"v_add_u32 v13, 0x80000, v13\n"
"v_add_u32 v42, s84, v42\n"
"v_mfma_f32_16x16x32_bf16 a[116:119], a[212:215], a[252:255], a[116:119]\n"
"v_mfma_f32_16x16x32_bf16 a[120:123], a[216:219], a[252:255], a[120:123]\n"
"v_mfma_f32_16x16x32_bf16 a[124:127], a[220:223], a[252:255], a[124:127]\n"
"s_waitcnt lgkmcnt(0)\n"
"v_mfma_f32_16x16x32_bf16 v[128:131], a[128:131], a[160:163], v[128:131]\n"
"ds_read_b64_tr_b16 a[224:225], v7 offset:0\n"
"v_add_u32 v1, 0xfffef800, v1\n"
"v_mfma_f32_16x16x32_bf16 v[132:135], a[132:135], a[160:163], v[132:135]\n"
"v_add_u32 v3, 0xfffef800, v3\n"
"v_add_u32 v4, 0xfffef800, v4\n"
"v_mfma_f32_16x16x32_bf16 v[136:139], a[136:139], a[160:163], v[136:139]\n"
"ds_read_b64_tr_b16 a[226:227], v7 offset:1024\n"
"v_add_u32 v5, 0xfffef800, v5\n"
"v_mfma_f32_16x16x32_bf16 v[140:143], a[140:143], a[160:163], v[140:143]\n"
"v_add_u32 v6, 0xfffef800, v6\n"
"v_mfma_f32_16x16x32_bf16 v[144:147], a[144:147], a[160:163], v[144:147]\n"
"ds_read_b128 a[192:195], v2 offset:0\n"
"v_mfma_f32_16x16x32_bf16 v[148:151], a[148:151], a[160:163], v[148:151]\n"
"v_mfma_f32_16x16x32_bf16 v[152:155], a[152:155], a[160:163], v[152:155]\n"
"ds_read_b128 a[196:199], v2 offset:2048\n"
"v_mfma_f32_16x16x32_bf16 v[156:159], a[156:159], a[160:163], v[156:159]\n"
"v_mfma_f32_16x16x32_bf16 v[160:163], a[128:131], a[164:167], v[160:163]\n"
"ds_read_b128 a[200:203], v2 offset:4096\n"
"v_mfma_f32_16x16x32_bf16 v[164:167], a[132:135], a[164:167], v[164:167]\n"
"v_mfma_f32_16x16x32_bf16 v[168:171], a[136:139], a[164:167], v[168:171]\n"
"ds_read_b128 a[204:207], v2 offset:6144\n"
"v_mfma_f32_16x16x32_bf16 v[172:175], a[140:143], a[164:167], v[172:175]\n"
"v_mfma_f32_16x16x32_bf16 v[176:179], a[144:147], a[164:167], v[176:179]\n"
"ds_read_b128 a[208:211], v2 offset:8192\n"
"v_mfma_f32_16x16x32_bf16 v[180:183], a[148:151], a[164:167], v[180:183]\n"
"v_mfma_f32_16x16x32_bf16 v[184:187], a[152:155], a[164:167], v[184:187]\n"
"ds_read_b128 a[212:215], v2 offset:10240\n"
"v_mfma_f32_16x16x32_bf16 v[188:191], a[156:159], a[164:167], v[188:191]\n"
"v_mfma_f32_16x16x32_bf16 v[192:195], a[128:131], a[168:171], v[192:195]\n"
"ds_read_b128 a[216:219], v2 offset:12288\n"
"v_mfma_f32_16x16x32_bf16 v[196:199], a[132:135], a[168:171], v[196:199]\n"
"v_mfma_f32_16x16x32_bf16 v[200:203], a[136:139], a[168:171], v[200:203]\n"
"ds_read_b128 a[220:223], v2 offset:14336\n"
"v_mfma_f32_16x16x32_bf16 v[204:207], a[140:143], a[168:171], v[204:207]\n"
"v_mfma_f32_16x16x32_bf16 v[208:211], a[144:147], a[168:171], v[208:211]\n"
"ds_read_b64_tr_b16 a[228:229], v8 offset:0\n"
"v_mfma_f32_16x16x32_bf16 v[212:215], a[148:151], a[168:171], v[212:215]\n"
"v_mfma_f32_16x16x32_bf16 v[216:219], a[152:155], a[168:171], v[216:219]\n"
"ds_read_b64_tr_b16 a[230:231], v8 offset:1024\n"
"v_mfma_f32_16x16x32_bf16 v[220:223], a[156:159], a[168:171], v[220:223]\n"
"v_mfma_f32_16x16x32_bf16 v[224:227], a[128:131], a[172:175], v[224:227]\n"
"ds_read_b64_tr_b16 a[232:233], v9 offset:0\n"
"v_mfma_f32_16x16x32_bf16 v[228:231], a[132:135], a[172:175], v[228:231]\n"
"v_mfma_f32_16x16x32_bf16 v[232:235], a[136:139], a[172:175], v[232:235]\n"
"ds_read_b64_tr_b16 a[234:235], v9 offset:1024\n"
"v_mfma_f32_16x16x32_bf16 v[236:239], a[140:143], a[172:175], v[236:239]\n"
"v_mfma_f32_16x16x32_bf16 v[240:243], a[144:147], a[172:175], v[240:243]\n"
"ds_read_b64_tr_b16 a[236:237], v10 offset:0\n"
"v_mfma_f32_16x16x32_bf16 v[244:247], a[148:151], a[172:175], v[244:247]\n"
"v_mfma_f32_16x16x32_bf16 v[248:251], a[152:155], a[172:175], v[248:251]\n"
"ds_read_b64_tr_b16 a[238:239], v10 offset:1024\n"
"v_mfma_f32_16x16x32_bf16 v[252:255], a[156:159], a[172:175], v[252:255]\n"
"v_mfma_f32_16x16x32_bf16 a[0:3], a[128:131], a[176:179], a[0:3]\n"
"ds_read_b64_tr_b16 a[240:241], v7 offset:128\n"
"v_mfma_f32_16x16x32_bf16 a[4:7], a[132:135], a[176:179], a[4:7]\n"
"v_mfma_f32_16x16x32_bf16 a[8:11], a[136:139], a[176:179], a[8:11]\n"
"ds_read_b64_tr_b16 a[242:243], v7 offset:1152\n"
"v_mfma_f32_16x16x32_bf16 a[12:15], a[140:143], a[176:179], a[12:15]\n"
"v_mfma_f32_16x16x32_bf16 a[16:19], a[144:147], a[176:179], a[16:19]\n"
"ds_read_b64_tr_b16 a[244:245], v8 offset:128\n"
"v_mfma_f32_16x16x32_bf16 a[20:23], a[148:151], a[176:179], a[20:23]\n"
"v_mfma_f32_16x16x32_bf16 a[24:27], a[152:155], a[176:179], a[24:27]\n"
"ds_read_b64_tr_b16 a[246:247], v8 offset:1152\n"
"v_mfma_f32_16x16x32_bf16 a[28:31], a[156:159], a[176:179], a[28:31]\n"
"v_mfma_f32_16x16x32_bf16 a[32:35], a[128:131], a[180:183], a[32:35]\n"
"ds_read_b64_tr_b16 a[248:249], v9 offset:128\n"
"v_mfma_f32_16x16x32_bf16 a[36:39], a[132:135], a[180:183], a[36:39]\n"
"v_mfma_f32_16x16x32_bf16 a[40:43], a[136:139], a[180:183], a[40:43]\n"
"ds_read_b64_tr_b16 a[250:251], v9 offset:1152\n"
"v_mfma_f32_16x16x32_bf16 a[44:47], a[140:143], a[180:183], a[44:47]\n"
"v_mfma_f32_16x16x32_bf16 a[48:51], a[144:147], a[180:183], a[48:51]\n"
"ds_read_b64_tr_b16 a[252:253], v10 offset:128\n"
"v_mfma_f32_16x16x32_bf16 a[52:55], a[148:151], a[180:183], a[52:55]\n"
"v_mfma_f32_16x16x32_bf16 a[56:59], a[152:155], a[180:183], a[56:59]\n"
"ds_read_b64_tr_b16 a[254:255], v10 offset:1152\n"
"v_mfma_f32_16x16x32_bf16 a[60:63], a[156:159], a[180:183], a[60:63]\n"
"v_mfma_f32_16x16x32_bf16 a[64:67], a[128:131], a[184:187], a[64:67]\n"
"v_mfma_f32_16x16x32_bf16 a[68:71], a[132:135], a[184:187], a[68:71]\n"
"v_mfma_f32_16x16x32_bf16 a[72:75], a[136:139], a[184:187], a[72:75]\n"
"v_mfma_f32_16x16x32_bf16 a[76:79], a[140:143], a[184:187], a[76:79]\n"
"v_mfma_f32_16x16x32_bf16 a[80:83], a[144:147], a[184:187], a[80:83]\n"
"v_mfma_f32_16x16x32_bf16 a[84:87], a[148:151], a[184:187], a[84:87]\n"
"v_mfma_f32_16x16x32_bf16 a[88:91], a[152:155], a[184:187], a[88:91]\n"
"v_mfma_f32_16x16x32_bf16 a[92:95], a[156:159], a[184:187], a[92:95]\n"
"v_mfma_f32_16x16x32_bf16 a[96:99], a[128:131], a[188:191], a[96:99]\n"
"v_mfma_f32_16x16x32_bf16 a[100:103], a[132:135], a[188:191], a[100:103]\n"
"v_mfma_f32_16x16x32_bf16 a[104:107], a[136:139], a[188:191], a[104:107]\n"
"v_mfma_f32_16x16x32_bf16 a[108:111], a[140:143], a[188:191], a[108:111]\n"
"v_mfma_f32_16x16x32_bf16 a[112:115], a[144:147], a[188:191], a[112:115]\n"
"v_mfma_f32_16x16x32_bf16 a[116:119], a[148:151], a[188:191], a[116:119]\n"
"v_mfma_f32_16x16x32_bf16 a[120:123], a[152:155], a[188:191], a[120:123]\n"
"v_mfma_f32_16x16x32_bf16 a[124:127], a[156:159], a[188:191], a[124:127]\n"
"s_waitcnt lgkmcnt(0)\n"
"v_mfma_f32_16x16x32_bf16 v[128:131], a[192:195], a[224:227], v[128:131]\n"
"s_waitcnt vmcnt(1)\n"
"s_barrier\n"
"v_mfma_f32_16x16x32_bf16 v[132:135], a[196:199], a[224:227], v[132:135]\n"
"s_add_u32 m0, s82, 0x10800\n"
"ds_read_b64_tr_b16 a[160:161], v3 offset:0\n"
"v_mfma_f32_16x16x32_bf16 v[136:139], a[200:203], a[224:227], v[136:139]\n"
"buffer_load_dwordx4 v11, s[48:51], s68 offen lds\n"
"v_mfma_f32_16x16x32_bf16 v[140:143], a[204:207], a[224:227], v[140:143]\n"
"ds_read_b64_tr_b16 a[162:163], v3 offset:1024\n"
"v_add_u32 v2, 0xfffef800, v2\n"
"v_mfma_f32_16x16x32_bf16 v[144:147], a[208:211], a[224:227], v[144:147]\n"
"s_add_u32 m0, s82, 0x10c00\n"
"v_add_u32 v7, 0xfffef800, v7\n"
"v_mfma_f32_16x16x32_bf16 v[148:151], a[212:215], a[224:227], v[148:151]\n"
"ds_read_b128 a[128:131], v1 offset:0\n"
"v_add_u32 v8, 0xfffef800, v8\n"
"v_mfma_f32_16x16x32_bf16 v[152:155], a[216:219], a[224:227], v[152:155]\n"
"buffer_load_dwordx4 v12, s[48:51], s69 offen lds\n"
"v_mfma_f32_16x16x32_bf16 v[156:159], a[220:223], a[224:227], v[156:159]\n"
"ds_read_b128 a[132:135], v1 offset:2048\n"
"v_add_u32 v9, 0xfffef800, v9\n"
"v_mfma_f32_16x16x32_bf16 v[160:163], a[192:195], a[228:231], v[160:163]\n"
"s_add_u32 m0, s82, 0x11000\n"
"v_add_u32 v10, 0xfffef800, v10\n"
"v_mfma_f32_16x16x32_bf16 v[164:167], a[196:199], a[228:231], v[164:167]\n"
"ds_read_b128 a[136:139], v1 offset:4096\n"
"v_mfma_f32_16x16x32_bf16 v[168:171], a[200:203], a[228:231], v[168:171]\n"
"buffer_load_dwordx4 v11, s[48:51], s70 offen lds\n"
"v_mfma_f32_16x16x32_bf16 v[172:175], a[204:207], a[228:231], v[172:175]\n"
"s_add_u32 m0, s82, 0x11400\n"
"ds_read_b128 a[140:143], v1 offset:6144\n"
"v_mfma_f32_16x16x32_bf16 v[176:179], a[208:211], a[228:231], v[176:179]\n"
"v_mfma_f32_16x16x32_bf16 v[180:183], a[212:215], a[228:231], v[180:183]\n"
"buffer_load_dwordx4 v12, s[48:51], s71 offen lds\n"
"v_mfma_f32_16x16x32_bf16 v[184:187], a[216:219], a[228:231], v[184:187]\n"
"ds_read_b128 a[144:147], v1 offset:8192\n"
"v_mfma_f32_16x16x32_bf16 v[188:191], a[220:223], a[228:231], v[188:191]\n"
"s_add_u32 m0, s82, 0x11800\n"
"ds_read_b128 a[148:151], v1 offset:10240\n"
"v_mfma_f32_16x16x32_bf16 v[192:195], a[192:195], a[232:235], v[192:195]\n"
"v_mfma_f32_16x16x32_bf16 v[196:199], a[196:199], a[232:235], v[196:199]\n"
"buffer_load_dwordx4 v11, s[48:51], s72 offen lds\n"
"v_mfma_f32_16x16x32_bf16 v[200:203], a[200:203], a[232:235], v[200:203]\n"
"ds_read_b128 a[152:155], v1 offset:12288\n"
"v_mfma_f32_16x16x32_bf16 v[204:207], a[204:207], a[232:235], v[204:207]\n"
"s_add_u32 m0, s82, 0x11c00\n"
"ds_read_b128 a[156:159], v1 offset:14336\n"
"v_mfma_f32_16x16x32_bf16 v[208:211], a[208:211], a[232:235], v[208:211]\n"
"v_mfma_f32_16x16x32_bf16 v[212:215], a[212:215], a[232:235], v[212:215]\n"
"buffer_load_dwordx4 v12, s[48:51], s73 offen lds\n"
"v_mfma_f32_16x16x32_bf16 v[216:219], a[216:219], a[232:235], v[216:219]\n"
"s_add_u32 m0, s82, 0x12000\n"
"ds_read_b64_tr_b16 a[164:165], v4 offset:0\n"
"v_mfma_f32_16x16x32_bf16 v[220:223], a[220:223], a[232:235], v[220:223]\n"
"ds_read_b64_tr_b16 a[166:167], v4 offset:1024\n"
"v_mfma_f32_16x16x32_bf16 v[224:227], a[192:195], a[236:239], v[224:227]\n"
"buffer_load_dwordx4 v11, s[48:51], s74 offen lds\n"
"v_mfma_f32_16x16x32_bf16 v[228:231], a[196:199], a[236:239], v[228:231]\n"
"ds_read_b64_tr_b16 a[168:169], v5 offset:0\n"
"v_mfma_f32_16x16x32_bf16 v[232:235], a[200:203], a[236:239], v[232:235]\n"
"s_add_u32 m0, s82, 0x12400\n"
"v_mfma_f32_16x16x32_bf16 v[236:239], a[204:207], a[236:239], v[236:239]\n"
"ds_read_b64_tr_b16 a[170:171], v5 offset:1024\n"
"v_mfma_f32_16x16x32_bf16 v[240:243], a[208:211], a[236:239], v[240:243]\n"
"buffer_load_dwordx4 v12, s[48:51], s75 offen lds\n"
"v_mfma_f32_16x16x32_bf16 v[244:247], a[212:215], a[236:239], v[244:247]\n"
"ds_read_b64_tr_b16 a[172:173], v6 offset:0\n"
"v_mfma_f32_16x16x32_bf16 v[248:251], a[216:219], a[236:239], v[248:251]\n"
"s_add_u32 m0, s83, 0x18800\n"
"v_mfma_f32_16x16x32_bf16 v[252:255], a[220:223], a[236:239], v[252:255]\n"
"buffer_load_dwordx4 v13, s[52:55], s76 offen lds\n"
"v_mfma_f32_16x16x32_bf16 a[0:3], a[192:195], a[240:243], a[0:3]\n"
"ds_read_b64_tr_b16 a[174:175], v6 offset:1024\n"
"v_mfma_f32_16x16x32_bf16 a[4:7], a[196:199], a[240:243], a[4:7]\n"
"s_add_u32 m0, s83, 0x18c00\n"
"ds_read_b64_tr_b16 a[176:177], v3 offset:128\n"
"v_mfma_f32_16x16x32_bf16 a[8:11], a[200:203], a[240:243], a[8:11]\n"
"v_mfma_f32_16x16x32_bf16 a[12:15], a[204:207], a[240:243], a[12:15]\n"
"buffer_load_dwordx4 v13, s[52:55], s77 offen lds\n"
"v_mfma_f32_16x16x32_bf16 a[16:19], a[208:211], a[240:243], a[16:19]\n"
"ds_read_b64_tr_b16 a[178:179], v3 offset:1152\n"
"v_mfma_f32_16x16x32_bf16 a[20:23], a[212:215], a[240:243], a[20:23]\n"
"s_add_u32 m0, s83, 0x19080\n"
"ds_read_b64_tr_b16 a[180:181], v4 offset:128\n"
"v_mfma_f32_16x16x32_bf16 a[24:27], a[216:219], a[240:243], a[24:27]\n"
"v_mfma_f32_16x16x32_bf16 a[28:31], a[220:223], a[240:243], a[28:31]\n"
"buffer_load_dwordx4 v13, s[52:55], s78 offen lds\n"
"v_mfma_f32_16x16x32_bf16 a[32:35], a[192:195], a[244:247], a[32:35]\n"
"ds_read_b64_tr_b16 a[182:183], v4 offset:1152\n"
"v_mfma_f32_16x16x32_bf16 a[36:39], a[196:199], a[244:247], a[36:39]\n"
"s_add_u32 m0, s83, 0x19480\n"
"ds_read_b64_tr_b16 a[184:185], v5 offset:128\n"
"v_mfma_f32_16x16x32_bf16 a[40:43], a[200:203], a[244:247], a[40:43]\n"
"buffer_load_dwordx4 v13, s[52:55], s79 offen lds\n"
"v_mfma_f32_16x16x32_bf16 a[44:47], a[204:207], a[244:247], a[44:47]\n"
"ds_read_b64_tr_b16 a[186:187], v5 offset:1152\n"
"v_mfma_f32_16x16x32_bf16 a[48:51], a[208:211], a[244:247], a[48:51]\n"
"s_add_u32 m0, s83, 0x1cc00\n"
"v_mfma_f32_16x16x32_bf16 a[52:55], a[212:215], a[244:247], a[52:55]\n"
"ds_read_b64_tr_b16 a[188:189], v6 offset:128\n"
"v_mfma_f32_16x16x32_bf16 a[56:59], a[216:219], a[244:247], a[56:59]\n"
"buffer_load_dwordx4 v13, s[56:59], s76 offen lds\n"
"v_mfma_f32_16x16x32_bf16 a[60:63], a[220:223], a[244:247], a[60:63]\n"
"ds_read_b64_tr_b16 a[190:191], v6 offset:1152\n"
"v_mfma_f32_16x16x32_bf16 a[64:67], a[192:195], a[248:251], a[64:67]\n"
"s_add_u32 m0, s83, 0x1d000\n"
"v_mfma_f32_16x16x32_bf16 a[68:71], a[196:199], a[248:251], a[68:71]\n"
"v_mfma_f32_16x16x32_bf16 a[72:75], a[200:203], a[248:251], a[72:75]\n"
"buffer_load_dwordx4 v13, s[56:59], s77 offen lds\n"
"v_mfma_f32_16x16x32_bf16 a[76:79], a[204:207], a[248:251], a[76:79]\n"
"s_add_u32 m0, s83, 0x1d480\n"
"v_mfma_f32_16x16x32_bf16 a[80:83], a[208:211], a[248:251], a[80:83]\n"
"v_mfma_f32_16x16x32_bf16 a[84:87], a[212:215], a[248:251], a[84:87]\n"
"buffer_load_dwordx4 v13, s[56:59], s78 offen lds\n"
"v_mfma_f32_16x16x32_bf16 a[88:91], a[216:219], a[248:251], a[88:91]\n"
"v_mfma_f32_16x16x32_bf16 a[92:95], a[220:223], a[248:251], a[92:95]\n"
"s_add_u32 m0, s83, 0x1d880\n"
"v_mfma_f32_16x16x32_bf16 a[96:99], a[192:195], a[252:255], a[96:99]\n"
"v_mfma_f32_16x16x32_bf16 a[100:103], a[196:199], a[252:255], a[100:103]\n"
"buffer_load_dwordx4 v13, s[56:59], s79 offen lds\n"
"v_mfma_f32_16x16x32_bf16 a[104:107], a[200:203], a[252:255], a[104:107]\n"
"v_mfma_f32_16x16x32_bf16 a[108:111], a[204:207], a[252:255], a[108:111]\n"
"buffer_load_dword v17, v42, s[60:63], 0 offen\n"
"v_mfma_f32_16x16x32_bf16 a[112:115], a[208:211], a[252:255], a[112:115]\n"
"v_add_u32 v11, 0x80, v11\n"
"v_add_u32 v12, 0x80, v12\n"
"v_add_u32 v13, 0x80000, v13\n"
"v_add_u32 v42, s84, v42\n"
"v_mfma_f32_16x16x32_bf16 a[116:119], a[212:215], a[252:255], a[116:119]\n"
"v_mfma_f32_16x16x32_bf16 a[120:123], a[216:219], a[252:255], a[120:123]\n"
"s_add_u32 s85, s85, 1\n"
"v_mfma_f32_16x16x32_bf16 a[124:127], a[220:223], a[252:255], a[124:127]\n"
"s_cbranch_scc0 LM_vloop\n"
"s_waitcnt lgkmcnt(0)\n"
"v_mfma_f32_16x16x32_bf16 v[128:131], a[128:131], a[160:163], v[128:131]\n"
"s_mov_b32 s48, s8\n"
"ds_read_b64_tr_b16 a[224:225], v7 offset:0\n"
"v_mfma_f32_16x16x32_bf16 v[132:135], a[132:135], a[160:163], v[132:135]\n"
"s_and_b32 s49, s9, 0xffff\n"
"v_add_u32 v1, 0x10800, v1\n"
"v_mfma_f32_16x16x32_bf16 v[136:139], a[136:139], a[160:163], v[136:139]\n"
"s_mov_b32 s50, 0x200000\n"
"ds_read_b64_tr_b16 a[226:227], v7 offset:1024\n"
"v_mfma_f32_16x16x32_bf16 v[140:143], a[140:143], a[160:163], v[140:143]\n"
"v_add_u32 v3, 0x10800, v3\n"
"v_add_u32 v4, 0x10800, v4\n"
"v_mfma_f32_16x16x32_bf16 v[144:147], a[144:147], a[160:163], v[144:147]\n"
"s_mov_b32 s51, 0x20000\n"
"ds_read_b128 a[192:195], v2 offset:0\n"
"v_mfma_f32_16x16x32_bf16 v[148:151], a[148:151], a[160:163], v[148:151]\n"
"s_mov_b32 s52, s12\n"
"v_add_u32 v5, 0x10800, v5\n"
"v_mfma_f32_16x16x32_bf16 v[152:155], a[152:155], a[160:163], v[152:155]\n"
"ds_read_b128 a[196:199], v2 offset:2048\n"
"v_add_u32 v6, 0x10800, v6\n"
"v_mfma_f32_16x16x32_bf16 v[156:159], a[156:159], a[160:163], v[156:159]\n"
"s_and_b32 s53, s13, 0xffff\n"
"v_mfma_f32_16x16x32_bf16 v[160:163], a[128:131], a[164:167], v[160:163]\n"
"s_mov_b32 s54, 0x400000\n"
"ds_read_b128 a[200:203], v2 offset:4096\n"
"v_mfma_f32_16x16x32_bf16 v[164:167], a[132:135], a[164:167], v[164:167]\n"
"v_mfma_f32_16x16x32_bf16 v[168:171], a[136:139], a[164:167], v[168:171]\n"
"s_mov_b32 s55, 0x20000\n"
"ds_read_b128 a[204:207], v2 offset:6144\n"
"v_mfma_f32_16x16x32_bf16 v[172:175], a[140:143], a[164:167], v[172:175]\n"
"s_add_u32 s44, s26, 0x3e00000\n"
"v_mfma_f32_16x16x32_bf16 v[176:179], a[144:147], a[164:167], v[176:179]\n"
"s_addc_u32 s45, s27, 0\n"
"ds_read_b128 a[208:211], v2 offset:8192\n"
"v_mfma_f32_16x16x32_bf16 v[180:183], a[148:151], a[164:167], v[180:183]\n"
"v_mfma_f32_16x16x32_bf16 v[184:187], a[152:155], a[164:167], v[184:187]\n"
"s_cmp_ge_u32 s37, 28\n"
"ds_read_b128 a[212:215], v2 offset:10240\n"
"v_mfma_f32_16x16x32_bf16 v[188:191], a[156:159], a[164:167], v[188:191]\n"
"s_cselect_b32 s56, s44, s20\n"
"v_mfma_f32_16x16x32_bf16 v[192:195], a[128:131], a[168:171], v[192:195]\n"
"ds_read_b128 a[216:219], v2 offset:12288\n"
"v_mfma_f32_16x16x32_bf16 v[196:199], a[132:135], a[168:171], v[196:199]\n"
"s_cselect_b32 s57, s45, s21\n"
"v_mfma_f32_16x16x32_bf16 v[200:203], a[136:139], a[168:171], v[200:203]\n"
"s_mov_b32 s58, 0x1c0000\n"
"ds_read_b128 a[220:223], v2 offset:14336\n"
"v_mfma_f32_16x16x32_bf16 v[204:207], a[140:143], a[168:171], v[204:207]\n"
"v_mfma_f32_16x16x32_bf16 v[208:211], a[144:147], a[168:171], v[208:211]\n"
"s_cselect_b32 s58, 0x40000, s58\n"
"ds_read_b64_tr_b16 a[228:229], v8 offset:0\n"
"v_mfma_f32_16x16x32_bf16 v[212:215], a[148:151], a[168:171], v[212:215]\n"
"s_mov_b32 s91, 0x38000\n"
"v_mfma_f32_16x16x32_bf16 v[216:219], a[152:155], a[168:171], v[216:219]\n"
"s_cselect_b32 s91, 1024, s91\n"
"ds_read_b64_tr_b16 a[230:231], v8 offset:1024\n"
"v_mfma_f32_16x16x32_bf16 v[220:223], a[156:159], a[168:171], v[220:223]\n"
"v_mfma_f32_16x16x32_bf16 v[224:227], a[128:131], a[172:175], v[224:227]\n"
"s_and_b32 s57, s57, 0xffff\n"
"ds_read_b64_tr_b16 a[232:233], v9 offset:0\n"
"v_mfma_f32_16x16x32_bf16 v[228:231], a[132:135], a[172:175], v[228:231]\n"
"s_mov_b32 s59, 0x20000\n"
"v_mfma_f32_16x16x32_bf16 v[232:235], a[136:139], a[172:175], v[232:235]\n"
"ds_read_b64_tr_b16 a[234:235], v9 offset:1024\n"
"v_mfma_f32_16x16x32_bf16 v[236:239], a[140:143], a[172:175], v[236:239]\n"
"s_lshl_b32 s44, s38, 17\n"
"v_mfma_f32_16x16x32_bf16 v[240:243], a[144:147], a[172:175], v[240:243]\n"
"s_lshl_b32 s45, s34, 15\n"
"ds_read_b64_tr_b16 a[236:237], v10 offset:0\n"
"v_mfma_f32_16x16x32_bf16 v[244:247], a[148:151], a[172:175], v[244:247]\n"
"v_mfma_f32_16x16x32_bf16 v[248:251], a[152:155], a[172:175], v[248:251]\n"
"s_add_u32 s46, s44, s45\n"
"ds_read_b64_tr_b16 a[238:239], v10 offset:1024\n"
"v_mfma_f32_16x16x32_bf16 v[252:255], a[156:159], a[172:175], v[252:255]\n"
"s_add_u32 s68, s46, 0\n"
"v_mfma_f32_16x16x32_bf16 a[0:3], a[128:131], a[176:179], a[0:3]\n"
"s_add_u32 s69, s46, 8192\n"
"ds_read_b64_tr_b16 a[240:241], v7 offset:128\n"
"v_mfma_f32_16x16x32_bf16 a[4:7], a[132:135], a[176:179], a[4:7]\n"
"v_mfma_f32_16x16x32_bf16 a[8:11], a[136:139], a[176:179], a[8:11]\n"
"s_add_u32 s70, s46, 16384\n"
"ds_read_b64_tr_b16 a[242:243], v7 offset:1152\n"
"v_mfma_f32_16x16x32_bf16 a[12:15], a[140:143], a[176:179], a[12:15]\n"
"s_add_u32 s71, s46, 24576\n"
"v_mfma_f32_16x16x32_bf16 a[16:19], a[144:147], a[176:179], a[16:19]\n"
"ds_read_b64_tr_b16 a[244:245], v8 offset:128\n"
"v_mfma_f32_16x16x32_bf16 a[20:23], a[148:151], a[176:179], a[20:23]\n"
"s_sub_u32 s46, 1966080, s44\n"
"v_mfma_f32_16x16x32_bf16 a[24:27], a[152:155], a[176:179], a[24:27]\n"
"s_add_u32 s46, s46, s45\n"
"ds_read_b64_tr_b16 a[246:247], v8 offset:1152\n"
"v_mfma_f32_16x16x32_bf16 a[28:31], a[156:159], a[176:179], a[28:31]\n"
"v_mfma_f32_16x16x32_bf16 a[32:35], a[128:131], a[180:183], a[32:35]\n"
"s_add_u32 s72, s46, 0\n"
"ds_read_b64_tr_b16 a[248:249], v9 offset:128\n"
"v_mfma_f32_16x16x32_bf16 a[36:39], a[132:135], a[180:183], a[36:39]\n"
"s_add_u32 s73, s46, 8192\n"
"v_mfma_f32_16x16x32_bf16 a[40:43], a[136:139], a[180:183], a[40:43]\n"
"s_add_u32 s74, s46, 16384\n"
"ds_read_b64_tr_b16 a[250:251], v9 offset:1152\n"
"v_mfma_f32_16x16x32_bf16 a[44:47], a[140:143], a[180:183], a[44:47]\n"
"v_mfma_f32_16x16x32_bf16 a[48:51], a[144:147], a[180:183], a[48:51]\n"
"s_add_u32 s75, s46, 24576\n"
"ds_read_b64_tr_b16 a[252:253], v10 offset:128\n"
"v_mfma_f32_16x16x32_bf16 a[52:55], a[148:151], a[180:183], a[52:55]\n"
"s_mul_i32 s44, s34, 57344\n"
"v_mfma_f32_16x16x32_bf16 a[56:59], a[152:155], a[180:183], a[56:59]\n"
"ds_read_b64_tr_b16 a[254:255], v10 offset:1152\n"
"v_mfma_f32_16x16x32_bf16 a[60:63], a[156:159], a[180:183], a[60:63]\n"
"s_lshl_b32 s45, s37, 7\n"
"v_mfma_f32_16x16x32_bf16 a[64:67], a[128:131], a[184:187], a[64:67]\n"
"s_add_u32 s80, s44, s45\n"
"v_mfma_f32_16x16x32_bf16 a[68:71], a[132:135], a[184:187], a[68:71]\n"
"v_mfma_f32_16x16x32_bf16 a[72:75], a[136:139], a[184:187], a[72:75]\n"
"s_add_u32 s81, s80, 28672\n"
"v_mfma_f32_16x16x32_bf16 a[76:79], a[140:143], a[184:187], a[76:79]\n"
"s_lshl_b32 s44, s34, 8\n"
"v_mfma_f32_16x16x32_bf16 a[80:83], a[144:147], a[184:187], a[80:83]\n"
"s_lshl_b32 s45, s36, 13\n"
"v_mfma_f32_16x16x32_bf16 a[84:87], a[148:151], a[184:187], a[84:87]\n"
"v_mfma_f32_16x16x32_bf16 a[88:91], a[152:155], a[184:187], a[88:91]\n"
"s_add_u32 s44, s44, s45\n"
"v_mfma_f32_16x16x32_bf16 a[92:95], a[156:159], a[184:187], a[92:95]\n"
"s_add_u32 s45, s44, 128\n"
"v_mfma_f32_16x16x32_bf16 a[96:99], a[128:131], a[188:191], a[96:99]\n"
"v_mfma_f32_16x16x32_bf16 a[100:103], a[132:135], a[188:191], a[100:103]\n"
"s_cmp_ge_u32 s37, 28\n"
"v_mfma_f32_16x16x32_bf16 a[104:107], a[136:139], a[188:191], a[104:107]\n"
"s_cselect_b32 s80, s44, s80\n"
"v_mfma_f32_16x16x32_bf16 a[108:111], a[140:143], a[188:191], a[108:111]\n"
"v_mfma_f32_16x16x32_bf16 a[112:115], a[144:147], a[188:191], a[112:115]\n"
"s_cselect_b32 s81, s45, s81\n"
"v_mfma_f32_16x16x32_bf16 a[116:119], a[148:151], a[188:191], a[116:119]\n"
"s_lshl_b32 s84, s34, 11\n"
"v_mfma_f32_16x16x32_bf16 a[120:123], a[152:155], a[188:191], a[120:123]\n"
"v_mfma_f32_16x16x32_bf16 a[124:127], a[156:159], a[188:191], a[124:127]\n"
"s_waitcnt lgkmcnt(0)\n"
"v_mfma_f32_16x16x32_bf16 v[128:131], a[192:195], a[224:227], v[128:131]\n"
"s_waitcnt vmcnt(0)\n"
"s_barrier\n"
"v_mfma_f32_16x16x32_bf16 v[132:135], a[196:199], a[224:227], v[132:135]\n"
"s_add_u32 m0, s82, 0x0\n"
"ds_read_b64_tr_b16 a[160:161], v3 offset:0\n"
"v_mfma_f32_16x16x32_bf16 v[136:139], a[200:203], a[224:227], v[136:139]\n"
"v_add_u32 v2, 0x10800, v2\n"
"v_add_u32 v7, 0x10800, v7\n"
"v_mfma_f32_16x16x32_bf16 v[140:143], a[204:207], a[224:227], v[140:143]\n"
"buffer_load_dwordx4 v36, s[48:51], s68 offen lds ;@Q0\n"
"v_mfma_f32_16x16x32_bf16 v[144:147], a[208:211], a[224:227], v[144:147]\n"
"ds_read_b64_tr_b16 a[162:163], v3 offset:1024\n"
"v_add_u32 v8, 0x10800, v8\n"
"v_mfma_f32_16x16x32_bf16 v[148:151], a[212:215], a[224:227], v[148:151]\n"
"s_add_u32 m0, s82, 0x400\n"
"ds_read_b128 a[128:131], v1 offset:0\n"
"v_mfma_f32_16x16x32_bf16 v[152:155], a[216:219], a[224:227], v[152:155]\n"
"v_add_u32 v9, 0x10800, v9\n"
"v_add_u32 v10, 0x10800, v10\n"
"v_mfma_f32_16x16x32_bf16 v[156:159], a[220:223], a[224:227], v[156:159]\n"
"buffer_load_dwordx4 v37, s[48:51], s69 offen lds ;@Q0\n"
"v_mfma_f32_16x16x32_bf16 v[160:163], a[192:195], a[228:231], v[160:163]\n"
"ds_read_b128 a[132:135], v1 offset:2048\n"
"v_mfma_f32_16x16x32_bf16 v[164:167], a[196:199], a[228:231], v[164:167]\n"
"s_add_u32 m0, s82, 0x800\n"
"ds_read_b128 a[136:139], v1 offset:4096\n"
"v_mfma_f32_16x16x32_bf16 v[168:171], a[200:203], a[228:231], v[168:171]\n"
"v_mfma_f32_16x16x32_bf16 v[172:175], a[204:207], a[228:231], v[172:175]\n"
"buffer_load_dwordx4 v36, s[48:51], s70 offen lds ;@Q0\n"
"v_mfma_f32_16x16x32_bf16 v[176:179], a[208:211], a[228:231], v[176:179]\n"
"ds_read_b128 a[140:143], v1 offset:6144\n"
"v_mfma_f32_16x16x32_bf16 v[180:183], a[212:215], a[228:231], v[180:183]\n"
"s_add_u32 m0, s82, 0xc00\n"
"ds_read_b128 a[144:147], v1 offset:8192\n"
"v_mfma_f32_16x16x32_bf16 v[184:187], a[216:219], a[228:231], v[184:187]\n"
"v_mfma_f32_16x16x32_bf16 v[188:191], a[220:223], a[228:231], v[188:191]\n"
"ds_read_b128 a[148:151], v1 offset:10240\n"
"v_mfma_f32_16x16x32_bf16 v[192:195], a[192:195], a[232:235], v[192:195]\n"
"buffer_load_dwordx4 v37, s[48:51], s71 offen lds ;@Q0\n"
"v_mfma_f32_16x16x32_bf16 v[196:199], a[196:199], a[232:235], v[196:199]\n"
"ds_read_b128 a[152:155], v1 offset:12288\n"
"v_mfma_f32_16x16x32_bf16 v[200:203], a[200:203], a[232:235], v[200:203]\n"
"s_add_u32 m0, s82, 0x1000\n"
"v_mfma_f32_16x16x32_bf16 v[204:207], a[204:207], a[232:235], v[204:207]\n"
"ds_read_b128 a[156:159], v1 offset:14336\n"
"v_mfma_f32_16x16x32_bf16 v[208:211], a[208:211], a[232:235], v[208:211]\n"
"buffer_load_dwordx4 v36, s[48:51], s72 offen lds ;@Q0\n"
"v_mfma_f32_16x16x32_bf16 v[212:215], a[212:215], a[232:235], v[212:215]\n"
"ds_read_b64_tr_b16 a[164:165], v4 offset:0\n"
"v_mfma_f32_16x16x32_bf16 v[216:219], a[216:219], a[232:235], v[216:219]\n"
"s_add_u32 m0, s82, 0x1400\n"
"v_mfma_f32_16x16x32_bf16 v[220:223], a[220:223], a[232:235], v[220:223]\n"
"ds_read_b64_tr_b16 a[166:167], v4 offset:1024\n"
"v_mfma_f32_16x16x32_bf16 v[224:227], a[192:195], a[236:239], v[224:227]\n"
"buffer_load_dwordx4 v37, s[48:51], s73 offen lds ;@Q0\n"
"v_mfma_f32_16x16x32_bf16 v[228:231], a[196:199], a[236:239], v[228:231]\n"
"ds_read_b64_tr_b16 a[168:169], v5 offset:0\n"
"v_mfma_f32_16x16x32_bf16 v[232:235], a[200:203], a[236:239], v[232:235]\n"
"s_add_u32 m0, s82, 0x1800\n"
"v_mfma_f32_16x16x32_bf16 v[236:239], a[204:207], a[236:239], v[236:239]\n"
"ds_read_b64_tr_b16 a[170:171], v5 offset:1024\n"
"v_mfma_f32_16x16x32_bf16 v[240:243], a[208:211], a[236:239], v[240:243]\n"
"buffer_load_dwordx4 v36, s[48:51], s74 offen lds ;@Q0\n"
"v_mfma_f32_16x16x32_bf16 v[244:247], a[212:215], a[236:239], v[244:247]\n"
"ds_read_b64_tr_b16 a[172:173], v6 offset:0\n"
"v_mfma_f32_16x16x32_bf16 v[248:251], a[216:219], a[236:239], v[248:251]\n"
"v_mfma_f32_16x16x32_bf16 v[252:255], a[220:223], a[236:239], v[252:255]\n"
"s_add_u32 m0, s82, 0x1c00\n"
"ds_read_b64_tr_b16 a[174:175], v6 offset:1024\n"
"v_mfma_f32_16x16x32_bf16 a[0:3], a[192:195], a[240:243], a[0:3]\n"
"v_mfma_f32_16x16x32_bf16 a[4:7], a[196:199], a[240:243], a[4:7]\n"
"buffer_load_dwordx4 v37, s[48:51], s75 offen lds ;@Q0\n"
"v_mfma_f32_16x16x32_bf16 a[8:11], a[200:203], a[240:243], a[8:11]\n"
"ds_read_b64_tr_b16 a[176:177], v3 offset:128\n"
"v_mfma_f32_16x16x32_bf16 a[12:15], a[204:207], a[240:243], a[12:15]\n"
"s_add_u32 m0, s83, 0x8000\n"
"ds_read_b64_tr_b16 a[178:179], v3 offset:1152\n"
"v_mfma_f32_16x16x32_bf16 a[16:19], a[208:211], a[240:243], a[16:19]\n"
"v_mfma_f32_16x16x32_bf16 a[20:23], a[212:215], a[240:243], a[20:23]\n"
"buffer_load_dwordx4 v38, s[52:55], s76 offen lds ;@Q0\n"
"v_mfma_f32_16x16x32_bf16 a[24:27], a[216:219], a[240:243], a[24:27]\n"
"ds_read_b64_tr_b16 a[180:181], v4 offset:128\n"
"v_mfma_f32_16x16x32_bf16 a[28:31], a[220:223], a[240:243], a[28:31]\n"
"s_add_u32 m0, s83, 0x8400\n"
"ds_read_b64_tr_b16 a[182:183], v4 offset:1152\n"
"v_mfma_f32_16x16x32_bf16 a[32:35], a[192:195], a[244:247], a[32:35]\n"
"v_mfma_f32_16x16x32_bf16 a[36:39], a[196:199], a[244:247], a[36:39]\n"
"buffer_load_dwordx4 v38, s[52:55], s77 offen lds ;@Q0\n"
"v_mfma_f32_16x16x32_bf16 a[40:43], a[200:203], a[244:247], a[40:43]\n"
"ds_read_b64_tr_b16 a[184:185], v5 offset:128\n"
"v_mfma_f32_16x16x32_bf16 a[44:47], a[204:207], a[244:247], a[44:47]\n"
"s_add_u32 m0, s83, 0x8880\n"
"ds_read_b64_tr_b16 a[186:187], v5 offset:1152\n"
"v_mfma_f32_16x16x32_bf16 a[48:51], a[208:211], a[244:247], a[48:51]\n"
"v_mfma_f32_16x16x32_bf16 a[52:55], a[212:215], a[244:247], a[52:55]\n"
"ds_read_b64_tr_b16 a[188:189], v6 offset:128\n"
"v_mfma_f32_16x16x32_bf16 a[56:59], a[216:219], a[244:247], a[56:59]\n"
"buffer_load_dwordx4 v38, s[52:55], s78 offen lds ;@Q0\n"
"v_mfma_f32_16x16x32_bf16 a[60:63], a[220:223], a[244:247], a[60:63]\n"
"ds_read_b64_tr_b16 a[190:191], v6 offset:1152\n"
"v_mfma_f32_16x16x32_bf16 a[64:67], a[192:195], a[248:251], a[64:67]\n"
"s_add_u32 m0, s83, 0x8c80\n"
"v_mfma_f32_16x16x32_bf16 a[68:71], a[196:199], a[248:251], a[68:71]\n"
"v_mfma_f32_16x16x32_bf16 a[72:75], a[200:203], a[248:251], a[72:75]\n"
"buffer_load_dwordx4 v38, s[52:55], s79 offen lds ;@Q0\n"
"v_mfma_f32_16x16x32_bf16 a[76:79], a[204:207], a[248:251], a[76:79]\n"
"v_mfma_f32_16x16x32_bf16 a[80:83], a[208:211], a[248:251], a[80:83]\n"
"s_add_u32 m0, s84, 0xc400\n"
"v_mfma_f32_16x16x32_bf16 a[84:87], a[212:215], a[248:251], a[84:87]\n"
"v_mfma_f32_16x16x32_bf16 a[88:91], a[216:219], a[248:251], a[88:91]\n"
"buffer_load_dwordx4 v39, s[56:59], s80 offen lds ;@Q0\n"
"v_mfma_f32_16x16x32_bf16 a[92:95], a[220:223], a[248:251], a[92:95]\n"
"v_mfma_f32_16x16x32_bf16 a[96:99], a[192:195], a[252:255], a[96:99]\n"
"s_add_u32 m0, s84, 0xc800\n"
"v_mfma_f32_16x16x32_bf16 a[100:103], a[196:199], a[252:255], a[100:103]\n"
"v_mfma_f32_16x16x32_bf16 a[104:107], a[200:203], a[252:255], a[104:107]\n"
"buffer_load_dwordx4 v40, s[56:59], s81 offen lds ;@Q0\n"
"v_mfma_f32_16x16x32_bf16 a[108:111], a[204:207], a[252:255], a[108:111]\n"
"v_add_u32 v36, 0x80, v36\n"
"v_add_u32 v37, 0x80, v37\n"
"v_mfma_f32_16x16x32_bf16 a[112:115], a[208:211], a[252:255], a[112:115]\n"
"v_add_u32 v38, 0x80000, v38\n"
"v_add_u32 v39, s91, v39\n"
"v_mfma_f32_16x16x32_bf16 a[116:119], a[212:215], a[252:255], a[116:119]\n"
"v_add_u32 v40, s91, v40\n"
"v_mfma_f32_16x16x32_bf16 a[120:123], a[216:219], a[252:255], a[120:123]\n"
"v_mfma_f32_16x16x32_bf16 a[124:127], a[220:223], a[252:255], a[124:127]\n"
"s_waitcnt lgkmcnt(0)\n"
"v_mfma_f32_16x16x32_bf16 v[128:131], a[128:131], a[160:163], v[128:131]\n"
"ds_read_b64_tr_b16 a[224:225], v7 offset:0\n"
"v_add_u32 v1, 0xfffef800, v1\n"
"v_mfma_f32_16x16x32_bf16 v[132:135], a[132:135], a[160:163], v[132:135]\n"
"v_add_u32 v3, 0xfffef800, v3\n"
"v_add_u32 v4, 0xfffef800, v4\n"
"v_mfma_f32_16x16x32_bf16 v[136:139], a[136:139], a[160:163], v[136:139]\n"
"ds_read_b64_tr_b16 a[226:227], v7 offset:1024\n"
"v_add_u32 v5, 0xfffef800, v5\n"
"v_mfma_f32_16x16x32_bf16 v[140:143], a[140:143], a[160:163], v[140:143]\n"
"v_add_u32 v6, 0xfffef800, v6\n"
"v_mfma_f32_16x16x32_bf16 v[144:147], a[144:147], a[160:163], v[144:147]\n"
"ds_read_b128 a[192:195], v2 offset:0\n"
"v_mfma_f32_16x16x32_bf16 v[148:151], a[148:151], a[160:163], v[148:151]\n"
"v_mfma_f32_16x16x32_bf16 v[152:155], a[152:155], a[160:163], v[152:155]\n"
"ds_read_b128 a[196:199], v2 offset:2048\n"
"v_mfma_f32_16x16x32_bf16 v[156:159], a[156:159], a[160:163], v[156:159]\n"
"v_mfma_f32_16x16x32_bf16 v[160:163], a[128:131], a[164:167], v[160:163]\n"
"ds_read_b128 a[200:203], v2 offset:4096\n"
"v_mfma_f32_16x16x32_bf16 v[164:167], a[132:135], a[164:167], v[164:167]\n"
"v_mfma_f32_16x16x32_bf16 v[168:171], a[136:139], a[164:167], v[168:171]\n"
"ds_read_b128 a[204:207], v2 offset:6144\n"
"v_mfma_f32_16x16x32_bf16 v[172:175], a[140:143], a[164:167], v[172:175]\n"
"v_mfma_f32_16x16x32_bf16 v[176:179], a[144:147], a[164:167], v[176:179]\n"
"ds_read_b128 a[208:211], v2 offset:8192\n"
"v_mfma_f32_16x16x32_bf16 v[180:183], a[148:151], a[164:167], v[180:183]\n"
"v_mfma_f32_16x16x32_bf16 v[184:187], a[152:155], a[164:167], v[184:187]\n"
"ds_read_b128 a[212:215], v2 offset:10240\n"
"v_mfma_f32_16x16x32_bf16 v[188:191], a[156:159], a[164:167], v[188:191]\n"
"v_mfma_f32_16x16x32_bf16 v[192:195], a[128:131], a[168:171], v[192:195]\n"
"ds_read_b128 a[216:219], v2 offset:12288\n"
"v_mfma_f32_16x16x32_bf16 v[196:199], a[132:135], a[168:171], v[196:199]\n"
"v_mfma_f32_16x16x32_bf16 v[200:203], a[136:139], a[168:171], v[200:203]\n"
"ds_read_b128 a[220:223], v2 offset:14336\n"
"v_mfma_f32_16x16x32_bf16 v[204:207], a[140:143], a[168:171], v[204:207]\n"
"v_mfma_f32_16x16x32_bf16 v[208:211], a[144:147], a[168:171], v[208:211]\n"
"ds_read_b64_tr_b16 a[228:229], v8 offset:0\n"
"v_mfma_f32_16x16x32_bf16 v[212:215], a[148:151], a[168:171], v[212:215]\n"
"v_mfma_f32_16x16x32_bf16 v[216:219], a[152:155], a[168:171], v[216:219]\n"
"ds_read_b64_tr_b16 a[230:231], v8 offset:1024\n"
"v_mfma_f32_16x16x32_bf16 v[220:223], a[156:159], a[168:171], v[220:223]\n"
"v_mfma_f32_16x16x32_bf16 v[224:227], a[128:131], a[172:175], v[224:227]\n"
"ds_read_b64_tr_b16 a[232:233], v9 offset:0\n"
"v_mfma_f32_16x16x32_bf16 v[228:231], a[132:135], a[172:175], v[228:231]\n"
"v_mfma_f32_16x16x32_bf16 v[232:235], a[136:139], a[172:175], v[232:235]\n"
"ds_read_b64_tr_b16 a[234:235], v9 offset:1024\n"
"v_mfma_f32_16x16x32_bf16 v[236:239], a[140:143], a[172:175], v[236:239]\n"
"v_mfma_f32_16x16x32_bf16 v[240:243], a[144:147], a[172:175], v[240:243]\n"
"ds_read_b64_tr_b16 a[236:237], v10 offset:0\n"
"v_mfma_f32_16x16x32_bf16 v[244:247], a[148:151], a[172:175], v[244:247]\n"
"v_mfma_f32_16x16x32_bf16 v[248:251], a[152:155], a[172:175], v[248:251]\n"
"ds_read_b64_tr_b16 a[238:239], v10 offset:1024\n"
"v_mfma_f32_16x16x32_bf16 v[252:255], a[156:159], a[172:175], v[252:255]\n"
"v_mfma_f32_16x16x32_bf16 a[0:3], a[128:131], a[176:179], a[0:3]\n"
"ds_read_b64_tr_b16 a[240:241], v7 offset:128\n"
"v_mfma_f32_16x16x32_bf16 a[4:7], a[132:135], a[176:179], a[4:7]\n"
"v_mfma_f32_16x16x32_bf16 a[8:11], a[136:139], a[176:179], a[8:11]\n"
"ds_read_b64_tr_b16 a[242:243], v7 offset:1152\n"
"v_mfma_f32_16x16x32_bf16 a[12:15], a[140:143], a[176:179], a[12:15]\n"
"v_mfma_f32_16x16x32_bf16 a[16:19], a[144:147], a[176:179], a[16:19]\n"
"ds_read_b64_tr_b16 a[244:245], v8 offset:128\n"
"v_mfma_f32_16x16x32_bf16 a[20:23], a[148:151], a[176:179], a[20:23]\n"
"v_mfma_f32_16x16x32_bf16 a[24:27], a[152:155], a[176:179], a[24:27]\n"
"ds_read_b64_tr_b16 a[246:247], v8 offset:1152\n"
"v_mfma_f32_16x16x32_bf16 a[28:31], a[156:159], a[176:179], a[28:31]\n"
"v_mfma_f32_16x16x32_bf16 a[32:35], a[128:131], a[180:183], a[32:35]\n"
"ds_read_b64_tr_b16 a[248:249], v9 offset:128\n"
"v_mfma_f32_16x16x32_bf16 a[36:39], a[132:135], a[180:183], a[36:39]\n"
"v_mfma_f32_16x16x32_bf16 a[40:43], a[136:139], a[180:183], a[40:43]\n"
"ds_read_b64_tr_b16 a[250:251], v9 offset:1152\n"
"v_mfma_f32_16x16x32_bf16 a[44:47], a[140:143], a[180:183], a[44:47]\n"
"v_mfma_f32_16x16x32_bf16 a[48:51], a[144:147], a[180:183], a[48:51]\n"
"ds_read_b64_tr_b16 a[252:253], v10 offset:128\n"
"v_mfma_f32_16x16x32_bf16 a[52:55], a[148:151], a[180:183], a[52:55]\n"
"v_mfma_f32_16x16x32_bf16 a[56:59], a[152:155], a[180:183], a[56:59]\n"
"ds_read_b64_tr_b16 a[254:255], v10 offset:1152\n"
"v_mfma_f32_16x16x32_bf16 a[60:63], a[156:159], a[180:183], a[60:63]\n"
"v_mfma_f32_16x16x32_bf16 a[64:67], a[128:131], a[184:187], a[64:67]\n"
"v_mfma_f32_16x16x32_bf16 a[68:71], a[132:135], a[184:187], a[68:71]\n"
"v_mfma_f32_16x16x32_bf16 a[72:75], a[136:139], a[184:187], a[72:75]\n"
"v_mfma_f32_16x16x32_bf16 a[76:79], a[140:143], a[184:187], a[76:79]\n"
"v_mfma_f32_16x16x32_bf16 a[80:83], a[144:147], a[184:187], a[80:83]\n"
"v_mfma_f32_16x16x32_bf16 a[84:87], a[148:151], a[184:187], a[84:87]\n"
"v_mfma_f32_16x16x32_bf16 a[88:91], a[152:155], a[184:187], a[88:91]\n"
"v_mfma_f32_16x16x32_bf16 a[92:95], a[156:159], a[184:187], a[92:95]\n"
"v_mfma_f32_16x16x32_bf16 a[96:99], a[128:131], a[188:191], a[96:99]\n"
"v_mfma_f32_16x16x32_bf16 a[100:103], a[132:135], a[188:191], a[100:103]\n"
"v_mfma_f32_16x16x32_bf16 a[104:107], a[136:139], a[188:191], a[104:107]\n"
"v_mfma_f32_16x16x32_bf16 a[108:111], a[140:143], a[188:191], a[108:111]\n"
"v_mfma_f32_16x16x32_bf16 a[112:115], a[144:147], a[188:191], a[112:115]\n"
"v_mfma_f32_16x16x32_bf16 a[116:119], a[148:151], a[188:191], a[116:119]\n"
"v_mfma_f32_16x16x32_bf16 a[120:123], a[152:155], a[188:191], a[120:123]\n"
"v_mfma_f32_16x16x32_bf16 a[124:127], a[156:159], a[188:191], a[124:127]\n"
"s_waitcnt lgkmcnt(0)\n"
"v_mfma_f32_16x16x32_bf16 v[128:131], a[192:195], a[224:227], v[128:131]\n"
"s_barrier\n"
"v_mfma_f32_16x16x32_bf16 v[132:135], a[196:199], a[224:227], v[132:135]\n"
"s_add_u32 m0, s82, 0xe400\n"
"v_mfma_f32_16x16x32_bf16 v[136:139], a[200:203], a[224:227], v[136:139]\n"
"v_mfma_f32_16x16x32_bf16 v[140:143], a[204:207], a[224:227], v[140:143]\n"
"buffer_load_dwordx4 v36, s[48:51], s68 offen lds ;@Q1\n"
"v_mfma_f32_16x16x32_bf16 v[144:147], a[208:211], a[224:227], v[144:147]\n"
"v_mfma_f32_16x16x32_bf16 v[148:151], a[212:215], a[224:227], v[148:151]\n"
"s_add_u32 m0, s82, 0xe800\n"
"v_mfma_f32_16x16x32_bf16 v[152:155], a[216:219], a[224:227], v[152:155]\n"
"v_mfma_f32_16x16x32_bf16 v[156:159], a[220:223], a[224:227], v[156:159]\n"
"buffer_load_dwordx4 v37, s[48:51], s69 offen lds ;@Q1\n"
"v_mfma_f32_16x16x32_bf16 v[160:163], a[192:195], a[228:231], v[160:163]\n"
"v_mfma_f32_16x16x32_bf16 v[164:167], a[196:199], a[228:231], v[164:167]\n"
"s_add_u32 m0, s82, 0xec00\n"
"v_mfma_f32_16x16x32_bf16 v[168:171], a[200:203], a[228:231], v[168:171]\n"
"v_mfma_f32_16x16x32_bf16 v[172:175], a[204:207], a[228:231], v[172:175]\n"
"buffer_load_dwordx4 v36, s[48:51], s70 offen lds ;@Q1\n"
"v_mfma_f32_16x16x32_bf16 v[176:179], a[208:211], a[228:231], v[176:179]\n"
"v_mfma_f32_16x16x32_bf16 v[180:183], a[212:215], a[228:231], v[180:183]\n"
"s_add_u32 m0, s82, 0xf000\n"
"v_mfma_f32_16x16x32_bf16 v[184:187], a[216:219], a[228:231], v[184:187]\n"
"v_mfma_f32_16x16x32_bf16 v[188:191], a[220:223], a[228:231], v[188:191]\n"
"v_mfma_f32_16x16x32_bf16 v[192:195], a[192:195], a[232:235], v[192:195]\n"
"buffer_load_dwordx4 v37, s[48:51], s71 offen lds ;@Q1\n"
"v_mfma_f32_16x16x32_bf16 v[196:199], a[196:199], a[232:235], v[196:199]\n"
"v_mfma_f32_16x16x32_bf16 v[200:203], a[200:203], a[232:235], v[200:203]\n"
"s_add_u32 m0, s82, 0xf400\n"
"v_mfma_f32_16x16x32_bf16 v[204:207], a[204:207], a[232:235], v[204:207]\n"
"v_mfma_f32_16x16x32_bf16 v[208:211], a[208:211], a[232:235], v[208:211]\n"
"buffer_load_dwordx4 v36, s[48:51], s72 offen lds ;@Q1\n"
"v_mfma_f32_16x16x32_bf16 v[212:215], a[212:215], a[232:235], v[212:215]\n"
"v_mfma_f32_16x16x32_bf16 v[216:219], a[216:219], a[232:235], v[216:219]\n"
"s_add_u32 m0, s82, 0xf800\n"
"v_mfma_f32_16x16x32_bf16 v[220:223], a[220:223], a[232:235], v[220:223]\n"
"v_mfma_f32_16x16x32_bf16 v[224:227], a[192:195], a[236:239], v[224:227]\n"
"buffer_load_dwordx4 v37, s[48:51], s73 offen lds ;@Q1\n"
"v_mfma_f32_16x16x32_bf16 v[228:231], a[196:199], a[236:239], v[228:231]\n"
"v_mfma_f32_16x16x32_bf16 v[232:235], a[200:203], a[236:239], v[232:235]\n"
"s_add_u32 m0, s82, 0xfc00\n"
"v_mfma_f32_16x16x32_bf16 v[236:239], a[204:207], a[236:239], v[236:239]\n"
"v_mfma_f32_16x16x32_bf16 v[240:243], a[208:211], a[236:239], v[240:243]\n"
"buffer_load_dwordx4 v36, s[48:51], s74 offen lds ;@Q1\n"
"v_mfma_f32_16x16x32_bf16 v[244:247], a[212:215], a[236:239], v[244:247]\n"
"v_mfma_f32_16x16x32_bf16 v[248:251], a[216:219], a[236:239], v[248:251]\n"
"v_mfma_f32_16x16x32_bf16 v[252:255], a[220:223], a[236:239], v[252:255]\n"
"s_add_u32 m0, s82, 0x10000\n"
"v_mfma_f32_16x16x32_bf16 a[0:3], a[192:195], a[240:243], a[0:3]\n"
"v_mfma_f32_16x16x32_bf16 a[4:7], a[196:199], a[240:243], a[4:7]\n"
"buffer_load_dwordx4 v37, s[48:51], s75 offen lds ;@Q1\n"
"v_mfma_f32_16x16x32_bf16 a[8:11], a[200:203], a[240:243], a[8:11]\n"
"v_mfma_f32_16x16x32_bf16 a[12:15], a[204:207], a[240:243], a[12:15]\n"
"s_add_u32 m0, s83, 0x16400\n"
"v_mfma_f32_16x16x32_bf16 a[16:19], a[208:211], a[240:243], a[16:19]\n"
"v_mfma_f32_16x16x32_bf16 a[20:23], a[212:215], a[240:243], a[20:23]\n"
"buffer_load_dwordx4 v38, s[52:55], s76 offen lds ;@Q1\n"
"v_mfma_f32_16x16x32_bf16 a[24:27], a[216:219], a[240:243], a[24:27]\n"
"v_mfma_f32_16x16x32_bf16 a[28:31], a[220:223], a[240:243], a[28:31]\n"
"s_add_u32 m0, s83, 0x16800\n"
"v_mfma_f32_16x16x32_bf16 a[32:35], a[192:195], a[244:247], a[32:35]\n"
"v_mfma_f32_16x16x32_bf16 a[36:39], a[196:199], a[244:247], a[36:39]\n"
"buffer_load_dwordx4 v38, s[52:55], s77 offen lds ;@Q1\n"
"v_mfma_f32_16x16x32_bf16 a[40:43], a[200:203], a[244:247], a[40:43]\n"
"v_mfma_f32_16x16x32_bf16 a[44:47], a[204:207], a[244:247], a[44:47]\n"
"s_add_u32 m0, s83, 0x16c80\n"
"v_mfma_f32_16x16x32_bf16 a[48:51], a[208:211], a[244:247], a[48:51]\n"
"v_mfma_f32_16x16x32_bf16 a[52:55], a[212:215], a[244:247], a[52:55]\n"
"v_mfma_f32_16x16x32_bf16 a[56:59], a[216:219], a[244:247], a[56:59]\n"
"buffer_load_dwordx4 v38, s[52:55], s78 offen lds ;@Q1\n"
"v_mfma_f32_16x16x32_bf16 a[60:63], a[220:223], a[244:247], a[60:63]\n"
"v_mfma_f32_16x16x32_bf16 a[64:67], a[192:195], a[248:251], a[64:67]\n"
"s_add_u32 m0, s83, 0x17080\n"
"v_mfma_f32_16x16x32_bf16 a[68:71], a[196:199], a[248:251], a[68:71]\n"
"v_mfma_f32_16x16x32_bf16 a[72:75], a[200:203], a[248:251], a[72:75]\n"
"buffer_load_dwordx4 v38, s[52:55], s79 offen lds ;@Q1\n"
"v_mfma_f32_16x16x32_bf16 a[76:79], a[204:207], a[248:251], a[76:79]\n"
"v_mfma_f32_16x16x32_bf16 a[80:83], a[208:211], a[248:251], a[80:83]\n"
"s_add_u32 m0, s84, 0x1a800\n"
"v_mfma_f32_16x16x32_bf16 a[84:87], a[212:215], a[248:251], a[84:87]\n"
"v_mfma_f32_16x16x32_bf16 a[88:91], a[216:219], a[248:251], a[88:91]\n"
"buffer_load_dwordx4 v39, s[56:59], s80 offen lds ;@Q1\n"
"v_mfma_f32_16x16x32_bf16 a[92:95], a[220:223], a[248:251], a[92:95]\n"
"v_mfma_f32_16x16x32_bf16 a[96:99], a[192:195], a[252:255], a[96:99]\n"
"s_add_u32 m0, s84, 0x1ac00\n"
"v_mfma_f32_16x16x32_bf16 a[100:103], a[196:199], a[252:255], a[100:103]\n"
"v_mfma_f32_16x16x32_bf16 a[104:107], a[200:203], a[252:255], a[104:107]\n"
"buffer_load_dwordx4 v40, s[56:59], s81 offen lds ;@Q1\n"
"v_mfma_f32_16x16x32_bf16 a[108:111], a[204:207], a[252:255], a[108:111]\n"
"v_add_u32 v36, 0x80, v36\n"
"v_add_u32 v37, 0x80, v37\n"
"v_mfma_f32_16x16x32_bf16 a[112:115], a[208:211], a[252:255], a[112:115]\n"
"v_add_u32 v38, 0x80000, v38\n"
"v_add_u32 v39, s91, v39\n"
"v_mfma_f32_16x16x32_bf16 a[116:119], a[212:215], a[252:255], a[116:119]\n"
"v_add_u32 v40, s91, v40\n"
"v_mfma_f32_16x16x32_bf16 a[120:123], a[216:219], a[252:255], a[120:123]\n"
"v_mfma_f32_16x16x32_bf16 a[124:127], a[220:223], a[252:255], a[124:127]\n"
"s_lshl_b32 s44, s37, 20\n"
"s_add_u32 s60, s26, s44\n"
"s_addc_u32 s61, s27, 0\n"
"s_and_b32 s61, s61, 0xffff\n"
"s_mov_b32 s62, 0x100000\n"
"s_mov_b32 s63, 0x20000\n"
"s_lshl_b32 s44, s38, 17\n"
"s_lshl_b32 s45, s39, 16\n"
"s_add_u32 s46, s44, s45\n"
"v_mov_b32 v17, v14\n"
"v_add_u32 v41, 0x1000, v14\n"
"s_add_u32 s86, s46, 8192\n"
"s_add_u32 s87, s46, 9216\n"
"s_add_u32 s88, s46, 10240\n"
"s_add_u32 s89, s46, 11264\n"
"v_accvgpr_read_b32 v48, a0\n"
"v_accvgpr_read_b32 v49, a1\n"
"v_accvgpr_read_b32 v50, a2\n"
"v_accvgpr_read_b32 v51, a3\n"
"v_accvgpr_read_b32 v52, a4\n"
"v_accvgpr_read_b32 v53, a5\n"
"v_accvgpr_read_b32 v54, a6\n"
"v_accvgpr_read_b32 v55, a7\n"
"v_cvt_pk_bf16_f32 v128, v128, v129\n"
"v_cvt_pk_bf16_f32 v129, v130, v131\n"
"v_cvt_pk_bf16_f32 v130, v132, v133\n"
"v_cvt_pk_bf16_f32 v131, v134, v135\n"
"v_cvt_pk_bf16_f32 v132, v48, v49\n"
"v_cvt_pk_bf16_f32 v133, v50, v51\n"
"v_cvt_pk_bf16_f32 v134, v52, v53\n"
"v_cvt_pk_bf16_f32 v135, v54, v55\n"
"v_accvgpr_read_b32 v56, a32\n"
"v_accvgpr_read_b32 v57, a33\n"
"v_accvgpr_read_b32 v58, a34\n"
"v_accvgpr_read_b32 v59, a35\n"
"v_accvgpr_read_b32 v60, a36\n"
"v_accvgpr_read_b32 v61, a37\n"
"v_accvgpr_read_b32 v62, a38\n"
"v_accvgpr_read_b32 v63, a39\n"
"v_cvt_pk_bf16_f32 v160, v160, v161\n"
"v_cvt_pk_bf16_f32 v161, v162, v163\n"
"v_cvt_pk_bf16_f32 v162, v164, v165\n"
"v_cvt_pk_bf16_f32 v163, v166, v167\n"
"v_cvt_pk_bf16_f32 v164, v56, v57\n"
"v_cvt_pk_bf16_f32 v165, v58, v59\n"
"v_cvt_pk_bf16_f32 v166, v60, v61\n"
"v_cvt_pk_bf16_f32 v167, v62, v63\n"
"v_accvgpr_read_b32 v48, a64\n"
"v_accvgpr_read_b32 v49, a65\n"
"v_accvgpr_read_b32 v50, a66\n"
"v_accvgpr_read_b32 v51, a67\n"
"v_accvgpr_read_b32 v52, a68\n"
"v_accvgpr_read_b32 v53, a69\n"
"v_accvgpr_read_b32 v54, a70\n"
"v_accvgpr_read_b32 v55, a71\n"
"v_cvt_pk_bf16_f32 v192, v192, v193\n"
"v_cvt_pk_bf16_f32 v193, v194, v195\n"
"v_cvt_pk_bf16_f32 v194, v196, v197\n"
"v_cvt_pk_bf16_f32 v195, v198, v199\n"
"v_cvt_pk_bf16_f32 v196, v48, v49\n"
"v_cvt_pk_bf16_f32 v197, v50, v51\n"
"v_cvt_pk_bf16_f32 v198, v52, v53\n"
"v_cvt_pk_bf16_f32 v199, v54, v55\n"
"v_accvgpr_read_b32 v56, a96\n"
"v_accvgpr_read_b32 v57, a97\n"
"v_accvgpr_read_b32 v58, a98\n"
"v_accvgpr_read_b32 v59, a99\n"
"v_accvgpr_read_b32 v60, a100\n"
"v_accvgpr_read_b32 v61, a101\n"
"v_accvgpr_read_b32 v62, a102\n"
"v_accvgpr_read_b32 v63, a103\n"
"v_cvt_pk_bf16_f32 v224, v224, v225\n"
"v_cvt_pk_bf16_f32 v225, v226, v227\n"
"v_cvt_pk_bf16_f32 v226, v228, v229\n"
"v_cvt_pk_bf16_f32 v227, v230, v231\n"
"v_cvt_pk_bf16_f32 v228, v56, v57\n"
"v_cvt_pk_bf16_f32 v229, v58, v59\n"
"v_cvt_pk_bf16_f32 v230, v60, v61\n"
"v_cvt_pk_bf16_f32 v231, v62, v63\n"
"v_accvgpr_read_b32 v48, a8\n"
"v_accvgpr_read_b32 v49, a9\n"
"v_accvgpr_read_b32 v50, a10\n"
"v_accvgpr_read_b32 v51, a11\n"
"v_accvgpr_read_b32 v52, a12\n"
"v_accvgpr_read_b32 v53, a13\n"
"v_accvgpr_read_b32 v54, a14\n"
"v_accvgpr_read_b32 v55, a15\n"
"v_cvt_pk_bf16_f32 v136, v136, v137\n"
"v_cvt_pk_bf16_f32 v137, v138, v139\n"
"v_cvt_pk_bf16_f32 v138, v140, v141\n"
"v_cvt_pk_bf16_f32 v139, v142, v143\n"
"v_cvt_pk_bf16_f32 v140, v48, v49\n"
"v_cvt_pk_bf16_f32 v141, v50, v51\n"
"v_cvt_pk_bf16_f32 v142, v52, v53\n"
"v_cvt_pk_bf16_f32 v143, v54, v55\n"
"v_accvgpr_read_b32 v56, a40\n"
"v_accvgpr_read_b32 v57, a41\n"
"v_accvgpr_read_b32 v58, a42\n"
"v_accvgpr_read_b32 v59, a43\n"
"v_accvgpr_read_b32 v60, a44\n"
"v_accvgpr_read_b32 v61, a45\n"
"v_accvgpr_read_b32 v62, a46\n"
"v_accvgpr_read_b32 v63, a47\n"
"v_cvt_pk_bf16_f32 v168, v168, v169\n"
"v_cvt_pk_bf16_f32 v169, v170, v171\n"
"v_cvt_pk_bf16_f32 v170, v172, v173\n"
"v_cvt_pk_bf16_f32 v171, v174, v175\n"
"v_cvt_pk_bf16_f32 v172, v56, v57\n"
"v_cvt_pk_bf16_f32 v173, v58, v59\n"
"v_cvt_pk_bf16_f32 v174, v60, v61\n"
"v_cvt_pk_bf16_f32 v175, v62, v63\n"
"v_accvgpr_read_b32 v48, a72\n"
"v_accvgpr_read_b32 v49, a73\n"
"v_accvgpr_read_b32 v50, a74\n"
"v_accvgpr_read_b32 v51, a75\n"
"v_accvgpr_read_b32 v52, a76\n"
"v_accvgpr_read_b32 v53, a77\n"
"v_accvgpr_read_b32 v54, a78\n"
"v_accvgpr_read_b32 v55, a79\n"
"v_cvt_pk_bf16_f32 v200, v200, v201\n"
"v_cvt_pk_bf16_f32 v201, v202, v203\n"
"v_cvt_pk_bf16_f32 v202, v204, v205\n"
"v_cvt_pk_bf16_f32 v203, v206, v207\n"
"v_cvt_pk_bf16_f32 v204, v48, v49\n"
"v_cvt_pk_bf16_f32 v205, v50, v51\n"
"v_cvt_pk_bf16_f32 v206, v52, v53\n"
"v_cvt_pk_bf16_f32 v207, v54, v55\n"
"v_accvgpr_read_b32 v56, a104\n"
"v_accvgpr_read_b32 v57, a105\n"
"v_accvgpr_read_b32 v58, a106\n"
"v_accvgpr_read_b32 v59, a107\n"
"v_accvgpr_read_b32 v60, a108\n"
"v_accvgpr_read_b32 v61, a109\n"
"v_accvgpr_read_b32 v62, a110\n"
"v_accvgpr_read_b32 v63, a111\n"
"v_cvt_pk_bf16_f32 v232, v232, v233\n"
"v_cvt_pk_bf16_f32 v233, v234, v235\n"
"v_cvt_pk_bf16_f32 v234, v236, v237\n"
"v_cvt_pk_bf16_f32 v235, v238, v239\n"
"v_cvt_pk_bf16_f32 v236, v56, v57\n"
"v_cvt_pk_bf16_f32 v237, v58, v59\n"
"v_cvt_pk_bf16_f32 v238, v60, v61\n"
"v_cvt_pk_bf16_f32 v239, v62, v63\n"
"v_accvgpr_read_b32 v48, a16\n"
"v_accvgpr_read_b32 v49, a17\n"
"v_accvgpr_read_b32 v50, a18\n"
"v_accvgpr_read_b32 v51, a19\n"
"v_accvgpr_read_b32 v52, a20\n"
"v_accvgpr_read_b32 v53, a21\n"
"v_accvgpr_read_b32 v54, a22\n"
"v_accvgpr_read_b32 v55, a23\n"
"v_cvt_pk_bf16_f32 v144, v144, v145\n"
"v_cvt_pk_bf16_f32 v145, v146, v147\n"
"v_cvt_pk_bf16_f32 v146, v148, v149\n"
"v_cvt_pk_bf16_f32 v147, v150, v151\n"
"v_cvt_pk_bf16_f32 v148, v48, v49\n"
"v_cvt_pk_bf16_f32 v149, v50, v51\n"
"v_cvt_pk_bf16_f32 v150, v52, v53\n"
"v_cvt_pk_bf16_f32 v151, v54, v55\n"
"v_accvgpr_read_b32 v56, a48\n"
"v_accvgpr_read_b32 v57, a49\n"
"v_accvgpr_read_b32 v58, a50\n"
"v_accvgpr_read_b32 v59, a51\n"
"v_accvgpr_read_b32 v60, a52\n"
"v_accvgpr_read_b32 v61, a53\n"
"v_accvgpr_read_b32 v62, a54\n"
"v_accvgpr_read_b32 v63, a55\n"
"v_cvt_pk_bf16_f32 v176, v176, v177\n"
"v_cvt_pk_bf16_f32 v177, v178, v179\n"
"v_cvt_pk_bf16_f32 v178, v180, v181\n"
"v_cvt_pk_bf16_f32 v179, v182, v183\n"
"v_cvt_pk_bf16_f32 v180, v56, v57\n"
"v_cvt_pk_bf16_f32 v181, v58, v59\n"
"v_cvt_pk_bf16_f32 v182, v60, v61\n"
"v_cvt_pk_bf16_f32 v183, v62, v63\n"
"v_accvgpr_read_b32 v48, a80\n"
"v_accvgpr_read_b32 v49, a81\n"
"v_accvgpr_read_b32 v50, a82\n"
"v_accvgpr_read_b32 v51, a83\n"
"v_accvgpr_read_b32 v52, a84\n"
"v_accvgpr_read_b32 v53, a85\n"
"v_accvgpr_read_b32 v54, a86\n"
"v_accvgpr_read_b32 v55, a87\n"
"v_cvt_pk_bf16_f32 v208, v208, v209\n"
"v_cvt_pk_bf16_f32 v209, v210, v211\n"
"v_cvt_pk_bf16_f32 v210, v212, v213\n"
"v_cvt_pk_bf16_f32 v211, v214, v215\n"
"v_cvt_pk_bf16_f32 v212, v48, v49\n"
"v_cvt_pk_bf16_f32 v213, v50, v51\n"
"v_cvt_pk_bf16_f32 v214, v52, v53\n"
"v_cvt_pk_bf16_f32 v215, v54, v55\n"
"v_accvgpr_read_b32 v56, a112\n"
"v_accvgpr_read_b32 v57, a113\n"
"v_accvgpr_read_b32 v58, a114\n"
"v_accvgpr_read_b32 v59, a115\n"
"v_accvgpr_read_b32 v60, a116\n"
"v_accvgpr_read_b32 v61, a117\n"
"v_accvgpr_read_b32 v62, a118\n"
"v_accvgpr_read_b32 v63, a119\n"
"v_cvt_pk_bf16_f32 v240, v240, v241\n"
"v_cvt_pk_bf16_f32 v241, v242, v243\n"
"v_cvt_pk_bf16_f32 v242, v244, v245\n"
"v_cvt_pk_bf16_f32 v243, v246, v247\n"
"v_cvt_pk_bf16_f32 v244, v56, v57\n"
"v_cvt_pk_bf16_f32 v245, v58, v59\n"
"v_cvt_pk_bf16_f32 v246, v60, v61\n"
"v_cvt_pk_bf16_f32 v247, v62, v63\n"
"v_accvgpr_read_b32 v48, a24\n"
"v_accvgpr_read_b32 v49, a25\n"
"v_accvgpr_read_b32 v50, a26\n"
"v_accvgpr_read_b32 v51, a27\n"
"v_accvgpr_read_b32 v52, a28\n"
"v_accvgpr_read_b32 v53, a29\n"
"v_accvgpr_read_b32 v54, a30\n"
"v_accvgpr_read_b32 v55, a31\n"
"v_cvt_pk_bf16_f32 v152, v152, v153\n"
"v_cvt_pk_bf16_f32 v153, v154, v155\n"
"v_cvt_pk_bf16_f32 v154, v156, v157\n"
"v_cvt_pk_bf16_f32 v155, v158, v159\n"
"v_cvt_pk_bf16_f32 v156, v48, v49\n"
"v_cvt_pk_bf16_f32 v157, v50, v51\n"
"v_cvt_pk_bf16_f32 v158, v52, v53\n"
"v_cvt_pk_bf16_f32 v159, v54, v55\n"
"v_accvgpr_read_b32 v56, a56\n"
"v_accvgpr_read_b32 v57, a57\n"
"v_accvgpr_read_b32 v58, a58\n"
"v_accvgpr_read_b32 v59, a59\n"
"v_accvgpr_read_b32 v60, a60\n"
"v_accvgpr_read_b32 v61, a61\n"
"v_accvgpr_read_b32 v62, a62\n"
"v_accvgpr_read_b32 v63, a63\n"
"v_cvt_pk_bf16_f32 v184, v184, v185\n"
"v_cvt_pk_bf16_f32 v185, v186, v187\n"
"v_cvt_pk_bf16_f32 v186, v188, v189\n"
"v_cvt_pk_bf16_f32 v187, v190, v191\n"
"v_cvt_pk_bf16_f32 v188, v56, v57\n"
"v_cvt_pk_bf16_f32 v189, v58, v59\n"
"v_cvt_pk_bf16_f32 v190, v60, v61\n"
"v_cvt_pk_bf16_f32 v191, v62, v63\n"
"v_accvgpr_read_b32 v48, a88\n"
"v_accvgpr_read_b32 v49, a89\n"
"v_accvgpr_read_b32 v50, a90\n"
"v_accvgpr_read_b32 v51, a91\n"
"v_accvgpr_read_b32 v52, a92\n"
"v_accvgpr_read_b32 v53, a93\n"
"v_accvgpr_read_b32 v54, a94\n"
"v_accvgpr_read_b32 v55, a95\n"
"v_cvt_pk_bf16_f32 v216, v216, v217\n"
"v_cvt_pk_bf16_f32 v217, v218, v219\n"
"v_cvt_pk_bf16_f32 v218, v220, v221\n"
"v_cvt_pk_bf16_f32 v219, v222, v223\n"
"v_cvt_pk_bf16_f32 v220, v48, v49\n"
"v_cvt_pk_bf16_f32 v221, v50, v51\n"
"v_cvt_pk_bf16_f32 v222, v52, v53\n"
"v_cvt_pk_bf16_f32 v223, v54, v55\n"
"v_accvgpr_read_b32 v56, a120\n"
"v_accvgpr_read_b32 v57, a121\n"
"v_accvgpr_read_b32 v58, a122\n"
"v_accvgpr_read_b32 v59, a123\n"
"v_accvgpr_read_b32 v60, a124\n"
"v_accvgpr_read_b32 v61, a125\n"
"v_accvgpr_read_b32 v62, a126\n"
"v_accvgpr_read_b32 v63, a127\n"
"v_cvt_pk_bf16_f32 v248, v248, v249\n"
"v_cvt_pk_bf16_f32 v249, v250, v251\n"
"v_cvt_pk_bf16_f32 v250, v252, v253\n"
"v_cvt_pk_bf16_f32 v251, v254, v255\n"
"v_cvt_pk_bf16_f32 v252, v56, v57\n"
"v_cvt_pk_bf16_f32 v253, v58, v59\n"
"v_cvt_pk_bf16_f32 v254, v60, v61\n"
"v_cvt_pk_bf16_f32 v255, v62, v63\n"
"LM_q:\n"
"s_waitcnt vmcnt(14)\n"
"s_barrier\n"
"ds_read_b64_tr_b16 a[144:145], v20 offset:0\n"
"ds_read_b64_tr_b16 a[146:147], v20 offset:1024\n"
"ds_read_b128 a[128:131], v18 offset:0\n"
"ds_read_b128 a[132:135], v18 offset:2048\n"
"ds_read_b128 a[136:139], v18 offset:4096\n"
"ds_read_b128 a[140:143], v18 offset:6144\n"
"ds_read_b64_tr_b16 a[148:149], v21 offset:0\n"
"ds_read_b64_tr_b16 a[150:151], v21 offset:1024\n"
"ds_read_b64_tr_b16 a[152:153], v22 offset:0\n"
"ds_read_b64_tr_b16 a[154:155], v22 offset:1024\n"
"ds_read_b64_tr_b16 a[156:157], v23 offset:0\n"
"ds_read_b64_tr_b16 a[158:159], v23 offset:1024\n"
"ds_read_b64_tr_b16 a[160:161], v20 offset:128\n"
"ds_read_b64_tr_b16 a[162:163], v20 offset:1152\n"
"ds_read_b64_tr_b16 a[164:165], v21 offset:128\n"
"ds_read_b64_tr_b16 a[166:167], v21 offset:1152\n"
"ds_read_b64_tr_b16 a[168:169], v22 offset:128\n"
"ds_read_b64_tr_b16 a[170:171], v22 offset:1152\n"
"ds_read_b64_tr_b16 a[172:173], v23 offset:128\n"
"ds_read_b64_tr_b16 a[174:175], v23 offset:1152\n"
"ds_read_b64_tr_b16 a[176:177], v28 offset:0\n"
"ds_read_b64_tr_b16 a[178:179], v28 offset:512\n"
"ds_read_b64_tr_b16 a[180:181], v29 offset:0\n"
"ds_read_b64_tr_b16 a[182:183], v29 offset:512\n"
"ds_read_b64_tr_b16 a[184:185], v30 offset:0\n"
"ds_read_b64_tr_b16 a[186:187], v30 offset:512\n"
"ds_read_b64_tr_b16 a[188:189], v31 offset:0\n"
"ds_read_b64_tr_b16 a[190:191], v31 offset:512\n"
"s_waitcnt lgkmcnt(0)\n"
"v_mfma_f32_16x16x32_bf16 a[0:3], a[144:147], a[128:131], 0\n"
"ds_read_b64_tr_b16 a[208:209], v24 offset:0\n"
"v_add_u32 v18, 0xe400, v18\n"
"v_mfma_f32_16x16x32_bf16 a[4:7], a[144:147], a[132:135], 0\n"
"ds_read_b64_tr_b16 a[210:211], v24 offset:1024\n"
"v_add_u32 v20, 0xe400, v20\n"
"v_mfma_f32_16x16x32_bf16 a[8:11], a[144:147], a[136:139], 0\n"
"s_add_u32 s47, s86, 0\n"
"ds_read_b128 a[192:195], v19 offset:0\n"
"v_mfma_f32_16x16x32_bf16 a[12:15], a[144:147], a[140:143], 0\n"
"v_add_u32 v21, 0xe400, v21\n"
"v_add_u32 v22, 0xe400, v22\n"
"v_mfma_f32_16x16x32_bf16 a[16:19], a[148:151], a[128:131], 0\n"
"ds_read_b128 a[196:199], v19 offset:2048\n"
"v_add_u32 v23, 0xe400, v23\n"
"v_mfma_f32_16x16x32_bf16 a[20:23], a[148:151], a[132:135], 0\n"
"ds_read_b128 a[200:203], v19 offset:4096\n"
"v_add_u32 v28, 0xe400, v28\n"
"v_mfma_f32_16x16x32_bf16 a[24:27], a[148:151], a[136:139], 0\n"
"ds_read_b128 a[204:207], v19 offset:6144\n"
"v_add_u32 v29, 0xe400, v29\n"
"v_mfma_f32_16x16x32_bf16 a[28:31], a[148:151], a[140:143], 0\n"
"buffer_store_dwordx4 v[128:131], v17, s[60:63], s47 offen sc1\n"
"v_mfma_f32_16x16x32_bf16 a[32:35], a[152:155], a[128:131], 0\n"
"ds_read_b64_tr_b16 a[212:213], v25 offset:0\n"
"v_add_u32 v30, 0xe400, v30\n"
"v_mfma_f32_16x16x32_bf16 a[36:39], a[152:155], a[132:135], 0\n"
"ds_read_b64_tr_b16 a[214:215], v25 offset:1024\n"
"v_add_u32 v31, 0xe400, v31\n"
"v_mfma_f32_16x16x32_bf16 a[40:43], a[152:155], a[136:139], 0\n"
"v_mfma_f32_16x16x32_bf16 a[44:47], a[152:155], a[140:143], 0\n"
"ds_read_b64_tr_b16 a[216:217], v26 offset:0\n"
"v_mfma_f32_16x16x32_bf16 a[48:51], a[156:159], a[128:131], 0\n"
"s_add_u32 s47, s86, 0\n"
"ds_read_b64_tr_b16 a[218:219], v26 offset:1024\n"
"v_mfma_f32_16x16x32_bf16 a[52:55], a[156:159], a[132:135], 0\n"
"ds_read_b64_tr_b16 a[220:221], v27 offset:0\n"
"v_mfma_f32_16x16x32_bf16 a[56:59], a[156:159], a[136:139], 0\n"
"v_mfma_f32_16x16x32_bf16 a[60:63], a[156:159], a[140:143], 0\n"
"ds_read_b64_tr_b16 a[222:223], v27 offset:1024\n"
"v_mfma_f32_16x16x32_bf16 a[64:67], a[160:163], a[128:131], 0\n"
"ds_read_b64_tr_b16 a[224:225], v24 offset:128\n"
"v_mfma_f32_16x16x32_bf16 a[68:71], a[160:163], a[132:135], 0\n"
"v_mfma_f32_16x16x32_bf16 a[72:75], a[160:163], a[136:139], 0\n"
"buffer_store_dwordx4 v[132:135], v41, s[60:63], s47 offen sc1\n"
"v_mfma_f32_16x16x32_bf16 a[76:79], a[160:163], a[140:143], 0\n"
"ds_read_b64_tr_b16 a[226:227], v24 offset:1152\n"
"ds_read_b64_tr_b16 a[228:229], v25 offset:128\n"
"v_mfma_f32_16x16x32_bf16 a[80:83], a[164:167], a[128:131], 0\n"
"ds_read_b64_tr_b16 a[230:231], v25 offset:1152\n"
"v_mfma_f32_16x16x32_bf16 a[84:87], a[164:167], a[132:135], 0\n"
"v_mfma_f32_16x16x32_bf16 a[88:91], a[164:167], a[136:139], 0\n"
"ds_read_b64_tr_b16 a[232:233], v26 offset:128\n"
"v_mfma_f32_16x16x32_bf16 a[92:95], a[164:167], a[140:143], 0\n"
"s_add_u32 s47, s86, 16384\n"
"ds_read_b64_tr_b16 a[234:235], v26 offset:1152\n"
"v_mfma_f32_16x16x32_bf16 a[96:99], a[168:171], a[128:131], 0\n"
"v_mfma_f32_16x16x32_bf16 a[100:103], a[168:171], a[132:135], 0\n"
"ds_read_b64_tr_b16 a[236:237], v27 offset:128\n"
"v_mfma_f32_16x16x32_bf16 a[104:107], a[168:171], a[136:139], 0\n"
"ds_read_b64_tr_b16 a[238:239], v27 offset:1152\n"
"v_mfma_f32_16x16x32_bf16 a[108:111], a[168:171], a[140:143], 0\n"
"ds_read_b64_tr_b16 a[240:241], v32 offset:0\n"
"v_mfma_f32_16x16x32_bf16 a[112:115], a[172:175], a[128:131], 0\n"
"buffer_store_dwordx4 v[136:139], v17, s[60:63], s47 offen sc1\n"
"v_mfma_f32_16x16x32_bf16 a[116:119], a[172:175], a[132:135], 0\n"
"ds_read_b64_tr_b16 a[242:243], v32 offset:512\n"
"v_mfma_f32_16x16x32_bf16 a[120:123], a[172:175], a[136:139], 0\n"
"ds_read_b64_tr_b16 a[244:245], v33 offset:0\n"
"v_mfma_f32_16x16x32_bf16 a[124:127], a[172:175], a[140:143], 0\n"
"v_mfma_f32_16x16x32_bf16 v[64:67], a[176:179], a[128:131], v[64:67]\n"
"ds_read_b64_tr_b16 a[246:247], v33 offset:512\n"
"v_mfma_f32_16x16x32_bf16 v[68:71], a[176:179], a[132:135], v[68:71]\n"
"ds_read_b64_tr_b16 a[248:249], v34 offset:0\n"
"v_mfma_f32_16x16x32_bf16 v[72:75], a[176:179], a[136:139], v[72:75]\n"
"s_add_u32 s47, s86, 16384\n"
"ds_read_b64_tr_b16 a[250:251], v34 offset:512\n"
"v_mfma_f32_16x16x32_bf16 v[76:79], a[176:179], a[140:143], v[76:79]\n"
"v_mfma_f32_16x16x32_bf16 v[80:83], a[180:183], a[128:131], v[80:83]\n"
"ds_read_b64_tr_b16 a[252:253], v35 offset:0\n"
"v_mfma_f32_16x16x32_bf16 v[84:87], a[180:183], a[132:135], v[84:87]\n"
"ds_read_b64_tr_b16 a[254:255], v35 offset:512\n"
"v_mfma_f32_16x16x32_bf16 v[88:91], a[180:183], a[136:139], v[88:91]\n"
"v_mfma_f32_16x16x32_bf16 v[92:95], a[180:183], a[140:143], v[92:95]\n"
"buffer_store_dwordx4 v[140:143], v41, s[60:63], s47 offen sc1\n"
"v_mfma_f32_16x16x32_bf16 v[96:99], a[184:187], a[128:131], v[96:99]\n"
"v_mfma_f32_16x16x32_bf16 v[100:103], a[184:187], a[132:135], v[100:103]\n"
"v_mfma_f32_16x16x32_bf16 v[104:107], a[184:187], a[136:139], v[104:107]\n"
"v_mfma_f32_16x16x32_bf16 v[108:111], a[184:187], a[140:143], v[108:111]\n"
"v_mfma_f32_16x16x32_bf16 v[112:115], a[188:191], a[128:131], v[112:115]\n"
"v_mfma_f32_16x16x32_bf16 v[116:119], a[188:191], a[132:135], v[116:119]\n"
"v_mfma_f32_16x16x32_bf16 v[120:123], a[188:191], a[136:139], v[120:123]\n"
"v_mfma_f32_16x16x32_bf16 v[124:127], a[188:191], a[140:143], v[124:127]\n"
"s_waitcnt lgkmcnt(0)\n"
"v_mfma_f32_16x16x32_bf16 a[0:3], a[208:211], a[192:195], a[0:3]\n"
"s_waitcnt vmcnt(4)\n"
"s_barrier\n"
"v_mfma_f32_16x16x32_bf16 a[4:7], a[208:211], a[196:199], a[4:7]\n"
"s_add_u32 m0, s82, 0x0\n"
"ds_read_b64_tr_b16 a[144:145], v20 offset:0\n"
"v_mfma_f32_16x16x32_bf16 a[8:11], a[208:211], a[200:203], a[8:11]\n"
"buffer_load_dwordx4 v36, s[48:51], s68 offen lds ;@Q2\n"
"v_mfma_f32_16x16x32_bf16 a[12:15], a[208:211], a[204:207], a[12:15]\n"
"ds_read_b64_tr_b16 a[146:147], v20 offset:1024\n"
"ds_read_b128 a[128:131], v18 offset:0\n"
"v_mfma_f32_16x16x32_bf16 a[16:19], a[212:215], a[192:195], a[16:19]\n"
"s_add_u32 m0, s82, 0x400\n"
"v_add_u32 v19, 0xe400, v19\n"
"v_mfma_f32_16x16x32_bf16 a[20:23], a[212:215], a[196:199], a[20:23]\n"
"buffer_load_dwordx4 v37, s[48:51], s69 offen lds ;@Q2\n"
"v_mfma_f32_16x16x32_bf16 a[24:27], a[212:215], a[200:203], a[24:27]\n"
"ds_read_b128 a[132:135], v18 offset:2048\n"
"ds_read_b128 a[136:139], v18 offset:4096\n"
"v_mfma_f32_16x16x32_bf16 a[28:31], a[212:215], a[204:207], a[28:31]\n"
"s_add_u32 m0, s82, 0x800\n"
"ds_read_b128 a[140:143], v18 offset:6144\n"
"v_mfma_f32_16x16x32_bf16 a[32:35], a[216:219], a[192:195], a[32:35]\n"
"buffer_load_dwordx4 v36, s[48:51], s70 offen lds ;@Q2\n"
"v_mfma_f32_16x16x32_bf16 a[36:39], a[216:219], a[196:199], a[36:39]\n"
"ds_read_b64_tr_b16 a[148:149], v21 offset:0\n"
"v_add_u32 v24, 0xe400, v24\n"
"v_mfma_f32_16x16x32_bf16 a[40:43], a[216:219], a[200:203], a[40:43]\n"
"s_add_u32 m0, s82, 0xc00\n"
"ds_read_b64_tr_b16 a[150:151], v21 offset:1024\n"
"v_mfma_f32_16x16x32_bf16 a[44:47], a[216:219], a[204:207], a[44:47]\n"
"ds_read_b64_tr_b16 a[152:153], v22 offset:0\n"
"v_add_u32 v25, 0xe400, v25\n"
"v_mfma_f32_16x16x32_bf16 a[48:51], a[220:223], a[192:195], a[48:51]\n"
"buffer_load_dwordx4 v37, s[48:51], s71 offen lds ;@Q2\n"
"v_mfma_f32_16x16x32_bf16 a[52:55], a[220:223], a[196:199], a[52:55]\n"
"s_add_u32 m0, s82, 0x1000\n"
"ds_read_b64_tr_b16 a[154:155], v22 offset:1024\n"
"v_mfma_f32_16x16x32_bf16 a[56:59], a[220:223], a[200:203], a[56:59]\n"
"ds_read_b64_tr_b16 a[156:157], v23 offset:0\n"
"v_add_u32 v26, 0xe400, v26\n"
"v_mfma_f32_16x16x32_bf16 a[60:63], a[220:223], a[204:207], a[60:63]\n"
"buffer_load_dwordx4 v36, s[48:51], s72 offen lds ;@Q2\n"
"v_mfma_f32_16x16x32_bf16 a[64:67], a[224:227], a[192:195], a[64:67]\n"
"s_add_u32 m0, s82, 0x1400\n"
"ds_read_b64_tr_b16 a[158:159], v23 offset:1024\n"
"v_mfma_f32_16x16x32_bf16 a[68:71], a[224:227], a[196:199], a[68:71]\n"
"ds_read_b64_tr_b16 a[160:161], v20 offset:128\n"
"v_add_u32 v27, 0xe400, v27\n"
"v_mfma_f32_16x16x32_bf16 a[72:75], a[224:227], a[200:203], a[72:75]\n"
"buffer_load_dwordx4 v37, s[48:51], s73 offen lds ;@Q2\n"
"v_mfma_f32_16x16x32_bf16 a[76:79], a[224:227], a[204:207], a[76:79]\n"
"s_add_u32 m0, s82, 0x1800\n"
"ds_read_b64_tr_b16 a[162:163], v20 offset:1152\n"
"v_mfma_f32_16x16x32_bf16 a[80:83], a[228:231], a[192:195], a[80:83]\n"
"ds_read_b64_tr_b16 a[164:165], v21 offset:128\n"
"v_add_u32 v32, 0xe400, v32\n"
"v_mfma_f32_16x16x32_bf16 a[84:87], a[228:231], a[196:199], a[84:87]\n"
"buffer_load_dwordx4 v36, s[48:51], s74 offen lds ;@Q2\n"
"v_mfma_f32_16x16x32_bf16 a[88:91], a[228:231], a[200:203], a[88:91]\n"
"ds_read_b64_tr_b16 a[166:167], v21 offset:1152\n"
"ds_read_b64_tr_b16 a[168:169], v22 offset:128\n"
"v_mfma_f32_16x16x32_bf16 a[92:95], a[228:231], a[204:207], a[92:95]\n"
"s_add_u32 m0, s82, 0x1c00\n"
"v_add_u32 v33, 0xe400, v33\n"
"v_mfma_f32_16x16x32_bf16 a[96:99], a[232:235], a[192:195], a[96:99]\n"
"buffer_load_dwordx4 v37, s[48:51], s75 offen lds ;@Q2\n"
"v_mfma_f32_16x16x32_bf16 a[100:103], a[232:235], a[196:199], a[100:103]\n"
"ds_read_b64_tr_b16 a[170:171], v22 offset:1152\n"
"ds_read_b64_tr_b16 a[172:173], v23 offset:128\n"
"v_mfma_f32_16x16x32_bf16 a[104:107], a[232:235], a[200:203], a[104:107]\n"
"s_add_u32 m0, s83, 0x8000\n"
"ds_read_b64_tr_b16 a[174:175], v23 offset:1152\n"
"v_mfma_f32_16x16x32_bf16 a[108:111], a[232:235], a[204:207], a[108:111]\n"
"buffer_load_dwordx4 v38, s[52:55], s76 offen lds ;@Q2\n"
"v_mfma_f32_16x16x32_bf16 a[112:115], a[236:239], a[192:195], a[112:115]\n"
"ds_read_b64_tr_b16 a[176:177], v28 offset:0\n"
"v_add_u32 v34, 0xe400, v34\n"
"v_mfma_f32_16x16x32_bf16 a[116:119], a[236:239], a[196:199], a[116:119]\n"
"s_add_u32 m0, s83, 0x8400\n"
"ds_read_b64_tr_b16 a[178:179], v28 offset:512\n"
"v_mfma_f32_16x16x32_bf16 a[120:123], a[236:239], a[200:203], a[120:123]\n"
"buffer_load_dwordx4 v38, s[52:55], s77 offen lds ;@Q2\n"
"v_mfma_f32_16x16x32_bf16 a[124:127], a[236:239], a[204:207], a[124:127]\n"
"ds_read_b64_tr_b16 a[180:181], v29 offset:0\n"
"v_add_u32 v35, 0xe400, v35\n"
"v_mfma_f32_16x16x32_bf16 v[64:67], a[240:243], a[192:195], v[64:67]\n"
"s_add_u32 m0, s83, 0x8880\n"
"ds_read_b64_tr_b16 a[182:183], v29 offset:512\n"
"v_mfma_f32_16x16x32_bf16 v[68:71], a[240:243], a[196:199], v[68:71]\n"
"ds_read_b64_tr_b16 a[184:185], v30 offset:0\n"
"v_mfma_f32_16x16x32_bf16 v[72:75], a[240:243], a[200:203], v[72:75]\n"
"buffer_load_dwordx4 v38, s[52:55], s78 offen lds ;@Q2\n"
"v_mfma_f32_16x16x32_bf16 v[76:79], a[240:243], a[204:207], v[76:79]\n"
"s_add_u32 m0, s83, 0x8c80\n"
"ds_read_b64_tr_b16 a[186:187], v30 offset:512\n"
"v_mfma_f32_16x16x32_bf16 v[80:83], a[244:247], a[192:195], v[80:83]\n"
"ds_read_b64_tr_b16 a[188:189], v31 offset:0\n"
"v_mfma_f32_16x16x32_bf16 v[84:87], a[244:247], a[196:199], v[84:87]\n"
"buffer_load_dwordx4 v38, s[52:55], s79 offen lds ;@Q2\n"
"v_mfma_f32_16x16x32_bf16 v[88:91], a[244:247], a[200:203], v[88:91]\n"
"s_add_u32 m0, s84, 0xc400\n"
"ds_read_b64_tr_b16 a[190:191], v31 offset:512\n"
"v_mfma_f32_16x16x32_bf16 v[92:95], a[244:247], a[204:207], v[92:95]\n"
"v_mfma_f32_16x16x32_bf16 v[96:99], a[248:251], a[192:195], v[96:99]\n"
"buffer_load_dwordx4 v39, s[56:59], s80 offen lds ;@Q2\n"
"v_mfma_f32_16x16x32_bf16 v[100:103], a[248:251], a[196:199], v[100:103]\n"
"s_add_u32 m0, s84, 0xc800\n"
"v_mfma_f32_16x16x32_bf16 v[104:107], a[248:251], a[200:203], v[104:107]\n"
"v_mfma_f32_16x16x32_bf16 v[108:111], a[248:251], a[204:207], v[108:111]\n"
"buffer_load_dwordx4 v40, s[56:59], s81 offen lds ;@Q2\n"
"v_mfma_f32_16x16x32_bf16 v[112:115], a[252:255], a[192:195], v[112:115]\n"
"v_add_u32 v36, 0x80, v36\n"
"v_add_u32 v37, 0x80, v37\n"
"v_mfma_f32_16x16x32_bf16 v[116:119], a[252:255], a[196:199], v[116:119]\n"
"v_add_u32 v38, 0x80000, v38\n"
"v_add_u32 v39, s91, v39\n"
"v_mfma_f32_16x16x32_bf16 v[120:123], a[252:255], a[200:203], v[120:123]\n"
"v_add_u32 v40, s91, v40\n"
"v_mfma_f32_16x16x32_bf16 v[124:127], a[252:255], a[204:207], v[124:127]\n"
"s_waitcnt lgkmcnt(0)\n"
"v_mfma_f32_16x16x32_bf16 a[0:3], a[144:147], a[128:131], a[0:3]\n"
"ds_read_b64_tr_b16 a[208:209], v24 offset:0\n"
"v_add_u32 v18, 0xffff1c00, v18\n"
"v_mfma_f32_16x16x32_bf16 a[4:7], a[144:147], a[132:135], a[4:7]\n"
"ds_read_b64_tr_b16 a[210:211], v24 offset:1024\n"
"v_add_u32 v20, 0xffff1c00, v20\n"
"v_mfma_f32_16x16x32_bf16 a[8:11], a[144:147], a[136:139], a[8:11]\n"
"s_add_u32 s47, s86, 32768\n"
"ds_read_b128 a[192:195], v19 offset:0\n"
"v_mfma_f32_16x16x32_bf16 a[12:15], a[144:147], a[140:143], a[12:15]\n"
"v_add_u32 v21, 0xffff1c00, v21\n"
"v_add_u32 v22, 0xffff1c00, v22\n"
"v_mfma_f32_16x16x32_bf16 a[16:19], a[148:151], a[128:131], a[16:19]\n"
"ds_read_b128 a[196:199], v19 offset:2048\n"
"v_add_u32 v23, 0xffff1c00, v23\n"
"v_mfma_f32_16x16x32_bf16 a[20:23], a[148:151], a[132:135], a[20:23]\n"
"ds_read_b128 a[200:203], v19 offset:4096\n"
"v_add_u32 v28, 0xffff1c00, v28\n"
"v_mfma_f32_16x16x32_bf16 a[24:27], a[148:151], a[136:139], a[24:27]\n"
"ds_read_b128 a[204:207], v19 offset:6144\n"
"v_add_u32 v29, 0xffff1c00, v29\n"
"v_mfma_f32_16x16x32_bf16 a[28:31], a[148:151], a[140:143], a[28:31]\n"
"buffer_store_dwordx4 v[144:147], v17, s[60:63], s47 offen sc1\n"
"v_mfma_f32_16x16x32_bf16 a[32:35], a[152:155], a[128:131], a[32:35]\n"
"ds_read_b64_tr_b16 a[212:213], v25 offset:0\n"
"v_add_u32 v30, 0xffff1c00, v30\n"
"v_mfma_f32_16x16x32_bf16 a[36:39], a[152:155], a[132:135], a[36:39]\n"
"ds_read_b64_tr_b16 a[214:215], v25 offset:1024\n"
"v_add_u32 v31, 0xffff1c00, v31\n"
"v_mfma_f32_16x16x32_bf16 a[40:43], a[152:155], a[136:139], a[40:43]\n"
"v_mfma_f32_16x16x32_bf16 a[44:47], a[152:155], a[140:143], a[44:47]\n"
"ds_read_b64_tr_b16 a[216:217], v26 offset:0\n"
"v_mfma_f32_16x16x32_bf16 a[48:51], a[156:159], a[128:131], a[48:51]\n"
"s_add_u32 s47, s86, 32768\n"
"ds_read_b64_tr_b16 a[218:219], v26 offset:1024\n"
"v_mfma_f32_16x16x32_bf16 a[52:55], a[156:159], a[132:135], a[52:55]\n"
"ds_read_b64_tr_b16 a[220:221], v27 offset:0\n"
"v_mfma_f32_16x16x32_bf16 a[56:59], a[156:159], a[136:139], a[56:59]\n"
"v_mfma_f32_16x16x32_bf16 a[60:63], a[156:159], a[140:143], a[60:63]\n"
"ds_read_b64_tr_b16 a[222:223], v27 offset:1024\n"
"v_mfma_f32_16x16x32_bf16 a[64:67], a[160:163], a[128:131], a[64:67]\n"
"ds_read_b64_tr_b16 a[224:225], v24 offset:128\n"
"v_mfma_f32_16x16x32_bf16 a[68:71], a[160:163], a[132:135], a[68:71]\n"
"v_mfma_f32_16x16x32_bf16 a[72:75], a[160:163], a[136:139], a[72:75]\n"
"buffer_store_dwordx4 v[148:151], v41, s[60:63], s47 offen sc1\n"
"v_mfma_f32_16x16x32_bf16 a[76:79], a[160:163], a[140:143], a[76:79]\n"
"ds_read_b64_tr_b16 a[226:227], v24 offset:1152\n"
"ds_read_b64_tr_b16 a[228:229], v25 offset:128\n"
"v_mfma_f32_16x16x32_bf16 a[80:83], a[164:167], a[128:131], a[80:83]\n"
"ds_read_b64_tr_b16 a[230:231], v25 offset:1152\n"
"v_mfma_f32_16x16x32_bf16 a[84:87], a[164:167], a[132:135], a[84:87]\n"
"v_mfma_f32_16x16x32_bf16 a[88:91], a[164:167], a[136:139], a[88:91]\n"
"ds_read_b64_tr_b16 a[232:233], v26 offset:128\n"
"v_mfma_f32_16x16x32_bf16 a[92:95], a[164:167], a[140:143], a[92:95]\n"
"s_add_u32 s47, s86, 49152\n"
"ds_read_b64_tr_b16 a[234:235], v26 offset:1152\n"
"v_mfma_f32_16x16x32_bf16 a[96:99], a[168:171], a[128:131], a[96:99]\n"
"v_mfma_f32_16x16x32_bf16 a[100:103], a[168:171], a[132:135], a[100:103]\n"
"ds_read_b64_tr_b16 a[236:237], v27 offset:128\n"
"v_mfma_f32_16x16x32_bf16 a[104:107], a[168:171], a[136:139], a[104:107]\n"
"ds_read_b64_tr_b16 a[238:239], v27 offset:1152\n"
"v_mfma_f32_16x16x32_bf16 a[108:111], a[168:171], a[140:143], a[108:111]\n"
"ds_read_b64_tr_b16 a[240:241], v32 offset:0\n"
"v_mfma_f32_16x16x32_bf16 a[112:115], a[172:175], a[128:131], a[112:115]\n"
"buffer_store_dwordx4 v[152:155], v17, s[60:63], s47 offen sc1\n"
"v_mfma_f32_16x16x32_bf16 a[116:119], a[172:175], a[132:135], a[116:119]\n"
"ds_read_b64_tr_b16 a[242:243], v32 offset:512\n"
"v_mfma_f32_16x16x32_bf16 a[120:123], a[172:175], a[136:139], a[120:123]\n"
"ds_read_b64_tr_b16 a[244:245], v33 offset:0\n"
"v_mfma_f32_16x16x32_bf16 a[124:127], a[172:175], a[140:143], a[124:127]\n"
"v_mfma_f32_16x16x32_bf16 v[64:67], a[176:179], a[128:131], v[64:67]\n"
"ds_read_b64_tr_b16 a[246:247], v33 offset:512\n"
"v_mfma_f32_16x16x32_bf16 v[68:71], a[176:179], a[132:135], v[68:71]\n"
"ds_read_b64_tr_b16 a[248:249], v34 offset:0\n"
"v_mfma_f32_16x16x32_bf16 v[72:75], a[176:179], a[136:139], v[72:75]\n"
"s_add_u32 s47, s86, 49152\n"
"ds_read_b64_tr_b16 a[250:251], v34 offset:512\n"
"v_mfma_f32_16x16x32_bf16 v[76:79], a[176:179], a[140:143], v[76:79]\n"
"v_mfma_f32_16x16x32_bf16 v[80:83], a[180:183], a[128:131], v[80:83]\n"
"ds_read_b64_tr_b16 a[252:253], v35 offset:0\n"
"v_mfma_f32_16x16x32_bf16 v[84:87], a[180:183], a[132:135], v[84:87]\n"
"ds_read_b64_tr_b16 a[254:255], v35 offset:512\n"
"v_mfma_f32_16x16x32_bf16 v[88:91], a[180:183], a[136:139], v[88:91]\n"
"v_mfma_f32_16x16x32_bf16 v[92:95], a[180:183], a[140:143], v[92:95]\n"
"buffer_store_dwordx4 v[156:159], v41, s[60:63], s47 offen sc1\n"
"v_mfma_f32_16x16x32_bf16 v[96:99], a[184:187], a[128:131], v[96:99]\n"
"v_mfma_f32_16x16x32_bf16 v[100:103], a[184:187], a[132:135], v[100:103]\n"
"v_mfma_f32_16x16x32_bf16 v[104:107], a[184:187], a[136:139], v[104:107]\n"
"v_mfma_f32_16x16x32_bf16 v[108:111], a[184:187], a[140:143], v[108:111]\n"
"v_mfma_f32_16x16x32_bf16 v[112:115], a[188:191], a[128:131], v[112:115]\n"
"v_mfma_f32_16x16x32_bf16 v[116:119], a[188:191], a[132:135], v[116:119]\n"
"v_mfma_f32_16x16x32_bf16 v[120:123], a[188:191], a[136:139], v[120:123]\n"
"v_mfma_f32_16x16x32_bf16 v[124:127], a[188:191], a[140:143], v[124:127]\n"
"s_waitcnt lgkmcnt(0)\n"
"v_mfma_f32_16x16x32_bf16 a[0:3], a[208:211], a[192:195], a[0:3]\n"
"s_waitcnt vmcnt(4)\n"
"s_barrier\n"
"v_mfma_f32_16x16x32_bf16 a[4:7], a[208:211], a[196:199], a[4:7]\n"
"s_add_u32 m0, s82, 0xe400\n"
"ds_read_b64_tr_b16 a[144:145], v20 offset:0\n"
"v_mfma_f32_16x16x32_bf16 a[8:11], a[208:211], a[200:203], a[8:11]\n"
"buffer_load_dwordx4 v36, s[48:51], s68 offen lds ;@Q3\n"
"v_mfma_f32_16x16x32_bf16 a[12:15], a[208:211], a[204:207], a[12:15]\n"
"ds_read_b64_tr_b16 a[146:147], v20 offset:1024\n"
"ds_read_b128 a[128:131], v18 offset:0\n"
"v_mfma_f32_16x16x32_bf16 a[16:19], a[212:215], a[192:195], a[16:19]\n"
"s_add_u32 m0, s82, 0xe800\n"
"v_add_u32 v19, 0xffff1c00, v19\n"
"v_mfma_f32_16x16x32_bf16 a[20:23], a[212:215], a[196:199], a[20:23]\n"
"buffer_load_dwordx4 v37, s[48:51], s69 offen lds ;@Q3\n"
"v_mfma_f32_16x16x32_bf16 a[24:27], a[212:215], a[200:203], a[24:27]\n"
"ds_read_b128 a[132:135], v18 offset:2048\n"
"ds_read_b128 a[136:139], v18 offset:4096\n"
"v_mfma_f32_16x16x32_bf16 a[28:31], a[212:215], a[204:207], a[28:31]\n"
"s_add_u32 m0, s82, 0xec00\n"
"ds_read_b128 a[140:143], v18 offset:6144\n"
"v_mfma_f32_16x16x32_bf16 a[32:35], a[216:219], a[192:195], a[32:35]\n"
"buffer_load_dwordx4 v36, s[48:51], s70 offen lds ;@Q3\n"
"v_mfma_f32_16x16x32_bf16 a[36:39], a[216:219], a[196:199], a[36:39]\n"
"ds_read_b64_tr_b16 a[148:149], v21 offset:0\n"
"v_add_u32 v24, 0xffff1c00, v24\n"
"v_mfma_f32_16x16x32_bf16 a[40:43], a[216:219], a[200:203], a[40:43]\n"
"s_add_u32 m0, s82, 0xf000\n"
"ds_read_b64_tr_b16 a[150:151], v21 offset:1024\n"
"v_mfma_f32_16x16x32_bf16 a[44:47], a[216:219], a[204:207], a[44:47]\n"
"ds_read_b64_tr_b16 a[152:153], v22 offset:0\n"
"v_add_u32 v25, 0xffff1c00, v25\n"
"v_mfma_f32_16x16x32_bf16 a[48:51], a[220:223], a[192:195], a[48:51]\n"
"buffer_load_dwordx4 v37, s[48:51], s71 offen lds ;@Q3\n"
"v_mfma_f32_16x16x32_bf16 a[52:55], a[220:223], a[196:199], a[52:55]\n"
"s_add_u32 m0, s82, 0xf400\n"
"ds_read_b64_tr_b16 a[154:155], v22 offset:1024\n"
"v_mfma_f32_16x16x32_bf16 a[56:59], a[220:223], a[200:203], a[56:59]\n"
"ds_read_b64_tr_b16 a[156:157], v23 offset:0\n"
"v_add_u32 v26, 0xffff1c00, v26\n"
"v_mfma_f32_16x16x32_bf16 a[60:63], a[220:223], a[204:207], a[60:63]\n"
"buffer_load_dwordx4 v36, s[48:51], s72 offen lds ;@Q3\n"
"v_mfma_f32_16x16x32_bf16 a[64:67], a[224:227], a[192:195], a[64:67]\n"
"s_add_u32 m0, s82, 0xf800\n"
"ds_read_b64_tr_b16 a[158:159], v23 offset:1024\n"
"v_mfma_f32_16x16x32_bf16 a[68:71], a[224:227], a[196:199], a[68:71]\n"
"ds_read_b64_tr_b16 a[160:161], v20 offset:128\n"
"v_add_u32 v27, 0xffff1c00, v27\n"
"v_mfma_f32_16x16x32_bf16 a[72:75], a[224:227], a[200:203], a[72:75]\n"
"buffer_load_dwordx4 v37, s[48:51], s73 offen lds ;@Q3\n"
"v_mfma_f32_16x16x32_bf16 a[76:79], a[224:227], a[204:207], a[76:79]\n"
"s_add_u32 m0, s82, 0xfc00\n"
"ds_read_b64_tr_b16 a[162:163], v20 offset:1152\n"
"v_mfma_f32_16x16x32_bf16 a[80:83], a[228:231], a[192:195], a[80:83]\n"
"ds_read_b64_tr_b16 a[164:165], v21 offset:128\n"
"v_add_u32 v32, 0xffff1c00, v32\n"
"v_mfma_f32_16x16x32_bf16 a[84:87], a[228:231], a[196:199], a[84:87]\n"
"buffer_load_dwordx4 v36, s[48:51], s74 offen lds ;@Q3\n"
"v_mfma_f32_16x16x32_bf16 a[88:91], a[228:231], a[200:203], a[88:91]\n"
"ds_read_b64_tr_b16 a[166:167], v21 offset:1152\n"
"ds_read_b64_tr_b16 a[168:169], v22 offset:128\n"
"v_mfma_f32_16x16x32_bf16 a[92:95], a[228:231], a[204:207], a[92:95]\n"
"s_add_u32 m0, s82, 0x10000\n"
"v_add_u32 v33, 0xffff1c00, v33\n"
"v_mfma_f32_16x16x32_bf16 a[96:99], a[232:235], a[192:195], a[96:99]\n"
"buffer_load_dwordx4 v37, s[48:51], s75 offen lds ;@Q3\n"
"v_mfma_f32_16x16x32_bf16 a[100:103], a[232:235], a[196:199], a[100:103]\n"
"ds_read_b64_tr_b16 a[170:171], v22 offset:1152\n"
"ds_read_b64_tr_b16 a[172:173], v23 offset:128\n"
"v_mfma_f32_16x16x32_bf16 a[104:107], a[232:235], a[200:203], a[104:107]\n"
"s_add_u32 m0, s83, 0x16400\n"
"ds_read_b64_tr_b16 a[174:175], v23 offset:1152\n"
"v_mfma_f32_16x16x32_bf16 a[108:111], a[232:235], a[204:207], a[108:111]\n"
"buffer_load_dwordx4 v38, s[52:55], s76 offen lds ;@Q3\n"
"v_mfma_f32_16x16x32_bf16 a[112:115], a[236:239], a[192:195], a[112:115]\n"
"ds_read_b64_tr_b16 a[176:177], v28 offset:0\n"
"v_add_u32 v34, 0xffff1c00, v34\n"
"v_mfma_f32_16x16x32_bf16 a[116:119], a[236:239], a[196:199], a[116:119]\n"
"s_add_u32 m0, s83, 0x16800\n"
"ds_read_b64_tr_b16 a[178:179], v28 offset:512\n"
"v_mfma_f32_16x16x32_bf16 a[120:123], a[236:239], a[200:203], a[120:123]\n"
"buffer_load_dwordx4 v38, s[52:55], s77 offen lds ;@Q3\n"
"v_mfma_f32_16x16x32_bf16 a[124:127], a[236:239], a[204:207], a[124:127]\n"
"ds_read_b64_tr_b16 a[180:181], v29 offset:0\n"
"v_add_u32 v35, 0xffff1c00, v35\n"
"v_mfma_f32_16x16x32_bf16 v[64:67], a[240:243], a[192:195], v[64:67]\n"
"s_add_u32 m0, s83, 0x16c80\n"
"ds_read_b64_tr_b16 a[182:183], v29 offset:512\n"
"v_mfma_f32_16x16x32_bf16 v[68:71], a[240:243], a[196:199], v[68:71]\n"
"ds_read_b64_tr_b16 a[184:185], v30 offset:0\n"
"v_mfma_f32_16x16x32_bf16 v[72:75], a[240:243], a[200:203], v[72:75]\n"
"buffer_load_dwordx4 v38, s[52:55], s78 offen lds ;@Q3\n"
"v_mfma_f32_16x16x32_bf16 v[76:79], a[240:243], a[204:207], v[76:79]\n"
"s_add_u32 m0, s83, 0x17080\n"
"ds_read_b64_tr_b16 a[186:187], v30 offset:512\n"
"v_mfma_f32_16x16x32_bf16 v[80:83], a[244:247], a[192:195], v[80:83]\n"
"ds_read_b64_tr_b16 a[188:189], v31 offset:0\n"
"v_mfma_f32_16x16x32_bf16 v[84:87], a[244:247], a[196:199], v[84:87]\n"
"buffer_load_dwordx4 v38, s[52:55], s79 offen lds ;@Q3\n"
"v_mfma_f32_16x16x32_bf16 v[88:91], a[244:247], a[200:203], v[88:91]\n"
"s_add_u32 m0, s84, 0x1a800\n"
"ds_read_b64_tr_b16 a[190:191], v31 offset:512\n"
"v_mfma_f32_16x16x32_bf16 v[92:95], a[244:247], a[204:207], v[92:95]\n"
"v_mfma_f32_16x16x32_bf16 v[96:99], a[248:251], a[192:195], v[96:99]\n"
"buffer_load_dwordx4 v39, s[56:59], s80 offen lds ;@Q3\n"
"v_mfma_f32_16x16x32_bf16 v[100:103], a[248:251], a[196:199], v[100:103]\n"
"s_add_u32 m0, s84, 0x1ac00\n"
"v_mfma_f32_16x16x32_bf16 v[104:107], a[248:251], a[200:203], v[104:107]\n"
"v_mfma_f32_16x16x32_bf16 v[108:111], a[248:251], a[204:207], v[108:111]\n"
"buffer_load_dwordx4 v40, s[56:59], s81 offen lds ;@Q3\n"
"v_mfma_f32_16x16x32_bf16 v[112:115], a[252:255], a[192:195], v[112:115]\n"
"v_add_u32 v36, 0x80, v36\n"
"v_add_u32 v37, 0x80, v37\n"
"v_mfma_f32_16x16x32_bf16 v[116:119], a[252:255], a[196:199], v[116:119]\n"
"v_add_u32 v38, 0x80000, v38\n"
"v_add_u32 v39, s91, v39\n"
"v_mfma_f32_16x16x32_bf16 v[120:123], a[252:255], a[200:203], v[120:123]\n"
"v_add_u32 v40, s91, v40\n"
"v_mfma_f32_16x16x32_bf16 v[124:127], a[252:255], a[204:207], v[124:127]\n"
"s_waitcnt lgkmcnt(0)\n"
"v_mfma_f32_16x16x32_bf16 a[0:3], a[144:147], a[128:131], a[0:3]\n"
"ds_read_b64_tr_b16 a[208:209], v24 offset:0\n"
"v_add_u32 v18, 0xe400, v18\n"
"v_mfma_f32_16x16x32_bf16 a[4:7], a[144:147], a[132:135], a[4:7]\n"
"ds_read_b64_tr_b16 a[210:211], v24 offset:1024\n"
"v_add_u32 v20, 0xe400, v20\n"
"v_mfma_f32_16x16x32_bf16 a[8:11], a[144:147], a[136:139], a[8:11]\n"
"s_add_u32 s47, s87, 0\n"
"ds_read_b128 a[192:195], v19 offset:0\n"
"v_mfma_f32_16x16x32_bf16 a[12:15], a[144:147], a[140:143], a[12:15]\n"
"v_add_u32 v21, 0xe400, v21\n"
"v_add_u32 v22, 0xe400, v22\n"
"v_mfma_f32_16x16x32_bf16 a[16:19], a[148:151], a[128:131], a[16:19]\n"
"ds_read_b128 a[196:199], v19 offset:2048\n"
"v_add_u32 v23, 0xe400, v23\n"
"v_mfma_f32_16x16x32_bf16 a[20:23], a[148:151], a[132:135], a[20:23]\n"
"ds_read_b128 a[200:203], v19 offset:4096\n"
"v_add_u32 v28, 0xe400, v28\n"
"v_mfma_f32_16x16x32_bf16 a[24:27], a[148:151], a[136:139], a[24:27]\n"
"ds_read_b128 a[204:207], v19 offset:6144\n"
"v_add_u32 v29, 0xe400, v29\n"
"v_mfma_f32_16x16x32_bf16 a[28:31], a[148:151], a[140:143], a[28:31]\n"
"buffer_store_dwordx4 v[160:163], v17, s[60:63], s47 offen sc1\n"
"v_mfma_f32_16x16x32_bf16 a[32:35], a[152:155], a[128:131], a[32:35]\n"
"ds_read_b64_tr_b16 a[212:213], v25 offset:0\n"
"v_add_u32 v30, 0xe400, v30\n"
"v_mfma_f32_16x16x32_bf16 a[36:39], a[152:155], a[132:135], a[36:39]\n"
"ds_read_b64_tr_b16 a[214:215], v25 offset:1024\n"
"v_add_u32 v31, 0xe400, v31\n"
"v_mfma_f32_16x16x32_bf16 a[40:43], a[152:155], a[136:139], a[40:43]\n"
"v_mfma_f32_16x16x32_bf16 a[44:47], a[152:155], a[140:143], a[44:47]\n"
"ds_read_b64_tr_b16 a[216:217], v26 offset:0\n"
"v_mfma_f32_16x16x32_bf16 a[48:51], a[156:159], a[128:131], a[48:51]\n"
"s_add_u32 s47, s87, 0\n"
"ds_read_b64_tr_b16 a[218:219], v26 offset:1024\n"
"v_mfma_f32_16x16x32_bf16 a[52:55], a[156:159], a[132:135], a[52:55]\n"
"ds_read_b64_tr_b16 a[220:221], v27 offset:0\n"
"v_mfma_f32_16x16x32_bf16 a[56:59], a[156:159], a[136:139], a[56:59]\n"
"v_mfma_f32_16x16x32_bf16 a[60:63], a[156:159], a[140:143], a[60:63]\n"
"ds_read_b64_tr_b16 a[222:223], v27 offset:1024\n"
"v_mfma_f32_16x16x32_bf16 a[64:67], a[160:163], a[128:131], a[64:67]\n"
"ds_read_b64_tr_b16 a[224:225], v24 offset:128\n"
"v_mfma_f32_16x16x32_bf16 a[68:71], a[160:163], a[132:135], a[68:71]\n"
"v_mfma_f32_16x16x32_bf16 a[72:75], a[160:163], a[136:139], a[72:75]\n"
"buffer_store_dwordx4 v[164:167], v41, s[60:63], s47 offen sc1\n"
"v_mfma_f32_16x16x32_bf16 a[76:79], a[160:163], a[140:143], a[76:79]\n"
"ds_read_b64_tr_b16 a[226:227], v24 offset:1152\n"
"ds_read_b64_tr_b16 a[228:229], v25 offset:128\n"
"v_mfma_f32_16x16x32_bf16 a[80:83], a[164:167], a[128:131], a[80:83]\n"
"ds_read_b64_tr_b16 a[230:231], v25 offset:1152\n"
"v_mfma_f32_16x16x32_bf16 a[84:87], a[164:167], a[132:135], a[84:87]\n"
"v_mfma_f32_16x16x32_bf16 a[88:91], a[164:167], a[136:139], a[88:91]\n"
"ds_read_b64_tr_b16 a[232:233], v26 offset:128\n"
"v_mfma_f32_16x16x32_bf16 a[92:95], a[164:167], a[140:143], a[92:95]\n"
"s_add_u32 s47, s87, 16384\n"
"ds_read_b64_tr_b16 a[234:235], v26 offset:1152\n"
"v_mfma_f32_16x16x32_bf16 a[96:99], a[168:171], a[128:131], a[96:99]\n"
"v_mfma_f32_16x16x32_bf16 a[100:103], a[168:171], a[132:135], a[100:103]\n"
"ds_read_b64_tr_b16 a[236:237], v27 offset:128\n"
"v_mfma_f32_16x16x32_bf16 a[104:107], a[168:171], a[136:139], a[104:107]\n"
"ds_read_b64_tr_b16 a[238:239], v27 offset:1152\n"
"v_mfma_f32_16x16x32_bf16 a[108:111], a[168:171], a[140:143], a[108:111]\n"
"ds_read_b64_tr_b16 a[240:241], v32 offset:0\n"
"v_mfma_f32_16x16x32_bf16 a[112:115], a[172:175], a[128:131], a[112:115]\n"
"buffer_store_dwordx4 v[168:171], v17, s[60:63], s47 offen sc1\n"
"v_mfma_f32_16x16x32_bf16 a[116:119], a[172:175], a[132:135], a[116:119]\n"
"ds_read_b64_tr_b16 a[242:243], v32 offset:512\n"
"v_mfma_f32_16x16x32_bf16 a[120:123], a[172:175], a[136:139], a[120:123]\n"
"ds_read_b64_tr_b16 a[244:245], v33 offset:0\n"
"v_mfma_f32_16x16x32_bf16 a[124:127], a[172:175], a[140:143], a[124:127]\n"
"v_mfma_f32_16x16x32_bf16 v[64:67], a[176:179], a[128:131], v[64:67]\n"
"ds_read_b64_tr_b16 a[246:247], v33 offset:512\n"
"v_mfma_f32_16x16x32_bf16 v[68:71], a[176:179], a[132:135], v[68:71]\n"
"ds_read_b64_tr_b16 a[248:249], v34 offset:0\n"
"v_mfma_f32_16x16x32_bf16 v[72:75], a[176:179], a[136:139], v[72:75]\n"
"s_add_u32 s47, s87, 16384\n"
"ds_read_b64_tr_b16 a[250:251], v34 offset:512\n"
"v_mfma_f32_16x16x32_bf16 v[76:79], a[176:179], a[140:143], v[76:79]\n"
"v_mfma_f32_16x16x32_bf16 v[80:83], a[180:183], a[128:131], v[80:83]\n"
"ds_read_b64_tr_b16 a[252:253], v35 offset:0\n"
"v_mfma_f32_16x16x32_bf16 v[84:87], a[180:183], a[132:135], v[84:87]\n"
"ds_read_b64_tr_b16 a[254:255], v35 offset:512\n"
"v_mfma_f32_16x16x32_bf16 v[88:91], a[180:183], a[136:139], v[88:91]\n"
"v_mfma_f32_16x16x32_bf16 v[92:95], a[180:183], a[140:143], v[92:95]\n"
"buffer_store_dwordx4 v[172:175], v41, s[60:63], s47 offen sc1\n"
"v_mfma_f32_16x16x32_bf16 v[96:99], a[184:187], a[128:131], v[96:99]\n"
"v_mfma_f32_16x16x32_bf16 v[100:103], a[184:187], a[132:135], v[100:103]\n"
"v_mfma_f32_16x16x32_bf16 v[104:107], a[184:187], a[136:139], v[104:107]\n"
"v_mfma_f32_16x16x32_bf16 v[108:111], a[184:187], a[140:143], v[108:111]\n"
"v_mfma_f32_16x16x32_bf16 v[112:115], a[188:191], a[128:131], v[112:115]\n"
"v_mfma_f32_16x16x32_bf16 v[116:119], a[188:191], a[132:135], v[116:119]\n"
"v_mfma_f32_16x16x32_bf16 v[120:123], a[188:191], a[136:139], v[120:123]\n"
"v_mfma_f32_16x16x32_bf16 v[124:127], a[188:191], a[140:143], v[124:127]\n"
"s_waitcnt lgkmcnt(0)\n"
"v_mfma_f32_16x16x32_bf16 a[0:3], a[208:211], a[192:195], a[0:3]\n"
"s_waitcnt vmcnt(4)\n"
"s_barrier\n"
"v_mfma_f32_16x16x32_bf16 a[4:7], a[208:211], a[196:199], a[4:7]\n"
"s_add_u32 m0, s82, 0x0\n"
"ds_read_b64_tr_b16 a[144:145], v20 offset:0\n"
"v_mfma_f32_16x16x32_bf16 a[8:11], a[208:211], a[200:203], a[8:11]\n"
"buffer_load_dwordx4 v36, s[48:51], s68 offen lds ;@Q4\n"
"v_mfma_f32_16x16x32_bf16 a[12:15], a[208:211], a[204:207], a[12:15]\n"
"ds_read_b64_tr_b16 a[146:147], v20 offset:1024\n"
"ds_read_b128 a[128:131], v18 offset:0\n"
"v_mfma_f32_16x16x32_bf16 a[16:19], a[212:215], a[192:195], a[16:19]\n"
"s_add_u32 m0, s82, 0x400\n"
"v_add_u32 v19, 0xe400, v19\n"
"v_mfma_f32_16x16x32_bf16 a[20:23], a[212:215], a[196:199], a[20:23]\n"
"buffer_load_dwordx4 v37, s[48:51], s69 offen lds ;@Q4\n"
"v_mfma_f32_16x16x32_bf16 a[24:27], a[212:215], a[200:203], a[24:27]\n"
"ds_read_b128 a[132:135], v18 offset:2048\n"
"ds_read_b128 a[136:139], v18 offset:4096\n"
"v_mfma_f32_16x16x32_bf16 a[28:31], a[212:215], a[204:207], a[28:31]\n"
"s_add_u32 m0, s82, 0x800\n"
"ds_read_b128 a[140:143], v18 offset:6144\n"
"v_mfma_f32_16x16x32_bf16 a[32:35], a[216:219], a[192:195], a[32:35]\n"
"buffer_load_dwordx4 v36, s[48:51], s70 offen lds ;@Q4\n"
"v_mfma_f32_16x16x32_bf16 a[36:39], a[216:219], a[196:199], a[36:39]\n"
"ds_read_b64_tr_b16 a[148:149], v21 offset:0\n"
"v_add_u32 v24, 0xe400, v24\n"
"v_mfma_f32_16x16x32_bf16 a[40:43], a[216:219], a[200:203], a[40:43]\n"
"s_add_u32 m0, s82, 0xc00\n"
"ds_read_b64_tr_b16 a[150:151], v21 offset:1024\n"
"v_mfma_f32_16x16x32_bf16 a[44:47], a[216:219], a[204:207], a[44:47]\n"
"ds_read_b64_tr_b16 a[152:153], v22 offset:0\n"
"v_add_u32 v25, 0xe400, v25\n"
"v_mfma_f32_16x16x32_bf16 a[48:51], a[220:223], a[192:195], a[48:51]\n"
"buffer_load_dwordx4 v37, s[48:51], s71 offen lds ;@Q4\n"
"v_mfma_f32_16x16x32_bf16 a[52:55], a[220:223], a[196:199], a[52:55]\n"
"s_add_u32 m0, s82, 0x1000\n"
"ds_read_b64_tr_b16 a[154:155], v22 offset:1024\n"
"v_mfma_f32_16x16x32_bf16 a[56:59], a[220:223], a[200:203], a[56:59]\n"
"ds_read_b64_tr_b16 a[156:157], v23 offset:0\n"
"v_add_u32 v26, 0xe400, v26\n"
"v_mfma_f32_16x16x32_bf16 a[60:63], a[220:223], a[204:207], a[60:63]\n"
"buffer_load_dwordx4 v36, s[48:51], s72 offen lds ;@Q4\n"
"v_mfma_f32_16x16x32_bf16 a[64:67], a[224:227], a[192:195], a[64:67]\n"
"s_add_u32 m0, s82, 0x1400\n"
"ds_read_b64_tr_b16 a[158:159], v23 offset:1024\n"
"v_mfma_f32_16x16x32_bf16 a[68:71], a[224:227], a[196:199], a[68:71]\n"
"ds_read_b64_tr_b16 a[160:161], v20 offset:128\n"
"v_add_u32 v27, 0xe400, v27\n"
"v_mfma_f32_16x16x32_bf16 a[72:75], a[224:227], a[200:203], a[72:75]\n"
"buffer_load_dwordx4 v37, s[48:51], s73 offen lds ;@Q4\n"
"v_mfma_f32_16x16x32_bf16 a[76:79], a[224:227], a[204:207], a[76:79]\n"
"s_add_u32 m0, s82, 0x1800\n"
"ds_read_b64_tr_b16 a[162:163], v20 offset:1152\n"
"v_mfma_f32_16x16x32_bf16 a[80:83], a[228:231], a[192:195], a[80:83]\n"
"ds_read_b64_tr_b16 a[164:165], v21 offset:128\n"
"v_add_u32 v32, 0xe400, v32\n"
"v_mfma_f32_16x16x32_bf16 a[84:87], a[228:231], a[196:199], a[84:87]\n"
"buffer_load_dwordx4 v36, s[48:51], s74 offen lds ;@Q4\n"
"v_mfma_f32_16x16x32_bf16 a[88:91], a[228:231], a[200:203], a[88:91]\n"
"ds_read_b64_tr_b16 a[166:167], v21 offset:1152\n"
"ds_read_b64_tr_b16 a[168:169], v22 offset:128\n"
"v_mfma_f32_16x16x32_bf16 a[92:95], a[228:231], a[204:207], a[92:95]\n"
"s_add_u32 m0, s82, 0x1c00\n"
"v_add_u32 v33, 0xe400, v33\n"
"v_mfma_f32_16x16x32_bf16 a[96:99], a[232:235], a[192:195], a[96:99]\n"
"buffer_load_dwordx4 v37, s[48:51], s75 offen lds ;@Q4\n"
"v_mfma_f32_16x16x32_bf16 a[100:103], a[232:235], a[196:199], a[100:103]\n"
"ds_read_b64_tr_b16 a[170:171], v22 offset:1152\n"
"ds_read_b64_tr_b16 a[172:173], v23 offset:128\n"
"v_mfma_f32_16x16x32_bf16 a[104:107], a[232:235], a[200:203], a[104:107]\n"
"s_add_u32 m0, s83, 0x8000\n"
"ds_read_b64_tr_b16 a[174:175], v23 offset:1152\n"
"v_mfma_f32_16x16x32_bf16 a[108:111], a[232:235], a[204:207], a[108:111]\n"
"buffer_load_dwordx4 v38, s[52:55], s76 offen lds ;@Q4\n"
"v_mfma_f32_16x16x32_bf16 a[112:115], a[236:239], a[192:195], a[112:115]\n"
"ds_read_b64_tr_b16 a[176:177], v28 offset:0\n"
"v_add_u32 v34, 0xe400, v34\n"
"v_mfma_f32_16x16x32_bf16 a[116:119], a[236:239], a[196:199], a[116:119]\n"
"s_add_u32 m0, s83, 0x8400\n"
"ds_read_b64_tr_b16 a[178:179], v28 offset:512\n"
"v_mfma_f32_16x16x32_bf16 a[120:123], a[236:239], a[200:203], a[120:123]\n"
"buffer_load_dwordx4 v38, s[52:55], s77 offen lds ;@Q4\n"
"v_mfma_f32_16x16x32_bf16 a[124:127], a[236:239], a[204:207], a[124:127]\n"
"ds_read_b64_tr_b16 a[180:181], v29 offset:0\n"
"v_add_u32 v35, 0xe400, v35\n"
"v_mfma_f32_16x16x32_bf16 v[64:67], a[240:243], a[192:195], v[64:67]\n"
"s_add_u32 m0, s83, 0x8880\n"
"ds_read_b64_tr_b16 a[182:183], v29 offset:512\n"
"v_mfma_f32_16x16x32_bf16 v[68:71], a[240:243], a[196:199], v[68:71]\n"
"ds_read_b64_tr_b16 a[184:185], v30 offset:0\n"
"v_mfma_f32_16x16x32_bf16 v[72:75], a[240:243], a[200:203], v[72:75]\n"
"buffer_load_dwordx4 v38, s[52:55], s78 offen lds ;@Q4\n"
"v_mfma_f32_16x16x32_bf16 v[76:79], a[240:243], a[204:207], v[76:79]\n"
"s_add_u32 m0, s83, 0x8c80\n"
"ds_read_b64_tr_b16 a[186:187], v30 offset:512\n"
"v_mfma_f32_16x16x32_bf16 v[80:83], a[244:247], a[192:195], v[80:83]\n"
"ds_read_b64_tr_b16 a[188:189], v31 offset:0\n"
"v_mfma_f32_16x16x32_bf16 v[84:87], a[244:247], a[196:199], v[84:87]\n"
"buffer_load_dwordx4 v38, s[52:55], s79 offen lds ;@Q4\n"
"v_mfma_f32_16x16x32_bf16 v[88:91], a[244:247], a[200:203], v[88:91]\n"
"s_add_u32 m0, s84, 0xc400\n"
"ds_read_b64_tr_b16 a[190:191], v31 offset:512\n"
"v_mfma_f32_16x16x32_bf16 v[92:95], a[244:247], a[204:207], v[92:95]\n"
"v_mfma_f32_16x16x32_bf16 v[96:99], a[248:251], a[192:195], v[96:99]\n"
"buffer_load_dwordx4 v39, s[56:59], s80 offen lds ;@Q4\n"
"v_mfma_f32_16x16x32_bf16 v[100:103], a[248:251], a[196:199], v[100:103]\n"
"s_add_u32 m0, s84, 0xc800\n"
"v_mfma_f32_16x16x32_bf16 v[104:107], a[248:251], a[200:203], v[104:107]\n"
"v_mfma_f32_16x16x32_bf16 v[108:111], a[248:251], a[204:207], v[108:111]\n"
"buffer_load_dwordx4 v40, s[56:59], s81 offen lds ;@Q4\n"
"v_mfma_f32_16x16x32_bf16 v[112:115], a[252:255], a[192:195], v[112:115]\n"
"v_add_u32 v36, 0x80, v36\n"
"v_add_u32 v37, 0x80, v37\n"
"v_mfma_f32_16x16x32_bf16 v[116:119], a[252:255], a[196:199], v[116:119]\n"
"v_add_u32 v38, 0x80000, v38\n"
"v_add_u32 v39, s91, v39\n"
"v_mfma_f32_16x16x32_bf16 v[120:123], a[252:255], a[200:203], v[120:123]\n"
"v_add_u32 v40, s91, v40\n"
"v_mfma_f32_16x16x32_bf16 v[124:127], a[252:255], a[204:207], v[124:127]\n"
"s_waitcnt lgkmcnt(0)\n"
"v_mfma_f32_16x16x32_bf16 a[0:3], a[144:147], a[128:131], a[0:3]\n"
"ds_read_b64_tr_b16 a[208:209], v24 offset:0\n"
"v_add_u32 v18, 0xffff1c00, v18\n"
"v_mfma_f32_16x16x32_bf16 a[4:7], a[144:147], a[132:135], a[4:7]\n"
"ds_read_b64_tr_b16 a[210:211], v24 offset:1024\n"
"v_add_u32 v20, 0xffff1c00, v20\n"
"v_mfma_f32_16x16x32_bf16 a[8:11], a[144:147], a[136:139], a[8:11]\n"
"s_add_u32 s47, s87, 32768\n"
"ds_read_b128 a[192:195], v19 offset:0\n"
"v_mfma_f32_16x16x32_bf16 a[12:15], a[144:147], a[140:143], a[12:15]\n"
"v_add_u32 v21, 0xffff1c00, v21\n"
"v_add_u32 v22, 0xffff1c00, v22\n"
"v_mfma_f32_16x16x32_bf16 a[16:19], a[148:151], a[128:131], a[16:19]\n"
"ds_read_b128 a[196:199], v19 offset:2048\n"
"v_add_u32 v23, 0xffff1c00, v23\n"
"v_mfma_f32_16x16x32_bf16 a[20:23], a[148:151], a[132:135], a[20:23]\n"
"ds_read_b128 a[200:203], v19 offset:4096\n"
"v_add_u32 v28, 0xffff1c00, v28\n"
"v_mfma_f32_16x16x32_bf16 a[24:27], a[148:151], a[136:139], a[24:27]\n"
"ds_read_b128 a[204:207], v19 offset:6144\n"
"v_add_u32 v29, 0xffff1c00, v29\n"
"v_mfma_f32_16x16x32_bf16 a[28:31], a[148:151], a[140:143], a[28:31]\n"
"buffer_store_dwordx4 v[176:179], v17, s[60:63], s47 offen sc1\n"
"v_mfma_f32_16x16x32_bf16 a[32:35], a[152:155], a[128:131], a[32:35]\n"
"ds_read_b64_tr_b16 a[212:213], v25 offset:0\n"
"v_add_u32 v30, 0xffff1c00, v30\n"
"v_mfma_f32_16x16x32_bf16 a[36:39], a[152:155], a[132:135], a[36:39]\n"
"ds_read_b64_tr_b16 a[214:215], v25 offset:1024\n"
"v_add_u32 v31, 0xffff1c00, v31\n"
"v_mfma_f32_16x16x32_bf16 a[40:43], a[152:155], a[136:139], a[40:43]\n"
"v_mfma_f32_16x16x32_bf16 a[44:47], a[152:155], a[140:143], a[44:47]\n"
"ds_read_b64_tr_b16 a[216:217], v26 offset:0\n"
"v_mfma_f32_16x16x32_bf16 a[48:51], a[156:159], a[128:131], a[48:51]\n"
"s_add_u32 s47, s87, 32768\n"
"ds_read_b64_tr_b16 a[218:219], v26 offset:1024\n"
"v_mfma_f32_16x16x32_bf16 a[52:55], a[156:159], a[132:135], a[52:55]\n"
"ds_read_b64_tr_b16 a[220:221], v27 offset:0\n"
"v_mfma_f32_16x16x32_bf16 a[56:59], a[156:159], a[136:139], a[56:59]\n"
"v_mfma_f32_16x16x32_bf16 a[60:63], a[156:159], a[140:143], a[60:63]\n"
"ds_read_b64_tr_b16 a[222:223], v27 offset:1024\n"
"v_mfma_f32_16x16x32_bf16 a[64:67], a[160:163], a[128:131], a[64:67]\n"
"ds_read_b64_tr_b16 a[224:225], v24 offset:128\n"
"v_mfma_f32_16x16x32_bf16 a[68:71], a[160:163], a[132:135], a[68:71]\n"
"v_mfma_f32_16x16x32_bf16 a[72:75], a[160:163], a[136:139], a[72:75]\n"
"buffer_store_dwordx4 v[180:183], v41, s[60:63], s47 offen sc1\n"
"v_mfma_f32_16x16x32_bf16 a[76:79], a[160:163], a[140:143], a[76:79]\n"
"ds_read_b64_tr_b16 a[226:227], v24 offset:1152\n"
"ds_read_b64_tr_b16 a[228:229], v25 offset:128\n"
"v_mfma_f32_16x16x32_bf16 a[80:83], a[164:167], a[128:131], a[80:83]\n"
"ds_read_b64_tr_b16 a[230:231], v25 offset:1152\n"
"v_mfma_f32_16x16x32_bf16 a[84:87], a[164:167], a[132:135], a[84:87]\n"
"v_mfma_f32_16x16x32_bf16 a[88:91], a[164:167], a[136:139], a[88:91]\n"
"ds_read_b64_tr_b16 a[232:233], v26 offset:128\n"
"v_mfma_f32_16x16x32_bf16 a[92:95], a[164:167], a[140:143], a[92:95]\n"
"s_add_u32 s47, s87, 49152\n"
"ds_read_b64_tr_b16 a[234:235], v26 offset:1152\n"
"v_mfma_f32_16x16x32_bf16 a[96:99], a[168:171], a[128:131], a[96:99]\n"
"v_mfma_f32_16x16x32_bf16 a[100:103], a[168:171], a[132:135], a[100:103]\n"
"ds_read_b64_tr_b16 a[236:237], v27 offset:128\n"
"v_mfma_f32_16x16x32_bf16 a[104:107], a[168:171], a[136:139], a[104:107]\n"
"ds_read_b64_tr_b16 a[238:239], v27 offset:1152\n"
"v_mfma_f32_16x16x32_bf16 a[108:111], a[168:171], a[140:143], a[108:111]\n"
"ds_read_b64_tr_b16 a[240:241], v32 offset:0\n"
"v_mfma_f32_16x16x32_bf16 a[112:115], a[172:175], a[128:131], a[112:115]\n"
"buffer_store_dwordx4 v[184:187], v17, s[60:63], s47 offen sc1\n"
"v_mfma_f32_16x16x32_bf16 a[116:119], a[172:175], a[132:135], a[116:119]\n"
"ds_read_b64_tr_b16 a[242:243], v32 offset:512\n"
"v_mfma_f32_16x16x32_bf16 a[120:123], a[172:175], a[136:139], a[120:123]\n"
"ds_read_b64_tr_b16 a[244:245], v33 offset:0\n"
"v_mfma_f32_16x16x32_bf16 a[124:127], a[172:175], a[140:143], a[124:127]\n"
"v_mfma_f32_16x16x32_bf16 v[64:67], a[176:179], a[128:131], v[64:67]\n"
"ds_read_b64_tr_b16 a[246:247], v33 offset:512\n"
"v_mfma_f32_16x16x32_bf16 v[68:71], a[176:179], a[132:135], v[68:71]\n"
"ds_read_b64_tr_b16 a[248:249], v34 offset:0\n"
"v_mfma_f32_16x16x32_bf16 v[72:75], a[176:179], a[136:139], v[72:75]\n"
"s_add_u32 s47, s87, 49152\n"
"ds_read_b64_tr_b16 a[250:251], v34 offset:512\n"
"v_mfma_f32_16x16x32_bf16 v[76:79], a[176:179], a[140:143], v[76:79]\n"
"v_mfma_f32_16x16x32_bf16 v[80:83], a[180:183], a[128:131], v[80:83]\n"
"ds_read_b64_tr_b16 a[252:253], v35 offset:0\n"
"v_mfma_f32_16x16x32_bf16 v[84:87], a[180:183], a[132:135], v[84:87]\n"
"ds_read_b64_tr_b16 a[254:255], v35 offset:512\n"
"v_mfma_f32_16x16x32_bf16 v[88:91], a[180:183], a[136:139], v[88:91]\n"
"v_mfma_f32_16x16x32_bf16 v[92:95], a[180:183], a[140:143], v[92:95]\n"
"buffer_store_dwordx4 v[188:191], v41, s[60:63], s47 offen sc1\n"
"v_mfma_f32_16x16x32_bf16 v[96:99], a[184:187], a[128:131], v[96:99]\n"
"v_mfma_f32_16x16x32_bf16 v[100:103], a[184:187], a[132:135], v[100:103]\n"
"v_mfma_f32_16x16x32_bf16 v[104:107], a[184:187], a[136:139], v[104:107]\n"
"v_mfma_f32_16x16x32_bf16 v[108:111], a[184:187], a[140:143], v[108:111]\n"
"v_mfma_f32_16x16x32_bf16 v[112:115], a[188:191], a[128:131], v[112:115]\n"
"v_mfma_f32_16x16x32_bf16 v[116:119], a[188:191], a[132:135], v[116:119]\n"
"v_mfma_f32_16x16x32_bf16 v[120:123], a[188:191], a[136:139], v[120:123]\n"
"v_mfma_f32_16x16x32_bf16 v[124:127], a[188:191], a[140:143], v[124:127]\n"
"s_waitcnt lgkmcnt(0)\n"
"v_mfma_f32_16x16x32_bf16 a[0:3], a[208:211], a[192:195], a[0:3]\n"
"s_waitcnt vmcnt(4)\n"
"s_barrier\n"
"v_mfma_f32_16x16x32_bf16 a[4:7], a[208:211], a[196:199], a[4:7]\n"
"s_add_u32 m0, s82, 0xe400\n"
"ds_read_b64_tr_b16 a[144:145], v20 offset:0\n"
"v_mfma_f32_16x16x32_bf16 a[8:11], a[208:211], a[200:203], a[8:11]\n"
"buffer_load_dwordx4 v36, s[48:51], s68 offen lds ;@Q5\n"
"v_mfma_f32_16x16x32_bf16 a[12:15], a[208:211], a[204:207], a[12:15]\n"
"ds_read_b64_tr_b16 a[146:147], v20 offset:1024\n"
"ds_read_b128 a[128:131], v18 offset:0\n"
"v_mfma_f32_16x16x32_bf16 a[16:19], a[212:215], a[192:195], a[16:19]\n"
"s_add_u32 m0, s82, 0xe800\n"
"v_add_u32 v19, 0xffff1c00, v19\n"
"v_mfma_f32_16x16x32_bf16 a[20:23], a[212:215], a[196:199], a[20:23]\n"
"buffer_load_dwordx4 v37, s[48:51], s69 offen lds ;@Q5\n"
"v_mfma_f32_16x16x32_bf16 a[24:27], a[212:215], a[200:203], a[24:27]\n"
"ds_read_b128 a[132:135], v18 offset:2048\n"
"ds_read_b128 a[136:139], v18 offset:4096\n"
"v_mfma_f32_16x16x32_bf16 a[28:31], a[212:215], a[204:207], a[28:31]\n"
"s_add_u32 m0, s82, 0xec00\n"
"ds_read_b128 a[140:143], v18 offset:6144\n"
"v_mfma_f32_16x16x32_bf16 a[32:35], a[216:219], a[192:195], a[32:35]\n"
"buffer_load_dwordx4 v36, s[48:51], s70 offen lds ;@Q5\n"
"v_mfma_f32_16x16x32_bf16 a[36:39], a[216:219], a[196:199], a[36:39]\n"
"ds_read_b64_tr_b16 a[148:149], v21 offset:0\n"
"v_add_u32 v24, 0xffff1c00, v24\n"
"v_mfma_f32_16x16x32_bf16 a[40:43], a[216:219], a[200:203], a[40:43]\n"
"s_add_u32 m0, s82, 0xf000\n"
"ds_read_b64_tr_b16 a[150:151], v21 offset:1024\n"
"v_mfma_f32_16x16x32_bf16 a[44:47], a[216:219], a[204:207], a[44:47]\n"
"ds_read_b64_tr_b16 a[152:153], v22 offset:0\n"
"v_add_u32 v25, 0xffff1c00, v25\n"
"v_mfma_f32_16x16x32_bf16 a[48:51], a[220:223], a[192:195], a[48:51]\n"
"buffer_load_dwordx4 v37, s[48:51], s71 offen lds ;@Q5\n"
"v_mfma_f32_16x16x32_bf16 a[52:55], a[220:223], a[196:199], a[52:55]\n"
"s_add_u32 m0, s82, 0xf400\n"
"ds_read_b64_tr_b16 a[154:155], v22 offset:1024\n"
"v_mfma_f32_16x16x32_bf16 a[56:59], a[220:223], a[200:203], a[56:59]\n"
"ds_read_b64_tr_b16 a[156:157], v23 offset:0\n"
"v_add_u32 v26, 0xffff1c00, v26\n"
"v_mfma_f32_16x16x32_bf16 a[60:63], a[220:223], a[204:207], a[60:63]\n"
"buffer_load_dwordx4 v36, s[48:51], s72 offen lds ;@Q5\n"
"v_mfma_f32_16x16x32_bf16 a[64:67], a[224:227], a[192:195], a[64:67]\n"
"s_add_u32 m0, s82, 0xf800\n"
"ds_read_b64_tr_b16 a[158:159], v23 offset:1024\n"
"v_mfma_f32_16x16x32_bf16 a[68:71], a[224:227], a[196:199], a[68:71]\n"
"ds_read_b64_tr_b16 a[160:161], v20 offset:128\n"
"v_add_u32 v27, 0xffff1c00, v27\n"
"v_mfma_f32_16x16x32_bf16 a[72:75], a[224:227], a[200:203], a[72:75]\n"
"buffer_load_dwordx4 v37, s[48:51], s73 offen lds ;@Q5\n"
"v_mfma_f32_16x16x32_bf16 a[76:79], a[224:227], a[204:207], a[76:79]\n"
"s_add_u32 m0, s82, 0xfc00\n"
"ds_read_b64_tr_b16 a[162:163], v20 offset:1152\n"
"v_mfma_f32_16x16x32_bf16 a[80:83], a[228:231], a[192:195], a[80:83]\n"
"ds_read_b64_tr_b16 a[164:165], v21 offset:128\n"
"v_add_u32 v32, 0xffff1c00, v32\n"
"v_mfma_f32_16x16x32_bf16 a[84:87], a[228:231], a[196:199], a[84:87]\n"
"buffer_load_dwordx4 v36, s[48:51], s74 offen lds ;@Q5\n"
"v_mfma_f32_16x16x32_bf16 a[88:91], a[228:231], a[200:203], a[88:91]\n"
"ds_read_b64_tr_b16 a[166:167], v21 offset:1152\n"
"ds_read_b64_tr_b16 a[168:169], v22 offset:128\n"
"v_mfma_f32_16x16x32_bf16 a[92:95], a[228:231], a[204:207], a[92:95]\n"
"s_add_u32 m0, s82, 0x10000\n"
"v_add_u32 v33, 0xffff1c00, v33\n"
"v_mfma_f32_16x16x32_bf16 a[96:99], a[232:235], a[192:195], a[96:99]\n"
"buffer_load_dwordx4 v37, s[48:51], s75 offen lds ;@Q5\n"
"v_mfma_f32_16x16x32_bf16 a[100:103], a[232:235], a[196:199], a[100:103]\n"
"ds_read_b64_tr_b16 a[170:171], v22 offset:1152\n"
"ds_read_b64_tr_b16 a[172:173], v23 offset:128\n"
"v_mfma_f32_16x16x32_bf16 a[104:107], a[232:235], a[200:203], a[104:107]\n"
"s_add_u32 m0, s83, 0x16400\n"
"ds_read_b64_tr_b16 a[174:175], v23 offset:1152\n"
"v_mfma_f32_16x16x32_bf16 a[108:111], a[232:235], a[204:207], a[108:111]\n"
"buffer_load_dwordx4 v38, s[52:55], s76 offen lds ;@Q5\n"
"v_mfma_f32_16x16x32_bf16 a[112:115], a[236:239], a[192:195], a[112:115]\n"
"ds_read_b64_tr_b16 a[176:177], v28 offset:0\n"
"v_add_u32 v34, 0xffff1c00, v34\n"
"v_mfma_f32_16x16x32_bf16 a[116:119], a[236:239], a[196:199], a[116:119]\n"
"s_add_u32 m0, s83, 0x16800\n"
"ds_read_b64_tr_b16 a[178:179], v28 offset:512\n"
"v_mfma_f32_16x16x32_bf16 a[120:123], a[236:239], a[200:203], a[120:123]\n"
"buffer_load_dwordx4 v38, s[52:55], s77 offen lds ;@Q5\n"
"v_mfma_f32_16x16x32_bf16 a[124:127], a[236:239], a[204:207], a[124:127]\n"
"ds_read_b64_tr_b16 a[180:181], v29 offset:0\n"
"v_add_u32 v35, 0xffff1c00, v35\n"
"v_mfma_f32_16x16x32_bf16 v[64:67], a[240:243], a[192:195], v[64:67]\n"
"s_add_u32 m0, s83, 0x16c80\n"
"ds_read_b64_tr_b16 a[182:183], v29 offset:512\n"
"v_mfma_f32_16x16x32_bf16 v[68:71], a[240:243], a[196:199], v[68:71]\n"
"ds_read_b64_tr_b16 a[184:185], v30 offset:0\n"
"v_mfma_f32_16x16x32_bf16 v[72:75], a[240:243], a[200:203], v[72:75]\n"
"buffer_load_dwordx4 v38, s[52:55], s78 offen lds ;@Q5\n"
"v_mfma_f32_16x16x32_bf16 v[76:79], a[240:243], a[204:207], v[76:79]\n"
"s_add_u32 m0, s83, 0x17080\n"
"ds_read_b64_tr_b16 a[186:187], v30 offset:512\n"
"v_mfma_f32_16x16x32_bf16 v[80:83], a[244:247], a[192:195], v[80:83]\n"
"ds_read_b64_tr_b16 a[188:189], v31 offset:0\n"
"v_mfma_f32_16x16x32_bf16 v[84:87], a[244:247], a[196:199], v[84:87]\n"
"buffer_load_dwordx4 v38, s[52:55], s79 offen lds ;@Q5\n"
"v_mfma_f32_16x16x32_bf16 v[88:91], a[244:247], a[200:203], v[88:91]\n"
"s_add_u32 m0, s84, 0x1a800\n"
"ds_read_b64_tr_b16 a[190:191], v31 offset:512\n"
"v_mfma_f32_16x16x32_bf16 v[92:95], a[244:247], a[204:207], v[92:95]\n"
"v_mfma_f32_16x16x32_bf16 v[96:99], a[248:251], a[192:195], v[96:99]\n"
"buffer_load_dwordx4 v39, s[56:59], s80 offen lds ;@Q5\n"
"v_mfma_f32_16x16x32_bf16 v[100:103], a[248:251], a[196:199], v[100:103]\n"
"s_add_u32 m0, s84, 0x1ac00\n"
"v_mfma_f32_16x16x32_bf16 v[104:107], a[248:251], a[200:203], v[104:107]\n"
"v_mfma_f32_16x16x32_bf16 v[108:111], a[248:251], a[204:207], v[108:111]\n"
"buffer_load_dwordx4 v40, s[56:59], s81 offen lds ;@Q5\n"
"v_mfma_f32_16x16x32_bf16 v[112:115], a[252:255], a[192:195], v[112:115]\n"
"v_add_u32 v36, 0x80, v36\n"
"v_add_u32 v37, 0x80, v37\n"
"v_mfma_f32_16x16x32_bf16 v[116:119], a[252:255], a[196:199], v[116:119]\n"
"v_add_u32 v38, 0x80000, v38\n"
"v_add_u32 v39, s91, v39\n"
"v_mfma_f32_16x16x32_bf16 v[120:123], a[252:255], a[200:203], v[120:123]\n"
"v_add_u32 v40, s91, v40\n"
"v_mfma_f32_16x16x32_bf16 v[124:127], a[252:255], a[204:207], v[124:127]\n"
"s_waitcnt lgkmcnt(0)\n"
"v_mfma_f32_16x16x32_bf16 a[0:3], a[144:147], a[128:131], a[0:3]\n"
"ds_read_b64_tr_b16 a[208:209], v24 offset:0\n"
"v_add_u32 v18, 0xe400, v18\n"
"v_mfma_f32_16x16x32_bf16 a[4:7], a[144:147], a[132:135], a[4:7]\n"
"ds_read_b64_tr_b16 a[210:211], v24 offset:1024\n"
"v_add_u32 v20, 0xe400, v20\n"
"v_mfma_f32_16x16x32_bf16 a[8:11], a[144:147], a[136:139], a[8:11]\n"
"s_add_u32 s47, s88, 0\n"
"ds_read_b128 a[192:195], v19 offset:0\n"
"v_mfma_f32_16x16x32_bf16 a[12:15], a[144:147], a[140:143], a[12:15]\n"
"v_add_u32 v21, 0xe400, v21\n"
"v_add_u32 v22, 0xe400, v22\n"
"v_mfma_f32_16x16x32_bf16 a[16:19], a[148:151], a[128:131], a[16:19]\n"
"ds_read_b128 a[196:199], v19 offset:2048\n"
"v_add_u32 v23, 0xe400, v23\n"
"v_mfma_f32_16x16x32_bf16 a[20:23], a[148:151], a[132:135], a[20:23]\n"
"ds_read_b128 a[200:203], v19 offset:4096\n"
"v_add_u32 v28, 0xe400, v28\n"
"v_mfma_f32_16x16x32_bf16 a[24:27], a[148:151], a[136:139], a[24:27]\n"
"ds_read_b128 a[204:207], v19 offset:6144\n"
"v_add_u32 v29, 0xe400, v29\n"
"v_mfma_f32_16x16x32_bf16 a[28:31], a[148:151], a[140:143], a[28:31]\n"
"buffer_store_dwordx4 v[192:195], v17, s[60:63], s47 offen sc1\n"
"v_mfma_f32_16x16x32_bf16 a[32:35], a[152:155], a[128:131], a[32:35]\n"
"ds_read_b64_tr_b16 a[212:213], v25 offset:0\n"
"v_add_u32 v30, 0xe400, v30\n"
"v_mfma_f32_16x16x32_bf16 a[36:39], a[152:155], a[132:135], a[36:39]\n"
"ds_read_b64_tr_b16 a[214:215], v25 offset:1024\n"
"v_add_u32 v31, 0xe400, v31\n"
"v_mfma_f32_16x16x32_bf16 a[40:43], a[152:155], a[136:139], a[40:43]\n"
"v_mfma_f32_16x16x32_bf16 a[44:47], a[152:155], a[140:143], a[44:47]\n"
"ds_read_b64_tr_b16 a[216:217], v26 offset:0\n"
"v_mfma_f32_16x16x32_bf16 a[48:51], a[156:159], a[128:131], a[48:51]\n"
"s_add_u32 s47, s88, 0\n"
"ds_read_b64_tr_b16 a[218:219], v26 offset:1024\n"
"v_mfma_f32_16x16x32_bf16 a[52:55], a[156:159], a[132:135], a[52:55]\n"
"ds_read_b64_tr_b16 a[220:221], v27 offset:0\n"
"v_mfma_f32_16x16x32_bf16 a[56:59], a[156:159], a[136:139], a[56:59]\n"
"v_mfma_f32_16x16x32_bf16 a[60:63], a[156:159], a[140:143], a[60:63]\n"
"ds_read_b64_tr_b16 a[222:223], v27 offset:1024\n"
"v_mfma_f32_16x16x32_bf16 a[64:67], a[160:163], a[128:131], a[64:67]\n"
"ds_read_b64_tr_b16 a[224:225], v24 offset:128\n"
"v_mfma_f32_16x16x32_bf16 a[68:71], a[160:163], a[132:135], a[68:71]\n"
"v_mfma_f32_16x16x32_bf16 a[72:75], a[160:163], a[136:139], a[72:75]\n"
"buffer_store_dwordx4 v[196:199], v41, s[60:63], s47 offen sc1\n"
"v_mfma_f32_16x16x32_bf16 a[76:79], a[160:163], a[140:143], a[76:79]\n"
"ds_read_b64_tr_b16 a[226:227], v24 offset:1152\n"
"ds_read_b64_tr_b16 a[228:229], v25 offset:128\n"
"v_mfma_f32_16x16x32_bf16 a[80:83], a[164:167], a[128:131], a[80:83]\n"
"ds_read_b64_tr_b16 a[230:231], v25 offset:1152\n"
"v_mfma_f32_16x16x32_bf16 a[84:87], a[164:167], a[132:135], a[84:87]\n"
"v_mfma_f32_16x16x32_bf16 a[88:91], a[164:167], a[136:139], a[88:91]\n"
"ds_read_b64_tr_b16 a[232:233], v26 offset:128\n"
"v_mfma_f32_16x16x32_bf16 a[92:95], a[164:167], a[140:143], a[92:95]\n"
"s_add_u32 s47, s88, 16384\n"
"ds_read_b64_tr_b16 a[234:235], v26 offset:1152\n"
"v_mfma_f32_16x16x32_bf16 a[96:99], a[168:171], a[128:131], a[96:99]\n"
"v_mfma_f32_16x16x32_bf16 a[100:103], a[168:171], a[132:135], a[100:103]\n"
"ds_read_b64_tr_b16 a[236:237], v27 offset:128\n"
"v_mfma_f32_16x16x32_bf16 a[104:107], a[168:171], a[136:139], a[104:107]\n"
"ds_read_b64_tr_b16 a[238:239], v27 offset:1152\n"
"v_mfma_f32_16x16x32_bf16 a[108:111], a[168:171], a[140:143], a[108:111]\n"
"ds_read_b64_tr_b16 a[240:241], v32 offset:0\n"
"v_mfma_f32_16x16x32_bf16 a[112:115], a[172:175], a[128:131], a[112:115]\n"
"buffer_store_dwordx4 v[200:203], v17, s[60:63], s47 offen sc1\n"
"v_mfma_f32_16x16x32_bf16 a[116:119], a[172:175], a[132:135], a[116:119]\n"
"ds_read_b64_tr_b16 a[242:243], v32 offset:512\n"
"v_mfma_f32_16x16x32_bf16 a[120:123], a[172:175], a[136:139], a[120:123]\n"
"ds_read_b64_tr_b16 a[244:245], v33 offset:0\n"
"v_mfma_f32_16x16x32_bf16 a[124:127], a[172:175], a[140:143], a[124:127]\n"
"v_mfma_f32_16x16x32_bf16 v[64:67], a[176:179], a[128:131], v[64:67]\n"
"ds_read_b64_tr_b16 a[246:247], v33 offset:512\n"
"v_mfma_f32_16x16x32_bf16 v[68:71], a[176:179], a[132:135], v[68:71]\n"
"ds_read_b64_tr_b16 a[248:249], v34 offset:0\n"
"v_mfma_f32_16x16x32_bf16 v[72:75], a[176:179], a[136:139], v[72:75]\n"
"s_add_u32 s47, s88, 16384\n"
"ds_read_b64_tr_b16 a[250:251], v34 offset:512\n"
"v_mfma_f32_16x16x32_bf16 v[76:79], a[176:179], a[140:143], v[76:79]\n"
"v_mfma_f32_16x16x32_bf16 v[80:83], a[180:183], a[128:131], v[80:83]\n"
"ds_read_b64_tr_b16 a[252:253], v35 offset:0\n"
"v_mfma_f32_16x16x32_bf16 v[84:87], a[180:183], a[132:135], v[84:87]\n"
"ds_read_b64_tr_b16 a[254:255], v35 offset:512\n"
"v_mfma_f32_16x16x32_bf16 v[88:91], a[180:183], a[136:139], v[88:91]\n"
"v_mfma_f32_16x16x32_bf16 v[92:95], a[180:183], a[140:143], v[92:95]\n"
"buffer_store_dwordx4 v[204:207], v41, s[60:63], s47 offen sc1\n"
"v_mfma_f32_16x16x32_bf16 v[96:99], a[184:187], a[128:131], v[96:99]\n"
"v_mfma_f32_16x16x32_bf16 v[100:103], a[184:187], a[132:135], v[100:103]\n"
"v_mfma_f32_16x16x32_bf16 v[104:107], a[184:187], a[136:139], v[104:107]\n"
"v_mfma_f32_16x16x32_bf16 v[108:111], a[184:187], a[140:143], v[108:111]\n"
"v_mfma_f32_16x16x32_bf16 v[112:115], a[188:191], a[128:131], v[112:115]\n"
"v_mfma_f32_16x16x32_bf16 v[116:119], a[188:191], a[132:135], v[116:119]\n"
"v_mfma_f32_16x16x32_bf16 v[120:123], a[188:191], a[136:139], v[120:123]\n"
"v_mfma_f32_16x16x32_bf16 v[124:127], a[188:191], a[140:143], v[124:127]\n"
"s_waitcnt lgkmcnt(0)\n"
"v_mfma_f32_16x16x32_bf16 a[0:3], a[208:211], a[192:195], a[0:3]\n"
"s_waitcnt vmcnt(4)\n"
"s_barrier\n"
"v_mfma_f32_16x16x32_bf16 a[4:7], a[208:211], a[196:199], a[4:7]\n"
"s_add_u32 m0, s82, 0x0\n"
"ds_read_b64_tr_b16 a[144:145], v20 offset:0\n"
"v_mfma_f32_16x16x32_bf16 a[8:11], a[208:211], a[200:203], a[8:11]\n"
"buffer_load_dwordx4 v36, s[48:51], s68 offen lds ;@Q6\n"
"v_mfma_f32_16x16x32_bf16 a[12:15], a[208:211], a[204:207], a[12:15]\n"
"ds_read_b64_tr_b16 a[146:147], v20 offset:1024\n"
"ds_read_b128 a[128:131], v18 offset:0\n"
"v_mfma_f32_16x16x32_bf16 a[16:19], a[212:215], a[192:195], a[16:19]\n"
"s_add_u32 m0, s82, 0x400\n"
"v_add_u32 v19, 0xe400, v19\n"
"v_mfma_f32_16x16x32_bf16 a[20:23], a[212:215], a[196:199], a[20:23]\n"
"buffer_load_dwordx4 v37, s[48:51], s69 offen lds ;@Q6\n"
"v_mfma_f32_16x16x32_bf16 a[24:27], a[212:215], a[200:203], a[24:27]\n"
"ds_read_b128 a[132:135], v18 offset:2048\n"
"ds_read_b128 a[136:139], v18 offset:4096\n"
"v_mfma_f32_16x16x32_bf16 a[28:31], a[212:215], a[204:207], a[28:31]\n"
"s_add_u32 m0, s82, 0x800\n"
"ds_read_b128 a[140:143], v18 offset:6144\n"
"v_mfma_f32_16x16x32_bf16 a[32:35], a[216:219], a[192:195], a[32:35]\n"
"buffer_load_dwordx4 v36, s[48:51], s70 offen lds ;@Q6\n"
"v_mfma_f32_16x16x32_bf16 a[36:39], a[216:219], a[196:199], a[36:39]\n"
"ds_read_b64_tr_b16 a[148:149], v21 offset:0\n"
"v_add_u32 v24, 0xe400, v24\n"
"v_mfma_f32_16x16x32_bf16 a[40:43], a[216:219], a[200:203], a[40:43]\n"
"s_add_u32 m0, s82, 0xc00\n"
"ds_read_b64_tr_b16 a[150:151], v21 offset:1024\n"
"v_mfma_f32_16x16x32_bf16 a[44:47], a[216:219], a[204:207], a[44:47]\n"
"ds_read_b64_tr_b16 a[152:153], v22 offset:0\n"
"v_add_u32 v25, 0xe400, v25\n"
"v_mfma_f32_16x16x32_bf16 a[48:51], a[220:223], a[192:195], a[48:51]\n"
"buffer_load_dwordx4 v37, s[48:51], s71 offen lds ;@Q6\n"
"v_mfma_f32_16x16x32_bf16 a[52:55], a[220:223], a[196:199], a[52:55]\n"
"s_add_u32 m0, s82, 0x1000\n"
"ds_read_b64_tr_b16 a[154:155], v22 offset:1024\n"
"v_mfma_f32_16x16x32_bf16 a[56:59], a[220:223], a[200:203], a[56:59]\n"
"ds_read_b64_tr_b16 a[156:157], v23 offset:0\n"
"v_add_u32 v26, 0xe400, v26\n"
"v_mfma_f32_16x16x32_bf16 a[60:63], a[220:223], a[204:207], a[60:63]\n"
"buffer_load_dwordx4 v36, s[48:51], s72 offen lds ;@Q6\n"
"v_mfma_f32_16x16x32_bf16 a[64:67], a[224:227], a[192:195], a[64:67]\n"
"s_add_u32 m0, s82, 0x1400\n"
"ds_read_b64_tr_b16 a[158:159], v23 offset:1024\n"
"v_mfma_f32_16x16x32_bf16 a[68:71], a[224:227], a[196:199], a[68:71]\n"
"ds_read_b64_tr_b16 a[160:161], v20 offset:128\n"
"v_add_u32 v27, 0xe400, v27\n"
"v_mfma_f32_16x16x32_bf16 a[72:75], a[224:227], a[200:203], a[72:75]\n"
"buffer_load_dwordx4 v37, s[48:51], s73 offen lds ;@Q6\n"
"v_mfma_f32_16x16x32_bf16 a[76:79], a[224:227], a[204:207], a[76:79]\n"
"s_add_u32 m0, s82, 0x1800\n"
"ds_read_b64_tr_b16 a[162:163], v20 offset:1152\n"
"v_mfma_f32_16x16x32_bf16 a[80:83], a[228:231], a[192:195], a[80:83]\n"
"ds_read_b64_tr_b16 a[164:165], v21 offset:128\n"
"v_add_u32 v32, 0xe400, v32\n"
"v_mfma_f32_16x16x32_bf16 a[84:87], a[228:231], a[196:199], a[84:87]\n"
"buffer_load_dwordx4 v36, s[48:51], s74 offen lds ;@Q6\n"
"v_mfma_f32_16x16x32_bf16 a[88:91], a[228:231], a[200:203], a[88:91]\n"
"ds_read_b64_tr_b16 a[166:167], v21 offset:1152\n"
"ds_read_b64_tr_b16 a[168:169], v22 offset:128\n"
"v_mfma_f32_16x16x32_bf16 a[92:95], a[228:231], a[204:207], a[92:95]\n"
"s_add_u32 m0, s82, 0x1c00\n"
"v_add_u32 v33, 0xe400, v33\n"
"v_mfma_f32_16x16x32_bf16 a[96:99], a[232:235], a[192:195], a[96:99]\n"
"buffer_load_dwordx4 v37, s[48:51], s75 offen lds ;@Q6\n"
"v_mfma_f32_16x16x32_bf16 a[100:103], a[232:235], a[196:199], a[100:103]\n"
"ds_read_b64_tr_b16 a[170:171], v22 offset:1152\n"
"ds_read_b64_tr_b16 a[172:173], v23 offset:128\n"
"v_mfma_f32_16x16x32_bf16 a[104:107], a[232:235], a[200:203], a[104:107]\n"
"s_add_u32 m0, s83, 0x8000\n"
"ds_read_b64_tr_b16 a[174:175], v23 offset:1152\n"
"v_mfma_f32_16x16x32_bf16 a[108:111], a[232:235], a[204:207], a[108:111]\n"
"buffer_load_dwordx4 v38, s[52:55], s76 offen lds ;@Q6\n"
"v_mfma_f32_16x16x32_bf16 a[112:115], a[236:239], a[192:195], a[112:115]\n"
"ds_read_b64_tr_b16 a[176:177], v28 offset:0\n"
"v_add_u32 v34, 0xe400, v34\n"
"v_mfma_f32_16x16x32_bf16 a[116:119], a[236:239], a[196:199], a[116:119]\n"
"s_add_u32 m0, s83, 0x8400\n"
"ds_read_b64_tr_b16 a[178:179], v28 offset:512\n"
"v_mfma_f32_16x16x32_bf16 a[120:123], a[236:239], a[200:203], a[120:123]\n"
"buffer_load_dwordx4 v38, s[52:55], s77 offen lds ;@Q6\n"
"v_mfma_f32_16x16x32_bf16 a[124:127], a[236:239], a[204:207], a[124:127]\n"
"ds_read_b64_tr_b16 a[180:181], v29 offset:0\n"
"v_add_u32 v35, 0xe400, v35\n"
"v_mfma_f32_16x16x32_bf16 v[64:67], a[240:243], a[192:195], v[64:67]\n"
"s_add_u32 m0, s83, 0x8880\n"
"ds_read_b64_tr_b16 a[182:183], v29 offset:512\n"
"v_mfma_f32_16x16x32_bf16 v[68:71], a[240:243], a[196:199], v[68:71]\n"
"ds_read_b64_tr_b16 a[184:185], v30 offset:0\n"
"v_mfma_f32_16x16x32_bf16 v[72:75], a[240:243], a[200:203], v[72:75]\n"
"buffer_load_dwordx4 v38, s[52:55], s78 offen lds ;@Q6\n"
"v_mfma_f32_16x16x32_bf16 v[76:79], a[240:243], a[204:207], v[76:79]\n"
"s_add_u32 m0, s83, 0x8c80\n"
"ds_read_b64_tr_b16 a[186:187], v30 offset:512\n"
"v_mfma_f32_16x16x32_bf16 v[80:83], a[244:247], a[192:195], v[80:83]\n"
"ds_read_b64_tr_b16 a[188:189], v31 offset:0\n"
"v_mfma_f32_16x16x32_bf16 v[84:87], a[244:247], a[196:199], v[84:87]\n"
"buffer_load_dwordx4 v38, s[52:55], s79 offen lds ;@Q6\n"
"v_mfma_f32_16x16x32_bf16 v[88:91], a[244:247], a[200:203], v[88:91]\n"
"s_add_u32 m0, s84, 0xc400\n"
"ds_read_b64_tr_b16 a[190:191], v31 offset:512\n"
"v_mfma_f32_16x16x32_bf16 v[92:95], a[244:247], a[204:207], v[92:95]\n"
"v_mfma_f32_16x16x32_bf16 v[96:99], a[248:251], a[192:195], v[96:99]\n"
"buffer_load_dwordx4 v39, s[56:59], s80 offen lds ;@Q6\n"
"v_mfma_f32_16x16x32_bf16 v[100:103], a[248:251], a[196:199], v[100:103]\n"
"s_add_u32 m0, s84, 0xc800\n"
"v_mfma_f32_16x16x32_bf16 v[104:107], a[248:251], a[200:203], v[104:107]\n"
"v_mfma_f32_16x16x32_bf16 v[108:111], a[248:251], a[204:207], v[108:111]\n"
"buffer_load_dwordx4 v40, s[56:59], s81 offen lds ;@Q6\n"
"v_mfma_f32_16x16x32_bf16 v[112:115], a[252:255], a[192:195], v[112:115]\n"
"v_add_u32 v36, 0x80, v36\n"
"v_add_u32 v37, 0x80, v37\n"
"v_mfma_f32_16x16x32_bf16 v[116:119], a[252:255], a[196:199], v[116:119]\n"
"v_add_u32 v38, 0x80000, v38\n"
"v_add_u32 v39, s91, v39\n"
"v_mfma_f32_16x16x32_bf16 v[120:123], a[252:255], a[200:203], v[120:123]\n"
"v_add_u32 v40, s91, v40\n"
"v_mfma_f32_16x16x32_bf16 v[124:127], a[252:255], a[204:207], v[124:127]\n"
"s_waitcnt lgkmcnt(0)\n"
"v_mfma_f32_16x16x32_bf16 a[0:3], a[144:147], a[128:131], a[0:3]\n"
"ds_read_b64_tr_b16 a[208:209], v24 offset:0\n"
"v_add_u32 v18, 0xffff1c00, v18\n"
"v_mfma_f32_16x16x32_bf16 a[4:7], a[144:147], a[132:135], a[4:7]\n"
"ds_read_b64_tr_b16 a[210:211], v24 offset:1024\n"
"v_add_u32 v20, 0xffff1c00, v20\n"
"v_mfma_f32_16x16x32_bf16 a[8:11], a[144:147], a[136:139], a[8:11]\n"
"s_add_u32 s47, s88, 32768\n"
"ds_read_b128 a[192:195], v19 offset:0\n"
"v_mfma_f32_16x16x32_bf16 a[12:15], a[144:147], a[140:143], a[12:15]\n"
"v_add_u32 v21, 0xffff1c00, v21\n"
"v_add_u32 v22, 0xffff1c00, v22\n"
"v_mfma_f32_16x16x32_bf16 a[16:19], a[148:151], a[128:131], a[16:19]\n"
"ds_read_b128 a[196:199], v19 offset:2048\n"
"v_add_u32 v23, 0xffff1c00, v23\n"
"v_mfma_f32_16x16x32_bf16 a[20:23], a[148:151], a[132:135], a[20:23]\n"
"ds_read_b128 a[200:203], v19 offset:4096\n"
"v_add_u32 v28, 0xffff1c00, v28\n"
"v_mfma_f32_16x16x32_bf16 a[24:27], a[148:151], a[136:139], a[24:27]\n"
"ds_read_b128 a[204:207], v19 offset:6144\n"
"v_add_u32 v29, 0xffff1c00, v29\n"
"v_mfma_f32_16x16x32_bf16 a[28:31], a[148:151], a[140:143], a[28:31]\n"
"buffer_store_dwordx4 v[208:211], v17, s[60:63], s47 offen sc1\n"
"v_mfma_f32_16x16x32_bf16 a[32:35], a[152:155], a[128:131], a[32:35]\n"
"ds_read_b64_tr_b16 a[212:213], v25 offset:0\n"
"v_add_u32 v30, 0xffff1c00, v30\n"
"v_mfma_f32_16x16x32_bf16 a[36:39], a[152:155], a[132:135], a[36:39]\n"
"ds_read_b64_tr_b16 a[214:215], v25 offset:1024\n"
"v_add_u32 v31, 0xffff1c00, v31\n"
"v_mfma_f32_16x16x32_bf16 a[40:43], a[152:155], a[136:139], a[40:43]\n"
"v_mfma_f32_16x16x32_bf16 a[44:47], a[152:155], a[140:143], a[44:47]\n"
"ds_read_b64_tr_b16 a[216:217], v26 offset:0\n"
"v_mfma_f32_16x16x32_bf16 a[48:51], a[156:159], a[128:131], a[48:51]\n"
"s_add_u32 s47, s88, 32768\n"
"ds_read_b64_tr_b16 a[218:219], v26 offset:1024\n"
"v_mfma_f32_16x16x32_bf16 a[52:55], a[156:159], a[132:135], a[52:55]\n"
"ds_read_b64_tr_b16 a[220:221], v27 offset:0\n"
"v_mfma_f32_16x16x32_bf16 a[56:59], a[156:159], a[136:139], a[56:59]\n"
"v_mfma_f32_16x16x32_bf16 a[60:63], a[156:159], a[140:143], a[60:63]\n"
"ds_read_b64_tr_b16 a[222:223], v27 offset:1024\n"
"v_mfma_f32_16x16x32_bf16 a[64:67], a[160:163], a[128:131], a[64:67]\n"
"ds_read_b64_tr_b16 a[224:225], v24 offset:128\n"
"v_mfma_f32_16x16x32_bf16 a[68:71], a[160:163], a[132:135], a[68:71]\n"
"v_mfma_f32_16x16x32_bf16 a[72:75], a[160:163], a[136:139], a[72:75]\n"
"buffer_store_dwordx4 v[212:215], v41, s[60:63], s47 offen sc1\n"
"v_mfma_f32_16x16x32_bf16 a[76:79], a[160:163], a[140:143], a[76:79]\n"
"ds_read_b64_tr_b16 a[226:227], v24 offset:1152\n"
"ds_read_b64_tr_b16 a[228:229], v25 offset:128\n"
"v_mfma_f32_16x16x32_bf16 a[80:83], a[164:167], a[128:131], a[80:83]\n"
"ds_read_b64_tr_b16 a[230:231], v25 offset:1152\n"
"v_mfma_f32_16x16x32_bf16 a[84:87], a[164:167], a[132:135], a[84:87]\n"
"v_mfma_f32_16x16x32_bf16 a[88:91], a[164:167], a[136:139], a[88:91]\n"
"ds_read_b64_tr_b16 a[232:233], v26 offset:128\n"
"v_mfma_f32_16x16x32_bf16 a[92:95], a[164:167], a[140:143], a[92:95]\n"
"s_add_u32 s47, s88, 49152\n"
"ds_read_b64_tr_b16 a[234:235], v26 offset:1152\n"
"v_mfma_f32_16x16x32_bf16 a[96:99], a[168:171], a[128:131], a[96:99]\n"
"v_mfma_f32_16x16x32_bf16 a[100:103], a[168:171], a[132:135], a[100:103]\n"
"ds_read_b64_tr_b16 a[236:237], v27 offset:128\n"
"v_mfma_f32_16x16x32_bf16 a[104:107], a[168:171], a[136:139], a[104:107]\n"
"ds_read_b64_tr_b16 a[238:239], v27 offset:1152\n"
"v_mfma_f32_16x16x32_bf16 a[108:111], a[168:171], a[140:143], a[108:111]\n"
"ds_read_b64_tr_b16 a[240:241], v32 offset:0\n"
"v_mfma_f32_16x16x32_bf16 a[112:115], a[172:175], a[128:131], a[112:115]\n"
"buffer_store_dwordx4 v[216:219], v17, s[60:63], s47 offen sc1\n"
"v_mfma_f32_16x16x32_bf16 a[116:119], a[172:175], a[132:135], a[116:119]\n"
"ds_read_b64_tr_b16 a[242:243], v32 offset:512\n"
"v_mfma_f32_16x16x32_bf16 a[120:123], a[172:175], a[136:139], a[120:123]\n"
"ds_read_b64_tr_b16 a[244:245], v33 offset:0\n"
"v_mfma_f32_16x16x32_bf16 a[124:127], a[172:175], a[140:143], a[124:127]\n"
"v_mfma_f32_16x16x32_bf16 v[64:67], a[176:179], a[128:131], v[64:67]\n"
"ds_read_b64_tr_b16 a[246:247], v33 offset:512\n"
"v_mfma_f32_16x16x32_bf16 v[68:71], a[176:179], a[132:135], v[68:71]\n"
"ds_read_b64_tr_b16 a[248:249], v34 offset:0\n"
"v_mfma_f32_16x16x32_bf16 v[72:75], a[176:179], a[136:139], v[72:75]\n"
"s_add_u32 s47, s88, 49152\n"
"ds_read_b64_tr_b16 a[250:251], v34 offset:512\n"
"v_mfma_f32_16x16x32_bf16 v[76:79], a[176:179], a[140:143], v[76:79]\n"
"v_mfma_f32_16x16x32_bf16 v[80:83], a[180:183], a[128:131], v[80:83]\n"
"ds_read_b64_tr_b16 a[252:253], v35 offset:0\n"
"v_mfma_f32_16x16x32_bf16 v[84:87], a[180:183], a[132:135], v[84:87]\n"
"ds_read_b64_tr_b16 a[254:255], v35 offset:512\n"
"v_mfma_f32_16x16x32_bf16 v[88:91], a[180:183], a[136:139], v[88:91]\n"
"v_mfma_f32_16x16x32_bf16 v[92:95], a[180:183], a[140:143], v[92:95]\n"
"buffer_store_dwordx4 v[220:223], v41, s[60:63], s47 offen sc1\n"
"v_mfma_f32_16x16x32_bf16 v[96:99], a[184:187], a[128:131], v[96:99]\n"
"v_mfma_f32_16x16x32_bf16 v[100:103], a[184:187], a[132:135], v[100:103]\n"
"v_mfma_f32_16x16x32_bf16 v[104:107], a[184:187], a[136:139], v[104:107]\n"
"v_mfma_f32_16x16x32_bf16 v[108:111], a[184:187], a[140:143], v[108:111]\n"
"v_mfma_f32_16x16x32_bf16 v[112:115], a[188:191], a[128:131], v[112:115]\n"
"v_mfma_f32_16x16x32_bf16 v[116:119], a[188:191], a[132:135], v[116:119]\n"
"v_mfma_f32_16x16x32_bf16 v[120:123], a[188:191], a[136:139], v[120:123]\n"
"v_mfma_f32_16x16x32_bf16 v[124:127], a[188:191], a[140:143], v[124:127]\n"
"s_waitcnt lgkmcnt(0)\n"
"v_mfma_f32_16x16x32_bf16 a[0:3], a[208:211], a[192:195], a[0:3]\n"
"s_waitcnt vmcnt(4)\n"
"s_barrier\n"
"v_mfma_f32_16x16x32_bf16 a[4:7], a[208:211], a[196:199], a[4:7]\n"
"s_add_u32 m0, s82, 0xe400\n"
"ds_read_b64_tr_b16 a[144:145], v20 offset:0\n"
"v_mfma_f32_16x16x32_bf16 a[8:11], a[208:211], a[200:203], a[8:11]\n"
"buffer_load_dwordx4 v36, s[48:51], s68 offen lds ;@Q7\n"
"v_mfma_f32_16x16x32_bf16 a[12:15], a[208:211], a[204:207], a[12:15]\n"
"ds_read_b64_tr_b16 a[146:147], v20 offset:1024\n"
"ds_read_b128 a[128:131], v18 offset:0\n"
"v_mfma_f32_16x16x32_bf16 a[16:19], a[212:215], a[192:195], a[16:19]\n"
"s_add_u32 m0, s82, 0xe800\n"
"v_add_u32 v19, 0xffff1c00, v19\n"
"v_mfma_f32_16x16x32_bf16 a[20:23], a[212:215], a[196:199], a[20:23]\n"
"buffer_load_dwordx4 v37, s[48:51], s69 offen lds ;@Q7\n"
"v_mfma_f32_16x16x32_bf16 a[24:27], a[212:215], a[200:203], a[24:27]\n"
"ds_read_b128 a[132:135], v18 offset:2048\n"
"ds_read_b128 a[136:139], v18 offset:4096\n"
"v_mfma_f32_16x16x32_bf16 a[28:31], a[212:215], a[204:207], a[28:31]\n"
"s_add_u32 m0, s82, 0xec00\n"
"ds_read_b128 a[140:143], v18 offset:6144\n"
"v_mfma_f32_16x16x32_bf16 a[32:35], a[216:219], a[192:195], a[32:35]\n"
"buffer_load_dwordx4 v36, s[48:51], s70 offen lds ;@Q7\n"
"v_mfma_f32_16x16x32_bf16 a[36:39], a[216:219], a[196:199], a[36:39]\n"
"ds_read_b64_tr_b16 a[148:149], v21 offset:0\n"
"v_add_u32 v24, 0xffff1c00, v24\n"
"v_mfma_f32_16x16x32_bf16 a[40:43], a[216:219], a[200:203], a[40:43]\n"
"s_add_u32 m0, s82, 0xf000\n"
"ds_read_b64_tr_b16 a[150:151], v21 offset:1024\n"
"v_mfma_f32_16x16x32_bf16 a[44:47], a[216:219], a[204:207], a[44:47]\n"
"ds_read_b64_tr_b16 a[152:153], v22 offset:0\n"
"v_add_u32 v25, 0xffff1c00, v25\n"
"v_mfma_f32_16x16x32_bf16 a[48:51], a[220:223], a[192:195], a[48:51]\n"
"buffer_load_dwordx4 v37, s[48:51], s71 offen lds ;@Q7\n"
"v_mfma_f32_16x16x32_bf16 a[52:55], a[220:223], a[196:199], a[52:55]\n"
"s_add_u32 m0, s82, 0xf400\n"
"ds_read_b64_tr_b16 a[154:155], v22 offset:1024\n"
"v_mfma_f32_16x16x32_bf16 a[56:59], a[220:223], a[200:203], a[56:59]\n"
"ds_read_b64_tr_b16 a[156:157], v23 offset:0\n"
"v_add_u32 v26, 0xffff1c00, v26\n"
"v_mfma_f32_16x16x32_bf16 a[60:63], a[220:223], a[204:207], a[60:63]\n"
"buffer_load_dwordx4 v36, s[48:51], s72 offen lds ;@Q7\n"
"v_mfma_f32_16x16x32_bf16 a[64:67], a[224:227], a[192:195], a[64:67]\n"
"s_add_u32 m0, s82, 0xf800\n"
"ds_read_b64_tr_b16 a[158:159], v23 offset:1024\n"
"v_mfma_f32_16x16x32_bf16 a[68:71], a[224:227], a[196:199], a[68:71]\n"
"ds_read_b64_tr_b16 a[160:161], v20 offset:128\n"
"v_add_u32 v27, 0xffff1c00, v27\n"
"v_mfma_f32_16x16x32_bf16 a[72:75], a[224:227], a[200:203], a[72:75]\n"
"buffer_load_dwordx4 v37, s[48:51], s73 offen lds ;@Q7\n"
"v_mfma_f32_16x16x32_bf16 a[76:79], a[224:227], a[204:207], a[76:79]\n"
"s_add_u32 m0, s82, 0xfc00\n"
"ds_read_b64_tr_b16 a[162:163], v20 offset:1152\n"
"v_mfma_f32_16x16x32_bf16 a[80:83], a[228:231], a[192:195], a[80:83]\n"
"ds_read_b64_tr_b16 a[164:165], v21 offset:128\n"
"v_add_u32 v32, 0xffff1c00, v32\n"
"v_mfma_f32_16x16x32_bf16 a[84:87], a[228:231], a[196:199], a[84:87]\n"
"buffer_load_dwordx4 v36, s[48:51], s74 offen lds ;@Q7\n"
"v_mfma_f32_16x16x32_bf16 a[88:91], a[228:231], a[200:203], a[88:91]\n"
"ds_read_b64_tr_b16 a[166:167], v21 offset:1152\n"
"ds_read_b64_tr_b16 a[168:169], v22 offset:128\n"
"v_mfma_f32_16x16x32_bf16 a[92:95], a[228:231], a[204:207], a[92:95]\n"
"s_add_u32 m0, s82, 0x10000\n"
"v_add_u32 v33, 0xffff1c00, v33\n"
"v_mfma_f32_16x16x32_bf16 a[96:99], a[232:235], a[192:195], a[96:99]\n"
"buffer_load_dwordx4 v37, s[48:51], s75 offen lds ;@Q7\n"
"v_mfma_f32_16x16x32_bf16 a[100:103], a[232:235], a[196:199], a[100:103]\n"
"ds_read_b64_tr_b16 a[170:171], v22 offset:1152\n"
"ds_read_b64_tr_b16 a[172:173], v23 offset:128\n"
"v_mfma_f32_16x16x32_bf16 a[104:107], a[232:235], a[200:203], a[104:107]\n"
"s_add_u32 m0, s83, 0x16400\n"
"ds_read_b64_tr_b16 a[174:175], v23 offset:1152\n"
"v_mfma_f32_16x16x32_bf16 a[108:111], a[232:235], a[204:207], a[108:111]\n"
"buffer_load_dwordx4 v38, s[52:55], s76 offen lds ;@Q7\n"
"v_mfma_f32_16x16x32_bf16 a[112:115], a[236:239], a[192:195], a[112:115]\n"
"ds_read_b64_tr_b16 a[176:177], v28 offset:0\n"
"v_add_u32 v34, 0xffff1c00, v34\n"
"v_mfma_f32_16x16x32_bf16 a[116:119], a[236:239], a[196:199], a[116:119]\n"
"s_add_u32 m0, s83, 0x16800\n"
"ds_read_b64_tr_b16 a[178:179], v28 offset:512\n"
"v_mfma_f32_16x16x32_bf16 a[120:123], a[236:239], a[200:203], a[120:123]\n"
"buffer_load_dwordx4 v38, s[52:55], s77 offen lds ;@Q7\n"
"v_mfma_f32_16x16x32_bf16 a[124:127], a[236:239], a[204:207], a[124:127]\n"
"ds_read_b64_tr_b16 a[180:181], v29 offset:0\n"
"v_add_u32 v35, 0xffff1c00, v35\n"
"v_mfma_f32_16x16x32_bf16 v[64:67], a[240:243], a[192:195], v[64:67]\n"
"s_add_u32 m0, s83, 0x16c80\n"
"ds_read_b64_tr_b16 a[182:183], v29 offset:512\n"
"v_mfma_f32_16x16x32_bf16 v[68:71], a[240:243], a[196:199], v[68:71]\n"
"ds_read_b64_tr_b16 a[184:185], v30 offset:0\n"
"v_mfma_f32_16x16x32_bf16 v[72:75], a[240:243], a[200:203], v[72:75]\n"
"buffer_load_dwordx4 v38, s[52:55], s78 offen lds ;@Q7\n"
"v_mfma_f32_16x16x32_bf16 v[76:79], a[240:243], a[204:207], v[76:79]\n"
"s_add_u32 m0, s83, 0x17080\n"
"ds_read_b64_tr_b16 a[186:187], v30 offset:512\n"
"v_mfma_f32_16x16x32_bf16 v[80:83], a[244:247], a[192:195], v[80:83]\n"
"ds_read_b64_tr_b16 a[188:189], v31 offset:0\n"
"v_mfma_f32_16x16x32_bf16 v[84:87], a[244:247], a[196:199], v[84:87]\n"
"buffer_load_dwordx4 v38, s[52:55], s79 offen lds ;@Q7\n"
"v_mfma_f32_16x16x32_bf16 v[88:91], a[244:247], a[200:203], v[88:91]\n"
"s_add_u32 m0, s84, 0x1a800\n"
"ds_read_b64_tr_b16 a[190:191], v31 offset:512\n"
"v_mfma_f32_16x16x32_bf16 v[92:95], a[244:247], a[204:207], v[92:95]\n"
"v_mfma_f32_16x16x32_bf16 v[96:99], a[248:251], a[192:195], v[96:99]\n"
"buffer_load_dwordx4 v39, s[56:59], s80 offen lds ;@Q7\n"
"v_mfma_f32_16x16x32_bf16 v[100:103], a[248:251], a[196:199], v[100:103]\n"
"s_add_u32 m0, s84, 0x1ac00\n"
"v_mfma_f32_16x16x32_bf16 v[104:107], a[248:251], a[200:203], v[104:107]\n"
"v_mfma_f32_16x16x32_bf16 v[108:111], a[248:251], a[204:207], v[108:111]\n"
"buffer_load_dwordx4 v40, s[56:59], s81 offen lds ;@Q7\n"
"v_mfma_f32_16x16x32_bf16 v[112:115], a[252:255], a[192:195], v[112:115]\n"
"v_add_u32 v36, 0x80, v36\n"
"v_add_u32 v37, 0x80, v37\n"
"v_mfma_f32_16x16x32_bf16 v[116:119], a[252:255], a[196:199], v[116:119]\n"
"v_add_u32 v38, 0x80000, v38\n"
"v_add_u32 v39, s91, v39\n"
"v_mfma_f32_16x16x32_bf16 v[120:123], a[252:255], a[200:203], v[120:123]\n"
"v_add_u32 v40, s91, v40\n"
"v_mfma_f32_16x16x32_bf16 v[124:127], a[252:255], a[204:207], v[124:127]\n"
"s_waitcnt lgkmcnt(0)\n"
"v_mfma_f32_16x16x32_bf16 a[0:3], a[144:147], a[128:131], a[0:3]\n"
"ds_read_b64_tr_b16 a[208:209], v24 offset:0\n"
"v_add_u32 v18, 0xe400, v18\n"
"v_mfma_f32_16x16x32_bf16 a[4:7], a[144:147], a[132:135], a[4:7]\n"
"ds_read_b64_tr_b16 a[210:211], v24 offset:1024\n"
"v_add_u32 v20, 0xe400, v20\n"
"v_mfma_f32_16x16x32_bf16 a[8:11], a[144:147], a[136:139], a[8:11]\n"
"s_add_u32 s47, s89, 0\n"
"ds_read_b128 a[192:195], v19 offset:0\n"
"v_mfma_f32_16x16x32_bf16 a[12:15], a[144:147], a[140:143], a[12:15]\n"
"v_add_u32 v21, 0xe400, v21\n"
"v_add_u32 v22, 0xe400, v22\n"
"v_mfma_f32_16x16x32_bf16 a[16:19], a[148:151], a[128:131], a[16:19]\n"
"ds_read_b128 a[196:199], v19 offset:2048\n"
"v_add_u32 v23, 0xe400, v23\n"
"v_mfma_f32_16x16x32_bf16 a[20:23], a[148:151], a[132:135], a[20:23]\n"
"ds_read_b128 a[200:203], v19 offset:4096\n"
"v_add_u32 v28, 0xe400, v28\n"
"v_mfma_f32_16x16x32_bf16 a[24:27], a[148:151], a[136:139], a[24:27]\n"
"ds_read_b128 a[204:207], v19 offset:6144\n"
"v_add_u32 v29, 0xe400, v29\n"
"v_mfma_f32_16x16x32_bf16 a[28:31], a[148:151], a[140:143], a[28:31]\n"
"buffer_store_dwordx4 v[224:227], v17, s[60:63], s47 offen sc1\n"
"v_mfma_f32_16x16x32_bf16 a[32:35], a[152:155], a[128:131], a[32:35]\n"
"ds_read_b64_tr_b16 a[212:213], v25 offset:0\n"
"v_add_u32 v30, 0xe400, v30\n"
"v_mfma_f32_16x16x32_bf16 a[36:39], a[152:155], a[132:135], a[36:39]\n"
"ds_read_b64_tr_b16 a[214:215], v25 offset:1024\n"
"v_add_u32 v31, 0xe400, v31\n"
"v_mfma_f32_16x16x32_bf16 a[40:43], a[152:155], a[136:139], a[40:43]\n"
"v_mfma_f32_16x16x32_bf16 a[44:47], a[152:155], a[140:143], a[44:47]\n"
"ds_read_b64_tr_b16 a[216:217], v26 offset:0\n"
"v_mfma_f32_16x16x32_bf16 a[48:51], a[156:159], a[128:131], a[48:51]\n"
"s_add_u32 s47, s89, 0\n"
"ds_read_b64_tr_b16 a[218:219], v26 offset:1024\n"
"v_mfma_f32_16x16x32_bf16 a[52:55], a[156:159], a[132:135], a[52:55]\n"
"ds_read_b64_tr_b16 a[220:221], v27 offset:0\n"
"v_mfma_f32_16x16x32_bf16 a[56:59], a[156:159], a[136:139], a[56:59]\n"
"v_mfma_f32_16x16x32_bf16 a[60:63], a[156:159], a[140:143], a[60:63]\n"
"ds_read_b64_tr_b16 a[222:223], v27 offset:1024\n"
"v_mfma_f32_16x16x32_bf16 a[64:67], a[160:163], a[128:131], a[64:67]\n"
"ds_read_b64_tr_b16 a[224:225], v24 offset:128\n"
"v_mfma_f32_16x16x32_bf16 a[68:71], a[160:163], a[132:135], a[68:71]\n"
"v_mfma_f32_16x16x32_bf16 a[72:75], a[160:163], a[136:139], a[72:75]\n"
"buffer_store_dwordx4 v[228:231], v41, s[60:63], s47 offen sc1\n"
"v_mfma_f32_16x16x32_bf16 a[76:79], a[160:163], a[140:143], a[76:79]\n"
"ds_read_b64_tr_b16 a[226:227], v24 offset:1152\n"
"ds_read_b64_tr_b16 a[228:229], v25 offset:128\n"
"v_mfma_f32_16x16x32_bf16 a[80:83], a[164:167], a[128:131], a[80:83]\n"
"ds_read_b64_tr_b16 a[230:231], v25 offset:1152\n"
"v_mfma_f32_16x16x32_bf16 a[84:87], a[164:167], a[132:135], a[84:87]\n"
"v_mfma_f32_16x16x32_bf16 a[88:91], a[164:167], a[136:139], a[88:91]\n"
"ds_read_b64_tr_b16 a[232:233], v26 offset:128\n"
"v_mfma_f32_16x16x32_bf16 a[92:95], a[164:167], a[140:143], a[92:95]\n"
"s_add_u32 s47, s89, 16384\n"
"ds_read_b64_tr_b16 a[234:235], v26 offset:1152\n"
"v_mfma_f32_16x16x32_bf16 a[96:99], a[168:171], a[128:131], a[96:99]\n"
"v_mfma_f32_16x16x32_bf16 a[100:103], a[168:171], a[132:135], a[100:103]\n"
"ds_read_b64_tr_b16 a[236:237], v27 offset:128\n"
"v_mfma_f32_16x16x32_bf16 a[104:107], a[168:171], a[136:139], a[104:107]\n"
"ds_read_b64_tr_b16 a[238:239], v27 offset:1152\n"
"v_mfma_f32_16x16x32_bf16 a[108:111], a[168:171], a[140:143], a[108:111]\n"
"ds_read_b64_tr_b16 a[240:241], v32 offset:0\n"
"v_mfma_f32_16x16x32_bf16 a[112:115], a[172:175], a[128:131], a[112:115]\n"
"buffer_store_dwordx4 v[232:235], v17, s[60:63], s47 offen sc1\n"
"v_mfma_f32_16x16x32_bf16 a[116:119], a[172:175], a[132:135], a[116:119]\n"
"ds_read_b64_tr_b16 a[242:243], v32 offset:512\n"
"v_mfma_f32_16x16x32_bf16 a[120:123], a[172:175], a[136:139], a[120:123]\n"
"ds_read_b64_tr_b16 a[244:245], v33 offset:0\n"
"v_mfma_f32_16x16x32_bf16 a[124:127], a[172:175], a[140:143], a[124:127]\n"
"v_mfma_f32_16x16x32_bf16 v[64:67], a[176:179], a[128:131], v[64:67]\n"
"ds_read_b64_tr_b16 a[246:247], v33 offset:512\n"
"v_mfma_f32_16x16x32_bf16 v[68:71], a[176:179], a[132:135], v[68:71]\n"
"ds_read_b64_tr_b16 a[248:249], v34 offset:0\n"
"v_mfma_f32_16x16x32_bf16 v[72:75], a[176:179], a[136:139], v[72:75]\n"
"s_add_u32 s47, s89, 16384\n"
"ds_read_b64_tr_b16 a[250:251], v34 offset:512\n"
"v_mfma_f32_16x16x32_bf16 v[76:79], a[176:179], a[140:143], v[76:79]\n"
"v_mfma_f32_16x16x32_bf16 v[80:83], a[180:183], a[128:131], v[80:83]\n"
"ds_read_b64_tr_b16 a[252:253], v35 offset:0\n"
"v_mfma_f32_16x16x32_bf16 v[84:87], a[180:183], a[132:135], v[84:87]\n"
"ds_read_b64_tr_b16 a[254:255], v35 offset:512\n"
"v_mfma_f32_16x16x32_bf16 v[88:91], a[180:183], a[136:139], v[88:91]\n"
"v_mfma_f32_16x16x32_bf16 v[92:95], a[180:183], a[140:143], v[92:95]\n"
"buffer_store_dwordx4 v[236:239], v41, s[60:63], s47 offen sc1\n"
"v_mfma_f32_16x16x32_bf16 v[96:99], a[184:187], a[128:131], v[96:99]\n"
"v_mfma_f32_16x16x32_bf16 v[100:103], a[184:187], a[132:135], v[100:103]\n"
"v_mfma_f32_16x16x32_bf16 v[104:107], a[184:187], a[136:139], v[104:107]\n"
"v_mfma_f32_16x16x32_bf16 v[108:111], a[184:187], a[140:143], v[108:111]\n"
"v_mfma_f32_16x16x32_bf16 v[112:115], a[188:191], a[128:131], v[112:115]\n"
"v_mfma_f32_16x16x32_bf16 v[116:119], a[188:191], a[132:135], v[116:119]\n"
"v_mfma_f32_16x16x32_bf16 v[120:123], a[188:191], a[136:139], v[120:123]\n"
"v_mfma_f32_16x16x32_bf16 v[124:127], a[188:191], a[140:143], v[124:127]\n"
"s_waitcnt lgkmcnt(0)\n"
"v_mfma_f32_16x16x32_bf16 a[0:3], a[208:211], a[192:195], a[0:3]\n"
"s_waitcnt vmcnt(4)\n"
"s_barrier\n"
"v_mfma_f32_16x16x32_bf16 a[4:7], a[208:211], a[196:199], a[4:7]\n"
"ds_read_b64_tr_b16 a[144:145], v20 offset:0\n"
"v_add_u32 v19, 0xe400, v19\n"
"v_mfma_f32_16x16x32_bf16 a[8:11], a[208:211], a[200:203], a[8:11]\n"
"ds_read_b64_tr_b16 a[146:147], v20 offset:1024\n"
"v_add_u32 v24, 0xe400, v24\n"
"v_mfma_f32_16x16x32_bf16 a[12:15], a[208:211], a[204:207], a[12:15]\n"
"ds_read_b128 a[128:131], v18 offset:0\n"
"v_add_u32 v25, 0xe400, v25\n"
"v_mfma_f32_16x16x32_bf16 a[16:19], a[212:215], a[192:195], a[16:19]\n"
"v_add_u32 v26, 0xe400, v26\n"
"v_add_u32 v27, 0xe400, v27\n"
"v_mfma_f32_16x16x32_bf16 a[20:23], a[212:215], a[196:199], a[20:23]\n"
"ds_read_b128 a[132:135], v18 offset:2048\n"
"v_add_u32 v32, 0xe400, v32\n"
"v_mfma_f32_16x16x32_bf16 a[24:27], a[212:215], a[200:203], a[24:27]\n"
"ds_read_b128 a[136:139], v18 offset:4096\n"
"v_add_u32 v33, 0xe400, v33\n"
"v_mfma_f32_16x16x32_bf16 a[28:31], a[212:215], a[204:207], a[28:31]\n"
"ds_read_b128 a[140:143], v18 offset:6144\n"
"v_add_u32 v34, 0xe400, v34\n"
"v_mfma_f32_16x16x32_bf16 a[32:35], a[216:219], a[192:195], a[32:35]\n"
"v_add_u32 v35, 0xe400, v35\n"
"v_mfma_f32_16x16x32_bf16 a[36:39], a[216:219], a[196:199], a[36:39]\n"
"ds_read_b64_tr_b16 a[148:149], v21 offset:0\n"
"v_mfma_f32_16x16x32_bf16 a[40:43], a[216:219], a[200:203], a[40:43]\n"
"ds_read_b64_tr_b16 a[150:151], v21 offset:1024\n"
"v_mfma_f32_16x16x32_bf16 a[44:47], a[216:219], a[204:207], a[44:47]\n"
"ds_read_b64_tr_b16 a[152:153], v22 offset:0\n"
"v_mfma_f32_16x16x32_bf16 a[48:51], a[220:223], a[192:195], a[48:51]\n"
"v_mfma_f32_16x16x32_bf16 a[52:55], a[220:223], a[196:199], a[52:55]\n"
"ds_read_b64_tr_b16 a[154:155], v22 offset:1024\n"
"v_mfma_f32_16x16x32_bf16 a[56:59], a[220:223], a[200:203], a[56:59]\n"
"ds_read_b64_tr_b16 a[156:157], v23 offset:0\n"
"v_mfma_f32_16x16x32_bf16 a[60:63], a[220:223], a[204:207], a[60:63]\n"
"ds_read_b64_tr_b16 a[158:159], v23 offset:1024\n"
"v_mfma_f32_16x16x32_bf16 a[64:67], a[224:227], a[192:195], a[64:67]\n"
"v_mfma_f32_16x16x32_bf16 a[68:71], a[224:227], a[196:199], a[68:71]\n"
"ds_read_b64_tr_b16 a[160:161], v20 offset:128\n"
"v_mfma_f32_16x16x32_bf16 a[72:75], a[224:227], a[200:203], a[72:75]\n"
"ds_read_b64_tr_b16 a[162:163], v20 offset:1152\n"
"v_mfma_f32_16x16x32_bf16 a[76:79], a[224:227], a[204:207], a[76:79]\n"
"v_mfma_f32_16x16x32_bf16 a[80:83], a[228:231], a[192:195], a[80:83]\n"
"ds_read_b64_tr_b16 a[164:165], v21 offset:128\n"
"v_mfma_f32_16x16x32_bf16 a[84:87], a[228:231], a[196:199], a[84:87]\n"
"ds_read_b64_tr_b16 a[166:167], v21 offset:1152\n"
"v_mfma_f32_16x16x32_bf16 a[88:91], a[228:231], a[200:203], a[88:91]\n"
"ds_read_b64_tr_b16 a[168:169], v22 offset:128\n"
"v_mfma_f32_16x16x32_bf16 a[92:95], a[228:231], a[204:207], a[92:95]\n"
"v_mfma_f32_16x16x32_bf16 a[96:99], a[232:235], a[192:195], a[96:99]\n"
"ds_read_b64_tr_b16 a[170:171], v22 offset:1152\n"
"v_mfma_f32_16x16x32_bf16 a[100:103], a[232:235], a[196:199], a[100:103]\n"
"ds_read_b64_tr_b16 a[172:173], v23 offset:128\n"
"v_mfma_f32_16x16x32_bf16 a[104:107], a[232:235], a[200:203], a[104:107]\n"
"ds_read_b64_tr_b16 a[174:175], v23 offset:1152\n"
"v_mfma_f32_16x16x32_bf16 a[108:111], a[232:235], a[204:207], a[108:111]\n"
"v_mfma_f32_16x16x32_bf16 a[112:115], a[236:239], a[192:195], a[112:115]\n"
"ds_read_b64_tr_b16 a[176:177], v28 offset:0\n"
"v_mfma_f32_16x16x32_bf16 a[116:119], a[236:239], a[196:199], a[116:119]\n"
"ds_read_b64_tr_b16 a[178:179], v28 offset:512\n"
"v_mfma_f32_16x16x32_bf16 a[120:123], a[236:239], a[200:203], a[120:123]\n"
"ds_read_b64_tr_b16 a[180:181], v29 offset:0\n"
"v_mfma_f32_16x16x32_bf16 a[124:127], a[236:239], a[204:207], a[124:127]\n"
"v_mfma_f32_16x16x32_bf16 v[64:67], a[240:243], a[192:195], v[64:67]\n"
"ds_read_b64_tr_b16 a[182:183], v29 offset:512\n"
"v_mfma_f32_16x16x32_bf16 v[68:71], a[240:243], a[196:199], v[68:71]\n"
"ds_read_b64_tr_b16 a[184:185], v30 offset:0\n"
"v_mfma_f32_16x16x32_bf16 v[72:75], a[240:243], a[200:203], v[72:75]\n"
"ds_read_b64_tr_b16 a[186:187], v30 offset:512\n"
"v_mfma_f32_16x16x32_bf16 v[76:79], a[240:243], a[204:207], v[76:79]\n"
"v_mfma_f32_16x16x32_bf16 v[80:83], a[244:247], a[192:195], v[80:83]\n"
"ds_read_b64_tr_b16 a[188:189], v31 offset:0\n"
"v_mfma_f32_16x16x32_bf16 v[84:87], a[244:247], a[196:199], v[84:87]\n"
"ds_read_b64_tr_b16 a[190:191], v31 offset:512\n"
"v_mfma_f32_16x16x32_bf16 v[88:91], a[244:247], a[200:203], v[88:91]\n"
"v_mfma_f32_16x16x32_bf16 v[92:95], a[244:247], a[204:207], v[92:95]\n"
"v_mfma_f32_16x16x32_bf16 v[96:99], a[248:251], a[192:195], v[96:99]\n"
"v_mfma_f32_16x16x32_bf16 v[100:103], a[248:251], a[196:199], v[100:103]\n"
"v_mfma_f32_16x16x32_bf16 v[104:107], a[248:251], a[200:203], v[104:107]\n"
"v_mfma_f32_16x16x32_bf16 v[108:111], a[248:251], a[204:207], v[108:111]\n"
"v_mfma_f32_16x16x32_bf16 v[112:115], a[252:255], a[192:195], v[112:115]\n"
"v_mfma_f32_16x16x32_bf16 v[116:119], a[252:255], a[196:199], v[116:119]\n"
"v_mfma_f32_16x16x32_bf16 v[120:123], a[252:255], a[200:203], v[120:123]\n"
"v_mfma_f32_16x16x32_bf16 v[124:127], a[252:255], a[204:207], v[124:127]\n"
"s_waitcnt lgkmcnt(0)\n"
"v_mfma_f32_16x16x32_bf16 a[0:3], a[144:147], a[128:131], a[0:3]\n"
"ds_read_b64_tr_b16 a[208:209], v24 offset:0\n"
"v_mfma_f32_16x16x32_bf16 a[4:7], a[144:147], a[132:135], a[4:7]\n"
"ds_read_b64_tr_b16 a[210:211], v24 offset:1024\n"
"v_mfma_f32_16x16x32_bf16 a[8:11], a[144:147], a[136:139], a[8:11]\n"
"s_add_u32 s47, s89, 32768\n"
"ds_read_b128 a[192:195], v19 offset:0\n"
"v_mfma_f32_16x16x32_bf16 a[12:15], a[144:147], a[140:143], a[12:15]\n"
"v_mfma_f32_16x16x32_bf16 a[16:19], a[148:151], a[128:131], a[16:19]\n"
"ds_read_b128 a[196:199], v19 offset:2048\n"
"v_mfma_f32_16x16x32_bf16 a[20:23], a[148:151], a[132:135], a[20:23]\n"
"ds_read_b128 a[200:203], v19 offset:4096\n"
"v_mfma_f32_16x16x32_bf16 a[24:27], a[148:151], a[136:139], a[24:27]\n"
"ds_read_b128 a[204:207], v19 offset:6144\n"
"v_mfma_f32_16x16x32_bf16 a[28:31], a[148:151], a[140:143], a[28:31]\n"
"buffer_store_dwordx4 v[240:243], v17, s[60:63], s47 offen sc1\n"
"v_mfma_f32_16x16x32_bf16 a[32:35], a[152:155], a[128:131], a[32:35]\n"
"ds_read_b64_tr_b16 a[212:213], v25 offset:0\n"
"v_mfma_f32_16x16x32_bf16 a[36:39], a[152:155], a[132:135], a[36:39]\n"
"ds_read_b64_tr_b16 a[214:215], v25 offset:1024\n"
"v_mfma_f32_16x16x32_bf16 a[40:43], a[152:155], a[136:139], a[40:43]\n"
"v_mfma_f32_16x16x32_bf16 a[44:47], a[152:155], a[140:143], a[44:47]\n"
"ds_read_b64_tr_b16 a[216:217], v26 offset:0\n"
"v_mfma_f32_16x16x32_bf16 a[48:51], a[156:159], a[128:131], a[48:51]\n"
"s_add_u32 s47, s89, 32768\n"
"ds_read_b64_tr_b16 a[218:219], v26 offset:1024\n"
"v_mfma_f32_16x16x32_bf16 a[52:55], a[156:159], a[132:135], a[52:55]\n"
"ds_read_b64_tr_b16 a[220:221], v27 offset:0\n"
"v_mfma_f32_16x16x32_bf16 a[56:59], a[156:159], a[136:139], a[56:59]\n"
"v_mfma_f32_16x16x32_bf16 a[60:63], a[156:159], a[140:143], a[60:63]\n"
"ds_read_b64_tr_b16 a[222:223], v27 offset:1024\n"
"v_mfma_f32_16x16x32_bf16 a[64:67], a[160:163], a[128:131], a[64:67]\n"
"ds_read_b64_tr_b16 a[224:225], v24 offset:128\n"
"v_mfma_f32_16x16x32_bf16 a[68:71], a[160:163], a[132:135], a[68:71]\n"
"v_mfma_f32_16x16x32_bf16 a[72:75], a[160:163], a[136:139], a[72:75]\n"
"buffer_store_dwordx4 v[244:247], v41, s[60:63], s47 offen sc1\n"
"v_mfma_f32_16x16x32_bf16 a[76:79], a[160:163], a[140:143], a[76:79]\n"
"ds_read_b64_tr_b16 a[226:227], v24 offset:1152\n"
"ds_read_b64_tr_b16 a[228:229], v25 offset:128\n"
"v_mfma_f32_16x16x32_bf16 a[80:83], a[164:167], a[128:131], a[80:83]\n"
"ds_read_b64_tr_b16 a[230:231], v25 offset:1152\n"
"v_mfma_f32_16x16x32_bf16 a[84:87], a[164:167], a[132:135], a[84:87]\n"
"v_mfma_f32_16x16x32_bf16 a[88:91], a[164:167], a[136:139], a[88:91]\n"
"ds_read_b64_tr_b16 a[232:233], v26 offset:128\n"
"v_mfma_f32_16x16x32_bf16 a[92:95], a[164:167], a[140:143], a[92:95]\n"
"s_add_u32 s47, s89, 49152\n"
"ds_read_b64_tr_b16 a[234:235], v26 offset:1152\n"
"v_mfma_f32_16x16x32_bf16 a[96:99], a[168:171], a[128:131], a[96:99]\n"
"v_mfma_f32_16x16x32_bf16 a[100:103], a[168:171], a[132:135], a[100:103]\n"
"ds_read_b64_tr_b16 a[236:237], v27 offset:128\n"
"v_mfma_f32_16x16x32_bf16 a[104:107], a[168:171], a[136:139], a[104:107]\n"
"ds_read_b64_tr_b16 a[238:239], v27 offset:1152\n"
"v_mfma_f32_16x16x32_bf16 a[108:111], a[168:171], a[140:143], a[108:111]\n"
"ds_read_b64_tr_b16 a[240:241], v32 offset:0\n"
"v_mfma_f32_16x16x32_bf16 a[112:115], a[172:175], a[128:131], a[112:115]\n"
"buffer_store_dwordx4 v[248:251], v17, s[60:63], s47 offen sc1\n"
"v_mfma_f32_16x16x32_bf16 a[116:119], a[172:175], a[132:135], a[116:119]\n"
"ds_read_b64_tr_b16 a[242:243], v32 offset:512\n"
"v_mfma_f32_16x16x32_bf16 a[120:123], a[172:175], a[136:139], a[120:123]\n"
"ds_read_b64_tr_b16 a[244:245], v33 offset:0\n"
"v_mfma_f32_16x16x32_bf16 a[124:127], a[172:175], a[140:143], a[124:127]\n"
"v_mfma_f32_16x16x32_bf16 v[64:67], a[176:179], a[128:131], v[64:67]\n"
"ds_read_b64_tr_b16 a[246:247], v33 offset:512\n"
"v_mfma_f32_16x16x32_bf16 v[68:71], a[176:179], a[132:135], v[68:71]\n"
"ds_read_b64_tr_b16 a[248:249], v34 offset:0\n"
"v_mfma_f32_16x16x32_bf16 v[72:75], a[176:179], a[136:139], v[72:75]\n"
"s_add_u32 s47, s89, 49152\n"
"ds_read_b64_tr_b16 a[250:251], v34 offset:512\n"
"v_mfma_f32_16x16x32_bf16 v[76:79], a[176:179], a[140:143], v[76:79]\n"
"v_mfma_f32_16x16x32_bf16 v[80:83], a[180:183], a[128:131], v[80:83]\n"
"ds_read_b64_tr_b16 a[252:253], v35 offset:0\n"
"v_mfma_f32_16x16x32_bf16 v[84:87], a[180:183], a[132:135], v[84:87]\n"
"ds_read_b64_tr_b16 a[254:255], v35 offset:512\n"
"v_mfma_f32_16x16x32_bf16 v[88:91], a[180:183], a[136:139], v[88:91]\n"
"v_mfma_f32_16x16x32_bf16 v[92:95], a[180:183], a[140:143], v[92:95]\n"
"buffer_store_dwordx4 v[252:255], v41, s[60:63], s47 offen sc1\n"
"v_mfma_f32_16x16x32_bf16 v[96:99], a[184:187], a[128:131], v[96:99]\n"
"v_mfma_f32_16x16x32_bf16 v[100:103], a[184:187], a[132:135], v[100:103]\n"
"v_mfma_f32_16x16x32_bf16 v[104:107], a[184:187], a[136:139], v[104:107]\n"
"v_mfma_f32_16x16x32_bf16 v[108:111], a[184:187], a[140:143], v[108:111]\n"
"v_mfma_f32_16x16x32_bf16 v[112:115], a[188:191], a[128:131], v[112:115]\n"
"v_mfma_f32_16x16x32_bf16 v[116:119], a[188:191], a[132:135], v[116:119]\n"
"v_mfma_f32_16x16x32_bf16 v[120:123], a[188:191], a[136:139], v[120:123]\n"
"v_mfma_f32_16x16x32_bf16 v[124:127], a[188:191], a[140:143], v[124:127]\n"
"s_waitcnt lgkmcnt(0)\n"
"v_mfma_f32_16x16x32_bf16 a[0:3], a[208:211], a[192:195], a[0:3]\n"
"v_mfma_f32_16x16x32_bf16 a[4:7], a[208:211], a[196:199], a[4:7]\n"
"v_mfma_f32_16x16x32_bf16 a[8:11], a[208:211], a[200:203], a[8:11]\n"
"v_mfma_f32_16x16x32_bf16 a[12:15], a[208:211], a[204:207], a[12:15]\n"
"v_mfma_f32_16x16x32_bf16 a[16:19], a[212:215], a[192:195], a[16:19]\n"
"v_mfma_f32_16x16x32_bf16 a[20:23], a[212:215], a[196:199], a[20:23]\n"
"v_mfma_f32_16x16x32_bf16 a[24:27], a[212:215], a[200:203], a[24:27]\n"
"v_mfma_f32_16x16x32_bf16 a[28:31], a[212:215], a[204:207], a[28:31]\n"
"v_mfma_f32_16x16x32_bf16 a[32:35], a[216:219], a[192:195], a[32:35]\n"
"v_mfma_f32_16x16x32_bf16 a[36:39], a[216:219], a[196:199], a[36:39]\n"
"v_mfma_f32_16x16x32_bf16 a[40:43], a[216:219], a[200:203], a[40:43]\n"
"v_mfma_f32_16x16x32_bf16 a[44:47], a[216:219], a[204:207], a[44:47]\n"
"v_mfma_f32_16x16x32_bf16 a[48:51], a[220:223], a[192:195], a[48:51]\n"
"v_mfma_f32_16x16x32_bf16 a[52:55], a[220:223], a[196:199], a[52:55]\n"
"v_mfma_f32_16x16x32_bf16 a[56:59], a[220:223], a[200:203], a[56:59]\n"
"v_mfma_f32_16x16x32_bf16 a[60:63], a[220:223], a[204:207], a[60:63]\n"
"v_mfma_f32_16x16x32_bf16 a[64:67], a[224:227], a[192:195], a[64:67]\n"
"v_mfma_f32_16x16x32_bf16 a[68:71], a[224:227], a[196:199], a[68:71]\n"
"v_mfma_f32_16x16x32_bf16 a[72:75], a[224:227], a[200:203], a[72:75]\n"
"v_mfma_f32_16x16x32_bf16 a[76:79], a[224:227], a[204:207], a[76:79]\n"
"v_mfma_f32_16x16x32_bf16 a[80:83], a[228:231], a[192:195], a[80:83]\n"
"v_mfma_f32_16x16x32_bf16 a[84:87], a[228:231], a[196:199], a[84:87]\n"
"v_mfma_f32_16x16x32_bf16 a[88:91], a[228:231], a[200:203], a[88:91]\n"
"v_mfma_f32_16x16x32_bf16 a[92:95], a[228:231], a[204:207], a[92:95]\n"
"v_mfma_f32_16x16x32_bf16 a[96:99], a[232:235], a[192:195], a[96:99]\n"
"v_mfma_f32_16x16x32_bf16 a[100:103], a[232:235], a[196:199], a[100:103]\n"
"v_mfma_f32_16x16x32_bf16 a[104:107], a[232:235], a[200:203], a[104:107]\n"
"v_mfma_f32_16x16x32_bf16 a[108:111], a[232:235], a[204:207], a[108:111]\n"
"v_mfma_f32_16x16x32_bf16 a[112:115], a[236:239], a[192:195], a[112:115]\n"
"v_mfma_f32_16x16x32_bf16 a[116:119], a[236:239], a[196:199], a[116:119]\n"
"v_mfma_f32_16x16x32_bf16 a[120:123], a[236:239], a[200:203], a[120:123]\n"
"v_mfma_f32_16x16x32_bf16 a[124:127], a[236:239], a[204:207], a[124:127]\n"
"v_mfma_f32_16x16x32_bf16 v[64:67], a[240:243], a[192:195], v[64:67]\n"
"v_mfma_f32_16x16x32_bf16 v[68:71], a[240:243], a[196:199], v[68:71]\n"
"v_mfma_f32_16x16x32_bf16 v[72:75], a[240:243], a[200:203], v[72:75]\n"
"v_mfma_f32_16x16x32_bf16 v[76:79], a[240:243], a[204:207], v[76:79]\n"
"v_mfma_f32_16x16x32_bf16 v[80:83], a[244:247], a[192:195], v[80:83]\n"
"v_mfma_f32_16x16x32_bf16 v[84:87], a[244:247], a[196:199], v[84:87]\n"
"v_mfma_f32_16x16x32_bf16 v[88:91], a[244:247], a[200:203], v[88:91]\n"
"v_mfma_f32_16x16x32_bf16 v[92:95], a[244:247], a[204:207], v[92:95]\n"
"v_mfma_f32_16x16x32_bf16 v[96:99], a[248:251], a[192:195], v[96:99]\n"
"v_mfma_f32_16x16x32_bf16 v[100:103], a[248:251], a[196:199], v[100:103]\n"
"v_mfma_f32_16x16x32_bf16 v[104:107], a[248:251], a[200:203], v[104:107]\n"
"v_mfma_f32_16x16x32_bf16 v[108:111], a[248:251], a[204:207], v[108:111]\n"
"v_mfma_f32_16x16x32_bf16 v[112:115], a[252:255], a[192:195], v[112:115]\n"
"v_mfma_f32_16x16x32_bf16 v[116:119], a[252:255], a[196:199], v[116:119]\n"
"v_mfma_f32_16x16x32_bf16 v[120:123], a[252:255], a[200:203], v[120:123]\n"
"v_mfma_f32_16x16x32_bf16 v[124:127], a[252:255], a[204:207], v[124:127]\n"
"s_mov_b32 s68, 0x3fb8aa3b\n"
"s_mov_b32 s69, 0x3fb8aa3b\n"
"s_mov_b32 s70, 0x40000000\n"
"s_mov_b32 s71, 0x40000000\n"
"s_mov_b32 s72, 0x3e0293ee\n"
"s_mov_b32 s73, 0x3e0293ee\n"
"s_lshl_b32 s41, s37, 3\n"
"s_add_u32 s41, s41, s38\n"
"s_lshl_b32 s41, s41, 7\n"
"s_add_u32 s41, s41, 4096\n"
"v_mov_b32 v44, s41\n"
"v_mov_b32 v46, s42\n"
"v_mov_b32 v47, s43\n"
"s_waitcnt vmcnt(0) lgkmcnt(0)\n"
"s_barrier\n"
"s_cmp_lg_u32 s34, 0\n"
"s_cbranch_scc1 LP1\n"
"s_mov_b64 exec, 1\n"
"buffer_store_dwordx2 v[46:47], v44, s[64:67], 0 offen sc1 ;@cond\n"
"s_mov_b64 exec, -1\n"
"LP1:\n"
"s_getpc_b64 s[48:49]\n"
"LM_pc1:\n"
"s_add_u32 s48, s48, LM_attn-LM_pc1\n"
"s_addc_u32 s49, s49, 0\n"
"s_and_b32 s49, s49, 0xffff\n"
"s_mov_b32 s50, LM_f0-LM_attn\n"
"s_mov_b32 s51, 0x20000\n"
"s_lshl_b32 s44, s36, 2\n"
"s_add_u32 s44, s44, s34\n"
"s_mul_i32 s44, s44, 512\n"
"v_and_b32 v42, 3, v0\n"
"v_lshlrev_b32 v42, 7, v42\n"
"buffer_load_dword v43, v42, s[48:51], s44 offen\n"
"s_nop 3\n"
"v_accvgpr_read_b32 v48, a0\n"
"v_accvgpr_read_b32 v49, a1\n"
"v_accvgpr_read_b32 v50, a2\n"
"v_accvgpr_read_b32 v51, a3\n"
"v_accvgpr_read_b32 v52, a16\n"
"v_accvgpr_read_b32 v53, a17\n"
"v_accvgpr_read_b32 v54, a18\n"
"v_accvgpr_read_b32 v55, a19\n"
"v_pk_mul_f32 v[48:49], v[48:49], s[72:73]\n"
"v_pk_mul_f32 v[50:51], v[50:51], s[72:73]\n"
"v_pk_mul_f32 v[52:53], v[52:53], s[72:73]\n"
"v_pk_mul_f32 v[54:55], v[54:55], s[72:73]\n"
"v_cvt_pk_bf16_f32 v20, v48, v49\n"
"v_cvt_pk_bf16_f32 v21, v50, v51\n"
"v_cvt_pk_bf16_f32 v22, v52, v53\n"
"v_cvt_pk_bf16_f32 v23, v54, v55\n"
"v_accvgpr_write_b32 a128, v20\n"
"v_accvgpr_write_b32 a129, v21\n"
"v_accvgpr_write_b32 a130, v22\n"
"v_accvgpr_write_b32 a131, v23\n"
"v_accvgpr_read_b32 v56, a32\n"
"v_accvgpr_read_b32 v57, a33\n"
"v_accvgpr_read_b32 v58, a34\n"
"v_accvgpr_read_b32 v59, a35\n"
"v_accvgpr_read_b32 v60, a48\n"
"v_accvgpr_read_b32 v61, a49\n"
"v_accvgpr_read_b32 v62, a50\n"
"v_accvgpr_read_b32 v63, a51\n"
"v_pk_mul_f32 v[56:57], v[56:57], s[72:73]\n"
"v_pk_mul_f32 v[58:59], v[58:59], s[72:73]\n"
"v_pk_mul_f32 v[60:61], v[60:61], s[72:73]\n"
"v_pk_mul_f32 v[62:63], v[62:63], s[72:73]\n"
"v_cvt_pk_bf16_f32 v24, v56, v57\n"
"v_cvt_pk_bf16_f32 v25, v58, v59\n"
"v_cvt_pk_bf16_f32 v26, v60, v61\n"
"v_cvt_pk_bf16_f32 v27, v62, v63\n"
"v_accvgpr_write_b32 a132, v24\n"
"v_accvgpr_write_b32 a133, v25\n"
"v_accvgpr_write_b32 a134, v26\n"
"v_accvgpr_write_b32 a135, v27\n"
"v_accvgpr_read_b32 v48, a64\n"
"v_accvgpr_read_b32 v49, a65\n"
"v_accvgpr_read_b32 v50, a66\n"
"v_accvgpr_read_b32 v51, a67\n"
"v_accvgpr_read_b32 v52, a80\n"
"v_accvgpr_read_b32 v53, a81\n"
"v_accvgpr_read_b32 v54, a82\n"
"v_accvgpr_read_b32 v55, a83\n"
"v_pk_mul_f32 v[48:49], v[48:49], s[72:73]\n"
"v_pk_mul_f32 v[50:51], v[50:51], s[72:73]\n"
"v_pk_mul_f32 v[52:53], v[52:53], s[72:73]\n"
"v_pk_mul_f32 v[54:55], v[54:55], s[72:73]\n"
"v_cvt_pk_bf16_f32 v20, v48, v49\n"
"v_cvt_pk_bf16_f32 v21, v50, v51\n"
"v_cvt_pk_bf16_f32 v22, v52, v53\n"
"v_cvt_pk_bf16_f32 v23, v54, v55\n"
"v_accvgpr_write_b32 a136, v20\n"
"v_accvgpr_write_b32 a137, v21\n"
"v_accvgpr_write_b32 a138, v22\n"
"v_accvgpr_write_b32 a139, v23\n"
"v_accvgpr_read_b32 v56, a96\n"
"v_accvgpr_read_b32 v57, a97\n"
"v_accvgpr_read_b32 v58, a98\n"
"v_accvgpr_read_b32 v59, a99\n"
"v_accvgpr_read_b32 v60, a112\n"
"v_accvgpr_read_b32 v61, a113\n"
"v_accvgpr_read_b32 v62, a114\n"
"v_accvgpr_read_b32 v63, a115\n"
"v_pk_mul_f32 v[56:57], v[56:57], s[72:73]\n"
"v_pk_mul_f32 v[58:59], v[58:59], s[72:73]\n"
"v_pk_mul_f32 v[60:61], v[60:61], s[72:73]\n"
"v_pk_mul_f32 v[62:63], v[62:63], s[72:73]\n"
"v_cvt_pk_bf16_f32 v24, v56, v57\n"
"v_cvt_pk_bf16_f32 v25, v58, v59\n"
"v_cvt_pk_bf16_f32 v26, v60, v61\n"
"v_cvt_pk_bf16_f32 v27, v62, v63\n"
"v_accvgpr_write_b32 a140, v24\n"
"v_accvgpr_write_b32 a141, v25\n"
"v_accvgpr_write_b32 a142, v26\n"
"v_accvgpr_write_b32 a143, v27\n"
"v_accvgpr_read_b32 v48, a4\n"
"v_accvgpr_read_b32 v49, a5\n"
"v_accvgpr_read_b32 v50, a6\n"
"v_accvgpr_read_b32 v51, a7\n"
"v_accvgpr_read_b32 v52, a20\n"
"v_accvgpr_read_b32 v53, a21\n"
"v_accvgpr_read_b32 v54, a22\n"
"v_accvgpr_read_b32 v55, a23\n"
"v_pk_mul_f32 v[48:49], v[48:49], s[72:73]\n"
"v_pk_mul_f32 v[50:51], v[50:51], s[72:73]\n"
"v_pk_mul_f32 v[52:53], v[52:53], s[72:73]\n"
"v_pk_mul_f32 v[54:55], v[54:55], s[72:73]\n"
"v_cvt_pk_bf16_f32 v20, v48, v49\n"
"v_cvt_pk_bf16_f32 v21, v50, v51\n"
"v_cvt_pk_bf16_f32 v22, v52, v53\n"
"v_cvt_pk_bf16_f32 v23, v54, v55\n"
"v_accvgpr_write_b32 a144, v20\n"
"v_accvgpr_write_b32 a145, v21\n"
"v_accvgpr_write_b32 a146, v22\n"
"v_accvgpr_write_b32 a147, v23\n"
"v_accvgpr_read_b32 v56, a36\n"
"v_accvgpr_read_b32 v57, a37\n"
"v_accvgpr_read_b32 v58, a38\n"
"v_accvgpr_read_b32 v59, a39\n"
"v_accvgpr_read_b32 v60, a52\n"
"v_accvgpr_read_b32 v61, a53\n"
"v_accvgpr_read_b32 v62, a54\n"
"v_accvgpr_read_b32 v63, a55\n"
"v_pk_mul_f32 v[56:57], v[56:57], s[72:73]\n"
"v_pk_mul_f32 v[58:59], v[58:59], s[72:73]\n"
"v_pk_mul_f32 v[60:61], v[60:61], s[72:73]\n"
"v_pk_mul_f32 v[62:63], v[62:63], s[72:73]\n"
"v_cvt_pk_bf16_f32 v24, v56, v57\n"
"v_cvt_pk_bf16_f32 v25, v58, v59\n"
"v_cvt_pk_bf16_f32 v26, v60, v61\n"
"v_cvt_pk_bf16_f32 v27, v62, v63\n"
"v_accvgpr_write_b32 a148, v24\n"
"v_accvgpr_write_b32 a149, v25\n"
"v_accvgpr_write_b32 a150, v26\n"
"v_accvgpr_write_b32 a151, v27\n"
"v_accvgpr_read_b32 v48, a68\n"
"v_accvgpr_read_b32 v49, a69\n"
"v_accvgpr_read_b32 v50, a70\n"
"v_accvgpr_read_b32 v51, a71\n"
"v_accvgpr_read_b32 v52, a84\n"
"v_accvgpr_read_b32 v53, a85\n"
"v_accvgpr_read_b32 v54, a86\n"
"v_accvgpr_read_b32 v55, a87\n"
"v_pk_mul_f32 v[48:49], v[48:49], s[72:73]\n"
"v_pk_mul_f32 v[50:51], v[50:51], s[72:73]\n"
"v_pk_mul_f32 v[52:53], v[52:53], s[72:73]\n"
"v_pk_mul_f32 v[54:55], v[54:55], s[72:73]\n"
"v_cvt_pk_bf16_f32 v20, v48, v49\n"
"v_cvt_pk_bf16_f32 v21, v50, v51\n"
"v_cvt_pk_bf16_f32 v22, v52, v53\n"
"v_cvt_pk_bf16_f32 v23, v54, v55\n"
"v_accvgpr_write_b32 a152, v20\n"
"v_accvgpr_write_b32 a153, v21\n"
"v_accvgpr_write_b32 a154, v22\n"
"v_accvgpr_write_b32 a155, v23\n"
"v_accvgpr_read_b32 v56, a100\n"
"v_accvgpr_read_b32 v57, a101\n"
"v_accvgpr_read_b32 v58, a102\n"
"v_accvgpr_read_b32 v59, a103\n"
"v_accvgpr_read_b32 v60, a116\n"
"v_accvgpr_read_b32 v61, a117\n"
"v_accvgpr_read_b32 v62, a118\n"
"v_accvgpr_read_b32 v63, a119\n"
"v_pk_mul_f32 v[56:57], v[56:57], s[72:73]\n"
"v_pk_mul_f32 v[58:59], v[58:59], s[72:73]\n"
"v_pk_mul_f32 v[60:61], v[60:61], s[72:73]\n"
"v_pk_mul_f32 v[62:63], v[62:63], s[72:73]\n"
"v_cvt_pk_bf16_f32 v24, v56, v57\n"
"v_cvt_pk_bf16_f32 v25, v58, v59\n"
"v_cvt_pk_bf16_f32 v26, v60, v61\n"
"v_cvt_pk_bf16_f32 v27, v62, v63\n"
"v_accvgpr_write_b32 a156, v24\n"
"v_accvgpr_write_b32 a157, v25\n"
"v_accvgpr_write_b32 a158, v26\n"
"v_accvgpr_write_b32 a159, v27\n"
"s_cmp_lg_u32 s34, 0\n"
"s_cbranch_scc1 LM_pkv_iss\n"
"v_and_b32 v44, 7, v0\n"
"v_lshlrev_b32 v44, 7, v44\n"
"s_lshl_b32 s44, s37, 10\n"
"s_add_u32 s44, s44, 4096\n"
"v_add_u32 v44, s44, v44\n"
"buffer_load_dwordx2 v[46:47], v44, s[64:67], 0 offen sc1 ;@cond\n"
"LM_pkv_iss:\n"
"v_accvgpr_read_b32 v48, a8\n"
"v_accvgpr_read_b32 v49, a9\n"
"v_accvgpr_read_b32 v50, a10\n"
"v_accvgpr_read_b32 v51, a11\n"
"v_accvgpr_read_b32 v52, a24\n"
"v_accvgpr_read_b32 v53, a25\n"
"v_accvgpr_read_b32 v54, a26\n"
"v_accvgpr_read_b32 v55, a27\n"
"v_pk_mul_f32 v[48:49], v[48:49], s[72:73]\n"
"v_pk_mul_f32 v[50:51], v[50:51], s[72:73]\n"
"v_pk_mul_f32 v[52:53], v[52:53], s[72:73]\n"
"v_pk_mul_f32 v[54:55], v[54:55], s[72:73]\n"
"v_cvt_pk_bf16_f32 v20, v48, v49\n"
"v_cvt_pk_bf16_f32 v21, v50, v51\n"
"v_cvt_pk_bf16_f32 v22, v52, v53\n"
"v_cvt_pk_bf16_f32 v23, v54, v55\n"
"v_accvgpr_write_b32 a160, v20\n"
"v_accvgpr_write_b32 a161, v21\n"
"v_accvgpr_write_b32 a162, v22\n"
"v_accvgpr_write_b32 a163, v23\n"
"v_accvgpr_read_b32 v56, a40\n"
"v_accvgpr_read_b32 v57, a41\n"
"v_accvgpr_read_b32 v58, a42\n"
"v_accvgpr_read_b32 v59, a43\n"
"v_accvgpr_read_b32 v60, a56\n"
"v_accvgpr_read_b32 v61, a57\n"
"v_accvgpr_read_b32 v62, a58\n"
"v_accvgpr_read_b32 v63, a59\n"
"v_pk_mul_f32 v[56:57], v[56:57], s[72:73]\n"
"v_pk_mul_f32 v[58:59], v[58:59], s[72:73]\n"
"v_pk_mul_f32 v[60:61], v[60:61], s[72:73]\n"
"v_pk_mul_f32 v[62:63], v[62:63], s[72:73]\n"
"v_cvt_pk_bf16_f32 v24, v56, v57\n"
"v_cvt_pk_bf16_f32 v25, v58, v59\n"
"v_cvt_pk_bf16_f32 v26, v60, v61\n"
"v_cvt_pk_bf16_f32 v27, v62, v63\n"
"v_accvgpr_write_b32 a164, v24\n"
"v_accvgpr_write_b32 a165, v25\n"
"v_accvgpr_write_b32 a166, v26\n"
"v_accvgpr_write_b32 a167, v27\n"
"v_accvgpr_read_b32 v48, a72\n"
"v_accvgpr_read_b32 v49, a73\n"
"v_accvgpr_read_b32 v50, a74\n"
"v_accvgpr_read_b32 v51, a75\n"
"v_accvgpr_read_b32 v52, a88\n"
"v_accvgpr_read_b32 v53, a89\n"
"v_accvgpr_read_b32 v54, a90\n"
"v_accvgpr_read_b32 v55, a91\n"
"v_pk_mul_f32 v[48:49], v[48:49], s[72:73]\n"
"v_pk_mul_f32 v[50:51], v[50:51], s[72:73]\n"
"v_pk_mul_f32 v[52:53], v[52:53], s[72:73]\n"
"v_pk_mul_f32 v[54:55], v[54:55], s[72:73]\n"
"v_cvt_pk_bf16_f32 v20, v48, v49\n"
"v_cvt_pk_bf16_f32 v21, v50, v51\n"
"v_cvt_pk_bf16_f32 v22, v52, v53\n"
"v_cvt_pk_bf16_f32 v23, v54, v55\n"
"v_accvgpr_write_b32 a168, v20\n"
"v_accvgpr_write_b32 a169, v21\n"
"v_accvgpr_write_b32 a170, v22\n"
"v_accvgpr_write_b32 a171, v23\n"
"v_accvgpr_read_b32 v56, a104\n"
"v_accvgpr_read_b32 v57, a105\n"
"v_accvgpr_read_b32 v58, a106\n"
"v_accvgpr_read_b32 v59, a107\n"
"v_accvgpr_read_b32 v60, a120\n"
"v_accvgpr_read_b32 v61, a121\n"
"v_accvgpr_read_b32 v62, a122\n"
"v_accvgpr_read_b32 v63, a123\n"
"v_pk_mul_f32 v[56:57], v[56:57], s[72:73]\n"
"v_pk_mul_f32 v[58:59], v[58:59], s[72:73]\n"
"v_pk_mul_f32 v[60:61], v[60:61], s[72:73]\n"
"v_pk_mul_f32 v[62:63], v[62:63], s[72:73]\n"
"v_cvt_pk_bf16_f32 v24, v56, v57\n"
"v_cvt_pk_bf16_f32 v25, v58, v59\n"
"v_cvt_pk_bf16_f32 v26, v60, v61\n"
"v_cvt_pk_bf16_f32 v27, v62, v63\n"
"v_accvgpr_write_b32 a172, v24\n"
"v_accvgpr_write_b32 a173, v25\n"
"v_accvgpr_write_b32 a174, v26\n"
"v_accvgpr_write_b32 a175, v27\n"
"v_accvgpr_read_b32 v48, a12\n"
"v_accvgpr_read_b32 v49, a13\n"
"v_accvgpr_read_b32 v50, a14\n"
"v_accvgpr_read_b32 v51, a15\n"
"v_accvgpr_read_b32 v52, a28\n"
"v_accvgpr_read_b32 v53, a29\n"
"v_accvgpr_read_b32 v54, a30\n"
"v_accvgpr_read_b32 v55, a31\n"
"v_pk_mul_f32 v[48:49], v[48:49], s[72:73]\n"
"v_pk_mul_f32 v[50:51], v[50:51], s[72:73]\n"
"v_pk_mul_f32 v[52:53], v[52:53], s[72:73]\n"
"v_pk_mul_f32 v[54:55], v[54:55], s[72:73]\n"
"v_cvt_pk_bf16_f32 v20, v48, v49\n"
"v_cvt_pk_bf16_f32 v21, v50, v51\n"
"v_cvt_pk_bf16_f32 v22, v52, v53\n"
"v_cvt_pk_bf16_f32 v23, v54, v55\n"
"v_accvgpr_write_b32 a176, v20\n"
"v_accvgpr_write_b32 a177, v21\n"
"v_accvgpr_write_b32 a178, v22\n"
"v_accvgpr_write_b32 a179, v23\n"
"v_accvgpr_read_b32 v56, a44\n"
"v_accvgpr_read_b32 v57, a45\n"
"v_accvgpr_read_b32 v58, a46\n"
"v_accvgpr_read_b32 v59, a47\n"
"v_accvgpr_read_b32 v60, a60\n"
"v_accvgpr_read_b32 v61, a61\n"
"v_accvgpr_read_b32 v62, a62\n"
"v_accvgpr_read_b32 v63, a63\n"
"v_pk_mul_f32 v[56:57], v[56:57], s[72:73]\n"
"v_pk_mul_f32 v[58:59], v[58:59], s[72:73]\n"
"v_pk_mul_f32 v[60:61], v[60:61], s[72:73]\n"
"v_pk_mul_f32 v[62:63], v[62:63], s[72:73]\n"
"v_cvt_pk_bf16_f32 v24, v56, v57\n"
"v_cvt_pk_bf16_f32 v25, v58, v59\n"
"v_cvt_pk_bf16_f32 v26, v60, v61\n"
"v_cvt_pk_bf16_f32 v27, v62, v63\n"
"v_accvgpr_write_b32 a180, v24\n"
"v_accvgpr_write_b32 a181, v25\n"
"v_accvgpr_write_b32 a182, v26\n"
"v_accvgpr_write_b32 a183, v27\n"
"v_accvgpr_read_b32 v48, a76\n"
"v_accvgpr_read_b32 v49, a77\n"
"v_accvgpr_read_b32 v50, a78\n"
"v_accvgpr_read_b32 v51, a79\n"
"v_accvgpr_read_b32 v52, a92\n"
"v_accvgpr_read_b32 v53, a93\n"
"v_accvgpr_read_b32 v54, a94\n"
"v_accvgpr_read_b32 v55, a95\n"
"v_pk_mul_f32 v[48:49], v[48:49], s[72:73]\n"
"v_pk_mul_f32 v[50:51], v[50:51], s[72:73]\n"
"v_pk_mul_f32 v[52:53], v[52:53], s[72:73]\n"
"v_pk_mul_f32 v[54:55], v[54:55], s[72:73]\n"
"v_cvt_pk_bf16_f32 v20, v48, v49\n"
"v_cvt_pk_bf16_f32 v21, v50, v51\n"
"v_cvt_pk_bf16_f32 v22, v52, v53\n"
"v_cvt_pk_bf16_f32 v23, v54, v55\n"
"v_accvgpr_write_b32 a184, v20\n"
"v_accvgpr_write_b32 a185, v21\n"
"v_accvgpr_write_b32 a186, v22\n"
"v_accvgpr_write_b32 a187, v23\n"
"v_accvgpr_read_b32 v56, a108\n"
"v_accvgpr_read_b32 v57, a109\n"
"v_accvgpr_read_b32 v58, a110\n"
"v_accvgpr_read_b32 v59, a111\n"
"v_accvgpr_read_b32 v60, a124\n"
"v_accvgpr_read_b32 v61, a125\n"
"v_accvgpr_read_b32 v62, a126\n"
"v_accvgpr_read_b32 v63, a127\n"
"v_pk_mul_f32 v[56:57], v[56:57], s[72:73]\n"
"v_pk_mul_f32 v[58:59], v[58:59], s[72:73]\n"
"v_pk_mul_f32 v[60:61], v[60:61], s[72:73]\n"
"v_pk_mul_f32 v[62:63], v[62:63], s[72:73]\n"
"v_cvt_pk_bf16_f32 v24, v56, v57\n"
"v_cvt_pk_bf16_f32 v25, v58, v59\n"
"v_cvt_pk_bf16_f32 v26, v60, v61\n"
"v_cvt_pk_bf16_f32 v27, v62, v63\n"
"v_accvgpr_write_b32 a188, v24\n"
"v_accvgpr_write_b32 a189, v25\n"
"v_accvgpr_write_b32 a190, v26\n"
"v_accvgpr_write_b32 a191, v27\n"
"s_cmp_lg_u32 s34, 0\n"
"s_cbranch_scc1 LM_pkv_done\n"
"s_mov_b32 s85, -40000\n"
"s_branch LM_pkv_chk\n"
"LM_pkv_spin:\n"
"s_sleep 1\n"
"buffer_load_dwordx2 v[46:47], v44, s[64:67], 0 offen sc1 ;@cond\n"
"LM_pkv_chk:\n"
"s_waitcnt vmcnt(0)\n"
"v_cmp_ne_u32 s[46:47], s42, v46\n"
"v_cmp_ne_u32 vcc, s43, v47\n"
"s_nop 1\n"
"s_or_b64 vcc, vcc, s[46:47]\n"
"s_cbranch_scc0 LM_pkv_done\n"
"s_add_u32 s85, s85, 1\n"
"s_cbranch_scc0 LM_pkv_spin\n"
"LM_pkv_done:\n"
"s_barrier\n"
"s_lshl_b32 s44, s37, 20\n"
"s_add_u32 s52, s26, s44\n"
"s_addc_u32 s53, s27, 0\n"
"s_and_b32 s53, s53, 0xffff\n"
"s_mov_b32 s54, 0x100000\n"
"s_mov_b32 s55, 0x20000\n"
"s_lshl_b32 s47, s34, 11\n"
"v_add_u32 v61, s47, v14\n"
"s_add_u32 s90, s47, 98304\n"
"s_mov_b32 s91, 81920\n"
"s_cmp_ge_u32 s37, 28\n"
"s_cbranch_scc1 LM_noH\n"
"s_add_u32 s56, s26, 0x2000000\n"
"s_addc_u32 s57, s27, 0\n"
"s_and_b32 s57, s57, 0xffff\n"
"s_mov_b32 s58, 0x1700000\n"
"s_mov_b32 s59, 0x20000\n"
"s_lshl_b32 s44, s37, 1\n"
"s_add_u32 s44, s44, 128\n"
"s_lshl_b32 s44, s44, 10\n"
"s_lshl_b32 s45, s38, 3\n"
"s_lshl_b32 s46, s34, 1\n"
"s_add_u32 s88, s45, s46\n"
"s_mul_i32 s88, s88, 188416\n"
"s_add_u32 s88, s88, s44\n"
"s_sub_u32 s45, 120, s45\n"
"s_add_u32 s89, s45, s46\n"
"s_mul_i32 s89, s89, 188416\n"
"s_add_u32 s89, s89, s44\n"
"s_add_u32 m0, s47, 0\n"
"s_mov_b32 s44, 0\n"
"buffer_load_dwordx4 v61, s[52:55], s44 offen sc1 lds\n"
"buffer_load_dwordx4 v61, s[52:55], s44 offen offset:1024 sc1 lds\n"
"s_add_u32 m0, s47, 16384\n"
"s_mov_b32 s44, 16384\n"
"buffer_load_dwordx4 v61, s[52:55], s44 offen sc1 lds\n"
"buffer_load_dwordx4 v61, s[52:55], s44 offen offset:1024 sc1 lds\n"
"s_add_u32 m0, s47, 24576\n"
"s_mov_b32 s44, 8192\n"
"buffer_load_dwordx4 v61, s[52:55], s44 offen sc1 lds\n"
"buffer_load_dwordx4 v61, s[52:55], s44 offen offset:1024 sc1 lds\n"
"v_pk_mul_f32 v[16:17], v[64:65], s[68:69]\n"
"v_pk_mul_f32 v[20:21], v[66:67], s[68:69]\n"
"v_min_f32 v16, 0x422c0000, v16\n"
"v_min_f32 v17, 0x422c0000, v17\n"
"v_min_f32 v20, 0x422c0000, v20\n"
"v_min_f32 v21, 0x422c0000, v21\n"
"v_exp_f32 v16, v16\n"
"v_exp_f32 v17, v17\n"
"v_exp_f32 v20, v20\n"
"v_exp_f32 v21, v21\n"
"v_pk_add_f32 v[18:19], v[16:17], s[70:71]\n"
"v_pk_add_f32 v[22:23], v[20:21], s[70:71]\n"
"v_pk_mul_f32 v[16:17], v[16:17], v[18:19]\n"
"v_pk_mul_f32 v[20:21], v[20:21], v[22:23]\n"
"v_pk_add_f32 v[18:19], v[16:17], s[70:71]\n"
"v_pk_add_f32 v[22:23], v[20:21], s[70:71]\n"
"v_rcp_f32 v18, v18\n"
"v_rcp_f32 v19, v19\n"
"v_rcp_f32 v22, v22\n"
"v_rcp_f32 v23, v23\n"
"v_pk_mul_f32 v[16:17], v[16:17], v[64:65]\n"
"v_pk_mul_f32 v[20:21], v[20:21], v[66:67]\n"
"v_pk_mul_f32 v[64:65], v[16:17], v[18:19]\n"
"v_pk_mul_f32 v[66:67], v[20:21], v[22:23]\n"
"v_pk_mul_f32 v[16:17], v[80:81], s[68:69]\n"
"v_pk_mul_f32 v[20:21], v[82:83], s[68:69]\n"
"s_add_u32 m0, s47, 32768\n"
"s_mov_b32 s44, 32768\n"
"buffer_load_dwordx4 v61, s[52:55], s44 offen sc1 lds\n"
"buffer_load_dwordx4 v61, s[52:55], s44 offen offset:1024 sc1 lds\n"
"v_min_f32 v16, 0x422c0000, v16\n"
"v_min_f32 v17, 0x422c0000, v17\n"
"v_min_f32 v20, 0x422c0000, v20\n"
"v_min_f32 v21, 0x422c0000, v21\n"
"v_exp_f32 v16, v16\n"
"v_exp_f32 v17, v17\n"
"v_exp_f32 v20, v20\n"
"v_exp_f32 v21, v21\n"
"v_pk_add_f32 v[18:19], v[16:17], s[70:71]\n"
"v_pk_add_f32 v[22:23], v[20:21], s[70:71]\n"
"v_pk_mul_f32 v[16:17], v[16:17], v[18:19]\n"
"v_pk_mul_f32 v[20:21], v[20:21], v[22:23]\n"
"v_pk_add_f32 v[18:19], v[16:17], s[70:71]\n"
"v_pk_add_f32 v[22:23], v[20:21], s[70:71]\n"
"v_rcp_f32 v18, v18\n"
"v_rcp_f32 v19, v19\n"
"v_rcp_f32 v22, v22\n"
"v_rcp_f32 v23, v23\n"
"v_pk_mul_f32 v[16:17], v[16:17], v[80:81]\n"
"v_pk_mul_f32 v[20:21], v[20:21], v[82:83]\n"
"v_pk_mul_f32 v[80:81], v[16:17], v[18:19]\n"
"v_pk_mul_f32 v[82:83], v[20:21], v[22:23]\n"
"v_cvt_pk_bf16_f32 v24, v64, v65\n"
"v_cvt_pk_bf16_f32 v25, v66, v67\n"
"v_cvt_pk_bf16_f32 v26, v80, v81\n"
"v_cvt_pk_bf16_f32 v27, v82, v83\n"
"s_add_u32 m0, s47, 40960\n"
"s_mov_b32 s44, 24576\n"
"buffer_load_dwordx4 v61, s[52:55], s44 offen sc1 lds\n"
"buffer_load_dwordx4 v61, s[52:55], s44 offen offset:1024 sc1 lds\n"
"s_add_u32 s45, s88, 0\n"
"buffer_store_dwordx4 v[24:27], v14, s[56:59], s45 offen sc1\n"
"v_pk_mul_f32 v[16:17], v[68:69], s[68:69]\n"
"v_pk_mul_f32 v[20:21], v[70:71], s[68:69]\n"
"v_min_f32 v16, 0x422c0000, v16\n"
"v_min_f32 v17, 0x422c0000, v17\n"
"v_min_f32 v20, 0x422c0000, v20\n"
"v_min_f32 v21, 0x422c0000, v21\n"
"v_exp_f32 v16, v16\n"
"v_exp_f32 v17, v17\n"
"v_exp_f32 v20, v20\n"
"v_exp_f32 v21, v21\n"
"v_pk_add_f32 v[18:19], v[16:17], s[70:71]\n"
"v_pk_add_f32 v[22:23], v[20:21], s[70:71]\n"
"v_pk_mul_f32 v[16:17], v[16:17], v[18:19]\n"
"v_pk_mul_f32 v[20:21], v[20:21], v[22:23]\n"
"v_pk_add_f32 v[18:19], v[16:17], s[70:71]\n"
"v_pk_add_f32 v[22:23], v[20:21], s[70:71]\n"
"v_rcp_f32 v18, v18\n"
"v_rcp_f32 v19, v19\n"
"v_rcp_f32 v22, v22\n"
"v_rcp_f32 v23, v23\n"
"v_pk_mul_f32 v[16:17], v[16:17], v[68:69]\n"
"v_pk_mul_f32 v[20:21], v[20:21], v[70:71]\n"
"v_pk_mul_f32 v[68:69], v[16:17], v[18:19]\n"
"v_pk_mul_f32 v[70:71], v[20:21], v[22:23]\n"
"v_pk_mul_f32 v[16:17], v[84:85], s[68:69]\n"
"v_pk_mul_f32 v[20:21], v[86:87], s[68:69]\n"
"s_add_u32 m0, s47, 49152\n"
"s_mov_b32 s44, 49152\n"
"buffer_load_dwordx4 v61, s[52:55], s44 offen sc1 lds\n"
"buffer_load_dwordx4 v61, s[52:55], s44 offen offset:1024 sc1 lds\n"
"v_min_f32 v16, 0x422c0000, v16\n"
"v_min_f32 v17, 0x422c0000, v17\n"
"v_min_f32 v20, 0x422c0000, v20\n"
"v_min_f32 v21, 0x422c0000, v21\n"
"v_exp_f32 v16, v16\n"
"v_exp_f32 v17, v17\n"
"v_exp_f32 v20, v20\n"
"v_exp_f32 v21, v21\n"
"v_pk_add_f32 v[18:19], v[16:17], s[70:71]\n"
"v_pk_add_f32 v[22:23], v[20:21], s[70:71]\n"
"v_pk_mul_f32 v[16:17], v[16:17], v[18:19]\n"
"v_pk_mul_f32 v[20:21], v[20:21], v[22:23]\n"
"v_pk_add_f32 v[18:19], v[16:17], s[70:71]\n"
"v_pk_add_f32 v[22:23], v[20:21], s[70:71]\n"
"v_rcp_f32 v18, v18\n"
"v_rcp_f32 v19, v19\n"
"v_rcp_f32 v22, v22\n"
"v_rcp_f32 v23, v23\n"
"v_pk_mul_f32 v[16:17], v[16:17], v[84:85]\n"
"v_pk_mul_f32 v[20:21], v[20:21], v[86:87]\n"
"v_pk_mul_f32 v[84:85], v[16:17], v[18:19]\n"
"v_pk_mul_f32 v[86:87], v[20:21], v[22:23]\n"
"v_cvt_pk_bf16_f32 v28, v68, v69\n"
"v_cvt_pk_bf16_f32 v29, v70, v71\n"
"v_cvt_pk_bf16_f32 v30, v84, v85\n"
"v_cvt_pk_bf16_f32 v31, v86, v87\n"
"s_add_u32 m0, s47, 57344\n"
"s_mov_b32 s44, 40960\n"
"buffer_load_dwordx4 v61, s[52:55], s44 offen sc1 lds\n"
"buffer_load_dwordx4 v61, s[52:55], s44 offen offset:1024 sc1 lds\n"
"s_add_u32 s45, s88, 188416\n"
"buffer_store_dwordx4 v[28:31], v14, s[56:59], s45 offen sc1\n"
"v_pk_mul_f32 v[16:17], v[72:73], s[68:69]\n"
"v_pk_mul_f32 v[20:21], v[74:75], s[68:69]\n"
"v_min_f32 v16, 0x422c0000, v16\n"
"v_min_f32 v17, 0x422c0000, v17\n"
"v_min_f32 v20, 0x422c0000, v20\n"
"v_min_f32 v21, 0x422c0000, v21\n"
"v_exp_f32 v16, v16\n"
"v_exp_f32 v17, v17\n"
"v_exp_f32 v20, v20\n"
"v_exp_f32 v21, v21\n"
"v_pk_add_f32 v[18:19], v[16:17], s[70:71]\n"
"v_pk_add_f32 v[22:23], v[20:21], s[70:71]\n"
"v_pk_mul_f32 v[16:17], v[16:17], v[18:19]\n"
"v_pk_mul_f32 v[20:21], v[20:21], v[22:23]\n"
"v_pk_add_f32 v[18:19], v[16:17], s[70:71]\n"
"v_pk_add_f32 v[22:23], v[20:21], s[70:71]\n"
"v_rcp_f32 v18, v18\n"
"v_rcp_f32 v19, v19\n"
"v_rcp_f32 v22, v22\n"
"v_rcp_f32 v23, v23\n"
"v_pk_mul_f32 v[16:17], v[16:17], v[72:73]\n"
"v_pk_mul_f32 v[20:21], v[20:21], v[74:75]\n"
"v_pk_mul_f32 v[72:73], v[16:17], v[18:19]\n"
"v_pk_mul_f32 v[74:75], v[20:21], v[22:23]\n"
"v_pk_mul_f32 v[16:17], v[88:89], s[68:69]\n"
"v_pk_mul_f32 v[20:21], v[90:91], s[68:69]\n"
"s_add_u32 m0, s47, 65536\n"
"s_mov_b32 s44, 65536\n"
"buffer_load_dwordx4 v61, s[52:55], s44 offen sc1 lds\n"
"buffer_load_dwordx4 v61, s[52:55], s44 offen offset:1024 sc1 lds\n"
"v_min_f32 v16, 0x422c0000, v16\n"
"v_min_f32 v17, 0x422c0000, v17\n"
"v_min_f32 v20, 0x422c0000, v20\n"
"v_min_f32 v21, 0x422c0000, v21\n"
"v_exp_f32 v16, v16\n"
"v_exp_f32 v17, v17\n"
"v_exp_f32 v20, v20\n"
"v_exp_f32 v21, v21\n"
"v_pk_add_f32 v[18:19], v[16:17], s[70:71]\n"
"v_pk_add_f32 v[22:23], v[20:21], s[70:71]\n"
"v_pk_mul_f32 v[16:17], v[16:17], v[18:19]\n"
"v_pk_mul_f32 v[20:21], v[20:21], v[22:23]\n"
"v_pk_add_f32 v[18:19], v[16:17], s[70:71]\n"
"v_pk_add_f32 v[22:23], v[20:21], s[70:71]\n"
"v_rcp_f32 v18, v18\n"
"v_rcp_f32 v19, v19\n"
"v_rcp_f32 v22, v22\n"
"v_rcp_f32 v23, v23\n"
"v_pk_mul_f32 v[16:17], v[16:17], v[88:89]\n"
"v_pk_mul_f32 v[20:21], v[20:21], v[90:91]\n"
"v_pk_mul_f32 v[88:89], v[16:17], v[18:19]\n"
"v_pk_mul_f32 v[90:91], v[20:21], v[22:23]\n"
"v_cvt_pk_bf16_f32 v24, v72, v73\n"
"v_cvt_pk_bf16_f32 v25, v74, v75\n"
"v_cvt_pk_bf16_f32 v26, v88, v89\n"
"v_cvt_pk_bf16_f32 v27, v90, v91\n"
"s_add_u32 m0, s47, 73728\n"
"s_mov_b32 s44, 57344\n"
"buffer_load_dwordx4 v61, s[52:55], s44 offen sc1 lds\n"
"buffer_load_dwordx4 v61, s[52:55], s44 offen offset:1024 sc1 lds\n"
"s_add_u32 s45, s89, 0\n"
"buffer_store_dwordx4 v[24:27], v14, s[56:59], s45 offen sc1\n"
"v_pk_mul_f32 v[16:17], v[76:77], s[68:69]\n"
"v_pk_mul_f32 v[20:21], v[78:79], s[68:69]\n"
"v_min_f32 v16, 0x422c0000, v16\n"
"v_min_f32 v17, 0x422c0000, v17\n"
"v_min_f32 v20, 0x422c0000, v20\n"
"v_min_f32 v21, 0x422c0000, v21\n"
"v_exp_f32 v16, v16\n"
"v_exp_f32 v17, v17\n"
"v_exp_f32 v20, v20\n"
"v_exp_f32 v21, v21\n"
"v_pk_add_f32 v[18:19], v[16:17], s[70:71]\n"
"v_pk_add_f32 v[22:23], v[20:21], s[70:71]\n"
"v_pk_mul_f32 v[16:17], v[16:17], v[18:19]\n"
"v_pk_mul_f32 v[20:21], v[20:21], v[22:23]\n"
"v_pk_add_f32 v[18:19], v[16:17], s[70:71]\n"
"v_pk_add_f32 v[22:23], v[20:21], s[70:71]\n"
"v_rcp_f32 v18, v18\n"
"v_rcp_f32 v19, v19\n"
"v_rcp_f32 v22, v22\n"
"v_rcp_f32 v23, v23\n"
"v_pk_mul_f32 v[16:17], v[16:17], v[76:77]\n"
"v_pk_mul_f32 v[20:21], v[20:21], v[78:79]\n"
"v_pk_mul_f32 v[76:77], v[16:17], v[18:19]\n"
"v_pk_mul_f32 v[78:79], v[20:21], v[22:23]\n"
"v_pk_mul_f32 v[16:17], v[92:93], s[68:69]\n"
"v_pk_mul_f32 v[20:21], v[94:95], s[68:69]\n"
"s_add_u32 m0, s47, 81920\n"
"s_mov_b32 s44, 81920\n"
"buffer_load_dwordx4 v61, s[52:55], s44 offen sc1 lds\n"
"buffer_load_dwordx4 v61, s[52:55], s44 offen offset:1024 sc1 lds\n"
"v_min_f32 v16, 0x422c0000, v16\n"
"v_min_f32 v17, 0x422c0000, v17\n"
"v_min_f32 v20, 0x422c0000, v20\n"
"v_min_f32 v21, 0x422c0000, v21\n"
"v_exp_f32 v16, v16\n"
"v_exp_f32 v17, v17\n"
"v_exp_f32 v20, v20\n"
"v_exp_f32 v21, v21\n"
"v_pk_add_f32 v[18:19], v[16:17], s[70:71]\n"
"v_pk_add_f32 v[22:23], v[20:21], s[70:71]\n"
"v_pk_mul_f32 v[16:17], v[16:17], v[18:19]\n"
"v_pk_mul_f32 v[20:21], v[20:21], v[22:23]\n"
"v_pk_add_f32 v[18:19], v[16:17], s[70:71]\n"
"v_pk_add_f32 v[22:23], v[20:21], s[70:71]\n"
"v_rcp_f32 v18, v18\n"
"v_rcp_f32 v19, v19\n"
"v_rcp_f32 v22, v22\n"
"v_rcp_f32 v23, v23\n"
"v_pk_mul_f32 v[16:17], v[16:17], v[92:93]\n"
"v_pk_mul_f32 v[20:21], v[20:21], v[94:95]\n"
"v_pk_mul_f32 v[92:93], v[16:17], v[18:19]\n"
"v_pk_mul_f32 v[94:95], v[20:21], v[22:23]\n"
"v_cvt_pk_bf16_f32 v28, v76, v77\n"
"v_cvt_pk_bf16_f32 v29, v78, v79\n"
"v_cvt_pk_bf16_f32 v30, v92, v93\n"
"v_cvt_pk_bf16_f32 v31, v94, v95\n"
"s_add_u32 m0, s47, 90112\n"
"s_mov_b32 s44, 73728\n"
"buffer_load_dwordx4 v61, s[52:55], s44 offen sc1 lds\n"
"buffer_load_dwordx4 v61, s[52:55], s44 offen offset:1024 sc1 lds\n"
"s_add_u32 s45, s89, 188416\n"
"buffer_store_dwordx4 v[28:31], v14, s[56:59], s45 offen sc1\n"
"v_pk_mul_f32 v[16:17], v[96:97], s[68:69]\n"
"v_pk_mul_f32 v[20:21], v[98:99], s[68:69]\n"
"v_min_f32 v16, 0x422c0000, v16\n"
"v_min_f32 v17, 0x422c0000, v17\n"
"v_min_f32 v20, 0x422c0000, v20\n"
"v_min_f32 v21, 0x422c0000, v21\n"
"v_exp_f32 v16, v16\n"
"v_exp_f32 v17, v17\n"
"v_exp_f32 v20, v20\n"
"v_exp_f32 v21, v21\n"
"v_pk_add_f32 v[18:19], v[16:17], s[70:71]\n"
"v_pk_add_f32 v[22:23], v[20:21], s[70:71]\n"
"v_pk_mul_f32 v[16:17], v[16:17], v[18:19]\n"
"v_pk_mul_f32 v[20:21], v[20:21], v[22:23]\n"
"v_pk_add_f32 v[18:19], v[16:17], s[70:71]\n"
"v_pk_add_f32 v[22:23], v[20:21], s[70:71]\n"
"v_rcp_f32 v18, v18\n"
"v_rcp_f32 v19, v19\n"
"v_rcp_f32 v22, v22\n"
"v_rcp_f32 v23, v23\n"
"v_pk_mul_f32 v[16:17], v[16:17], v[96:97]\n"
"v_pk_mul_f32 v[20:21], v[20:21], v[98:99]\n"
"v_pk_mul_f32 v[96:97], v[16:17], v[18:19]\n"
"v_pk_mul_f32 v[98:99], v[20:21], v[22:23]\n"
"v_pk_mul_f32 v[16:17], v[112:113], s[68:69]\n"
"v_pk_mul_f32 v[20:21], v[114:115], s[68:69]\n"
"v_min_f32 v16, 0x422c0000, v16\n"
"v_min_f32 v17, 0x422c0000, v17\n"
"v_min_f32 v20, 0x422c0000, v20\n"
"v_min_f32 v21, 0x422c0000, v21\n"
"v_exp_f32 v16, v16\n"
"v_exp_f32 v17, v17\n"
"v_exp_f32 v20, v20\n"
"v_exp_f32 v21, v21\n"
"v_pk_add_f32 v[18:19], v[16:17], s[70:71]\n"
"v_pk_add_f32 v[22:23], v[20:21], s[70:71]\n"
"v_pk_mul_f32 v[16:17], v[16:17], v[18:19]\n"
"v_pk_mul_f32 v[20:21], v[20:21], v[22:23]\n"
"v_pk_add_f32 v[18:19], v[16:17], s[70:71]\n"
"v_pk_add_f32 v[22:23], v[20:21], s[70:71]\n"
"v_rcp_f32 v18, v18\n"
"v_rcp_f32 v19, v19\n"
"v_rcp_f32 v22, v22\n"
"v_rcp_f32 v23, v23\n"
"v_pk_mul_f32 v[16:17], v[16:17], v[112:113]\n"
"v_pk_mul_f32 v[20:21], v[20:21], v[114:115]\n"
"v_pk_mul_f32 v[112:113], v[16:17], v[18:19]\n"
"v_pk_mul_f32 v[114:115], v[20:21], v[22:23]\n"
"v_cvt_pk_bf16_f32 v24, v96, v97\n"
"v_cvt_pk_bf16_f32 v25, v98, v99\n"
"v_cvt_pk_bf16_f32 v26, v112, v113\n"
"v_cvt_pk_bf16_f32 v27, v114, v115\n"
"s_add_u32 s45, s88, 1024\n"
"buffer_store_dwordx4 v[24:27], v14, s[56:59], s45 offen sc1\n"
"v_pk_mul_f32 v[16:17], v[100:101], s[68:69]\n"
"v_pk_mul_f32 v[20:21], v[102:103], s[68:69]\n"
"v_min_f32 v16, 0x422c0000, v16\n"
"v_min_f32 v17, 0x422c0000, v17\n"
"v_min_f32 v20, 0x422c0000, v20\n"
"v_min_f32 v21, 0x422c0000, v21\n"
"v_exp_f32 v16, v16\n"
"v_exp_f32 v17, v17\n"
"v_exp_f32 v20, v20\n"
"v_exp_f32 v21, v21\n"
"v_pk_add_f32 v[18:19], v[16:17], s[70:71]\n"
"v_pk_add_f32 v[22:23], v[20:21], s[70:71]\n"
"v_pk_mul_f32 v[16:17], v[16:17], v[18:19]\n"
"v_pk_mul_f32 v[20:21], v[20:21], v[22:23]\n"
"v_pk_add_f32 v[18:19], v[16:17], s[70:71]\n"
"v_pk_add_f32 v[22:23], v[20:21], s[70:71]\n"
"v_rcp_f32 v18, v18\n"
"v_rcp_f32 v19, v19\n"
"v_rcp_f32 v22, v22\n"
"v_rcp_f32 v23, v23\n"
"v_pk_mul_f32 v[16:17], v[16:17], v[100:101]\n"
"v_pk_mul_f32 v[20:21], v[20:21], v[102:103]\n"
"v_pk_mul_f32 v[100:101], v[16:17], v[18:19]\n"
"v_pk_mul_f32 v[102:103], v[20:21], v[22:23]\n"
"v_pk_mul_f32 v[16:17], v[116:117], s[68:69]\n"
"v_pk_mul_f32 v[20:21], v[118:119], s[68:69]\n"
"v_min_f32 v16, 0x422c0000, v16\n"
"v_min_f32 v17, 0x422c0000, v17\n"
"v_min_f32 v20, 0x422c0000, v20\n"
"v_min_f32 v21, 0x422c0000, v21\n"
"v_exp_f32 v16, v16\n"
"v_exp_f32 v17, v17\n"
"v_exp_f32 v20, v20\n"
"v_exp_f32 v21, v21\n"
"v_pk_add_f32 v[18:19], v[16:17], s[70:71]\n"
"v_pk_add_f32 v[22:23], v[20:21], s[70:71]\n"
"v_pk_mul_f32 v[16:17], v[16:17], v[18:19]\n"
"v_pk_mul_f32 v[20:21], v[20:21], v[22:23]\n"
"v_pk_add_f32 v[18:19], v[16:17], s[70:71]\n"
"v_pk_add_f32 v[22:23], v[20:21], s[70:71]\n"
"v_rcp_f32 v18, v18\n"
"v_rcp_f32 v19, v19\n"
"v_rcp_f32 v22, v22\n"
"v_rcp_f32 v23, v23\n"
"v_pk_mul_f32 v[16:17], v[16:17], v[116:117]\n"
"v_pk_mul_f32 v[20:21], v[20:21], v[118:119]\n"
"v_pk_mul_f32 v[116:117], v[16:17], v[18:19]\n"
"v_pk_mul_f32 v[118:119], v[20:21], v[22:23]\n"
"v_cvt_pk_bf16_f32 v28, v100, v101\n"
"v_cvt_pk_bf16_f32 v29, v102, v103\n"
"v_cvt_pk_bf16_f32 v30, v116, v117\n"
"v_cvt_pk_bf16_f32 v31, v118, v119\n"
"s_add_u32 s45, s88, 189440\n"
"buffer_store_dwordx4 v[28:31], v14, s[56:59], s45 offen sc1\n"
"v_pk_mul_f32 v[16:17], v[104:105], s[68:69]\n"
"v_pk_mul_f32 v[20:21], v[106:107], s[68:69]\n"
"v_min_f32 v16, 0x422c0000, v16\n"
"v_min_f32 v17, 0x422c0000, v17\n"
"v_min_f32 v20, 0x422c0000, v20\n"
"v_min_f32 v21, 0x422c0000, v21\n"
"v_exp_f32 v16, v16\n"
"v_exp_f32 v17, v17\n"
"v_exp_f32 v20, v20\n"
"v_exp_f32 v21, v21\n"
"v_pk_add_f32 v[18:19], v[16:17], s[70:71]\n"
"v_pk_add_f32 v[22:23], v[20:21], s[70:71]\n"
"v_pk_mul_f32 v[16:17], v[16:17], v[18:19]\n"
"v_pk_mul_f32 v[20:21], v[20:21], v[22:23]\n"
"v_pk_add_f32 v[18:19], v[16:17], s[70:71]\n"
"v_pk_add_f32 v[22:23], v[20:21], s[70:71]\n"
"v_rcp_f32 v18, v18\n"
"v_rcp_f32 v19, v19\n"
"v_rcp_f32 v22, v22\n"
"v_rcp_f32 v23, v23\n"
"v_pk_mul_f32 v[16:17], v[16:17], v[104:105]\n"
"v_pk_mul_f32 v[20:21], v[20:21], v[106:107]\n"
"v_pk_mul_f32 v[104:105], v[16:17], v[18:19]\n"
"v_pk_mul_f32 v[106:107], v[20:21], v[22:23]\n"
"v_pk_mul_f32 v[16:17], v[120:121], s[68:69]\n"
"v_pk_mul_f32 v[20:21], v[122:123], s[68:69]\n"
"v_min_f32 v16, 0x422c0000, v16\n"
"v_min_f32 v17, 0x422c0000, v17\n"
"v_min_f32 v20, 0x422c0000, v20\n"
"v_min_f32 v21, 0x422c0000, v21\n"
"v_exp_f32 v16, v16\n"
"v_exp_f32 v17, v17\n"
"v_exp_f32 v20, v20\n"
"v_exp_f32 v21, v21\n"
"v_pk_add_f32 v[18:19], v[16:17], s[70:71]\n"
"v_pk_add_f32 v[22:23], v[20:21], s[70:71]\n"
"v_pk_mul_f32 v[16:17], v[16:17], v[18:19]\n"
"v_pk_mul_f32 v[20:21], v[20:21], v[22:23]\n"
"v_pk_add_f32 v[18:19], v[16:17], s[70:71]\n"
"v_pk_add_f32 v[22:23], v[20:21], s[70:71]\n"
"v_rcp_f32 v18, v18\n"
"v_rcp_f32 v19, v19\n"
"v_rcp_f32 v22, v22\n"
"v_rcp_f32 v23, v23\n"
"v_pk_mul_f32 v[16:17], v[16:17], v[120:121]\n"
"v_pk_mul_f32 v[20:21], v[20:21], v[122:123]\n"
"v_pk_mul_f32 v[120:121], v[16:17], v[18:19]\n"
"v_pk_mul_f32 v[122:123], v[20:21], v[22:23]\n"
"v_cvt_pk_bf16_f32 v24, v104, v105\n"
"v_cvt_pk_bf16_f32 v25, v106, v107\n"
"v_cvt_pk_bf16_f32 v26, v120, v121\n"
"v_cvt_pk_bf16_f32 v27, v122, v123\n"
"s_add_u32 s45, s89, 1024\n"
"buffer_store_dwordx4 v[24:27], v14, s[56:59], s45 offen sc1\n"
"v_pk_mul_f32 v[16:17], v[108:109], s[68:69]\n"
"v_pk_mul_f32 v[20:21], v[110:111], s[68:69]\n"
"v_min_f32 v16, 0x422c0000, v16\n"
"v_min_f32 v17, 0x422c0000, v17\n"
"v_min_f32 v20, 0x422c0000, v20\n"
"v_min_f32 v21, 0x422c0000, v21\n"
"v_exp_f32 v16, v16\n"
"v_exp_f32 v17, v17\n"
"v_exp_f32 v20, v20\n"
"v_exp_f32 v21, v21\n"
"v_pk_add_f32 v[18:19], v[16:17], s[70:71]\n"
"v_pk_add_f32 v[22:23], v[20:21], s[70:71]\n"
"v_pk_mul_f32 v[16:17], v[16:17], v[18:19]\n"
"v_pk_mul_f32 v[20:21], v[20:21], v[22:23]\n"
"v_pk_add_f32 v[18:19], v[16:17], s[70:71]\n"
"v_pk_add_f32 v[22:23], v[20:21], s[70:71]\n"
"v_rcp_f32 v18, v18\n"
"v_rcp_f32 v19, v19\n"
"v_rcp_f32 v22, v22\n"
"v_rcp_f32 v23, v23\n"
"v_pk_mul_f32 v[16:17], v[16:17], v[108:109]\n"
"v_pk_mul_f32 v[20:21], v[20:21], v[110:111]\n"
"v_pk_mul_f32 v[108:109], v[16:17], v[18:19]\n"
"v_pk_mul_f32 v[110:111], v[20:21], v[22:23]\n"
"v_pk_mul_f32 v[16:17], v[124:125], s[68:69]\n"
"v_pk_mul_f32 v[20:21], v[126:127], s[68:69]\n"
"v_min_f32 v16, 0x422c0000, v16\n"
"v_min_f32 v17, 0x422c0000, v17\n"
"v_min_f32 v20, 0x422c0000, v20\n"
"v_min_f32 v21, 0x422c0000, v21\n"
"v_exp_f32 v16, v16\n"
"v_exp_f32 v17, v17\n"
"v_exp_f32 v20, v20\n"
"v_exp_f32 v21, v21\n"
"v_pk_add_f32 v[18:19], v[16:17], s[70:71]\n"
"v_pk_add_f32 v[22:23], v[20:21], s[70:71]\n"
"v_pk_mul_f32 v[16:17], v[16:17], v[18:19]\n"
"v_pk_mul_f32 v[20:21], v[20:21], v[22:23]\n"
"v_pk_add_f32 v[18:19], v[16:17], s[70:71]\n"
"v_pk_add_f32 v[22:23], v[20:21], s[70:71]\n"
"v_rcp_f32 v18, v18\n"
"v_rcp_f32 v19, v19\n"
"v_rcp_f32 v22, v22\n"
"v_rcp_f32 v23, v23\n"
"v_pk_mul_f32 v[16:17], v[16:17], v[124:125]\n"
"v_pk_mul_f32 v[20:21], v[20:21], v[126:127]\n"
"v_pk_mul_f32 v[124:125], v[16:17], v[18:19]\n"
"v_pk_mul_f32 v[126:127], v[20:21], v[22:23]\n"
"v_cvt_pk_bf16_f32 v28, v108, v109\n"
"v_cvt_pk_bf16_f32 v29, v110, v111\n"
"v_cvt_pk_bf16_f32 v30, v124, v125\n"
"v_cvt_pk_bf16_f32 v31, v126, v127\n"
"s_add_u32 s45, s89, 189440\n"
"buffer_store_dwordx4 v[28:31], v14, s[56:59], s45 offen sc1\n"
"s_branch LM_attn\n"
"LM_noH:\n"
"s_add_u32 m0, s47, 0\n"
"s_mov_b32 s44, 0\n"
"buffer_load_dwordx4 v61, s[52:55], s44 offen sc1 lds\n"
"buffer_load_dwordx4 v61, s[52:55], s44 offen offset:1024 sc1 lds\n"
"s_add_u32 m0, s47, 16384\n"
"s_mov_b32 s44, 16384\n"
"buffer_load_dwordx4 v61, s[52:55], s44 offen sc1 lds\n"
"buffer_load_dwordx4 v61, s[52:55], s44 offen offset:1024 sc1 lds\n"
"s_add_u32 m0, s47, 24576\n"
"s_mov_b32 s44, 8192\n"
"buffer_load_dwordx4 v61, s[52:55], s44 offen sc1 lds\n"
"buffer_load_dwordx4 v61, s[52:55], s44 offen offset:1024 sc1 lds\n"
"s_add_u32 m0, s47, 32768\n"
"s_mov_b32 s44, 32768\n"
"buffer_load_dwordx4 v61, s[52:55], s44 offen sc1 lds\n"
"buffer_load_dwordx4 v61, s[52:55], s44 offen offset:1024 sc1 lds\n"
"s_add_u32 m0, s47, 40960\n"
"s_mov_b32 s44, 24576\n"
"buffer_load_dwordx4 v61, s[52:55], s44 offen sc1 lds\n"
"buffer_load_dwordx4 v61, s[52:55], s44 offen offset:1024 sc1 lds\n"
"s_add_u32 m0, s47, 49152\n"
"s_mov_b32 s44, 49152\n"
"buffer_load_dwordx4 v61, s[52:55], s44 offen sc1 lds\n"
"buffer_load_dwordx4 v61, s[52:55], s44 offen offset:1024 sc1 lds\n"
"s_add_u32 m0, s47, 57344\n"
"s_mov_b32 s44, 40960\n"
"buffer_load_dwordx4 v61, s[52:55], s44 offen sc1 lds\n"
"buffer_load_dwordx4 v61, s[52:55], s44 offen offset:1024 sc1 lds\n"
"s_add_u32 m0, s47, 65536\n"
"s_mov_b32 s44, 65536\n"
"buffer_load_dwordx4 v61, s[52:55], s44 offen sc1 lds\n"
"buffer_load_dwordx4 v61, s[52:55], s44 offen offset:1024 sc1 lds\n"
"s_add_u32 m0, s47, 73728\n"
"s_mov_b32 s44, 57344\n"
"buffer_load_dwordx4 v61, s[52:55], s44 offen sc1 lds\n"
"buffer_load_dwordx4 v61, s[52:55], s44 offen offset:1024 sc1 lds\n"
"s_add_u32 m0, s47, 81920\n"
"s_mov_b32 s44, 81920\n"
"buffer_load_dwordx4 v61, s[52:55], s44 offen sc1 lds\n"
"buffer_load_dwordx4 v61, s[52:55], s44 offen offset:1024 sc1 lds\n"
"s_add_u32 m0, s47, 90112\n"
"s_mov_b32 s44, 73728\n"
"buffer_load_dwordx4 v61, s[52:55], s44 offen sc1 lds\n"
"buffer_load_dwordx4 v61, s[52:55], s44 offen offset:1024 sc1 lds\n"
"s_cmp_lg_u32 s37, 28\n"
"s_cbranch_scc1 LM_attn\n"
"v_mul_f32 v20, 0xbfb8aa3b, v64\n"
"v_mul_f32 v21, 0xbfb8aa3b, v68\n"
"v_mul_f32 v22, 0xbfb8aa3b, v72\n"
"v_mul_f32 v23, 0xbfb8aa3b, v76\n"
"v_exp_f32 v20, v20\n"
"v_exp_f32 v21, v21\n"
"v_exp_f32 v22, v22\n"
"v_exp_f32 v23, v23\n"
"v_add_f32 v20, 1.0, v20\n"
"v_add_f32 v21, 1.0, v21\n"
"v_add_f32 v22, 1.0, v22\n"
"v_add_f32 v23, 1.0, v23\n"
"v_rcp_f32 v20, v20\n"
"v_rcp_f32 v21, v21\n"
"v_rcp_f32 v22, v22\n"
"v_rcp_f32 v23, v23\n"
"s_add_u32 s48, s26, 0x3d00000\n"
"s_addc_u32 s49, s27, 0\n"
"s_and_b32 s49, s49, 0xffff\n"
"s_mov_b32 s50, 0x2000\n"
"s_mov_b32 s51, 0x20000\n"
"s_lshl_b32 s44, s38, 9\n"
"s_lshl_b32 s45, s34, 7\n"
"s_add_u32 s46, s44, s45\n"
"s_sub_u32 s44, 7680, s44\n"
"s_add_u32 s44, s44, s45\n"
"v_lshlrev_b32 v24, 2, v15\n"
"s_mov_b64 exec, 0xffff\n"
"buffer_store_dword v20, v24, s[48:51], s46 offen offset:0 sc1\n"
"buffer_store_dword v21, v24, s[48:51], s46 offen offset:64 sc1\n"
"buffer_store_dword v22, v24, s[48:51], s44 offen offset:0 sc1\n"
"buffer_store_dword v23, v24, s[48:51], s44 offen offset:64 sc1\n"
"s_mov_b64 exec, -1\n"
"LM_attn:\n"
"v_and_b32 v12, 63, v0\n"
"v_lshlrev_b32 v1, 4, v12\n"
"s_lshl_b32 s47, s34, 11\n"
"v_add_u32 v12, s47, v1\n"
"v_add_u32 v3, 0x4000, v12\n"
"v_add_u32 v4, 0x2000, v12\n"
"s_add_u32 s60, s26, 0x2000000\n"
"s_addc_u32 s61, s27, 0\n"
"s_and_b32 s61, s61, 0xffff\n"
"s_mov_b32 s62, 0x1700000\n"
"s_mov_b32 s63, 0x20000\n"
"s_lshl_b32 s44, s38, 2\n"
"s_add_u32 s86, s44, s34\n"
"s_sub_u32 s87, 60, s44\n"
"s_add_u32 s87, s87, s34\n"
"s_mov_b32 s31, 0\n"
"v_mov_b32 v228, 0x5d800000\n"
"v_mov_b32 v224, 0x3f803f80\n"
"v_mov_b32 v225, 0x3f803f80\n"
"v_mov_b32 v226, 0x3f803f80\n"
"v_mov_b32 v227, 0x3f803f80\n"
"v_and_b32 v12, 15, v0\n"
"v_lshrrev_b32 v13, 4, v0\n"
"v_and_b32 v13, 3, v13\n"
"v_lshlrev_b32 v13, 2, v13\n"
"v_sub_u32 v12, v12, v13\n"
"s_lshl_b32 s44, s34, 5\n"
"v_add_u32 v12, s44, v12\n"
"v_cvt_f32_i32 v12, v12\n"
"v_add_f32 v12, 0.5, v12\n"
"v_mul_f32 v11, 0x6c800000, v12\n"
"v_mov_b32 v5, 0\n"
"v_mov_b32 v6, 0\n"
"v_mov_b32 v7, 0\n"
"v_mov_b32 v8, 0\n"
"v_add_f32 v9, 0, v11\n"
"v_add_f32 v10, 0x6e800000, v11\n"
"s_cmp_eq_u32 s38, 0\n"
"s_cselect_b32 s44, 0, 0x72800000\n"
"v_add_f32 v9, s44, v9\n"
"v_add_f32 v10, s44, v10\n"
"v_add_f32 v144, 0, v9\n"
"v_min_f32 v144, v144, v5\n"
"v_add_f32 v145, 0xec800000, v9\n"
"v_min_f32 v145, v145, v5\n"
"v_add_f32 v146, 0xed000000, v9\n"
"v_min_f32 v146, v146, v5\n"
"v_add_f32 v147, 0xed400000, v9\n"
"v_min_f32 v147, v147, v5\n"
"v_add_f32 v148, 0, v10\n"
"v_min_f32 v148, v148, v6\n"
"v_add_f32 v149, 0xec800000, v10\n"
"v_min_f32 v149, v149, v6\n"
"v_add_f32 v150, 0xed000000, v10\n"
"v_min_f32 v150, v150, v6\n"
"v_add_f32 v151, 0xed400000, v10\n"
"v_min_f32 v151, v151, v6\n"
"v_add_f32 v152, 0xee800000, v9\n"
"v_min_f32 v152, v152, v5\n"
"v_add_f32 v153, 0xee880000, v9\n"
"v_min_f32 v153, v153, v5\n"
"v_add_f32 v154, 0xee900000, v9\n"
"v_min_f32 v154, v154, v5\n"
"v_add_f32 v155, 0xee980000, v9\n"
"v_min_f32 v155, v155, v5\n"
"v_add_f32 v156, 0xee800000, v10\n"
"v_min_f32 v156, v156, v6\n"
"v_add_f32 v157, 0xee880000, v10\n"
"v_min_f32 v157, v157, v6\n"
"v_add_f32 v158, 0xee900000, v10\n"
"v_min_f32 v158, v158, v6\n"
"v_add_f32 v159, 0xee980000, v10\n"
"v_min_f32 v159, v159, v6\n"
"v_add_f32 v9, 0xef000000, v9\n"
"v_add_f32 v10, 0xef000000, v10\n"
"v_add_u32 v2, 0x4000, v1\n"
"s_waitcnt vmcnt(16)\n"
"s_barrier\n"
"ds_read_b128 a[192:195], v1 offset:0\n"
"ds_read_b128 a[196:199], v1 offset:1024\n"
"ds_read_b128 a[200:203], v1 offset:2048\n"
"ds_read_b128 a[204:207], v1 offset:3072\n"
"ds_read_b128 a[208:211], v1 offset:4096\n"
"ds_read_b128 a[212:215], v1 offset:5120\n"
"ds_read_b128 a[216:219], v1 offset:6144\n"
"ds_read_b128 a[220:223], v1 offset:7168\n"
"ds_read_b128 a[224:227], v2 offset:0\n"
"ds_read_b128 a[228:231], v2 offset:1024\n"
"ds_read_b128 a[232:235], v2 offset:2048\n"
"ds_read_b128 a[236:239], v2 offset:3072\n"
"ds_read_b128 a[240:243], v2 offset:4096\n"
"ds_read_b128 a[244:247], v2 offset:5120\n"
"ds_read_b128 a[248:251], v2 offset:6144\n"
"ds_read_b128 a[252:255], v2 offset:7168\n"
"s_waitcnt lgkmcnt(8)\n"
"v_mfma_f32_16x16x32_bf16 v[16:19], a[192:195], a[128:131], v[144:147]\n"
"v_mfma_f32_16x16x32_bf16 v[16:19], a[196:199], a[132:135], v[16:19]\n"
"v_mfma_f32_16x16x32_bf16 v[16:19], a[200:203], a[136:139], v[16:19]\n"
"v_mfma_f32_16x16x32_bf16 v[16:19], a[204:207], a[140:143], v[16:19]\n"
"v_mfma_f32_16x16x32_bf16 v[20:23], a[192:195], a[144:147], v[148:151]\n"
"v_mfma_f32_16x16x32_bf16 v[20:23], a[196:199], a[148:151], v[20:23]\n"
"v_mfma_f32_16x16x32_bf16 v[20:23], a[200:203], a[152:155], v[20:23]\n"
"v_mfma_f32_16x16x32_bf16 v[20:23], a[204:207], a[156:159], v[20:23]\n"
"v_mfma_f32_16x16x32_bf16 v[24:27], a[208:211], a[128:131], v[152:155]\n"
"v_mfma_f32_16x16x32_bf16 v[24:27], a[212:215], a[132:135], v[24:27]\n"
"v_mfma_f32_16x16x32_bf16 v[24:27], a[216:219], a[136:139], v[24:27]\n"
"v_mfma_f32_16x16x32_bf16 v[24:27], a[220:223], a[140:143], v[24:27]\n"
"v_mfma_f32_16x16x32_bf16 v[28:31], a[208:211], a[144:147], v[156:159]\n"
"v_mfma_f32_16x16x32_bf16 v[28:31], a[212:215], a[148:151], v[28:31]\n"
"v_mfma_f32_16x16x32_bf16 v[28:31], a[216:219], a[152:155], v[28:31]\n"
"v_mfma_f32_16x16x32_bf16 v[28:31], a[220:223], a[156:159], v[28:31]\n"
"ds_read_b128 v[160:163], v2 offset:8192\n"
"ds_read_b128 v[164:167], v2 offset:9216\n"
"ds_read_b128 v[168:171], v2 offset:10240\n"
"ds_read_b128 v[172:175], v2 offset:11264\n"
"ds_read_b128 v[176:179], v2 offset:12288\n"
"ds_read_b128 v[180:183], v2 offset:13312\n"
"ds_read_b128 v[184:187], v2 offset:14336\n"
"ds_read_b128 v[188:191], v2 offset:15360\n"
"v_mfma_f32_16x16x32_bf16 v[48:51], a[192:195], a[160:163], 0\n"
"s_and_b32 m0, s90, 0x1ffff\n"
"v_mfma_f32_16x16x32_bf16 v[48:51], a[196:199], a[164:167], v[48:51]\n"
"buffer_load_dwordx4 v3, s[52:55], s91 offen sc1 lds\n"
"s_add_u32 s90, s90, 0x4000\n"
"v_mfma_f32_16x16x32_bf16 v[48:51], a[200:203], a[168:171], v[48:51]\n"
"buffer_load_dwordx4 v3, s[52:55], s91 offen offset:1024 sc1 lds\n"
"s_add_u32 m0, m0, 0x2000\n"
"v_mfma_f32_16x16x32_bf16 v[48:51], a[204:207], a[172:175], v[48:51]\n"
"buffer_load_dwordx4 v4, s[52:55], s91 offen sc1 lds\n"
"v_mfma_f32_16x16x32_bf16 v[52:55], a[192:195], a[176:179], 0\n"
"buffer_load_dwordx4 v4, s[52:55], s91 offen offset:1024 sc1 lds\n"
"s_add_u32 s91, s91, 0x4000\n"
"v_mfma_f32_16x16x32_bf16 v[52:55], a[196:199], a[180:183], v[52:55]\n"
"v_max3_f32 v12, v16, v17, v18\n"
"v_max3_f32 v13, v19, v24, v25\n"
"v_max3_f32 v12, v12, v26, v27\n"
"v_max_f32 v12, v12, v13\n"
"v_mfma_f32_16x16x32_bf16 v[52:55], a[200:203], a[184:187], v[52:55]\n"
"v_mov_b32 v13, v12\n"
"s_nop 1\n"
"v_permlane32_swap_b32 v12, v13\n"
"s_nop 1\n"
"v_mfma_f32_16x16x32_bf16 v[52:55], a[204:207], a[188:191], v[52:55]\n"
"v_max_f32 v12, v12, v13\n"
"v_mov_b32 v13, v12\n"
"s_nop 1\n"
"v_permlane16_swap_b32 v12, v13\n"
"v_mfma_f32_16x16x32_bf16 v[56:59], a[208:211], a[160:163], 0\n"
"s_nop 1\n"
"v_max_f32 v12, v12, v13\n"
"v_sub_f32 v5, 0, v12\n"
"v_max3_f32 v14, v20, v21, v22\n"
"v_mfma_f32_16x16x32_bf16 v[56:59], a[212:215], a[164:167], v[56:59]\n"
"v_max3_f32 v15, v23, v28, v29\n"
"v_max3_f32 v14, v14, v30, v31\n"
"v_max_f32 v14, v14, v15\n"
"v_mov_b32 v15, v14\n"
"v_mfma_f32_16x16x32_bf16 v[56:59], a[216:219], a[168:171], v[56:59]\n"
"s_nop 1\n"
"v_permlane32_swap_b32 v14, v15\n"
"s_nop 1\n"
"v_max_f32 v14, v14, v15\n"
"v_mfma_f32_16x16x32_bf16 v[56:59], a[220:223], a[172:175], v[56:59]\n"
"v_mov_b32 v15, v14\n"
"s_nop 1\n"
"v_permlane16_swap_b32 v14, v15\n"
"s_nop 1\n"
"v_mfma_f32_16x16x32_bf16 v[60:63], a[208:211], a[176:179], 0\n"
"v_max_f32 v14, v14, v15\n"
"v_sub_f32 v6, 0, v14\n"
"v_add_f32 v16, v5, v16\n"
"v_add_f32 v17, v5, v17\n"
"v_mfma_f32_16x16x32_bf16 v[60:63], a[212:215], a[180:183], v[60:63]\n"
"v_add_f32 v18, v5, v18\n"
"v_add_f32 v19, v5, v19\n"
"v_add_f32 v24, v5, v24\n"
"v_add_f32 v25, v5, v25\n"
"v_mfma_f32_16x16x32_bf16 v[60:63], a[216:219], a[184:187], v[60:63]\n"
"v_add_f32 v26, v5, v26\n"
"v_add_f32 v27, v5, v27\n"
"v_mov_b32 v112, v5\n"
"v_mov_b32 v113, v5\n"
"v_mfma_f32_16x16x32_bf16 v[60:63], a[220:223], a[188:191], v[60:63]\n"
"v_mov_b32 v114, v5\n"
"v_mov_b32 v115, v5\n"
"v_add_f32 v20, v6, v20\n"
"v_add_f32 v21, v6, v21\n"
"v_add_f32 v22, v6, v22\n"
"v_add_f32 v23, v6, v23\n"
"v_add_f32 v28, v6, v28\n"
"v_add_f32 v29, v6, v29\n"
"v_add_f32 v30, v6, v30\n"
"v_add_f32 v31, v6, v31\n"
"v_mov_b32 v116, v6\n"
"v_mov_b32 v117, v6\n"
"v_mov_b32 v118, v6\n"
"v_mov_b32 v119, v6\n"
"v_add_u32 v2, 0x4000, v2\n"
"v_and_b32 v2, 0x1ffff, v2\n"
"s_nop 3\n"
"v_max3_f32 v12, v48, v49, v50\n"
"v_max3_f32 v13, v51, v56, v57\n"
"v_max3_f32 v12, v12, v58, v59\n"
"v_max_f32 v12, v12, v13\n"
"v_mov_b32 v13, v12\n"
"s_nop 1\n"
"v_permlane32_swap_b32 v12, v13\n"
"s_nop 1\n"
"v_max_f32 v12, v12, v13\n"
"v_mov_b32 v13, v12\n"
"s_nop 1\n"
"v_permlane16_swap_b32 v12, v13\n"
"s_nop 1\n"
"v_max_f32 v12, v12, v13\n"
"v_sub_f32 v7, 0, v12\n"
"v_max3_f32 v14, v52, v53, v54\n"
"v_max3_f32 v15, v55, v60, v61\n"
"v_max3_f32 v14, v14, v62, v63\n"
"v_max_f32 v14, v14, v15\n"
"v_mov_b32 v15, v14\n"
"s_nop 1\n"
"v_permlane32_swap_b32 v14, v15\n"
"s_nop 1\n"
"v_max_f32 v14, v14, v15\n"
"v_mov_b32 v15, v14\n"
"s_nop 1\n"
"v_permlane16_swap_b32 v14, v15\n"
"s_nop 1\n"
"v_max_f32 v14, v14, v15\n"
"v_sub_f32 v8, 0, v14\n"
"v_add_f32 v48, v7, v48\n"
"v_add_f32 v49, v7, v49\n"
"v_add_f32 v50, v7, v50\n"
"v_add_f32 v51, v7, v51\n"
"v_add_f32 v56, v7, v56\n"
"v_add_f32 v57, v7, v57\n"
"v_add_f32 v58, v7, v58\n"
"v_add_f32 v59, v7, v59\n"
"v_mov_b32 v120, v7\n"
"v_mov_b32 v121, v7\n"
"v_mov_b32 v122, v7\n"
"v_mov_b32 v123, v7\n"
"v_add_f32 v52, v8, v52\n"
"v_add_f32 v53, v8, v53\n"
"v_add_f32 v54, v8, v54\n"
"v_add_f32 v55, v8, v55\n"
"v_add_f32 v60, v8, v60\n"
"v_add_f32 v61, v8, v61\n"
"v_add_f32 v62, v8, v62\n"
"v_add_f32 v63, v8, v63\n"
"v_mov_b32 v124, v8\n"
"v_mov_b32 v125, v8\n"
"v_mov_b32 v126, v8\n"
"v_mov_b32 v127, v8\n"
"v_exp_f32 v16, v16\n"
"v_exp_f32 v17, v17\n"
"s_nop 0\n"
"v_cvt_pk_bf16_f32 v80, v16, v17\n"
"v_exp_f32 v18, v18\n"
"v_exp_f32 v19, v19\n"
"s_nop 0\n"
"v_cvt_pk_bf16_f32 v81, v18, v19\n"
"v_exp_f32 v20, v20\n"
"v_exp_f32 v21, v21\n"
"s_nop 0\n"
"v_cvt_pk_bf16_f32 v84, v20, v21\n"
"v_exp_f32 v22, v22\n"
"v_exp_f32 v23, v23\n"
"s_nop 0\n"
"v_cvt_pk_bf16_f32 v85, v22, v23\n"
"v_exp_f32 v24, v24\n"
"v_exp_f32 v25, v25\n"
"s_nop 0\n"
"v_cvt_pk_bf16_f32 v82, v24, v25\n"
"v_exp_f32 v26, v26\n"
"v_exp_f32 v27, v27\n"
"s_nop 0\n"
"v_cvt_pk_bf16_f32 v83, v26, v27\n"
"v_exp_f32 v48, v48\n"
"v_exp_f32 v49, v49\n"
"s_nop 0\n"
"v_cvt_pk_bf16_f32 v96, v48, v49\n"
"v_exp_f32 v50, v50\n"
"v_exp_f32 v51, v51\n"
"s_nop 0\n"
"v_cvt_pk_bf16_f32 v97, v50, v51\n"
"v_exp_f32 v56, v56\n"
"v_exp_f32 v57, v57\n"
"s_nop 0\n"
"v_cvt_pk_bf16_f32 v98, v56, v57\n"
"s_nop 1\n"
"s_cmp_eq_u32 s38, 0\n"
"s_cbranch_scc1 LA_j0\n"
"s_waitcnt lgkmcnt(8)\n"
"v_mfma_f32_16x16x32_bf16 v[32:35], a[224:227], a[128:131], v[112:115]\n"
"s_waitcnt vmcnt(12)\n"
"s_barrier\n"
"v_mfma_f32_16x16x32_bf16 v[32:35], a[228:231], a[132:135], v[32:35]\n"
"v_exp_f32 v28, v28\n"
"v_mfma_f32_16x16x32_bf16 v[32:35], a[232:235], a[136:139], v[32:35]\n"
"v_exp_f32 v29, v29\n"
"v_mfma_f32_16x16x32_bf16 v[32:35], a[236:239], a[140:143], v[32:35]\n"
"ds_read_b128 a[192:195], v2 offset:0\n"
"s_and_b32 m0, s90, 0x1ffff\n"
"v_mfma_f32_16x16x32_bf16 v[36:39], a[224:227], a[144:147], v[116:119]\n"
"ds_read_b128 a[196:199], v2 offset:1024\n"
"v_mfma_f32_16x16x32_bf16 v[36:39], a[228:231], a[148:151], v[36:39]\n"
"v_cvt_pk_bf16_f32 v86, v28, v29\n"
"v_mfma_f32_16x16x32_bf16 v[36:39], a[232:235], a[152:155], v[36:39]\n"
"v_exp_f32 v58, v58\n"
"v_mfma_f32_16x16x32_bf16 v[36:39], a[236:239], a[156:159], v[36:39]\n"
"v_exp_f32 v59, v59\n"
"v_mfma_f32_16x16x32_bf16 v[40:43], a[240:243], a[128:131], v[112:115]\n"
"v_exp_f32 v30, v30\n"
"v_mfma_f32_16x16x32_bf16 v[40:43], a[244:247], a[132:135], v[40:43]\n"
"ds_read_b128 a[200:203], v2 offset:2048\n"
"v_cvt_pk_bf16_f32 v99, v58, v59\n"
"v_mfma_f32_16x16x32_bf16 v[40:43], a[248:251], a[136:139], v[40:43]\n"
"v_exp_f32 v52, v52\n"
"v_mfma_f32_16x16x32_bf16 v[40:43], a[252:255], a[140:143], v[40:43]\n"
"v_exp_f32 v31, v31\n"
"v_mfma_f32_16x16x32_bf16 v[44:47], a[240:243], a[144:147], v[116:119]\n"
"ds_read_b128 a[204:207], v2 offset:3072\n"
"v_mfma_f32_16x16x32_bf16 v[44:47], a[244:247], a[148:151], v[44:47]\n"
"v_exp_f32 v53, v53\n"
"v_mfma_f32_16x16x32_bf16 v[44:47], a[248:251], a[152:155], v[44:47]\n"
"v_cvt_pk_bf16_f32 v87, v30, v31\n"
"v_cvt_pk_bf16_f32 v100, v52, v53\n"
"v_mfma_f32_16x16x32_bf16 v[44:47], a[252:255], a[156:159], v[44:47]\n"
"buffer_load_dwordx4 v3, s[52:55], s91 offen sc1 lds\n"
"v_mfma_f32_16x16x32_bf16 v[64:67], a[224:227], a[160:163], v[120:123]\n"
"ds_read_b128 a[208:211], v2 offset:4096\n"
"v_mfma_f32_16x16x32_bf16 v[64:67], a[228:231], a[164:167], v[64:67]\n"
"v_exp_f32 v54, v54\n"
"v_mfma_f32_16x16x32_bf16 v[64:67], a[232:235], a[168:171], v[64:67]\n"
"v_exp_f32 v32, v32\n"
"v_mfma_f32_16x16x32_bf16 v[64:67], a[236:239], a[172:175], v[64:67]\n"
"ds_read_b128 a[212:215], v2 offset:5120\n"
"s_add_u32 s90, s90, 0x4000\n"
"v_mfma_f32_16x16x32_bf16 v[68:71], a[224:227], a[176:179], v[124:127]\n"
"v_exp_f32 v55, v55\n"
"v_mfma_f32_16x16x32_bf16 v[68:71], a[228:231], a[180:183], v[68:71]\n"
"v_exp_f32 v33, v33\n"
"v_mfma_f32_16x16x32_bf16 v[68:71], a[232:235], a[184:187], v[68:71]\n"
"v_cvt_pk_bf16_f32 v101, v54, v55\n"
"v_cvt_pk_bf16_f32 v88, v32, v33\n"
"v_mfma_f32_16x16x32_bf16 v[68:71], a[236:239], a[188:191], v[68:71]\n"
"ds_read_b128 a[216:219], v2 offset:6144\n"
"v_mfma_f32_16x16x32_bf16 v[72:75], a[240:243], a[160:163], v[120:123]\n"
"v_mfma_f32_16x16x32_bf16 v[72:75], a[244:247], a[164:167], v[72:75]\n"
"v_exp_f32 v60, v60\n"
"v_mfma_f32_16x16x32_bf16 v[72:75], a[248:251], a[168:171], v[72:75]\n"
"v_exp_f32 v34, v34\n"
"v_mfma_f32_16x16x32_bf16 v[72:75], a[252:255], a[172:175], v[72:75]\n"
"ds_read_b128 a[220:223], v2 offset:7168\n"
"v_mfma_f32_16x16x32_bf16 v[76:79], a[240:243], a[176:179], v[124:127]\n"
"v_exp_f32 v61, v61\n"
"v_mfma_f32_16x16x32_bf16 v[76:79], a[244:247], a[180:183], v[76:79]\n"
"buffer_load_dwordx4 v3, s[52:55], s91 offen offset:1024 sc1 lds\n"
"v_mfma_f32_16x16x32_bf16 v[76:79], a[248:251], a[184:187], v[76:79]\n"
"v_exp_f32 v35, v35\n"
"v_mfma_f32_16x16x32_bf16 v[76:79], a[252:255], a[188:191], v[76:79]\n"
"s_waitcnt lgkmcnt(8)\n"
"v_cvt_pk_bf16_f32 v102, v60, v61\n"
"v_cvt_pk_bf16_f32 v89, v34, v35\n"
"v_mfma_f32_16x16x32_bf16 v[128:131], v[224:227], v[80:83], 0\n"
"ds_read_b128 v[192:195], v2 offset:8192\n"
"v_mfma_f32_16x16x32_bf16 a[0:3], v[160:163], v[80:83], 0\n"
"s_add_u32 m0, m0, 0x2000\n"
"v_mfma_f32_16x16x32_bf16 a[8:11], v[164:167], v[80:83], 0\n"
"v_exp_f32 v62, v62\n"
"v_mfma_f32_16x16x32_bf16 a[16:19], v[168:171], v[80:83], 0\n"
"v_exp_f32 v36, v36\n"
"v_mfma_f32_16x16x32_bf16 a[24:27], v[172:175], v[80:83], 0\n"
"v_exp_f32 v63, v63\n"
"v_mfma_f32_16x16x32_bf16 a[32:35], v[176:179], v[80:83], 0\n"
"ds_read_b128 v[196:199], v2 offset:9216\n"
"v_mfma_f32_16x16x32_bf16 a[40:43], v[180:183], v[80:83], 0\n"
"v_exp_f32 v37, v37\n"
"v_mfma_f32_16x16x32_bf16 a[48:51], v[184:187], v[80:83], 0\n"
"v_cvt_pk_bf16_f32 v103, v62, v63\n"
"v_cvt_pk_bf16_f32 v92, v36, v37\n"
"v_mfma_f32_16x16x32_bf16 a[56:59], v[188:191], v[80:83], 0\n"
"ds_read_b128 v[200:203], v2 offset:10240\n"
"v_mfma_f32_16x16x32_bf16 v[132:135], v[224:227], v[84:87], 0\n"
"buffer_load_dwordx4 v4, s[52:55], s91 offen sc1 lds\n"
"v_mfma_f32_16x16x32_bf16 a[4:7], v[160:163], v[84:87], 0\n"
"v_exp_f32 v38, v38\n"
"v_mfma_f32_16x16x32_bf16 a[12:15], v[164:167], v[84:87], 0\n"
"v_exp_f32 v64, v64\n"
"v_mfma_f32_16x16x32_bf16 a[20:23], v[168:171], v[84:87], 0\n"
"ds_read_b128 v[204:207], v2 offset:11264\n"
"v_mfma_f32_16x16x32_bf16 a[28:31], v[172:175], v[84:87], 0\n"
"v_exp_f32 v39, v39\n"
"v_mfma_f32_16x16x32_bf16 a[36:39], v[176:179], v[84:87], 0\n"
"v_exp_f32 v65, v65\n"
"v_mfma_f32_16x16x32_bf16 a[44:47], v[180:183], v[84:87], 0\n"
"v_mfma_f32_16x16x32_bf16 a[52:55], v[184:187], v[84:87], 0\n"
"v_cvt_pk_bf16_f32 v93, v38, v39\n"
"v_cvt_pk_bf16_f32 v104, v64, v65\n"
"v_mfma_f32_16x16x32_bf16 a[60:63], v[188:191], v[84:87], 0\n"
"ds_read_b128 v[208:211], v2 offset:12288\n"
"v_mfma_f32_16x16x32_bf16 v[136:139], v[224:227], v[96:99], 0\n"
"buffer_load_dwordx4 v4, s[52:55], s91 offen offset:1024 sc1 lds\n"
"v_mfma_f32_16x16x32_bf16 a[64:67], v[160:163], v[96:99], 0\n"
"v_exp_f32 v40, v40\n"
"v_mfma_f32_16x16x32_bf16 a[72:75], v[164:167], v[96:99], 0\n"
"v_exp_f32 v66, v66\n"
"v_mfma_f32_16x16x32_bf16 a[80:83], v[168:171], v[96:99], 0\n"
"ds_read_b128 v[212:215], v2 offset:13312\n"
"v_mfma_f32_16x16x32_bf16 a[88:91], v[172:175], v[96:99], 0\n"
"v_exp_f32 v41, v41\n"
"v_mfma_f32_16x16x32_bf16 a[96:99], v[176:179], v[96:99], 0\n"
"v_exp_f32 v67, v67\n"
"v_mfma_f32_16x16x32_bf16 a[104:107], v[180:183], v[96:99], 0\n"
"v_cvt_pk_bf16_f32 v90, v40, v41\n"
"v_cvt_pk_bf16_f32 v105, v66, v67\n"
"v_mfma_f32_16x16x32_bf16 a[112:115], v[184:187], v[96:99], 0\n"
"ds_read_b128 v[216:219], v2 offset:14336\n"
"s_add_u32 s91, s91, 0x4000\n"
"v_mfma_f32_16x16x32_bf16 a[120:123], v[188:191], v[96:99], 0\n"
"v_mfma_f32_16x16x32_bf16 v[140:143], v[224:227], v[100:103], 0\n"
"v_exp_f32 v42, v42\n"
"v_mfma_f32_16x16x32_bf16 a[68:71], v[160:163], v[100:103], 0\n"
"v_exp_f32 v72, v72\n"
"v_mfma_f32_16x16x32_bf16 a[76:79], v[164:167], v[100:103], 0\n"
"ds_read_b128 v[220:223], v2 offset:15360\n"
"v_mfma_f32_16x16x32_bf16 a[84:87], v[168:171], v[100:103], 0\n"
"v_exp_f32 v43, v43\n"
"v_mfma_f32_16x16x32_bf16 a[92:95], v[172:175], v[100:103], 0\n"
"v_exp_f32 v73, v73\n"
"v_mfma_f32_16x16x32_bf16 a[100:103], v[176:179], v[100:103], 0\n"
"v_mfma_f32_16x16x32_bf16 a[108:111], v[180:183], v[100:103], 0\n"
"v_cvt_pk_bf16_f32 v91, v42, v43\n"
"v_cvt_pk_bf16_f32 v106, v72, v73\n"
"v_mfma_f32_16x16x32_bf16 a[116:119], v[184:187], v[100:103], 0\n"
"v_mfma_f32_16x16x32_bf16 a[124:127], v[188:191], v[100:103], 0\n"
"v_add_u32 v2, 0x4000, v2\n"
"v_and_b32 v2, 0x1ffff, v2\n"
"s_lshl_b32 s85, s38, 1\n"
"s_sub_u32 s85, 1, s85\n"
"LA_dloop:\n"
"s_waitcnt lgkmcnt(8)\n"
"v_mfma_f32_16x16x32_bf16 v[16:19], a[192:195], a[128:131], v[112:115]\n"
"v_exp_f32 v44, v44\n"
"v_mfma_f32_16x16x32_bf16 v[16:19], a[196:199], a[132:135], v[16:19]\n"
"ds_read_b128 a[224:227], v2 offset:0\n"
"s_and_b32 m0, s90, 0x1ffff\n"
"v_mfma_f32_16x16x32_bf16 v[16:19], a[200:203], a[136:139], v[16:19]\n"
"v_exp_f32 v45, v45\n"
"v_mfma_f32_16x16x32_bf16 v[16:19], a[204:207], a[140:143], v[16:19]\n"
"ds_read_b128 a[228:231], v2 offset:1024\n"
"v_mfma_f32_16x16x32_bf16 v[20:23], a[192:195], a[144:147], v[116:119]\n"
"v_exp_f32 v74, v74\n"
"v_mfma_f32_16x16x32_bf16 v[20:23], a[196:199], a[148:151], v[20:23]\n"
"v_cvt_pk_bf16_f32 v94, v44, v45\n"
"v_mfma_f32_16x16x32_bf16 v[20:23], a[200:203], a[152:155], v[20:23]\n"
"v_exp_f32 v75, v75\n"
"v_mfma_f32_16x16x32_bf16 v[20:23], a[204:207], a[156:159], v[20:23]\n"
"ds_read_b128 a[232:235], v2 offset:2048\n"
"v_cvt_pk_bf16_f32 v107, v74, v75\n"
"v_mfma_f32_16x16x32_bf16 v[24:27], a[208:211], a[128:131], v[112:115]\n"
"v_exp_f32 v46, v46\n"
"v_mfma_f32_16x16x32_bf16 v[24:27], a[212:215], a[132:135], v[24:27]\n"
"v_exp_f32 v68, v68\n"
"v_mfma_f32_16x16x32_bf16 v[24:27], a[216:219], a[136:139], v[24:27]\n"
"buffer_load_dwordx4 v3, s[52:55], s91 offen sc1 lds\n"
"v_mfma_f32_16x16x32_bf16 v[24:27], a[220:223], a[140:143], v[24:27]\n"
"v_exp_f32 v47, v47\n"
"v_mfma_f32_16x16x32_bf16 v[28:31], a[208:211], a[144:147], v[116:119]\n"
"ds_read_b128 a[236:239], v2 offset:3072\n"
"v_mfma_f32_16x16x32_bf16 v[28:31], a[212:215], a[148:151], v[28:31]\n"
"v_exp_f32 v69, v69\n"
"v_mfma_f32_16x16x32_bf16 v[28:31], a[216:219], a[152:155], v[28:31]\n"
"v_cvt_pk_bf16_f32 v95, v46, v47\n"
"v_cvt_pk_bf16_f32 v108, v68, v69\n"
"v_mfma_f32_16x16x32_bf16 v[28:31], a[220:223], a[156:159], v[28:31]\n"
"ds_read_b128 a[240:243], v2 offset:4096\n"
"v_mfma_f32_16x16x32_bf16 v[48:51], a[192:195], a[160:163], v[120:123]\n"
"s_add_u32 s90, s90, 0x4000\n"
"v_mfma_f32_16x16x32_bf16 v[48:51], a[196:199], a[164:167], v[48:51]\n"
"v_exp_f32 v70, v70\n"
"v_mfma_f32_16x16x32_bf16 v[48:51], a[200:203], a[168:171], v[48:51]\n"
"v_exp_f32 v16, v16\n"
"v_mfma_f32_16x16x32_bf16 v[48:51], a[204:207], a[172:175], v[48:51]\n"
"ds_read_b128 a[244:247], v2 offset:5120\n"
"v_mfma_f32_16x16x32_bf16 v[52:55], a[192:195], a[176:179], v[124:127]\n"
"v_exp_f32 v71, v71\n"
"v_mfma_f32_16x16x32_bf16 v[52:55], a[196:199], a[180:183], v[52:55]\n"
"v_exp_f32 v17, v17\n"
"v_mfma_f32_16x16x32_bf16 v[52:55], a[200:203], a[184:187], v[52:55]\n"
"v_cvt_pk_bf16_f32 v109, v70, v71\n"
"v_cvt_pk_bf16_f32 v80, v16, v17\n"
"v_mfma_f32_16x16x32_bf16 v[52:55], a[204:207], a[188:191], v[52:55]\n"
"ds_read_b128 a[248:251], v2 offset:6144\n"
"v_mfma_f32_16x16x32_bf16 v[56:59], a[208:211], a[160:163], v[120:123]\n"
"buffer_load_dwordx4 v3, s[52:55], s91 offen offset:1024 sc1 lds\n"
"v_mfma_f32_16x16x32_bf16 v[56:59], a[212:215], a[164:167], v[56:59]\n"
"v_exp_f32 v76, v76\n"
"v_mfma_f32_16x16x32_bf16 v[56:59], a[216:219], a[168:171], v[56:59]\n"
"v_exp_f32 v18, v18\n"
"v_mfma_f32_16x16x32_bf16 v[56:59], a[220:223], a[172:175], v[56:59]\n"
"ds_read_b128 a[252:255], v2 offset:7168\n"
"v_mfma_f32_16x16x32_bf16 v[60:63], a[208:211], a[176:179], v[124:127]\n"
"v_exp_f32 v77, v77\n"
"v_mfma_f32_16x16x32_bf16 v[60:63], a[212:215], a[180:183], v[60:63]\n"
"v_exp_f32 v19, v19\n"
"v_mfma_f32_16x16x32_bf16 v[60:63], a[216:219], a[184:187], v[60:63]\n"
"v_mfma_f32_16x16x32_bf16 v[60:63], a[220:223], a[188:191], v[60:63]\n"
"s_waitcnt lgkmcnt(8)\n"
"v_cvt_pk_bf16_f32 v110, v76, v77\n"
"v_cvt_pk_bf16_f32 v81, v18, v19\n"
"v_mfma_f32_16x16x32_bf16 v[128:131], v[224:227], v[88:91], v[128:131]\n"
"ds_read_b128 v[160:163], v2 offset:8192\n"
"v_mfma_f32_16x16x32_bf16 a[0:3], v[192:195], v[88:91], a[0:3]\n"
"s_add_u32 m0, m0, 0x2000\n"
"v_mfma_f32_16x16x32_bf16 a[8:11], v[196:199], v[88:91], a[8:11]\n"
"v_exp_f32 v78, v78\n"
"v_mfma_f32_16x16x32_bf16 a[16:19], v[200:203], v[88:91], a[16:19]\n"
"v_exp_f32 v20, v20\n"
"v_mfma_f32_16x16x32_bf16 a[24:27], v[204:207], v[88:91], a[24:27]\n"
"v_exp_f32 v79, v79\n"
"v_mfma_f32_16x16x32_bf16 a[32:35], v[208:211], v[88:91], a[32:35]\n"
"ds_read_b128 v[164:167], v2 offset:9216\n"
"v_mfma_f32_16x16x32_bf16 a[40:43], v[212:215], v[88:91], a[40:43]\n"
"v_exp_f32 v21, v21\n"
"v_mfma_f32_16x16x32_bf16 a[48:51], v[216:219], v[88:91], a[48:51]\n"
"v_cvt_pk_bf16_f32 v111, v78, v79\n"
"v_cvt_pk_bf16_f32 v84, v20, v21\n"
"v_mfma_f32_16x16x32_bf16 a[56:59], v[220:223], v[88:91], a[56:59]\n"
"ds_read_b128 v[168:171], v2 offset:10240\n"
"v_mfma_f32_16x16x32_bf16 v[132:135], v[224:227], v[92:95], v[132:135]\n"
"buffer_load_dwordx4 v4, s[52:55], s91 offen sc1 lds\n"
"v_mfma_f32_16x16x32_bf16 a[4:7], v[192:195], v[92:95], a[4:7]\n"
"v_exp_f32 v22, v22\n"
"v_mfma_f32_16x16x32_bf16 a[12:15], v[196:199], v[92:95], a[12:15]\n"
"v_exp_f32 v48, v48\n"
"v_mfma_f32_16x16x32_bf16 a[20:23], v[200:203], v[92:95], a[20:23]\n"
"ds_read_b128 v[172:175], v2 offset:11264\n"
"v_mfma_f32_16x16x32_bf16 a[28:31], v[204:207], v[92:95], a[28:31]\n"
"v_exp_f32 v23, v23\n"
"v_mfma_f32_16x16x32_bf16 a[36:39], v[208:211], v[92:95], a[36:39]\n"
"v_exp_f32 v49, v49\n"
"v_mfma_f32_16x16x32_bf16 a[44:47], v[212:215], v[92:95], a[44:47]\n"
"v_mfma_f32_16x16x32_bf16 a[52:55], v[216:219], v[92:95], a[52:55]\n"
"v_cvt_pk_bf16_f32 v85, v22, v23\n"
"v_cvt_pk_bf16_f32 v96, v48, v49\n"
"v_mfma_f32_16x16x32_bf16 a[60:63], v[220:223], v[92:95], a[60:63]\n"
"ds_read_b128 v[176:179], v2 offset:12288\n"
"v_mfma_f32_16x16x32_bf16 v[136:139], v[224:227], v[104:107], v[136:139]\n"
"buffer_load_dwordx4 v4, s[52:55], s91 offen offset:1024 sc1 lds\n"
"v_mfma_f32_16x16x32_bf16 a[64:67], v[192:195], v[104:107], a[64:67]\n"
"v_exp_f32 v24, v24\n"
"v_mfma_f32_16x16x32_bf16 a[72:75], v[196:199], v[104:107], a[72:75]\n"
"v_exp_f32 v50, v50\n"
"v_mfma_f32_16x16x32_bf16 a[80:83], v[200:203], v[104:107], a[80:83]\n"
"ds_read_b128 v[180:183], v2 offset:13312\n"
"v_mfma_f32_16x16x32_bf16 a[88:91], v[204:207], v[104:107], a[88:91]\n"
"v_exp_f32 v25, v25\n"
"v_mfma_f32_16x16x32_bf16 a[96:99], v[208:211], v[104:107], a[96:99]\n"
"v_exp_f32 v51, v51\n"
"v_mfma_f32_16x16x32_bf16 a[104:107], v[212:215], v[104:107], a[104:107]\n"
"v_cvt_pk_bf16_f32 v82, v24, v25\n"
"v_cvt_pk_bf16_f32 v97, v50, v51\n"
"v_mfma_f32_16x16x32_bf16 a[112:115], v[216:219], v[104:107], a[112:115]\n"
"ds_read_b128 v[184:187], v2 offset:14336\n"
"s_add_u32 s91, s91, 0x4000\n"
"v_mfma_f32_16x16x32_bf16 a[120:123], v[220:223], v[104:107], a[120:123]\n"
"v_mfma_f32_16x16x32_bf16 v[140:143], v[224:227], v[108:111], v[140:143]\n"
"v_exp_f32 v26, v26\n"
"v_mfma_f32_16x16x32_bf16 a[68:71], v[192:195], v[108:111], a[68:71]\n"
"v_exp_f32 v56, v56\n"
"v_mfma_f32_16x16x32_bf16 a[76:79], v[196:199], v[108:111], a[76:79]\n"
"ds_read_b128 v[188:191], v2 offset:15360\n"
"v_mfma_f32_16x16x32_bf16 a[84:87], v[200:203], v[108:111], a[84:87]\n"
"v_exp_f32 v27, v27\n"
"v_mfma_f32_16x16x32_bf16 a[92:95], v[204:207], v[108:111], a[92:95]\n"
"v_exp_f32 v57, v57\n"
"v_mfma_f32_16x16x32_bf16 a[100:103], v[208:211], v[108:111], a[100:103]\n"
"v_mfma_f32_16x16x32_bf16 a[108:111], v[212:215], v[108:111], a[108:111]\n"
"v_cvt_pk_bf16_f32 v83, v26, v27\n"
"v_cvt_pk_bf16_f32 v98, v56, v57\n"
"v_mfma_f32_16x16x32_bf16 a[116:119], v[216:219], v[108:111], a[116:119]\n"
"v_mfma_f32_16x16x32_bf16 a[124:127], v[220:223], v[108:111], a[124:127]\n"
"v_add_u32 v2, 0x4000, v2\n"
"v_and_b32 v2, 0x1ffff, v2\n"
"s_waitcnt lgkmcnt(8)\n"
"v_mfma_f32_16x16x32_bf16 v[32:35], a[224:227], a[128:131], v[112:115]\n"
"s_waitcnt vmcnt(12)\n"
"s_barrier\n"
"v_mfma_f32_16x16x32_bf16 v[32:35], a[228:231], a[132:135], v[32:35]\n"
"v_exp_f32 v28, v28\n"
"v_mfma_f32_16x16x32_bf16 v[32:35], a[232:235], a[136:139], v[32:35]\n"
"v_exp_f32 v29, v29\n"
"v_mfma_f32_16x16x32_bf16 v[32:35], a[236:239], a[140:143], v[32:35]\n"
"ds_read_b128 a[192:195], v2 offset:0\n"
"s_and_b32 m0, s90, 0x1ffff\n"
"v_mfma_f32_16x16x32_bf16 v[36:39], a[224:227], a[144:147], v[116:119]\n"
"ds_read_b128 a[196:199], v2 offset:1024\n"
"v_mfma_f32_16x16x32_bf16 v[36:39], a[228:231], a[148:151], v[36:39]\n"
"v_cvt_pk_bf16_f32 v86, v28, v29\n"
"v_mfma_f32_16x16x32_bf16 v[36:39], a[232:235], a[152:155], v[36:39]\n"
"v_exp_f32 v58, v58\n"
"v_mfma_f32_16x16x32_bf16 v[36:39], a[236:239], a[156:159], v[36:39]\n"
"v_exp_f32 v59, v59\n"
"v_mfma_f32_16x16x32_bf16 v[40:43], a[240:243], a[128:131], v[112:115]\n"
"v_exp_f32 v30, v30\n"
"v_mfma_f32_16x16x32_bf16 v[40:43], a[244:247], a[132:135], v[40:43]\n"
"ds_read_b128 a[200:203], v2 offset:2048\n"
"v_cvt_pk_bf16_f32 v99, v58, v59\n"
"v_mfma_f32_16x16x32_bf16 v[40:43], a[248:251], a[136:139], v[40:43]\n"
"v_exp_f32 v52, v52\n"
"v_mfma_f32_16x16x32_bf16 v[40:43], a[252:255], a[140:143], v[40:43]\n"
"v_exp_f32 v31, v31\n"
"v_mfma_f32_16x16x32_bf16 v[44:47], a[240:243], a[144:147], v[116:119]\n"
"ds_read_b128 a[204:207], v2 offset:3072\n"
"v_mfma_f32_16x16x32_bf16 v[44:47], a[244:247], a[148:151], v[44:47]\n"
"v_exp_f32 v53, v53\n"
"v_mfma_f32_16x16x32_bf16 v[44:47], a[248:251], a[152:155], v[44:47]\n"
"v_cvt_pk_bf16_f32 v87, v30, v31\n"
"v_cvt_pk_bf16_f32 v100, v52, v53\n"
"v_mfma_f32_16x16x32_bf16 v[44:47], a[252:255], a[156:159], v[44:47]\n"
"buffer_load_dwordx4 v3, s[52:55], s91 offen sc1 lds\n"
"v_mfma_f32_16x16x32_bf16 v[64:67], a[224:227], a[160:163], v[120:123]\n"
"ds_read_b128 a[208:211], v2 offset:4096\n"
"s_add_u32 s90, s90, 0x4000\n"
"v_mfma_f32_16x16x32_bf16 v[64:67], a[228:231], a[164:167], v[64:67]\n"
"v_exp_f32 v54, v54\n"
"v_mfma_f32_16x16x32_bf16 v[64:67], a[232:235], a[168:171], v[64:67]\n"
"v_exp_f32 v32, v32\n"
"v_mfma_f32_16x16x32_bf16 v[64:67], a[236:239], a[172:175], v[64:67]\n"
"ds_read_b128 a[212:215], v2 offset:5120\n"
"v_mfma_f32_16x16x32_bf16 v[68:71], a[224:227], a[176:179], v[124:127]\n"
"v_exp_f32 v55, v55\n"
"v_mfma_f32_16x16x32_bf16 v[68:71], a[228:231], a[180:183], v[68:71]\n"
"v_exp_f32 v33, v33\n"
"v_mfma_f32_16x16x32_bf16 v[68:71], a[232:235], a[184:187], v[68:71]\n"
"v_cvt_pk_bf16_f32 v101, v54, v55\n"
"v_cvt_pk_bf16_f32 v88, v32, v33\n"
"v_mfma_f32_16x16x32_bf16 v[68:71], a[236:239], a[188:191], v[68:71]\n"
"ds_read_b128 a[216:219], v2 offset:6144\n"
"v_mfma_f32_16x16x32_bf16 v[72:75], a[240:243], a[160:163], v[120:123]\n"
"buffer_load_dwordx4 v3, s[52:55], s91 offen offset:1024 sc1 lds\n"
"v_mfma_f32_16x16x32_bf16 v[72:75], a[244:247], a[164:167], v[72:75]\n"
"v_exp_f32 v60, v60\n"
"v_mfma_f32_16x16x32_bf16 v[72:75], a[248:251], a[168:171], v[72:75]\n"
"v_exp_f32 v34, v34\n"
"v_mfma_f32_16x16x32_bf16 v[72:75], a[252:255], a[172:175], v[72:75]\n"
"ds_read_b128 a[220:223], v2 offset:7168\n"
"v_mfma_f32_16x16x32_bf16 v[76:79], a[240:243], a[176:179], v[124:127]\n"
"v_exp_f32 v61, v61\n"
"v_mfma_f32_16x16x32_bf16 v[76:79], a[244:247], a[180:183], v[76:79]\n"
"v_exp_f32 v35, v35\n"
"v_mfma_f32_16x16x32_bf16 v[76:79], a[248:251], a[184:187], v[76:79]\n"
"s_add_u32 m0, m0, 0x2000\n"
"v_mfma_f32_16x16x32_bf16 v[76:79], a[252:255], a[188:191], v[76:79]\n"
"s_waitcnt lgkmcnt(8)\n"
"v_cvt_pk_bf16_f32 v102, v60, v61\n"
"v_cvt_pk_bf16_f32 v89, v34, v35\n"
"v_mfma_f32_16x16x32_bf16 v[128:131], v[224:227], v[80:83], v[128:131]\n"
"ds_read_b128 v[192:195], v2 offset:8192\n"
"v_mfma_f32_16x16x32_bf16 a[0:3], v[160:163], v[80:83], a[0:3]\n"
"v_mfma_f32_16x16x32_bf16 a[8:11], v[164:167], v[80:83], a[8:11]\n"
"v_exp_f32 v62, v62\n"
"v_mfma_f32_16x16x32_bf16 a[16:19], v[168:171], v[80:83], a[16:19]\n"
"v_exp_f32 v36, v36\n"
"v_mfma_f32_16x16x32_bf16 a[24:27], v[172:175], v[80:83], a[24:27]\n"
"v_exp_f32 v63, v63\n"
"v_mfma_f32_16x16x32_bf16 a[32:35], v[176:179], v[80:83], a[32:35]\n"
"ds_read_b128 v[196:199], v2 offset:9216\n"
"v_mfma_f32_16x16x32_bf16 a[40:43], v[180:183], v[80:83], a[40:43]\n"
"buffer_load_dwordx4 v4, s[52:55], s91 offen sc1 lds\n"
"v_mfma_f32_16x16x32_bf16 a[48:51], v[184:187], v[80:83], a[48:51]\n"
"v_cvt_pk_bf16_f32 v103, v62, v63\n"
"v_mfma_f32_16x16x32_bf16 a[56:59], v[188:191], v[80:83], a[56:59]\n"
"ds_read_b128 v[200:203], v2 offset:10240\n"
"v_mfma_f32_16x16x32_bf16 v[132:135], v[224:227], v[84:87], v[132:135]\n"
"v_exp_f32 v37, v37\n"
"v_mfma_f32_16x16x32_bf16 a[4:7], v[160:163], v[84:87], a[4:7]\n"
"v_exp_f32 v64, v64\n"
"v_mfma_f32_16x16x32_bf16 a[12:15], v[164:167], v[84:87], a[12:15]\n"
"v_cvt_pk_bf16_f32 v92, v36, v37\n"
"v_mfma_f32_16x16x32_bf16 a[20:23], v[168:171], v[84:87], a[20:23]\n"
"v_exp_f32 v38, v38\n"
"v_mfma_f32_16x16x32_bf16 a[28:31], v[172:175], v[84:87], a[28:31]\n"
"ds_read_b128 v[204:207], v2 offset:11264\n"
"v_mfma_f32_16x16x32_bf16 a[36:39], v[176:179], v[84:87], a[36:39]\n"
"buffer_load_dwordx4 v4, s[52:55], s91 offen offset:1024 sc1 lds\n"
"v_mfma_f32_16x16x32_bf16 a[44:47], v[180:183], v[84:87], a[44:47]\n"
"v_exp_f32 v39, v39\n"
"v_mfma_f32_16x16x32_bf16 a[52:55], v[184:187], v[84:87], a[52:55]\n"
"v_exp_f32 v65, v65\n"
"v_mfma_f32_16x16x32_bf16 a[60:63], v[188:191], v[84:87], a[60:63]\n"
"ds_read_b128 v[208:211], v2 offset:12288\n"
"v_cvt_pk_bf16_f32 v93, v38, v39\n"
"v_mfma_f32_16x16x32_bf16 v[136:139], v[224:227], v[96:99], v[136:139]\n"
"v_cvt_pk_bf16_f32 v104, v64, v65\n"
"v_mfma_f32_16x16x32_bf16 a[64:67], v[160:163], v[96:99], a[64:67]\n"
"v_exp_f32 v40, v40\n"
"v_mfma_f32_16x16x32_bf16 a[72:75], v[164:167], v[96:99], a[72:75]\n"
"v_exp_f32 v66, v66\n"
"v_mfma_f32_16x16x32_bf16 a[80:83], v[168:171], v[96:99], a[80:83]\n"
"s_add_u32 s91, s91, 0x4000\n"
"ds_read_b128 v[212:215], v2 offset:13312\n"
"v_mfma_f32_16x16x32_bf16 a[88:91], v[172:175], v[96:99], a[88:91]\n"
"v_exp_f32 v41, v41\n"
"v_mfma_f32_16x16x32_bf16 a[96:99], v[176:179], v[96:99], a[96:99]\n"
"v_exp_f32 v67, v67\n"
"v_mfma_f32_16x16x32_bf16 a[104:107], v[180:183], v[96:99], a[104:107]\n"
"v_cvt_pk_bf16_f32 v90, v40, v41\n"
"v_cvt_pk_bf16_f32 v105, v66, v67\n"
"v_mfma_f32_16x16x32_bf16 a[112:115], v[184:187], v[96:99], a[112:115]\n"
"ds_read_b128 v[216:219], v2 offset:14336\n"
"v_mfma_f32_16x16x32_bf16 a[120:123], v[188:191], v[96:99], a[120:123]\n"
"v_mfma_f32_16x16x32_bf16 v[140:143], v[224:227], v[100:103], v[140:143]\n"
"v_exp_f32 v42, v42\n"
"v_mfma_f32_16x16x32_bf16 a[68:71], v[160:163], v[100:103], a[68:71]\n"
"v_exp_f32 v72, v72\n"
"v_mfma_f32_16x16x32_bf16 a[76:79], v[164:167], v[100:103], a[76:79]\n"
"ds_read_b128 v[220:223], v2 offset:15360\n"
"s_add_u32 s85, s85, 1\n"
"v_mfma_f32_16x16x32_bf16 a[84:87], v[168:171], v[100:103], a[84:87]\n"
"v_exp_f32 v43, v43\n"
"v_mfma_f32_16x16x32_bf16 a[92:95], v[172:175], v[100:103], a[92:95]\n"
"v_exp_f32 v73, v73\n"
"v_mfma_f32_16x16x32_bf16 a[100:103], v[176:179], v[100:103], a[100:103]\n"
"v_mfma_f32_16x16x32_bf16 a[108:111], v[180:183], v[100:103], a[108:111]\n"
"v_cvt_pk_bf16_f32 v91, v42, v43\n"
"v_cvt_pk_bf16_f32 v106, v72, v73\n"
"v_mfma_f32_16x16x32_bf16 a[116:119], v[184:187], v[100:103], a[116:119]\n"
"v_mfma_f32_16x16x32_bf16 a[124:127], v[188:191], v[100:103], a[124:127]\n"
"v_add_u32 v2, 0x4000, v2\n"
"v_and_b32 v2, 0x1ffff, v2\n"
"s_cbranch_scc0 LA_dloop\n"
"v_add_f32 v9, 0, v11\n"
"v_add_f32 v10, 0x6e800000, v11\n"
"v_add_f32 v144, 0, v9\n"
"v_min_f32 v144, v144, v5\n"
"v_add_f32 v145, 0xec800000, v9\n"
"v_min_f32 v145, v145, v5\n"
"v_add_f32 v146, 0xed000000, v9\n"
"v_min_f32 v146, v146, v5\n"
"v_add_f32 v147, 0xed400000, v9\n"
"v_min_f32 v147, v147, v5\n"
"v_add_f32 v148, 0, v10\n"
"v_min_f32 v148, v148, v6\n"
"v_add_f32 v149, 0xec800000, v10\n"
"v_min_f32 v149, v149, v6\n"
"v_add_f32 v150, 0xed000000, v10\n"
"v_min_f32 v150, v150, v6\n"
"v_add_f32 v151, 0xed400000, v10\n"
"v_min_f32 v151, v151, v6\n"
"v_add_f32 v152, 0xee800000, v9\n"
"v_min_f32 v152, v152, v5\n"
"v_add_f32 v153, 0xee880000, v9\n"
"v_min_f32 v153, v153, v5\n"
"v_add_f32 v154, 0xee900000, v9\n"
"v_min_f32 v154, v154, v5\n"
"v_add_f32 v155, 0xee980000, v9\n"
"v_min_f32 v155, v155, v5\n"
"v_add_f32 v156, 0xee800000, v10\n"
"v_min_f32 v156, v156, v6\n"
"v_add_f32 v157, 0xee880000, v10\n"
"v_min_f32 v157, v157, v6\n"
"v_add_f32 v158, 0xee900000, v10\n"
"v_min_f32 v158, v158, v6\n"
"v_add_f32 v159, 0xee980000, v10\n"
"v_min_f32 v159, v159, v6\n"
"v_add_f32 v9, 0xef000000, v9\n"
"v_add_f32 v10, 0xef000000, v10\n"
"s_nop 1\n"
"s_mov_b32 s85, -2\n"
"s_branch LA_dm\n"
"LA_j0:\n"
"v_add_f32 v9, 0xef000000, v11\n"
"v_add_f32 v10, 0xee800000, v11\n"
"v_add_f32 v144, 0, v9\n"
"v_min_f32 v144, v144, v5\n"
"v_add_f32 v145, 0xec800000, v9\n"
"v_min_f32 v145, v145, v5\n"
"v_add_f32 v146, 0xed000000, v9\n"
"v_min_f32 v146, v146, v5\n"
"v_add_f32 v147, 0xed400000, v9\n"
"v_min_f32 v147, v147, v5\n"
"v_add_f32 v148, 0, v10\n"
"v_min_f32 v148, v148, v6\n"
"v_add_f32 v149, 0xec800000, v10\n"
"v_min_f32 v149, v149, v6\n"
"v_add_f32 v150, 0xed000000, v10\n"
"v_min_f32 v150, v150, v6\n"
"v_add_f32 v151, 0xed400000, v10\n"
"v_min_f32 v151, v151, v6\n"
"v_add_f32 v152, 0xee800000, v9\n"
"v_min_f32 v152, v152, v5\n"
"v_add_f32 v153, 0xee880000, v9\n"
"v_min_f32 v153, v153, v5\n"
"v_add_f32 v154, 0xee900000, v9\n"
"v_min_f32 v154, v154, v5\n"
"v_add_f32 v155, 0xee980000, v9\n"
"v_min_f32 v155, v155, v5\n"
"v_add_f32 v156, 0xee800000, v10\n"
"v_min_f32 v156, v156, v6\n"
"v_add_f32 v157, 0xee880000, v10\n"
"v_min_f32 v157, v157, v6\n"
"v_add_f32 v158, 0xee900000, v10\n"
"v_min_f32 v158, v158, v6\n"
"v_add_f32 v159, 0xee980000, v10\n"
"v_min_f32 v159, v159, v6\n"
"v_add_f32 v9, 0xef000000, v9\n"
"v_add_f32 v10, 0xef000000, v10\n"
"s_nop 1\n"
"s_waitcnt lgkmcnt(8)\n"
"v_mfma_f32_16x16x32_bf16 v[32:35], a[224:227], a[128:131], v[144:147]\n"
"s_waitcnt vmcnt(12)\n"
"s_barrier\n"
"v_mfma_f32_16x16x32_bf16 v[32:35], a[228:231], a[132:135], v[32:35]\n"
"ds_read_b128 a[192:195], v2 offset:0\n"
"v_exp_f32 v28, v28\n"
"v_mfma_f32_16x16x32_bf16 v[32:35], a[232:235], a[136:139], v[32:35]\n"
"s_and_b32 m0, s90, 0x1ffff\n"
"v_exp_f32 v29, v29\n"
"v_mfma_f32_16x16x32_bf16 v[32:35], a[236:239], a[140:143], v[32:35]\n"
"v_exp_f32 v58, v58\n"
"v_mfma_f32_16x16x32_bf16 v[36:39], a[224:227], a[144:147], v[148:151]\n"
"ds_read_b128 a[196:199], v2 offset:1024\n"
"v_exp_f32 v59, v59\n"
"v_mfma_f32_16x16x32_bf16 v[36:39], a[228:231], a[148:151], v[36:39]\n"
"v_cvt_pk_bf16_f32 v86, v28, v29\n"
"v_cvt_pk_bf16_f32 v99, v58, v59\n"
"v_mfma_f32_16x16x32_bf16 v[36:39], a[232:235], a[152:155], v[36:39]\n"
"v_mfma_f32_16x16x32_bf16 v[36:39], a[236:239], a[156:159], v[36:39]\n"
"v_mfma_f32_16x16x32_bf16 v[40:43], a[240:243], a[128:131], v[152:155]\n"
"ds_read_b128 a[200:203], v2 offset:2048\n"
"v_exp_f32 v30, v30\n"
"v_mfma_f32_16x16x32_bf16 v[40:43], a[244:247], a[132:135], v[40:43]\n"
"v_exp_f32 v52, v52\n"
"v_mfma_f32_16x16x32_bf16 v[40:43], a[248:251], a[136:139], v[40:43]\n"
"buffer_load_dwordx4 v3, s[52:55], s91 offen sc1 lds\n"
"v_mfma_f32_16x16x32_bf16 v[40:43], a[252:255], a[140:143], v[40:43]\n"
"v_exp_f32 v31, v31\n"
"v_mfma_f32_16x16x32_bf16 v[44:47], a[240:243], a[144:147], v[156:159]\n"
"ds_read_b128 a[204:207], v2 offset:3072\n"
"v_exp_f32 v53, v53\n"
"v_mfma_f32_16x16x32_bf16 v[44:47], a[244:247], a[148:151], v[44:47]\n"
"v_mfma_f32_16x16x32_bf16 v[44:47], a[248:251], a[152:155], v[44:47]\n"
"v_cvt_pk_bf16_f32 v87, v30, v31\n"
"v_cvt_pk_bf16_f32 v100, v52, v53\n"
"v_mfma_f32_16x16x32_bf16 v[44:47], a[252:255], a[156:159], v[44:47]\n"
"v_mfma_f32_16x16x32_bf16 v[64:67], a[224:227], a[160:163], v[120:123]\n"
"ds_read_b128 a[208:211], v2 offset:4096\n"
"v_mfma_f32_16x16x32_bf16 v[64:67], a[228:231], a[164:167], v[64:67]\n"
"s_add_u32 s90, s90, 0x4000\n"
"v_exp_f32 v54, v54\n"
"v_mfma_f32_16x16x32_bf16 v[64:67], a[232:235], a[168:171], v[64:67]\n"
"v_exp_f32 v32, v32\n"
"v_mfma_f32_16x16x32_bf16 v[64:67], a[236:239], a[172:175], v[64:67]\n"
"ds_read_b128 a[212:215], v2 offset:5120\n"
"v_exp_f32 v55, v55\n"
"v_mfma_f32_16x16x32_bf16 v[68:71], a[224:227], a[176:179], v[124:127]\n"
"v_exp_f32 v33, v33\n"
"v_add_f32 v144, 0, v9\n"
"v_mfma_f32_16x16x32_bf16 v[68:71], a[228:231], a[180:183], v[68:71]\n"
"v_min_f32 v144, v144, v5\n"
"v_add_f32 v145, 0xec800000, v9\n"
"v_mfma_f32_16x16x32_bf16 v[68:71], a[232:235], a[184:187], v[68:71]\n"
"v_cvt_pk_bf16_f32 v101, v54, v55\n"
"v_cvt_pk_bf16_f32 v88, v32, v33\n"
"v_mfma_f32_16x16x32_bf16 v[68:71], a[236:239], a[188:191], v[68:71]\n"
"ds_read_b128 a[216:219], v2 offset:6144\n"
"v_min_f32 v145, v145, v5\n"
"v_mfma_f32_16x16x32_bf16 v[72:75], a[240:243], a[160:163], v[120:123]\n"
"v_add_f32 v146, 0xed000000, v9\n"
"v_mfma_f32_16x16x32_bf16 v[72:75], a[244:247], a[164:167], v[72:75]\n"
"v_exp_f32 v60, v60\n"
"v_mfma_f32_16x16x32_bf16 v[72:75], a[248:251], a[168:171], v[72:75]\n"
"v_exp_f32 v34, v34\n"
"v_min_f32 v146, v146, v5\n"
"v_mfma_f32_16x16x32_bf16 v[72:75], a[252:255], a[172:175], v[72:75]\n"
"ds_read_b128 a[220:223], v2 offset:7168\n"
"buffer_load_dwordx4 v3, s[52:55], s91 offen offset:1024 sc1 lds\n"
"v_mfma_f32_16x16x32_bf16 v[76:79], a[240:243], a[176:179], v[124:127]\n"
"v_exp_f32 v61, v61\n"
"v_add_f32 v147, 0xed400000, v9\n"
"v_mfma_f32_16x16x32_bf16 v[76:79], a[244:247], a[180:183], v[76:79]\n"
"v_exp_f32 v35, v35\n"
"v_min_f32 v147, v147, v5\n"
"v_mfma_f32_16x16x32_bf16 v[76:79], a[248:251], a[184:187], v[76:79]\n"
"v_add_f32 v148, 0, v10\n"
"v_mfma_f32_16x16x32_bf16 v[76:79], a[252:255], a[188:191], v[76:79]\n"
"s_waitcnt lgkmcnt(8)\n"
"v_cvt_pk_bf16_f32 v102, v60, v61\n"
"v_cvt_pk_bf16_f32 v89, v34, v35\n"
"v_min_f32 v148, v148, v6\n"
"v_mfma_f32_16x16x32_bf16 v[128:131], v[224:227], v[80:83], 0\n"
"ds_read_b128 v[192:195], v2 offset:8192\n"
"v_mfma_f32_16x16x32_bf16 a[0:3], v[160:163], v[80:83], 0\n"
"s_add_u32 m0, m0, 0x2000\n"
"v_add_f32 v149, 0xec800000, v10\n"
"v_mfma_f32_16x16x32_bf16 a[8:11], v[164:167], v[80:83], 0\n"
"v_exp_f32 v62, v62\n"
"v_min_f32 v149, v149, v6\n"
"v_mfma_f32_16x16x32_bf16 a[16:19], v[168:171], v[80:83], 0\n"
"v_exp_f32 v36, v36\n"
"v_mfma_f32_16x16x32_bf16 a[24:27], v[172:175], v[80:83], 0\n"
"ds_read_b128 v[196:199], v2 offset:9216\n"
"v_exp_f32 v63, v63\n"
"v_mfma_f32_16x16x32_bf16 a[32:35], v[176:179], v[80:83], 0\n"
"v_exp_f32 v37, v37\n"
"v_add_f32 v150, 0xed000000, v10\n"
"v_mfma_f32_16x16x32_bf16 a[40:43], v[180:183], v[80:83], 0\n"
"v_min_f32 v150, v150, v6\n"
"v_mfma_f32_16x16x32_bf16 a[48:51], v[184:187], v[80:83], 0\n"
"v_cvt_pk_bf16_f32 v103, v62, v63\n"
"v_add_f32 v151, 0xed400000, v10\n"
"v_cvt_pk_bf16_f32 v92, v36, v37\n"
"v_mfma_f32_16x16x32_bf16 a[56:59], v[188:191], v[80:83], 0\n"
"ds_read_b128 v[200:203], v2 offset:10240\n"
"v_min_f32 v151, v151, v6\n"
"v_mfma_f32_16x16x32_bf16 v[132:135], v[224:227], v[84:87], 0\n"
"buffer_load_dwordx4 v4, s[52:55], s91 offen sc1 lds\n"
"v_mfma_f32_16x16x32_bf16 a[4:7], v[160:163], v[84:87], 0\n"
"v_exp_f32 v38, v38\n"
"v_add_f32 v152, 0xee800000, v9\n"
"v_mfma_f32_16x16x32_bf16 a[12:15], v[164:167], v[84:87], 0\n"
"v_exp_f32 v64, v64\n"
"v_min_f32 v152, v152, v5\n"
"v_mfma_f32_16x16x32_bf16 a[20:23], v[168:171], v[84:87], 0\n"
"ds_read_b128 v[204:207], v2 offset:11264\n"
"v_add_f32 v153, 0xee880000, v9\n"
"v_mfma_f32_16x16x32_bf16 a[28:31], v[172:175], v[84:87], 0\n"
"v_exp_f32 v39, v39\n"
"v_mfma_f32_16x16x32_bf16 a[36:39], v[176:179], v[84:87], 0\n"
"v_exp_f32 v65, v65\n"
"v_min_f32 v153, v153, v5\n"
"v_mfma_f32_16x16x32_bf16 a[44:47], v[180:183], v[84:87], 0\n"
"v_add_f32 v154, 0xee900000, v9\n"
"v_mfma_f32_16x16x32_bf16 a[52:55], v[184:187], v[84:87], 0\n"
"v_cvt_pk_bf16_f32 v93, v38, v39\n"
"v_cvt_pk_bf16_f32 v104, v64, v65\n"
"v_mfma_f32_16x16x32_bf16 a[60:63], v[188:191], v[84:87], 0\n"
"ds_read_b128 v[208:211], v2 offset:12288\n"
"buffer_load_dwordx4 v4, s[52:55], s91 offen offset:1024 sc1 lds\n"
"v_mfma_f32_16x16x32_bf16 v[136:139], v[224:227], v[96:99], 0\n"
"v_min_f32 v154, v154, v5\n"
"v_add_f32 v155, 0xee980000, v9\n"
"v_mfma_f32_16x16x32_bf16 a[64:67], v[160:163], v[96:99], 0\n"
"v_exp_f32 v40, v40\n"
"v_mfma_f32_16x16x32_bf16 a[72:75], v[164:167], v[96:99], 0\n"
"v_exp_f32 v66, v66\n"
"v_min_f32 v155, v155, v5\n"
"v_mfma_f32_16x16x32_bf16 a[80:83], v[168:171], v[96:99], 0\n"
"ds_read_b128 v[212:215], v2 offset:13312\n"
"v_exp_f32 v41, v41\n"
"v_mfma_f32_16x16x32_bf16 a[88:91], v[172:175], v[96:99], 0\n"
"v_exp_f32 v67, v67\n"
"v_add_f32 v156, 0xee800000, v10\n"
"v_mfma_f32_16x16x32_bf16 a[96:99], v[176:179], v[96:99], 0\n"
"v_min_f32 v156, v156, v6\n"
"v_mfma_f32_16x16x32_bf16 a[104:107], v[180:183], v[96:99], 0\n"
"v_add_f32 v157, 0xee880000, v10\n"
"v_cvt_pk_bf16_f32 v90, v40, v41\n"
"v_cvt_pk_bf16_f32 v105, v66, v67\n"
"v_mfma_f32_16x16x32_bf16 a[112:115], v[184:187], v[96:99], 0\n"
"ds_read_b128 v[216:219], v2 offset:14336\n"
"s_add_u32 s91, s91, 0x4000\n"
"v_min_f32 v157, v157, v6\n"
"v_mfma_f32_16x16x32_bf16 a[120:123], v[188:191], v[96:99], 0\n"
"v_mfma_f32_16x16x32_bf16 v[140:143], v[224:227], v[100:103], 0\n"
"v_exp_f32 v42, v42\n"
"v_add_f32 v158, 0xee900000, v10\n"
"v_mfma_f32_16x16x32_bf16 a[68:71], v[160:163], v[100:103], 0\n"
"v_exp_f32 v72, v72\n"
"v_min_f32 v158, v158, v6\n"
"v_mfma_f32_16x16x32_bf16 a[76:79], v[164:167], v[100:103], 0\n"
"ds_read_b128 v[220:223], v2 offset:15360\n"
"v_mfma_f32_16x16x32_bf16 a[84:87], v[168:171], v[100:103], 0\n"
"v_exp_f32 v43, v43\n"
"v_add_f32 v159, 0xee980000, v10\n"
"v_mfma_f32_16x16x32_bf16 a[92:95], v[172:175], v[100:103], 0\n"
"v_exp_f32 v73, v73\n"
"v_min_f32 v159, v159, v6\n"
"v_mfma_f32_16x16x32_bf16 a[100:103], v[176:179], v[100:103], 0\n"
"v_mfma_f32_16x16x32_bf16 a[108:111], v[180:183], v[100:103], 0\n"
"v_add_f32 v9, 0xef000000, v9\n"
"v_cvt_pk_bf16_f32 v91, v42, v43\n"
"v_cvt_pk_bf16_f32 v106, v72, v73\n"
"v_mfma_f32_16x16x32_bf16 a[116:119], v[184:187], v[100:103], 0\n"
"v_add_f32 v10, 0xef000000, v10\n"
"v_mfma_f32_16x16x32_bf16 a[124:127], v[188:191], v[100:103], 0\n"
"v_add_u32 v2, 0x4000, v2\n"
"v_and_b32 v2, 0x1ffff, v2\n"
"s_mov_b32 s85, -1\n"
"LA_dm:\n"
"s_waitcnt lgkmcnt(8)\n"
"v_mfma_f32_16x16x32_bf16 v[16:19], a[192:195], a[128:131], v[144:147]\n"
"ds_read_b128 a[224:227], v2 offset:0\n"
"v_exp_f32 v44, v44\n"
"v_mfma_f32_16x16x32_bf16 v[16:19], a[196:199], a[132:135], v[16:19]\n"
"s_and_b32 m0, s90, 0x1ffff\n"
"v_exp_f32 v74, v74\n"
"v_mfma_f32_16x16x32_bf16 v[16:19], a[200:203], a[136:139], v[16:19]\n"
"v_exp_f32 v45, v45\n"
"v_mfma_f32_16x16x32_bf16 v[16:19], a[204:207], a[140:143], v[16:19]\n"
"ds_read_b128 a[228:231], v2 offset:1024\n"
"v_exp_f32 v75, v75\n"
"v_mfma_f32_16x16x32_bf16 v[20:23], a[192:195], a[144:147], v[148:151]\n"
"v_mfma_f32_16x16x32_bf16 v[20:23], a[196:199], a[148:151], v[20:23]\n"
"v_cvt_pk_bf16_f32 v94, v44, v45\n"
"v_cvt_pk_bf16_f32 v107, v74, v75\n"
"v_mfma_f32_16x16x32_bf16 v[20:23], a[200:203], a[152:155], v[20:23]\n"
"v_mfma_f32_16x16x32_bf16 v[20:23], a[204:207], a[156:159], v[20:23]\n"
"ds_read_b128 a[232:235], v2 offset:2048\n"
"v_mfma_f32_16x16x32_bf16 v[24:27], a[208:211], a[128:131], v[152:155]\n"
"v_exp_f32 v46, v46\n"
"v_mfma_f32_16x16x32_bf16 v[24:27], a[212:215], a[132:135], v[24:27]\n"
"v_exp_f32 v68, v68\n"
"v_mfma_f32_16x16x32_bf16 v[24:27], a[216:219], a[136:139], v[24:27]\n"
"buffer_load_dwordx4 v3, s[52:55], s91 offen sc1 lds\n"
"v_mfma_f32_16x16x32_bf16 v[24:27], a[220:223], a[140:143], v[24:27]\n"
"ds_read_b128 a[236:239], v2 offset:3072\n"
"v_exp_f32 v47, v47\n"
"v_mfma_f32_16x16x32_bf16 v[28:31], a[208:211], a[144:147], v[156:159]\n"
"v_exp_f32 v69, v69\n"
"v_mfma_f32_16x16x32_bf16 v[28:31], a[212:215], a[148:151], v[28:31]\n"
"v_mfma_f32_16x16x32_bf16 v[28:31], a[216:219], a[152:155], v[28:31]\n"
"v_cvt_pk_bf16_f32 v95, v46, v47\n"
"v_cvt_pk_bf16_f32 v108, v68, v69\n"
"v_mfma_f32_16x16x32_bf16 v[28:31], a[220:223], a[156:159], v[28:31]\n"
"ds_read_b128 a[240:243], v2 offset:4096\n"
"v_mfma_f32_16x16x32_bf16 v[48:51], a[192:195], a[160:163], v[120:123]\n"
"s_add_u32 s90, s90, 0x4000\n"
"v_mfma_f32_16x16x32_bf16 v[48:51], a[196:199], a[164:167], v[48:51]\n"
"v_exp_f32 v70, v70\n"
"v_mfma_f32_16x16x32_bf16 v[48:51], a[200:203], a[168:171], v[48:51]\n"
"v_exp_f32 v16, v16\n"
"v_mfma_f32_16x16x32_bf16 v[48:51], a[204:207], a[172:175], v[48:51]\n"
"ds_read_b128 a[244:247], v2 offset:5120\n"
"v_exp_f32 v71, v71\n"
"v_mfma_f32_16x16x32_bf16 v[52:55], a[192:195], a[176:179], v[124:127]\n"
"v_exp_f32 v17, v17\n"
"v_add_f32 v144, 0, v9\n"
"v_mfma_f32_16x16x32_bf16 v[52:55], a[196:199], a[180:183], v[52:55]\n"
"v_min_f32 v144, v144, v5\n"
"v_add_f32 v145, 0xec800000, v9\n"
"v_mfma_f32_16x16x32_bf16 v[52:55], a[200:203], a[184:187], v[52:55]\n"
"v_cvt_pk_bf16_f32 v109, v70, v71\n"
"v_cvt_pk_bf16_f32 v80, v16, v17\n"
"v_mfma_f32_16x16x32_bf16 v[52:55], a[204:207], a[188:191], v[52:55]\n"
"ds_read_b128 a[248:251], v2 offset:6144\n"
"v_min_f32 v145, v145, v5\n"
"v_mfma_f32_16x16x32_bf16 v[56:59], a[208:211], a[160:163], v[120:123]\n"
"buffer_load_dwordx4 v3, s[52:55], s91 offen offset:1024 sc1 lds\n"
"v_add_f32 v146, 0xed000000, v9\n"
"v_mfma_f32_16x16x32_bf16 v[56:59], a[212:215], a[164:167], v[56:59]\n"
"v_exp_f32 v76, v76\n"
"v_mfma_f32_16x16x32_bf16 v[56:59], a[216:219], a[168:171], v[56:59]\n"
"v_exp_f32 v18, v18\n"
"v_min_f32 v146, v146, v5\n"
"v_mfma_f32_16x16x32_bf16 v[56:59], a[220:223], a[172:175], v[56:59]\n"
"ds_read_b128 a[252:255], v2 offset:7168\n"
"v_add_f32 v147, 0xed400000, v9\n"
"v_mfma_f32_16x16x32_bf16 v[60:63], a[208:211], a[176:179], v[124:127]\n"
"v_exp_f32 v77, v77\n"
"v_mfma_f32_16x16x32_bf16 v[60:63], a[212:215], a[180:183], v[60:63]\n"
"v_exp_f32 v19, v19\n"
"v_min_f32 v147, v147, v5\n"
"v_mfma_f32_16x16x32_bf16 v[60:63], a[216:219], a[184:187], v[60:63]\n"
"v_add_f32 v148, 0, v10\n"
"v_mfma_f32_16x16x32_bf16 v[60:63], a[220:223], a[188:191], v[60:63]\n"
"s_waitcnt lgkmcnt(8)\n"
"v_cvt_pk_bf16_f32 v110, v76, v77\n"
"v_cvt_pk_bf16_f32 v81, v18, v19\n"
"v_min_f32 v148, v148, v6\n"
"v_mfma_f32_16x16x32_bf16 v[128:131], v[224:227], v[88:91], v[128:131]\n"
"ds_read_b128 v[160:163], v2 offset:8192\n"
"v_mfma_f32_16x16x32_bf16 a[0:3], v[192:195], v[88:91], a[0:3]\n"
"s_add_u32 m0, m0, 0x2000\n"
"v_add_f32 v149, 0xec800000, v10\n"
"v_mfma_f32_16x16x32_bf16 a[8:11], v[196:199], v[88:91], a[8:11]\n"
"v_exp_f32 v78, v78\n"
"v_min_f32 v149, v149, v6\n"
"v_mfma_f32_16x16x32_bf16 a[16:19], v[200:203], v[88:91], a[16:19]\n"
"v_exp_f32 v20, v20\n"
"v_mfma_f32_16x16x32_bf16 a[24:27], v[204:207], v[88:91], a[24:27]\n"
"ds_read_b128 v[164:167], v2 offset:9216\n"
"v_exp_f32 v79, v79\n"
"v_mfma_f32_16x16x32_bf16 a[32:35], v[208:211], v[88:91], a[32:35]\n"
"v_exp_f32 v21, v21\n"
"v_add_f32 v150, 0xed000000, v10\n"
"v_mfma_f32_16x16x32_bf16 a[40:43], v[212:215], v[88:91], a[40:43]\n"
"v_min_f32 v150, v150, v6\n"
"v_mfma_f32_16x16x32_bf16 a[48:51], v[216:219], v[88:91], a[48:51]\n"
"v_cvt_pk_bf16_f32 v111, v78, v79\n"
"v_add_f32 v151, 0xed400000, v10\n"
"v_cvt_pk_bf16_f32 v84, v20, v21\n"
"v_mfma_f32_16x16x32_bf16 a[56:59], v[220:223], v[88:91], a[56:59]\n"
"ds_read_b128 v[168:171], v2 offset:10240\n"
"v_min_f32 v151, v151, v6\n"
"v_mfma_f32_16x16x32_bf16 v[132:135], v[224:227], v[92:95], v[132:135]\n"
"buffer_load_dwordx4 v4, s[52:55], s91 offen sc1 lds\n"
"v_mfma_f32_16x16x32_bf16 a[4:7], v[192:195], v[92:95], a[4:7]\n"
"v_exp_f32 v22, v22\n"
"v_add_f32 v152, 0xee800000, v9\n"
"v_mfma_f32_16x16x32_bf16 a[12:15], v[196:199], v[92:95], a[12:15]\n"
"v_exp_f32 v48, v48\n"
"v_min_f32 v152, v152, v5\n"
"v_mfma_f32_16x16x32_bf16 a[20:23], v[200:203], v[92:95], a[20:23]\n"
"ds_read_b128 v[172:175], v2 offset:11264\n"
"v_add_f32 v153, 0xee880000, v9\n"
"v_mfma_f32_16x16x32_bf16 a[28:31], v[204:207], v[92:95], a[28:31]\n"
"v_exp_f32 v23, v23\n"
"v_mfma_f32_16x16x32_bf16 a[36:39], v[208:211], v[92:95], a[36:39]\n"
"v_exp_f32 v49, v49\n"
"v_min_f32 v153, v153, v5\n"
"v_mfma_f32_16x16x32_bf16 a[44:47], v[212:215], v[92:95], a[44:47]\n"
"v_add_f32 v154, 0xee900000, v9\n"
"v_mfma_f32_16x16x32_bf16 a[52:55], v[216:219], v[92:95], a[52:55]\n"
"v_cvt_pk_bf16_f32 v85, v22, v23\n"
"v_cvt_pk_bf16_f32 v96, v48, v49\n"
"v_mfma_f32_16x16x32_bf16 a[60:63], v[220:223], v[92:95], a[60:63]\n"
"ds_read_b128 v[176:179], v2 offset:12288\n"
"buffer_load_dwordx4 v4, s[52:55], s91 offen offset:1024 sc1 lds\n"
"v_mfma_f32_16x16x32_bf16 v[136:139], v[224:227], v[104:107], v[136:139]\n"
"v_min_f32 v154, v154, v5\n"
"v_add_f32 v155, 0xee980000, v9\n"
"v_mfma_f32_16x16x32_bf16 a[64:67], v[192:195], v[104:107], a[64:67]\n"
"v_exp_f32 v24, v24\n"
"v_mfma_f32_16x16x32_bf16 a[72:75], v[196:199], v[104:107], a[72:75]\n"
"v_exp_f32 v50, v50\n"
"v_min_f32 v155, v155, v5\n"
"v_mfma_f32_16x16x32_bf16 a[80:83], v[200:203], v[104:107], a[80:83]\n"
"ds_read_b128 v[180:183], v2 offset:13312\n"
"v_exp_f32 v25, v25\n"
"v_mfma_f32_16x16x32_bf16 a[88:91], v[204:207], v[104:107], a[88:91]\n"
"v_exp_f32 v51, v51\n"
"v_add_f32 v156, 0xee800000, v10\n"
"v_mfma_f32_16x16x32_bf16 a[96:99], v[208:211], v[104:107], a[96:99]\n"
"v_min_f32 v156, v156, v6\n"
"v_mfma_f32_16x16x32_bf16 a[104:107], v[212:215], v[104:107], a[104:107]\n"
"v_add_f32 v157, 0xee880000, v10\n"
"v_cvt_pk_bf16_f32 v82, v24, v25\n"
"v_cvt_pk_bf16_f32 v97, v50, v51\n"
"v_mfma_f32_16x16x32_bf16 a[112:115], v[216:219], v[104:107], a[112:115]\n"
"ds_read_b128 v[184:187], v2 offset:14336\n"
"s_add_u32 s91, s91, 0x4000\n"
"v_min_f32 v157, v157, v6\n"
"v_mfma_f32_16x16x32_bf16 a[120:123], v[220:223], v[104:107], a[120:123]\n"
"v_mfma_f32_16x16x32_bf16 v[140:143], v[224:227], v[108:111], v[140:143]\n"
"v_exp_f32 v26, v26\n"
"v_add_f32 v158, 0xee900000, v10\n"
"v_mfma_f32_16x16x32_bf16 a[68:71], v[192:195], v[108:111], a[68:71]\n"
"v_exp_f32 v56, v56\n"
"v_min_f32 v158, v158, v6\n"
"v_mfma_f32_16x16x32_bf16 a[76:79], v[196:199], v[108:111], a[76:79]\n"
"ds_read_b128 v[188:191], v2 offset:15360\n"
"v_mfma_f32_16x16x32_bf16 a[84:87], v[200:203], v[108:111], a[84:87]\n"
"v_exp_f32 v27, v27\n"
"v_add_f32 v159, 0xee980000, v10\n"
"v_mfma_f32_16x16x32_bf16 a[92:95], v[204:207], v[108:111], a[92:95]\n"
"v_exp_f32 v57, v57\n"
"v_min_f32 v159, v159, v6\n"
"v_mfma_f32_16x16x32_bf16 a[100:103], v[208:211], v[108:111], a[100:103]\n"
"v_mfma_f32_16x16x32_bf16 a[108:111], v[212:215], v[108:111], a[108:111]\n"
"v_add_f32 v9, 0xef000000, v9\n"
"v_cvt_pk_bf16_f32 v83, v26, v27\n"
"v_cvt_pk_bf16_f32 v98, v56, v57\n"
"v_mfma_f32_16x16x32_bf16 a[116:119], v[216:219], v[108:111], a[116:119]\n"
"v_add_f32 v10, 0xef000000, v10\n"
"v_mfma_f32_16x16x32_bf16 a[124:127], v[220:223], v[108:111], a[124:127]\n"
"v_add_u32 v2, 0x4000, v2\n"
"v_and_b32 v2, 0x1ffff, v2\n"
"s_waitcnt lgkmcnt(8)\n"
"v_mfma_f32_16x16x32_bf16 v[32:35], a[224:227], a[128:131], v[144:147]\n"
"s_waitcnt vmcnt(12)\n"
"s_barrier\n"
"v_mfma_f32_16x16x32_bf16 v[32:35], a[228:231], a[132:135], v[32:35]\n"
"ds_read_b128 a[192:195], v2 offset:0\n"
"v_exp_f32 v28, v28\n"
"v_mfma_f32_16x16x32_bf16 v[32:35], a[232:235], a[136:139], v[32:35]\n"
"s_and_b32 m0, s90, 0x1ffff\n"
"v_exp_f32 v29, v29\n"
"v_mfma_f32_16x16x32_bf16 v[32:35], a[236:239], a[140:143], v[32:35]\n"
"v_exp_f32 v58, v58\n"
"v_mfma_f32_16x16x32_bf16 v[36:39], a[224:227], a[144:147], v[148:151]\n"
"ds_read_b128 a[196:199], v2 offset:1024\n"
"v_exp_f32 v59, v59\n"
"v_mfma_f32_16x16x32_bf16 v[36:39], a[228:231], a[148:151], v[36:39]\n"
"v_cvt_pk_bf16_f32 v86, v28, v29\n"
"v_cvt_pk_bf16_f32 v99, v58, v59\n"
"v_mfma_f32_16x16x32_bf16 v[36:39], a[232:235], a[152:155], v[36:39]\n"
"v_mfma_f32_16x16x32_bf16 v[36:39], a[236:239], a[156:159], v[36:39]\n"
"v_mfma_f32_16x16x32_bf16 v[40:43], a[240:243], a[128:131], v[152:155]\n"
"ds_read_b128 a[200:203], v2 offset:2048\n"
"v_exp_f32 v30, v30\n"
"v_mfma_f32_16x16x32_bf16 v[40:43], a[244:247], a[132:135], v[40:43]\n"
"v_exp_f32 v52, v52\n"
"v_mfma_f32_16x16x32_bf16 v[40:43], a[248:251], a[136:139], v[40:43]\n"
"buffer_load_dwordx4 v3, s[52:55], s91 offen sc1 lds\n"
"v_mfma_f32_16x16x32_bf16 v[40:43], a[252:255], a[140:143], v[40:43]\n"
"v_exp_f32 v31, v31\n"
"v_mfma_f32_16x16x32_bf16 v[44:47], a[240:243], a[144:147], v[156:159]\n"
"ds_read_b128 a[204:207], v2 offset:3072\n"
"v_exp_f32 v53, v53\n"
"v_mfma_f32_16x16x32_bf16 v[44:47], a[244:247], a[148:151], v[44:47]\n"
"v_mfma_f32_16x16x32_bf16 v[44:47], a[248:251], a[152:155], v[44:47]\n"
"v_cvt_pk_bf16_f32 v87, v30, v31\n"
"v_cvt_pk_bf16_f32 v100, v52, v53\n"
"v_mfma_f32_16x16x32_bf16 v[44:47], a[252:255], a[156:159], v[44:47]\n"
"s_add_u32 s90, s90, 0x4000\n"
"v_mfma_f32_16x16x32_bf16 v[64:67], a[224:227], a[160:163], v[120:123]\n"
"ds_read_b128 a[208:211], v2 offset:4096\n"
"v_mfma_f32_16x16x32_bf16 v[64:67], a[228:231], a[164:167], v[64:67]\n"
"v_exp_f32 v54, v54\n"
"v_mfma_f32_16x16x32_bf16 v[64:67], a[232:235], a[168:171], v[64:67]\n"
"v_exp_f32 v32, v32\n"
"v_mfma_f32_16x16x32_bf16 v[64:67], a[236:239], a[172:175], v[64:67]\n"
"ds_read_b128 a[212:215], v2 offset:5120\n"
"v_exp_f32 v55, v55\n"
"v_mfma_f32_16x16x32_bf16 v[68:71], a[224:227], a[176:179], v[124:127]\n"
"v_exp_f32 v33, v33\n"
"v_add_f32 v144, 0, v9\n"
"v_mfma_f32_16x16x32_bf16 v[68:71], a[228:231], a[180:183], v[68:71]\n"
"v_min_f32 v144, v144, v5\n"
"v_add_f32 v145, 0xec800000, v9\n"
"v_mfma_f32_16x16x32_bf16 v[68:71], a[232:235], a[184:187], v[68:71]\n"
"v_cvt_pk_bf16_f32 v101, v54, v55\n"
"v_cvt_pk_bf16_f32 v88, v32, v33\n"
"v_mfma_f32_16x16x32_bf16 v[68:71], a[236:239], a[188:191], v[68:71]\n"
"ds_read_b128 a[216:219], v2 offset:6144\n"
"buffer_load_dwordx4 v3, s[52:55], s91 offen offset:1024 sc1 lds\n"
"v_mfma_f32_16x16x32_bf16 v[72:75], a[240:243], a[160:163], v[120:123]\n"
"v_min_f32 v145, v145, v5\n"
"v_add_f32 v146, 0xed000000, v9\n"
"v_mfma_f32_16x16x32_bf16 v[72:75], a[244:247], a[164:167], v[72:75]\n"
"v_exp_f32 v60, v60\n"
"v_mfma_f32_16x16x32_bf16 v[72:75], a[248:251], a[168:171], v[72:75]\n"
"v_exp_f32 v34, v34\n"
"v_min_f32 v146, v146, v5\n"
"v_mfma_f32_16x16x32_bf16 v[72:75], a[252:255], a[172:175], v[72:75]\n"
"ds_read_b128 a[220:223], v2 offset:7168\n"
"v_add_f32 v147, 0xed400000, v9\n"
"v_mfma_f32_16x16x32_bf16 v[76:79], a[240:243], a[176:179], v[124:127]\n"
"v_exp_f32 v61, v61\n"
"v_mfma_f32_16x16x32_bf16 v[76:79], a[244:247], a[180:183], v[76:79]\n"
"v_exp_f32 v35, v35\n"
"v_min_f32 v147, v147, v5\n"
"v_mfma_f32_16x16x32_bf16 v[76:79], a[248:251], a[184:187], v[76:79]\n"
"s_add_u32 m0, m0, 0x2000\n"
"v_add_f32 v148, 0, v10\n"
"v_mfma_f32_16x16x32_bf16 v[76:79], a[252:255], a[188:191], v[76:79]\n"
"s_waitcnt lgkmcnt(8)\n"
"v_cvt_pk_bf16_f32 v102, v60, v61\n"
"v_cvt_pk_bf16_f32 v89, v34, v35\n"
"v_min_f32 v148, v148, v6\n"
"v_mfma_f32_16x16x32_bf16 v[128:131], v[224:227], v[80:83], v[128:131]\n"
"ds_read_b128 v[192:195], v2 offset:8192\n"
"v_mfma_f32_16x16x32_bf16 a[0:3], v[160:163], v[80:83], a[0:3]\n"
"v_add_f32 v149, 0xec800000, v10\n"
"v_mfma_f32_16x16x32_bf16 a[8:11], v[164:167], v[80:83], a[8:11]\n"
"v_exp_f32 v62, v62\n"
"v_min_f32 v149, v149, v6\n"
"v_mfma_f32_16x16x32_bf16 a[16:19], v[168:171], v[80:83], a[16:19]\n"
"v_exp_f32 v36, v36\n"
"v_mfma_f32_16x16x32_bf16 a[24:27], v[172:175], v[80:83], a[24:27]\n"
"ds_read_b128 v[196:199], v2 offset:9216\n"
"v_exp_f32 v63, v63\n"
"v_mfma_f32_16x16x32_bf16 a[32:35], v[176:179], v[80:83], a[32:35]\n"
"buffer_load_dwordx4 v4, s[52:55], s91 offen sc1 lds\n"
"v_add_f32 v150, 0xed000000, v10\n"
"v_mfma_f32_16x16x32_bf16 a[40:43], v[180:183], v[80:83], a[40:43]\n"
"v_exp_f32 v37, v37\n"
"v_min_f32 v150, v150, v6\n"
"v_mfma_f32_16x16x32_bf16 a[48:51], v[184:187], v[80:83], a[48:51]\n"
"v_cvt_pk_bf16_f32 v103, v62, v63\n"
"v_add_f32 v151, 0xed400000, v10\n"
"v_cvt_pk_bf16_f32 v92, v36, v37\n"
"v_mfma_f32_16x16x32_bf16 a[56:59], v[188:191], v[80:83], a[56:59]\n"
"ds_read_b128 v[200:203], v2 offset:10240\n"
"v_min_f32 v151, v151, v6\n"
"v_mfma_f32_16x16x32_bf16 v[132:135], v[224:227], v[84:87], v[132:135]\n"
"v_mfma_f32_16x16x32_bf16 a[4:7], v[160:163], v[84:87], a[4:7]\n"
"v_exp_f32 v38, v38\n"
"v_add_f32 v152, 0xee800000, v9\n"
"v_mfma_f32_16x16x32_bf16 a[12:15], v[164:167], v[84:87], a[12:15]\n"
"v_exp_f32 v64, v64\n"
"v_min_f32 v152, v152, v5\n"
"v_mfma_f32_16x16x32_bf16 a[20:23], v[168:171], v[84:87], a[20:23]\n"
"ds_read_b128 v[204:207], v2 offset:11264\n"
"v_add_f32 v153, 0xee880000, v9\n"
"v_mfma_f32_16x16x32_bf16 a[28:31], v[172:175], v[84:87], a[28:31]\n"
"buffer_load_dwordx4 v4, s[52:55], s91 offen offset:1024 sc1 lds\n"
"v_mfma_f32_16x16x32_bf16 a[36:39], v[176:179], v[84:87], a[36:39]\n"
"v_exp_f32 v39, v39\n"
"v_min_f32 v153, v153, v5\n"
"v_mfma_f32_16x16x32_bf16 a[44:47], v[180:183], v[84:87], a[44:47]\n"
"v_exp_f32 v65, v65\n"
"v_add_f32 v154, 0xee900000, v9\n"
"v_mfma_f32_16x16x32_bf16 a[52:55], v[184:187], v[84:87], a[52:55]\n"
"v_cvt_pk_bf16_f32 v93, v38, v39\n"
"v_cvt_pk_bf16_f32 v104, v64, v65\n"
"v_mfma_f32_16x16x32_bf16 a[60:63], v[188:191], v[84:87], a[60:63]\n"
"ds_read_b128 v[208:211], v2 offset:12288\n"
"v_min_f32 v154, v154, v5\n"
"v_mfma_f32_16x16x32_bf16 v[136:139], v[224:227], v[96:99], v[136:139]\n"
"v_add_f32 v155, 0xee980000, v9\n"
"v_mfma_f32_16x16x32_bf16 a[64:67], v[160:163], v[96:99], a[64:67]\n"
"v_exp_f32 v40, v40\n"
"v_mfma_f32_16x16x32_bf16 a[72:75], v[164:167], v[96:99], a[72:75]\n"
"s_add_u32 s91, s91, 0x4000\n"
"v_exp_f32 v66, v66\n"
"v_mfma_f32_16x16x32_bf16 a[80:83], v[168:171], v[96:99], a[80:83]\n"
"ds_read_b128 v[212:215], v2 offset:13312\n"
"v_exp_f32 v41, v41\n"
"v_mfma_f32_16x16x32_bf16 a[88:91], v[172:175], v[96:99], a[88:91]\n"
"v_exp_f32 v67, v67\n"
"v_min_f32 v155, v155, v5\n"
"v_mfma_f32_16x16x32_bf16 a[96:99], v[176:179], v[96:99], a[96:99]\n"
"v_add_f32 v156, 0xee800000, v10\n"
"v_min_f32 v156, v156, v6\n"
"v_mfma_f32_16x16x32_bf16 a[104:107], v[180:183], v[96:99], a[104:107]\n"
"v_add_f32 v157, 0xee880000, v10\n"
"v_cvt_pk_bf16_f32 v90, v40, v41\n"
"v_cvt_pk_bf16_f32 v105, v66, v67\n"
"v_mfma_f32_16x16x32_bf16 a[112:115], v[184:187], v[96:99], a[112:115]\n"
"ds_read_b128 v[216:219], v2 offset:14336\n"
"v_min_f32 v157, v157, v6\n"
"v_mfma_f32_16x16x32_bf16 a[120:123], v[188:191], v[96:99], a[120:123]\n"
"v_mfma_f32_16x16x32_bf16 v[140:143], v[224:227], v[100:103], v[140:143]\n"
"v_exp_f32 v42, v42\n"
"v_add_f32 v158, 0xee900000, v10\n"
"v_mfma_f32_16x16x32_bf16 a[68:71], v[160:163], v[100:103], a[68:71]\n"
"s_add_u32 s85, s85, 1\n"
"v_exp_f32 v72, v72\n"
"v_mfma_f32_16x16x32_bf16 a[76:79], v[164:167], v[100:103], a[76:79]\n"
"ds_read_b128 v[220:223], v2 offset:15360\n"
"v_min_f32 v158, v158, v6\n"
"v_mfma_f32_16x16x32_bf16 a[84:87], v[168:171], v[100:103], a[84:87]\n"
"v_exp_f32 v43, v43\n"
"v_add_f32 v159, 0xee980000, v10\n"
"v_mfma_f32_16x16x32_bf16 a[92:95], v[172:175], v[100:103], a[92:95]\n"
"v_exp_f32 v73, v73\n"
"v_min_f32 v159, v159, v6\n"
"v_mfma_f32_16x16x32_bf16 a[100:103], v[176:179], v[100:103], a[100:103]\n"
"v_mfma_f32_16x16x32_bf16 a[108:111], v[180:183], v[100:103], a[108:111]\n"
"v_add_f32 v9, 0xef000000, v9\n"
"v_cvt_pk_bf16_f32 v91, v42, v43\n"
"v_cvt_pk_bf16_f32 v106, v72, v73\n"
"v_mfma_f32_16x16x32_bf16 a[116:119], v[184:187], v[100:103], a[116:119]\n"
"v_add_f32 v10, 0xef000000, v10\n"
"v_mfma_f32_16x16x32_bf16 a[124:127], v[188:191], v[100:103], a[124:127]\n"
"v_add_u32 v2, 0x4000, v2\n"
"v_and_b32 v2, 0x1ffff, v2\n"
"s_cbranch_scc0 LA_dm\n"
"v_and_b32 v255, 63, v0\n"
"v_lshlrev_b32 v255, 7, v255\n"
"s_cmp_lg_u32 s34, 0\n"
"s_cbranch_scc1 LM_pf2_0\n"
"s_mov_b32 s48, s18\n"
"s_and_b32 s49, s19, 0xffff\n"
"s_mov_b32 s50, 0x400000\n"
"s_mov_b32 s51, 0x20000\n"
"s_mul_i32 s46, s30, 16384\n"
"s_add_u32 s46, s46, 0\n"
"buffer_load_dword v254, v255, s[48:51], s46 offen ;@cond\n"
"LM_pf2_0:\n"
"s_cmp_lg_u32 s34, 1\n"
"s_cbranch_scc1 LM_pf2_1\n"
"s_mov_b32 s48, s18\n"
"s_and_b32 s49, s19, 0xffff\n"
"s_mov_b32 s50, 0x400000\n"
"s_mov_b32 s51, 0x20000\n"
"s_mul_i32 s46, s30, 16384\n"
"s_add_u32 s46, s46, 8192\n"
"buffer_load_dword v254, v255, s[48:51], s46 offen ;@cond\n"
"LM_pf2_1:\n"
"s_cmp_lg_u32 s34, 2\n"
"s_cbranch_scc1 LM_pf2_2\n"
"s_mov_b32 s48, s24\n"
"s_and_b32 s49, s25, 0xffff\n"
"s_mov_b32 s50, 0x1c0000\n"
"s_mov_b32 s51, 0x20000\n"
"s_mul_i32 s46, s30, 7168\n"
"s_add_u32 s46, s46, 0\n"
"buffer_load_dword v254, v255, s[48:51], s46 offen ;@cond\n"
"LM_pf2_2:\n"
"s_mul_i32 s41, s38, 28\n"
"s_add_u32 s41, s41, s37\n"
"s_lshl_b32 s41, s41, 7\n"
"s_add_u32 s41, s41, 36864\n"
"s_lshl_b32 s44, s38, 7\n"
"s_add_u32 s44, s44, 69632\n"
"s_cmp_ge_u32 s37, 28\n"
"s_cselect_b32 s41, s44, s41\n"
"s_cmp_gt_u32 s37, 28\n"
"s_cbranch_scc1 LM_noactpub\n"
"v_mov_b32 v250, s41\n"
"v_mov_b32 v252, s42\n"
"v_mov_b32 v253, s43\n"
"s_cmp_lg_u32 s34, 0\n"
"s_cbranch_scc1 LP2\n"
"s_mov_b64 exec, 1\n"
"buffer_store_dwordx2 v[252:253], v250, s[64:67], 0 offen sc1 ;@cond\n"
"s_mov_b64 exec, -1\n"
"LP2:\n"
"LM_noactpub:\n"
"s_cmp_eq_u32 s38, 7\n"
"s_cbranch_scc1 LA_j7\n"
"s_waitcnt lgkmcnt(8)\n"
"v_mfma_f32_16x16x32_bf16 v[48:51], a[192:195], a[160:163], v[120:123]\n"
"v_exp_f32 v44, v44\n"
"v_mfma_f32_16x16x32_bf16 v[48:51], a[196:199], a[164:167], v[48:51]\n"
"ds_read_b128 a[224:227], v2 offset:0\n"
"ds_read_b128 a[228:231], v2 offset:1024\n"
"v_mfma_f32_16x16x32_bf16 v[48:51], a[200:203], a[168:171], v[48:51]\n"
"v_exp_f32 v74, v74\n"
"v_mfma_f32_16x16x32_bf16 v[48:51], a[204:207], a[172:175], v[48:51]\n"
"v_exp_f32 v45, v45\n"
"v_mfma_f32_16x16x32_bf16 v[52:55], a[192:195], a[176:179], v[124:127]\n"
"ds_read_b128 a[232:235], v2 offset:2048\n"
"s_and_b32 m0, s90, 0x1ffff\n"
"v_mfma_f32_16x16x32_bf16 v[52:55], a[196:199], a[180:183], v[52:55]\n"
"ds_read_b128 a[236:239], v2 offset:3072\n"
"v_mfma_f32_16x16x32_bf16 v[52:55], a[200:203], a[184:187], v[52:55]\n"
"v_exp_f32 v75, v75\n"
"v_mfma_f32_16x16x32_bf16 v[52:55], a[204:207], a[188:191], v[52:55]\n"
"ds_read_b128 a[240:243], v2 offset:4096\n"
"v_cvt_pk_bf16_f32 v94, v44, v45\n"
"v_mfma_f32_16x16x32_bf16 v[56:59], a[208:211], a[160:163], v[120:123]\n"
"v_cvt_pk_bf16_f32 v107, v74, v75\n"
"v_mfma_f32_16x16x32_bf16 v[56:59], a[212:215], a[164:167], v[56:59]\n"
"ds_read_b128 a[244:247], v2 offset:5120\n"
"v_mfma_f32_16x16x32_bf16 v[56:59], a[216:219], a[168:171], v[56:59]\n"
"v_exp_f32 v68, v68\n"
"v_mfma_f32_16x16x32_bf16 v[56:59], a[220:223], a[172:175], v[56:59]\n"
"ds_read_b128 a[248:251], v2 offset:6144\n"
"v_mfma_f32_16x16x32_bf16 v[60:63], a[208:211], a[176:179], v[124:127]\n"
"ds_read_b128 a[252:255], v2 offset:7168\n"
"v_mfma_f32_16x16x32_bf16 v[60:63], a[212:215], a[180:183], v[60:63]\n"
"v_exp_f32 v46, v46\n"
"v_mfma_f32_16x16x32_bf16 v[60:63], a[216:219], a[184:187], v[60:63]\n"
"v_exp_f32 v69, v69\n"
"v_mfma_f32_16x16x32_bf16 v[60:63], a[220:223], a[188:191], v[60:63]\n"
"s_waitcnt lgkmcnt(8)\n"
"v_exp_f32 v47, v47\n"
"v_mfma_f32_16x16x32_bf16 v[128:131], v[224:227], v[88:91], v[128:131]\n"
"ds_read_b128 v[160:163], v2 offset:8192\n"
"v_cvt_pk_bf16_f32 v108, v68, v69\n"
"v_mfma_f32_16x16x32_bf16 a[0:3], v[192:195], v[88:91], a[0:3]\n"
"v_exp_f32 v70, v70\n"
"v_mfma_f32_16x16x32_bf16 a[8:11], v[196:199], v[88:91], a[8:11]\n"
"v_exp_f32 v71, v71\n"
"v_mfma_f32_16x16x32_bf16 a[16:19], v[200:203], v[88:91], a[16:19]\n"
"v_cvt_pk_bf16_f32 v95, v46, v47\n"
"v_cvt_pk_bf16_f32 v109, v70, v71\n"
"v_mfma_f32_16x16x32_bf16 a[24:27], v[204:207], v[88:91], a[24:27]\n"
"v_exp_f32 v76, v76\n"
"v_mfma_f32_16x16x32_bf16 a[32:35], v[208:211], v[88:91], a[32:35]\n"
"v_exp_f32 v77, v77\n"
"v_mfma_f32_16x16x32_bf16 a[40:43], v[212:215], v[88:91], a[40:43]\n"
"buffer_load_dwordx4 v3, s[52:55], s91 offen sc1 lds\n"
"v_mfma_f32_16x16x32_bf16 a[48:51], v[216:219], v[88:91], a[48:51]\n"
"s_add_u32 s90, s90, 0x4000\n"
"v_cvt_pk_bf16_f32 v110, v76, v77\n"
"v_mfma_f32_16x16x32_bf16 a[56:59], v[220:223], v[88:91], a[56:59]\n"
"ds_read_b128 v[164:167], v2 offset:9216\n"
"ds_read_b128 v[168:171], v2 offset:10240\n"
"v_mfma_f32_16x16x32_bf16 v[132:135], v[224:227], v[92:95], v[132:135]\n"
"buffer_load_dwordx4 v3, s[52:55], s91 offen offset:1024 sc1 lds\n"
"v_mfma_f32_16x16x32_bf16 a[4:7], v[192:195], v[92:95], a[4:7]\n"
"v_exp_f32 v78, v78\n"
"v_mfma_f32_16x16x32_bf16 a[12:15], v[196:199], v[92:95], a[12:15]\n"
"s_add_u32 m0, m0, 0x2000\n"
"v_mfma_f32_16x16x32_bf16 a[20:23], v[200:203], v[92:95], a[20:23]\n"
"v_exp_f32 v79, v79\n"
"v_mfma_f32_16x16x32_bf16 a[28:31], v[204:207], v[92:95], a[28:31]\n"
"ds_read_b128 v[172:175], v2 offset:11264\n"
"v_mfma_f32_16x16x32_bf16 a[36:39], v[208:211], v[92:95], a[36:39]\n"
"v_cvt_pk_bf16_f32 v111, v78, v79\n"
"v_mfma_f32_16x16x32_bf16 a[44:47], v[212:215], v[92:95], a[44:47]\n"
"buffer_load_dwordx4 v4, s[52:55], s91 offen sc1 lds\n"
"v_mfma_f32_16x16x32_bf16 a[52:55], v[216:219], v[92:95], a[52:55]\n"
"v_exp_f32 v48, v48\n"
"v_mfma_f32_16x16x32_bf16 a[60:63], v[220:223], v[92:95], a[60:63]\n"
"ds_read_b128 v[176:179], v2 offset:12288\n"
"v_mfma_f32_16x16x32_bf16 v[136:139], v[224:227], v[104:107], v[136:139]\n"
"v_exp_f32 v49, v49\n"
"v_mfma_f32_16x16x32_bf16 a[64:67], v[192:195], v[104:107], a[64:67]\n"
"v_mfma_f32_16x16x32_bf16 a[72:75], v[196:199], v[104:107], a[72:75]\n"
"v_cvt_pk_bf16_f32 v96, v48, v49\n"
"v_mfma_f32_16x16x32_bf16 a[80:83], v[200:203], v[104:107], a[80:83]\n"
"ds_read_b128 v[180:183], v2 offset:13312\n"
"v_mfma_f32_16x16x32_bf16 a[88:91], v[204:207], v[104:107], a[88:91]\n"
"buffer_load_dwordx4 v4, s[52:55], s91 offen offset:1024 sc1 lds\n"
"v_mfma_f32_16x16x32_bf16 a[96:99], v[208:211], v[104:107], a[96:99]\n"
"v_exp_f32 v50, v50\n"
"v_mfma_f32_16x16x32_bf16 a[104:107], v[212:215], v[104:107], a[104:107]\n"
"v_mfma_f32_16x16x32_bf16 a[112:115], v[216:219], v[104:107], a[112:115]\n"
"ds_read_b128 v[184:187], v2 offset:14336\n"
"v_mfma_f32_16x16x32_bf16 a[120:123], v[220:223], v[104:107], a[120:123]\n"
"v_exp_f32 v51, v51\n"
"v_mfma_f32_16x16x32_bf16 v[140:143], v[224:227], v[108:111], v[140:143]\n"
"s_add_u32 s91, s91, 0x4000\n"
"v_cvt_pk_bf16_f32 v97, v50, v51\n"
"v_mfma_f32_16x16x32_bf16 a[68:71], v[192:195], v[108:111], a[68:71]\n"
"v_mfma_f32_16x16x32_bf16 a[76:79], v[196:199], v[108:111], a[76:79]\n"
"v_exp_f32 v56, v56\n"
"v_mfma_f32_16x16x32_bf16 a[84:87], v[200:203], v[108:111], a[84:87]\n"
"ds_read_b128 v[188:191], v2 offset:15360\n"
"v_mfma_f32_16x16x32_bf16 a[92:95], v[204:207], v[108:111], a[92:95]\n"
"v_exp_f32 v57, v57\n"
"v_mfma_f32_16x16x32_bf16 a[100:103], v[208:211], v[108:111], a[100:103]\n"
"v_mfma_f32_16x16x32_bf16 a[108:111], v[212:215], v[108:111], a[108:111]\n"
"v_cvt_pk_bf16_f32 v98, v56, v57\n"
"v_mfma_f32_16x16x32_bf16 a[116:119], v[216:219], v[108:111], a[116:119]\n"
"v_mfma_f32_16x16x32_bf16 a[124:127], v[220:223], v[108:111], a[124:127]\n"
"v_add_u32 v2, 0x4000, v2\n"
"v_and_b32 v2, 0x1ffff, v2\n"
"s_waitcnt lgkmcnt(8)\n"
"v_mfma_f32_16x16x32_bf16 v[64:67], a[224:227], a[160:163], v[120:123]\n"
"s_waitcnt vmcnt(12)\n"
"s_barrier\n"
"v_mfma_f32_16x16x32_bf16 v[64:67], a[228:231], a[164:167], v[64:67]\n"
"v_exp_f32 v58, v58\n"
"v_mfma_f32_16x16x32_bf16 v[64:67], a[232:235], a[168:171], v[64:67]\n"
"v_exp_f32 v59, v59\n"
"v_mfma_f32_16x16x32_bf16 v[64:67], a[236:239], a[172:175], v[64:67]\n"
"ds_read_b128 a[192:195], v2 offset:0\n"
"v_cvt_pk_bf16_f32 v99, v58, v59\n"
"v_mfma_f32_16x16x32_bf16 v[68:71], a[224:227], a[176:179], v[124:127]\n"
"ds_read_b128 a[196:199], v2 offset:1024\n"
"ds_read_b128 a[200:203], v2 offset:2048\n"
"v_mfma_f32_16x16x32_bf16 v[68:71], a[228:231], a[180:183], v[68:71]\n"
"v_exp_f32 v52, v52\n"
"v_mfma_f32_16x16x32_bf16 v[68:71], a[232:235], a[184:187], v[68:71]\n"
"ds_read_b128 a[204:207], v2 offset:3072\n"
"s_and_b32 m0, s90, 0x1ffff\n"
"v_mfma_f32_16x16x32_bf16 v[68:71], a[236:239], a[188:191], v[68:71]\n"
"v_exp_f32 v53, v53\n"
"v_mfma_f32_16x16x32_bf16 v[72:75], a[240:243], a[160:163], v[120:123]\n"
"ds_read_b128 a[208:211], v2 offset:4096\n"
"v_cvt_pk_bf16_f32 v100, v52, v53\n"
"v_mfma_f32_16x16x32_bf16 v[72:75], a[244:247], a[164:167], v[72:75]\n"
"ds_read_b128 a[212:215], v2 offset:5120\n"
"v_mfma_f32_16x16x32_bf16 v[72:75], a[248:251], a[168:171], v[72:75]\n"
"v_exp_f32 v54, v54\n"
"v_mfma_f32_16x16x32_bf16 v[72:75], a[252:255], a[172:175], v[72:75]\n"
"ds_read_b128 a[216:219], v2 offset:6144\n"
"v_mfma_f32_16x16x32_bf16 v[76:79], a[240:243], a[176:179], v[124:127]\n"
"ds_read_b128 a[220:223], v2 offset:7168\n"
"v_mfma_f32_16x16x32_bf16 v[76:79], a[244:247], a[180:183], v[76:79]\n"
"v_exp_f32 v55, v55\n"
"v_mfma_f32_16x16x32_bf16 v[76:79], a[248:251], a[184:187], v[76:79]\n"
"v_cvt_pk_bf16_f32 v101, v54, v55\n"
"v_mfma_f32_16x16x32_bf16 v[76:79], a[252:255], a[188:191], v[76:79]\n"
"s_waitcnt lgkmcnt(8)\n"
"v_exp_f32 v60, v60\n"
"v_mfma_f32_16x16x32_bf16 v[136:139], v[224:227], v[96:99], v[136:139]\n"
"v_exp_f32 v61, v61\n"
"v_mfma_f32_16x16x32_bf16 a[64:67], v[160:163], v[96:99], a[64:67]\n"
"ds_read_b128 v[192:195], v2 offset:8192\n"
"v_cvt_pk_bf16_f32 v102, v60, v61\n"
"v_mfma_f32_16x16x32_bf16 a[72:75], v[164:167], v[96:99], a[72:75]\n"
"v_exp_f32 v62, v62\n"
"v_mfma_f32_16x16x32_bf16 a[80:83], v[168:171], v[96:99], a[80:83]\n"
"v_exp_f32 v63, v63\n"
"v_mfma_f32_16x16x32_bf16 a[88:91], v[172:175], v[96:99], a[88:91]\n"
"ds_read_b128 v[196:199], v2 offset:9216\n"
"v_cvt_pk_bf16_f32 v103, v62, v63\n"
"v_mfma_f32_16x16x32_bf16 a[96:99], v[176:179], v[96:99], a[96:99]\n"
"buffer_load_dwordx4 v3, s[52:55], s91 offen sc1 lds\n"
"v_mfma_f32_16x16x32_bf16 a[104:107], v[180:183], v[96:99], a[104:107]\n"
"s_add_u32 s90, s90, 0x4000\n"
"ds_read_b128 v[200:203], v2 offset:10240\n"
"v_mfma_f32_16x16x32_bf16 a[112:115], v[184:187], v[96:99], a[112:115]\n"
"buffer_load_dwordx4 v3, s[52:55], s91 offen offset:1024 sc1 lds\n"
"v_mfma_f32_16x16x32_bf16 a[120:123], v[188:191], v[96:99], a[120:123]\n"
"v_exp_f32 v64, v64\n"
"v_mfma_f32_16x16x32_bf16 v[140:143], v[224:227], v[100:103], v[140:143]\n"
"s_add_u32 m0, m0, 0x2000\n"
"ds_read_b128 v[204:207], v2 offset:11264\n"
"v_mfma_f32_16x16x32_bf16 a[68:71], v[160:163], v[100:103], a[68:71]\n"
"buffer_load_dwordx4 v4, s[52:55], s91 offen sc1 lds\n"
"v_mfma_f32_16x16x32_bf16 a[76:79], v[164:167], v[100:103], a[76:79]\n"
"v_exp_f32 v65, v65\n"
"v_mfma_f32_16x16x32_bf16 a[84:87], v[168:171], v[100:103], a[84:87]\n"
"ds_read_b128 v[208:211], v2 offset:12288\n"
"v_cvt_pk_bf16_f32 v104, v64, v65\n"
"v_mfma_f32_16x16x32_bf16 a[92:95], v[172:175], v[100:103], a[92:95]\n"
"v_exp_f32 v66, v66\n"
"v_mfma_f32_16x16x32_bf16 a[100:103], v[176:179], v[100:103], a[100:103]\n"
"v_exp_f32 v67, v67\n"
"v_mfma_f32_16x16x32_bf16 a[108:111], v[180:183], v[100:103], a[108:111]\n"
"ds_read_b128 v[212:215], v2 offset:13312\n"
"ds_read_b128 v[216:219], v2 offset:14336\n"
"buffer_load_dwordx4 v4, s[52:55], s91 offen offset:1024 sc1 lds\n"
"ds_read_b128 v[220:223], v2 offset:15360\n"
"s_add_u32 s91, s91, 0x4000\n"
"v_cvt_pk_bf16_f32 v105, v66, v67\n"
"v_exp_f32 v72, v72\n"
"v_mfma_f32_16x16x32_bf16 a[116:119], v[184:187], v[100:103], a[116:119]\n"
"v_exp_f32 v73, v73\n"
"v_mfma_f32_16x16x32_bf16 a[124:127], v[188:191], v[100:103], a[124:127]\n"
"v_cvt_pk_bf16_f32 v106, v72, v73\n"
"v_add_u32 v2, 0x4000, v2\n"
"v_and_b32 v2, 0x1ffff, v2\n"
"s_mul_i32 s44, s86, 376832\n"
"s_lshl_b32 s45, s37, 12\n"
"s_add_u32 s44, s44, s45\n"
"s_add_u32 s45, s44, 188416\n"
"v_cmp_nlt_f32 vcc, v128, v228\n"
"v_rcp_f32 v13, v128\n"
"s_nop 0\n"
"s_or_b32 s31, s31, vcc_lo\n"
"s_or_b32 s31, s31, vcc_hi\n"
"v_mov_b32 v12, v13\n"
"v_accvgpr_read_b32 v230, a0\n"
"v_accvgpr_read_b32 v231, a1\n"
"v_accvgpr_read_b32 v232, a2\n"
"v_accvgpr_read_b32 v233, a3\n"
"v_accvgpr_read_b32 v234, a8\n"
"v_accvgpr_read_b32 v235, a9\n"
"v_accvgpr_read_b32 v236, a10\n"
"v_accvgpr_read_b32 v237, a11\n"
"v_pk_mul_f32 v[230:231], v[230:231], v[12:13]\n"
"v_pk_mul_f32 v[232:233], v[232:233], v[12:13]\n"
"v_pk_mul_f32 v[234:235], v[234:235], v[12:13]\n"
"v_pk_mul_f32 v[236:237], v[236:237], v[12:13]\n"
"v_cvt_pk_bf16_f32 v246, v230, v231\n"
"v_cvt_pk_bf16_f32 v247, v232, v233\n"
"v_cvt_pk_bf16_f32 v248, v234, v235\n"
"v_cvt_pk_bf16_f32 v249, v236, v237\n"
"buffer_store_dwordx4 v[246:249], v1, s[60:63], s44 offen offset:0 sc1\n"
"v_accvgpr_read_b32 v238, a16\n"
"v_accvgpr_read_b32 v239, a17\n"
"v_accvgpr_read_b32 v240, a18\n"
"v_accvgpr_read_b32 v241, a19\n"
"v_accvgpr_read_b32 v242, a24\n"
"v_accvgpr_read_b32 v243, a25\n"
"v_accvgpr_read_b32 v244, a26\n"
"v_accvgpr_read_b32 v245, a27\n"
"v_pk_mul_f32 v[238:239], v[238:239], v[12:13]\n"
"v_pk_mul_f32 v[240:241], v[240:241], v[12:13]\n"
"v_pk_mul_f32 v[242:243], v[242:243], v[12:13]\n"
"v_pk_mul_f32 v[244:245], v[244:245], v[12:13]\n"
"v_cvt_pk_bf16_f32 v250, v238, v239\n"
"v_cvt_pk_bf16_f32 v251, v240, v241\n"
"v_cvt_pk_bf16_f32 v252, v242, v243\n"
"v_cvt_pk_bf16_f32 v253, v244, v245\n"
"buffer_store_dwordx4 v[250:253], v1, s[60:63], s44 offen offset:1024 sc1\n"
"v_accvgpr_read_b32 v230, a32\n"
"v_accvgpr_read_b32 v231, a33\n"
"v_accvgpr_read_b32 v232, a34\n"
"v_accvgpr_read_b32 v233, a35\n"
"v_accvgpr_read_b32 v234, a40\n"
"v_accvgpr_read_b32 v235, a41\n"
"v_accvgpr_read_b32 v236, a42\n"
"v_accvgpr_read_b32 v237, a43\n"
"v_pk_mul_f32 v[230:231], v[230:231], v[12:13]\n"
"v_pk_mul_f32 v[232:233], v[232:233], v[12:13]\n"
"v_pk_mul_f32 v[234:235], v[234:235], v[12:13]\n"
"v_pk_mul_f32 v[236:237], v[236:237], v[12:13]\n"
"v_cvt_pk_bf16_f32 v246, v230, v231\n"
"v_cvt_pk_bf16_f32 v247, v232, v233\n"
"v_cvt_pk_bf16_f32 v248, v234, v235\n"
"v_cvt_pk_bf16_f32 v249, v236, v237\n"
"buffer_store_dwordx4 v[246:249], v1, s[60:63], s44 offen offset:2048 sc1\n"
"v_accvgpr_read_b32 v238, a48\n"
"v_accvgpr_read_b32 v239, a49\n"
"v_accvgpr_read_b32 v240, a50\n"
"v_accvgpr_read_b32 v241, a51\n"
"v_accvgpr_read_b32 v242, a56\n"
"v_accvgpr_read_b32 v243, a57\n"
"v_accvgpr_read_b32 v244, a58\n"
"v_accvgpr_read_b32 v245, a59\n"
"v_pk_mul_f32 v[238:239], v[238:239], v[12:13]\n"
"v_pk_mul_f32 v[240:241], v[240:241], v[12:13]\n"
"v_pk_mul_f32 v[242:243], v[242:243], v[12:13]\n"
"v_pk_mul_f32 v[244:245], v[244:245], v[12:13]\n"
"v_cvt_pk_bf16_f32 v250, v238, v239\n"
"v_cvt_pk_bf16_f32 v251, v240, v241\n"
"v_cvt_pk_bf16_f32 v252, v242, v243\n"
"v_cvt_pk_bf16_f32 v253, v244, v245\n"
"buffer_store_dwordx4 v[250:253], v1, s[60:63], s44 offen offset:3072 sc1\n"
"v_cmp_nlt_f32 vcc, v132, v228\n"
"v_rcp_f32 v13, v132\n"
"s_nop 0\n"
"s_or_b32 s31, s31, vcc_lo\n"
"s_or_b32 s31, s31, vcc_hi\n"
"v_mov_b32 v12, v13\n"
"v_accvgpr_read_b32 v230, a4\n"
"v_accvgpr_read_b32 v231, a5\n"
"v_accvgpr_read_b32 v232, a6\n"
"v_accvgpr_read_b32 v233, a7\n"
"v_accvgpr_read_b32 v234, a12\n"
"v_accvgpr_read_b32 v235, a13\n"
"v_accvgpr_read_b32 v236, a14\n"
"v_accvgpr_read_b32 v237, a15\n"
"v_pk_mul_f32 v[230:231], v[230:231], v[12:13]\n"
"v_pk_mul_f32 v[232:233], v[232:233], v[12:13]\n"
"v_pk_mul_f32 v[234:235], v[234:235], v[12:13]\n"
"v_pk_mul_f32 v[236:237], v[236:237], v[12:13]\n"
"v_cvt_pk_bf16_f32 v246, v230, v231\n"
"v_cvt_pk_bf16_f32 v247, v232, v233\n"
"v_cvt_pk_bf16_f32 v248, v234, v235\n"
"v_cvt_pk_bf16_f32 v249, v236, v237\n"
"buffer_store_dwordx4 v[246:249], v1, s[60:63], s45 offen offset:0 sc1\n"
"v_accvgpr_read_b32 v238, a20\n"
"v_accvgpr_read_b32 v239, a21\n"
"v_accvgpr_read_b32 v240, a22\n"
"v_accvgpr_read_b32 v241, a23\n"
"v_accvgpr_read_b32 v242, a28\n"
"v_accvgpr_read_b32 v243, a29\n"
"v_accvgpr_read_b32 v244, a30\n"
"v_accvgpr_read_b32 v245, a31\n"
"v_pk_mul_f32 v[238:239], v[238:239], v[12:13]\n"
"v_pk_mul_f32 v[240:241], v[240:241], v[12:13]\n"
"v_pk_mul_f32 v[242:243], v[242:243], v[12:13]\n"
"v_pk_mul_f32 v[244:245], v[244:245], v[12:13]\n"
"v_cvt_pk_bf16_f32 v250, v238, v239\n"
"v_cvt_pk_bf16_f32 v251, v240, v241\n"
"v_cvt_pk_bf16_f32 v252, v242, v243\n"
"v_cvt_pk_bf16_f32 v253, v244, v245\n"
"buffer_store_dwordx4 v[250:253], v1, s[60:63], s45 offen offset:1024 sc1\n"
"v_accvgpr_read_b32 v230, a36\n"
"v_accvgpr_read_b32 v231, a37\n"
"v_accvgpr_read_b32 v232, a38\n"
"v_accvgpr_read_b32 v233, a39\n"
"v_accvgpr_read_b32 v234, a44\n"
"v_accvgpr_read_b32 v235, a45\n"
"v_accvgpr_read_b32 v236, a46\n"
"v_accvgpr_read_b32 v237, a47\n"
"v_pk_mul_f32 v[230:231], v[230:231], v[12:13]\n"
"v_pk_mul_f32 v[232:233], v[232:233], v[12:13]\n"
"v_pk_mul_f32 v[234:235], v[234:235], v[12:13]\n"
"v_pk_mul_f32 v[236:237], v[236:237], v[12:13]\n"
"v_cvt_pk_bf16_f32 v246, v230, v231\n"
"v_cvt_pk_bf16_f32 v247, v232, v233\n"
"v_cvt_pk_bf16_f32 v248, v234, v235\n"
"v_cvt_pk_bf16_f32 v249, v236, v237\n"
"buffer_store_dwordx4 v[246:249], v1, s[60:63], s45 offen offset:2048 sc1\n"
"v_accvgpr_read_b32 v238, a52\n"
"v_accvgpr_read_b32 v239, a53\n"
"v_accvgpr_read_b32 v240, a54\n"
"v_accvgpr_read_b32 v241, a55\n"
"v_accvgpr_read_b32 v242, a60\n"
"v_accvgpr_read_b32 v243, a61\n"
"v_accvgpr_read_b32 v244, a62\n"
"v_accvgpr_read_b32 v245, a63\n"
"v_pk_mul_f32 v[238:239], v[238:239], v[12:13]\n"
"v_pk_mul_f32 v[240:241], v[240:241], v[12:13]\n"
"v_pk_mul_f32 v[242:243], v[242:243], v[12:13]\n"
"v_pk_mul_f32 v[244:245], v[244:245], v[12:13]\n"
"v_cvt_pk_bf16_f32 v250, v238, v239\n"
"v_cvt_pk_bf16_f32 v251, v240, v241\n"
"v_cvt_pk_bf16_f32 v252, v242, v243\n"
"v_cvt_pk_bf16_f32 v253, v244, v245\n"
"buffer_store_dwordx4 v[250:253], v1, s[60:63], s45 offen offset:3072 sc1\n"
"s_lshl_b32 s44, s38, 2\n"
"s_sub_u32 s85, s44, 27\n"
"LA_sloop:\n"
"s_waitcnt lgkmcnt(8)\n"
"v_mfma_f32_16x16x32_bf16 v[48:51], a[192:195], a[160:163], v[120:123]\n"
"v_exp_f32 v74, v74\n"
"v_mfma_f32_16x16x32_bf16 v[48:51], a[196:199], a[164:167], v[48:51]\n"
"v_exp_f32 v75, v75\n"
"v_mfma_f32_16x16x32_bf16 v[48:51], a[200:203], a[168:171], v[48:51]\n"
"ds_read_b128 a[224:227], v2 offset:0\n"
"v_cvt_pk_bf16_f32 v107, v74, v75\n"
"v_mfma_f32_16x16x32_bf16 v[48:51], a[204:207], a[172:175], v[48:51]\n"
"ds_read_b128 a[228:231], v2 offset:1024\n"
"ds_read_b128 a[232:235], v2 offset:2048\n"
"v_mfma_f32_16x16x32_bf16 v[52:55], a[192:195], a[176:179], v[124:127]\n"
"v_exp_f32 v68, v68\n"
"v_mfma_f32_16x16x32_bf16 v[52:55], a[196:199], a[180:183], v[52:55]\n"
"ds_read_b128 a[236:239], v2 offset:3072\n"
"s_and_b32 m0, s90, 0x1ffff\n"
"v_mfma_f32_16x16x32_bf16 v[52:55], a[200:203], a[184:187], v[52:55]\n"
"v_exp_f32 v69, v69\n"
"v_mfma_f32_16x16x32_bf16 v[52:55], a[204:207], a[188:191], v[52:55]\n"
"ds_read_b128 a[240:243], v2 offset:4096\n"
"v_cvt_pk_bf16_f32 v108, v68, v69\n"
"v_mfma_f32_16x16x32_bf16 v[56:59], a[208:211], a[160:163], v[120:123]\n"
"v_exp_f32 v70, v70\n"
"v_mfma_f32_16x16x32_bf16 v[56:59], a[212:215], a[164:167], v[56:59]\n"
"ds_read_b128 a[244:247], v2 offset:5120\n"
"v_mfma_f32_16x16x32_bf16 v[56:59], a[216:219], a[168:171], v[56:59]\n"
"v_exp_f32 v71, v71\n"
"v_mfma_f32_16x16x32_bf16 v[56:59], a[220:223], a[172:175], v[56:59]\n"
"ds_read_b128 a[248:251], v2 offset:6144\n"
"v_cvt_pk_bf16_f32 v109, v70, v71\n"
"v_mfma_f32_16x16x32_bf16 v[60:63], a[208:211], a[176:179], v[124:127]\n"
"ds_read_b128 a[252:255], v2 offset:7168\n"
"v_mfma_f32_16x16x32_bf16 v[60:63], a[212:215], a[180:183], v[60:63]\n"
"v_exp_f32 v76, v76\n"
"v_mfma_f32_16x16x32_bf16 v[60:63], a[216:219], a[184:187], v[60:63]\n"
"v_exp_f32 v77, v77\n"
"v_mfma_f32_16x16x32_bf16 v[60:63], a[220:223], a[188:191], v[60:63]\n"
"s_waitcnt lgkmcnt(8)\n"
"v_cvt_pk_bf16_f32 v110, v76, v77\n"
"v_mfma_f32_16x16x32_bf16 v[136:139], v[224:227], v[104:107], v[136:139]\n"
"buffer_load_dwordx4 v3, s[52:55], s91 offen sc1 lds\n"
"v_mfma_f32_16x16x32_bf16 a[64:67], v[192:195], v[104:107], a[64:67]\n"
"v_exp_f32 v78, v78\n"
"v_mfma_f32_16x16x32_bf16 a[72:75], v[196:199], v[104:107], a[72:75]\n"
"v_exp_f32 v79, v79\n"
"v_mfma_f32_16x16x32_bf16 a[80:83], v[200:203], v[104:107], a[80:83]\n"
"s_add_u32 s90, s90, 0x4000\n"
"v_cvt_pk_bf16_f32 v111, v78, v79\n"
"v_mfma_f32_16x16x32_bf16 a[88:91], v[204:207], v[104:107], a[88:91]\n"
"ds_read_b128 v[160:163], v2 offset:8192\n"
"ds_read_b128 v[164:167], v2 offset:9216\n"
"v_mfma_f32_16x16x32_bf16 a[96:99], v[208:211], v[104:107], a[96:99]\n"
"buffer_load_dwordx4 v3, s[52:55], s91 offen offset:1024 sc1 lds\n"
"v_mfma_f32_16x16x32_bf16 a[104:107], v[212:215], v[104:107], a[104:107]\n"
"s_add_u32 m0, m0, 0x2000\n"
"ds_read_b128 v[168:171], v2 offset:10240\n"
"v_mfma_f32_16x16x32_bf16 a[112:115], v[216:219], v[104:107], a[112:115]\n"
"v_exp_f32 v48, v48\n"
"v_mfma_f32_16x16x32_bf16 a[120:123], v[220:223], v[104:107], a[120:123]\n"
"ds_read_b128 v[172:175], v2 offset:11264\n"
"ds_read_b128 v[176:179], v2 offset:12288\n"
"v_mfma_f32_16x16x32_bf16 v[140:143], v[224:227], v[108:111], v[140:143]\n"
"buffer_load_dwordx4 v4, s[52:55], s91 offen sc1 lds\n"
"v_mfma_f32_16x16x32_bf16 a[68:71], v[192:195], v[108:111], a[68:71]\n"
"buffer_load_dwordx4 v4, s[52:55], s91 offen offset:1024 sc1 lds\n"
"v_mfma_f32_16x16x32_bf16 a[76:79], v[196:199], v[108:111], a[76:79]\n"
"v_exp_f32 v49, v49\n"
"v_mfma_f32_16x16x32_bf16 a[84:87], v[200:203], v[108:111], a[84:87]\n"
"ds_read_b128 v[180:183], v2 offset:13312\n"
"v_cvt_pk_bf16_f32 v96, v48, v49\n"
"v_mfma_f32_16x16x32_bf16 a[92:95], v[204:207], v[108:111], a[92:95]\n"
"v_exp_f32 v50, v50\n"
"v_mfma_f32_16x16x32_bf16 a[100:103], v[208:211], v[108:111], a[100:103]\n"
"v_exp_f32 v51, v51\n"
"v_mfma_f32_16x16x32_bf16 a[108:111], v[212:215], v[108:111], a[108:111]\n"
"ds_read_b128 v[184:187], v2 offset:14336\n"
"ds_read_b128 v[188:191], v2 offset:15360\n"
"s_add_u32 s91, s91, 0x4000\n"
"v_cvt_pk_bf16_f32 v97, v50, v51\n"
"v_exp_f32 v56, v56\n"
"v_mfma_f32_16x16x32_bf16 a[116:119], v[216:219], v[108:111], a[116:119]\n"
"v_exp_f32 v57, v57\n"
"v_mfma_f32_16x16x32_bf16 a[124:127], v[220:223], v[108:111], a[124:127]\n"
"v_cvt_pk_bf16_f32 v98, v56, v57\n"
"v_add_u32 v2, 0x4000, v2\n"
"v_and_b32 v2, 0x1ffff, v2\n"
"s_waitcnt lgkmcnt(8)\n"
"v_mfma_f32_16x16x32_bf16 v[64:67], a[224:227], a[160:163], v[120:123]\n"
"s_waitcnt vmcnt(12)\n"
"s_barrier\n"
"v_mfma_f32_16x16x32_bf16 v[64:67], a[228:231], a[164:167], v[64:67]\n"
"v_exp_f32 v58, v58\n"
"v_mfma_f32_16x16x32_bf16 v[64:67], a[232:235], a[168:171], v[64:67]\n"
"v_exp_f32 v59, v59\n"
"v_mfma_f32_16x16x32_bf16 v[64:67], a[236:239], a[172:175], v[64:67]\n"
"ds_read_b128 a[192:195], v2 offset:0\n"
"v_cvt_pk_bf16_f32 v99, v58, v59\n"
"v_mfma_f32_16x16x32_bf16 v[68:71], a[224:227], a[176:179], v[124:127]\n"
"ds_read_b128 a[196:199], v2 offset:1024\n"
"ds_read_b128 a[200:203], v2 offset:2048\n"
"v_mfma_f32_16x16x32_bf16 v[68:71], a[228:231], a[180:183], v[68:71]\n"
"v_exp_f32 v52, v52\n"
"v_mfma_f32_16x16x32_bf16 v[68:71], a[232:235], a[184:187], v[68:71]\n"
"ds_read_b128 a[204:207], v2 offset:3072\n"
"s_and_b32 m0, s90, 0x1ffff\n"
"v_mfma_f32_16x16x32_bf16 v[68:71], a[236:239], a[188:191], v[68:71]\n"
"v_exp_f32 v53, v53\n"
"v_mfma_f32_16x16x32_bf16 v[72:75], a[240:243], a[160:163], v[120:123]\n"
"ds_read_b128 a[208:211], v2 offset:4096\n"
"v_cvt_pk_bf16_f32 v100, v52, v53\n"
"v_mfma_f32_16x16x32_bf16 v[72:75], a[244:247], a[164:167], v[72:75]\n"
"ds_read_b128 a[212:215], v2 offset:5120\n"
"v_mfma_f32_16x16x32_bf16 v[72:75], a[248:251], a[168:171], v[72:75]\n"
"v_exp_f32 v54, v54\n"
"v_mfma_f32_16x16x32_bf16 v[72:75], a[252:255], a[172:175], v[72:75]\n"
"ds_read_b128 a[216:219], v2 offset:6144\n"
"v_mfma_f32_16x16x32_bf16 v[76:79], a[240:243], a[176:179], v[124:127]\n"
"ds_read_b128 a[220:223], v2 offset:7168\n"
"v_mfma_f32_16x16x32_bf16 v[76:79], a[244:247], a[180:183], v[76:79]\n"
"v_exp_f32 v55, v55\n"
"v_mfma_f32_16x16x32_bf16 v[76:79], a[248:251], a[184:187], v[76:79]\n"
"v_cvt_pk_bf16_f32 v101, v54, v55\n"
"v_mfma_f32_16x16x32_bf16 v[76:79], a[252:255], a[188:191], v[76:79]\n"
"s_waitcnt lgkmcnt(8)\n"
"v_exp_f32 v60, v60\n"
"v_mfma_f32_16x16x32_bf16 v[136:139], v[224:227], v[96:99], v[136:139]\n"
"v_exp_f32 v61, v61\n"
"v_mfma_f32_16x16x32_bf16 a[64:67], v[160:163], v[96:99], a[64:67]\n"
"ds_read_b128 v[192:195], v2 offset:8192\n"
"v_cvt_pk_bf16_f32 v102, v60, v61\n"
"v_mfma_f32_16x16x32_bf16 a[72:75], v[164:167], v[96:99], a[72:75]\n"
"v_exp_f32 v62, v62\n"
"v_mfma_f32_16x16x32_bf16 a[80:83], v[168:171], v[96:99], a[80:83]\n"
"v_exp_f32 v63, v63\n"
"v_mfma_f32_16x16x32_bf16 a[88:91], v[172:175], v[96:99], a[88:91]\n"
"ds_read_b128 v[196:199], v2 offset:9216\n"
"v_cvt_pk_bf16_f32 v103, v62, v63\n"
"v_mfma_f32_16x16x32_bf16 a[96:99], v[176:179], v[96:99], a[96:99]\n"
"buffer_load_dwordx4 v3, s[52:55], s91 offen sc1 lds\n"
"v_mfma_f32_16x16x32_bf16 a[104:107], v[180:183], v[96:99], a[104:107]\n"
"s_add_u32 s90, s90, 0x4000\n"
"ds_read_b128 v[200:203], v2 offset:10240\n"
"v_mfma_f32_16x16x32_bf16 a[112:115], v[184:187], v[96:99], a[112:115]\n"
"buffer_load_dwordx4 v3, s[52:55], s91 offen offset:1024 sc1 lds\n"
"v_mfma_f32_16x16x32_bf16 a[120:123], v[188:191], v[96:99], a[120:123]\n"
"s_add_u32 m0, m0, 0x2000\n"
"ds_read_b128 v[204:207], v2 offset:11264\n"
"v_mfma_f32_16x16x32_bf16 v[140:143], v[224:227], v[100:103], v[140:143]\n"
"v_exp_f32 v64, v64\n"
"v_mfma_f32_16x16x32_bf16 a[68:71], v[160:163], v[100:103], a[68:71]\n"
"buffer_load_dwordx4 v4, s[52:55], s91 offen sc1 lds\n"
"v_mfma_f32_16x16x32_bf16 a[76:79], v[164:167], v[100:103], a[76:79]\n"
"v_exp_f32 v65, v65\n"
"v_mfma_f32_16x16x32_bf16 a[84:87], v[168:171], v[100:103], a[84:87]\n"
"ds_read_b128 v[208:211], v2 offset:12288\n"
"v_cvt_pk_bf16_f32 v104, v64, v65\n"
"v_mfma_f32_16x16x32_bf16 a[92:95], v[172:175], v[100:103], a[92:95]\n"
"v_exp_f32 v66, v66\n"
"v_mfma_f32_16x16x32_bf16 a[100:103], v[176:179], v[100:103], a[100:103]\n"
"v_exp_f32 v67, v67\n"
"v_mfma_f32_16x16x32_bf16 a[108:111], v[180:183], v[100:103], a[108:111]\n"
"ds_read_b128 v[212:215], v2 offset:13312\n"
"buffer_load_dwordx4 v4, s[52:55], s91 offen offset:1024 sc1 lds\n"
"ds_read_b128 v[216:219], v2 offset:14336\n"
"s_add_u32 s91, s91, 0x4000\n"
"ds_read_b128 v[220:223], v2 offset:15360\n"
"v_mfma_f32_16x16x32_bf16 a[116:119], v[184:187], v[100:103], a[116:119]\n"
"s_add_u32 s85, s85, 1\n"
"v_cvt_pk_bf16_f32 v105, v66, v67\n"
"v_exp_f32 v72, v72\n"
"v_exp_f32 v73, v73\n"
"v_mfma_f32_16x16x32_bf16 a[124:127], v[188:191], v[100:103], a[124:127]\n"
"v_cvt_pk_bf16_f32 v106, v72, v73\n"
"v_add_u32 v2, 0x4000, v2\n"
"v_and_b32 v2, 0x1ffff, v2\n"
"s_cbranch_scc0 LA_sloop\n"
"v_and_b32 v52, 63, v0\n"
"v_lshlrev_b32 v38, 4, v52\n"
"v_and_b32 v39, 15, v52\n"
"v_lshrrev_b32 v40, 4, v52\n"
"s_and_b32 s80, s30, 3\n"
"s_lshr_b32 s44, s30, 2\n"
"s_and_b32 s44, s44, 1\n"
"s_lshl_b32 s44, s44, 3\n"
"s_lshr_b32 s45, s30, 5\n"
"s_add_u32 s81, s44, s45\n"
"s_lshr_b32 s45, s30, 3\n"
"s_and_b32 s84, s45, 3\n"
"s_lshl_b32 s88, s81, 2\n"
"s_add_u32 s88, s88, s84\n"
"s_mov_b32 s52, s18\n"
"s_and_b32 s53, s19, 0xffff\n"
"s_mov_b32 s54, 0x400000\n"
"s_mov_b32 s55, 0x20000\n"
"s_mov_b32 s56, s24\n"
"s_and_b32 s57, s25, 0xffff\n"
"s_mov_b32 s58, 0x1c0000\n"
"s_mov_b32 s59, 0x20000\n"
"s_add_u32 s60, s26, 0x2000000\n"
"s_addc_u32 s61, s27, 0\n"
"s_and_b32 s61, s61, 0xffff\n"
"s_mov_b32 s62, 0x1700000\n"
"s_mov_b32 s63, 0x20000\n"
"s_lshl_b32 s44, s34, 1\n"
"s_add_u32 s44, s44, 0\n"
"s_and_b32 s45, s44, 4\n"
"s_add_u32 s44, s44, s80\n"
"s_and_b32 s44, s44, 3\n"
"s_add_u32 s44, s44, s45\n"
"s_lshl_b32 s45, s81, 3\n"
"s_add_u32 s44, s44, s45\n"
"s_mul_i32 s44, s44, 188416\n"
"s_mul_i32 s45, s80, 14336\n"
"s_add_u32 s68, s44, s45\n"
"s_add_u32 s68, s68, 131072\n"
"s_lshl_b32 s45, s80, 15\n"
"s_add_u32 s70, s44, s45\n"
"s_lshl_b32 s44, s34, 1\n"
"s_add_u32 s44, s44, 1\n"
"s_and_b32 s45, s44, 4\n"
"s_add_u32 s44, s44, s80\n"
"s_and_b32 s44, s44, 3\n"
"s_add_u32 s44, s44, s45\n"
"s_lshl_b32 s45, s81, 3\n"
"s_add_u32 s44, s44, s45\n"
"s_mul_i32 s44, s44, 188416\n"
"s_mul_i32 s45, s80, 14336\n"
"s_add_u32 s69, s44, s45\n"
"s_add_u32 s69, s69, 131072\n"
"s_lshl_b32 s45, s80, 15\n"
"s_add_u32 s71, s44, s45\n"
"s_lshl_b32 s82, s34, 12\n"
"s_lshl_b32 s83, s34, 12\n"
"s_lshl_b32 s46, s84, 8\n"
"s_lshl_b32 s44, s39, 5\n"
"s_lshl_b32 s45, s40, 3\n"
"s_add_u32 s44, s44, s45\n"
"s_add_u32 s44, s44, 0\n"
"s_lshl_b32 s45, s80, 10\n"
"s_add_u32 s44, s44, s45\n"
"s_lshl_b32 s44, s44, 10\n"
"s_add_u32 s76, s44, s46\n"
"s_lshl_b32 s44, s39, 5\n"
"s_lshl_b32 s45, s40, 3\n"
"s_add_u32 s44, s44, s45\n"
"s_add_u32 s44, s44, 0\n"
"s_mul_i32 s45, s80, 448\n"
"s_add_u32 s44, s44, s45\n"
"s_lshl_b32 s44, s44, 10\n"
"s_add_u32 s72, s44, s46\n"
"s_lshl_b32 s44, s39, 5\n"
"s_lshl_b32 s45, s40, 3\n"
"s_add_u32 s44, s44, s45\n"
"s_add_u32 s44, s44, 16\n"
"s_lshl_b32 s45, s80, 10\n"
"s_add_u32 s44, s44, s45\n"
"s_lshl_b32 s44, s44, 10\n"
"s_add_u32 s77, s44, s46\n"
"s_lshl_b32 s44, s39, 5\n"
"s_lshl_b32 s45, s40, 3\n"
"s_add_u32 s44, s44, s45\n"
"s_add_u32 s44, s44, 16\n"
"s_mul_i32 s45, s80, 448\n"
"s_add_u32 s44, s44, s45\n"
"s_lshl_b32 s44, s44, 10\n"
"s_add_u32 s73, s44, s46\n"
"s_lshl_b32 s44, s39, 5\n"
"s_lshl_b32 s45, s40, 3\n"
"s_add_u32 s44, s44, s45\n"
"s_add_u32 s44, s44, 4\n"
"s_lshl_b32 s45, s80, 10\n"
"s_add_u32 s44, s44, s45\n"
"s_lshl_b32 s44, s44, 10\n"
"s_add_u32 s78, s44, s46\n"
"s_lshl_b32 s44, s39, 5\n"
"s_lshl_b32 s45, s40, 3\n"
"s_add_u32 s44, s44, s45\n"
"s_add_u32 s44, s44, 4\n"
"s_mul_i32 s45, s80, 448\n"
"s_add_u32 s44, s44, s45\n"
"s_lshl_b32 s44, s44, 10\n"
"s_add_u32 s74, s44, s46\n"
"s_lshl_b32 s44, s39, 5\n"
"s_lshl_b32 s45, s40, 3\n"
"s_add_u32 s44, s44, s45\n"
"s_add_u32 s44, s44, 20\n"
"s_lshl_b32 s45, s80, 10\n"
"s_add_u32 s44, s44, s45\n"
"s_lshl_b32 s44, s44, 10\n"
"s_add_u32 s79, s44, s46\n"
"s_lshl_b32 s44, s39, 5\n"
"s_lshl_b32 s45, s40, 3\n"
"s_add_u32 s44, s44, s45\n"
"s_add_u32 s44, s44, 20\n"
"s_mul_i32 s45, s80, 448\n"
"s_add_u32 s44, s44, s45\n"
"s_lshl_b32 s44, s44, 10\n"
"s_add_u32 s75, s44, s46\n"
"v_add_u32 v34, 0x0, v38\n"
"v_mov_b32 v35, v38\n"
"v_lshrrev_b32 v52, 1, v39\n"
"v_xor_b32 v52, v52, v40\n"
"v_lshlrev_b32 v52, 5, v52\n"
"v_and_b32 v53, 1, v39\n"
"v_lshl_add_u32 v52, v53, 4, v52\n"
"v_lshl_add_u32 v37, v40, 10, v52\n"
"v_xor_b32 v42, 0x80, v37\n"
"v_add_u32 v36, 0x0, v37\n"
"v_add_u32 v41, 0x0, v42\n"
"s_lshl_b32 s44, s39, 13\n"
"s_add_u32 s44, s44, 0x0\n"
"v_add_u32 v24, s44, v38\n"
"v_add_u32 v25, 0x400, v24\n"
"v_lshrrev_b32 v52, 2, v39\n"
"v_and_b32 v53, 3, v39\n"
"v_lshlrev_b32 v54, 11, v40\n"
"v_lshl_add_u32 v54, v52, 8, v54\n"
"v_lshl_add_u32 v54, v53, 3, v54\n"
"v_add_u32 v54, 0x4000, v54\n"
"v_and_b32 v53, 1, v40\n"
"v_lshl_or_b32 v52, v53, 2, v52\n"
"s_lshl_b32 s45, s40, 2\n"
"s_add_u32 s44, s45, 0\n"
"v_xor_b32 v55, s44, v52\n"
"v_lshl_add_u32 v26, v55, 5, v54\n"
"v_add_u32 v30, 0x2000, v26\n"
"s_add_u32 s44, s45, 1\n"
"v_xor_b32 v55, s44, v52\n"
"v_lshl_add_u32 v27, v55, 5, v54\n"
"v_add_u32 v31, 0x2000, v27\n"
"s_add_u32 s44, s45, 2\n"
"v_xor_b32 v55, s44, v52\n"
"v_lshl_add_u32 v28, v55, 5, v54\n"
"v_add_u32 v32, 0x2000, v28\n"
"s_add_u32 s44, s45, 3\n"
"v_xor_b32 v55, s44, v52\n"
"v_lshl_add_u32 v29, v55, 5, v54\n"
"v_add_u32 v33, 0x2000, v29\n"
"s_getpc_b64 s[48:49]\n"
"LM_pc2:\n"
"s_add_u32 s48, s48, LB_tailcode-LM_pc2\n"
"s_addc_u32 s49, s49, 0\n"
"s_and_b32 s49, s49, 0xffff\n"
"s_mov_b32 s50, LM_codeend-LB_tailcode\n"
"s_mov_b32 s51, 0x20000\n"
"s_lshl_b32 s44, s36, 2\n"
"s_add_u32 s44, s44, s34\n"
"s_mul_i32 s44, s44, 256\n"
"v_and_b32 v19, 1, v0\n"
"v_lshlrev_b32 v19, 7, v19\n"
"buffer_load_dword v18, v19, s[48:51], s44 offen\n"
"s_cmp_lg_u32 s34, 0\n"
"s_cbranch_scc1 LM_pact_iss_0\n"
"v_and_b32 v43, 63, v0\n"
"s_sub_u32 s45, 15, s81\n"
"s_min_u32 s46, s81, s45\n"
"s_mul_i32 s44, s46, 28\n"
"s_mul_i32 s45, s80, 7\n"
"s_add_u32 s44, s44, s45\n"
"s_lshl_b32 s44, s44, 7\n"
"s_add_u32 s44, s44, 36864\n"
"v_lshlrev_b32 v44, 7, v43\n"
"v_add_u32 v44, s44, v44\n"
"s_lshl_b32 s45, s46, 7\n"
"s_add_u32 s45, s45, 69632\n"
"v_mov_b32 v45, s45\n"
"v_cmp_gt_u32 vcc, 7, v43\n"
"s_nop 1\n"
"v_cndmask_b32 v44, v45, v44, vcc\n"
"buffer_load_dwordx2 v[46:47], v44, s[64:67], 0 offen sc1 ;@cond\n"
"LM_pact_iss_0:\n"
"s_lshl_b32 s44, s34, 5\n"
"v_cvt_f32_u32 v12, s44\n"
"v_mul_f32 v12, 0xec800000, v12\n"
"v_add_f32 v9, v12, v11\n"
"v_add_f32 v10, 0x6e800000, v9\n"
"v_add_f32 v144, 0, v9\n"
"v_min_f32 v144, v144, v7\n"
"v_add_f32 v145, 0xec800000, v9\n"
"v_min_f32 v145, v145, v7\n"
"v_add_f32 v146, 0xed000000, v9\n"
"v_min_f32 v146, v146, v7\n"
"v_add_f32 v147, 0xed400000, v9\n"
"v_min_f32 v147, v147, v7\n"
"v_add_f32 v148, 0, v10\n"
"v_min_f32 v148, v148, v8\n"
"v_add_f32 v149, 0xec800000, v10\n"
"v_min_f32 v149, v149, v8\n"
"v_add_f32 v150, 0xed000000, v10\n"
"v_min_f32 v150, v150, v8\n"
"v_add_f32 v151, 0xed400000, v10\n"
"v_min_f32 v151, v151, v8\n"
"v_add_f32 v152, 0xee800000, v9\n"
"v_min_f32 v152, v152, v7\n"
"v_add_f32 v153, 0xee880000, v9\n"
"v_min_f32 v153, v153, v7\n"
"v_add_f32 v154, 0xee900000, v9\n"
"v_min_f32 v154, v154, v7\n"
"v_add_f32 v155, 0xee980000, v9\n"
"v_min_f32 v155, v155, v7\n"
"v_add_f32 v156, 0xee800000, v10\n"
"v_min_f32 v156, v156, v8\n"
"v_add_f32 v157, 0xee880000, v10\n"
"v_min_f32 v157, v157, v8\n"
"v_add_f32 v158, 0xee900000, v10\n"
"v_min_f32 v158, v158, v8\n"
"v_add_f32 v159, 0xee980000, v10\n"
"v_min_f32 v159, v159, v8\n"
"s_waitcnt vmcnt(4)\n"
"s_barrier\n"
"s_cmp_eq_u32 s34, 0\n"
"s_cbranch_scc1 LB_w0\n"
"s_cmp_eq_u32 s34, 1\n"
"s_cbranch_scc1 LB_w1\n"
"s_cmp_eq_u32 s34, 2\n"
"s_cbranch_scc1 LB_w2\n"
"s_waitcnt lgkmcnt(8)\n"
"v_mfma_f32_16x16x32_bf16 v[48:51], a[192:195], a[160:163], v[120:123]\n"
"v_exp_f32 v74, v74\n"
"v_mfma_f32_16x16x32_bf16 v[48:51], a[196:199], a[164:167], v[48:51]\n"
"v_exp_f32 v75, v75\n"
"v_mfma_f32_16x16x32_bf16 v[48:51], a[200:203], a[168:171], v[48:51]\n"
"ds_read_b128 a[224:227], v2 offset:0\n"
"v_cvt_pk_bf16_f32 v107, v74, v75\n"
"v_mfma_f32_16x16x32_bf16 v[48:51], a[204:207], a[172:175], v[48:51]\n"
"ds_read_b128 a[228:231], v2 offset:1024\n"
"ds_read_b128 a[232:235], v2 offset:2048\n"
"v_mfma_f32_16x16x32_bf16 v[52:55], a[192:195], a[176:179], v[124:127]\n"
"v_exp_f32 v68, v68\n"
"v_mfma_f32_16x16x32_bf16 v[52:55], a[196:199], a[180:183], v[52:55]\n"
"ds_read_b128 a[236:239], v2 offset:3072\n"
"v_mfma_f32_16x16x32_bf16 v[52:55], a[200:203], a[184:187], v[52:55]\n"
"v_exp_f32 v69, v69\n"
"v_mfma_f32_16x16x32_bf16 v[52:55], a[204:207], a[188:191], v[52:55]\n"
"ds_read_b128 a[240:243], v2 offset:4096\n"
"v_cvt_pk_bf16_f32 v108, v68, v69\n"
"v_mfma_f32_16x16x32_bf16 v[56:59], a[208:211], a[160:163], v[120:123]\n"
"v_exp_f32 v70, v70\n"
"v_mfma_f32_16x16x32_bf16 v[56:59], a[212:215], a[164:167], v[56:59]\n"
"ds_read_b128 a[244:247], v2 offset:5120\n"
"v_mfma_f32_16x16x32_bf16 v[56:59], a[216:219], a[168:171], v[56:59]\n"
"v_exp_f32 v71, v71\n"
"v_mfma_f32_16x16x32_bf16 v[56:59], a[220:223], a[172:175], v[56:59]\n"
"ds_read_b128 a[248:251], v2 offset:6144\n"
"v_cvt_pk_bf16_f32 v109, v70, v71\n"
"v_mfma_f32_16x16x32_bf16 v[60:63], a[208:211], a[176:179], v[124:127]\n"
"ds_read_b128 a[252:255], v2 offset:7168\n"
"v_mfma_f32_16x16x32_bf16 v[60:63], a[212:215], a[180:183], v[60:63]\n"
"v_exp_f32 v76, v76\n"
"v_mfma_f32_16x16x32_bf16 v[60:63], a[216:219], a[184:187], v[60:63]\n"
"v_exp_f32 v77, v77\n"
"v_mfma_f32_16x16x32_bf16 v[60:63], a[220:223], a[188:191], v[60:63]\n"
"s_waitcnt lgkmcnt(8)\n"
"v_cvt_pk_bf16_f32 v110, v76, v77\n"
"v_mfma_f32_16x16x32_bf16 v[136:139], v[224:227], v[104:107], v[136:139]\n"
"ds_read_b128 v[160:163], v2 offset:8192\n"
"v_mfma_f32_16x16x32_bf16 a[64:67], v[192:195], v[104:107], a[64:67]\n"
"v_exp_f32 v78, v78\n"
"v_mfma_f32_16x16x32_bf16 a[72:75], v[196:199], v[104:107], a[72:75]\n"
"v_exp_f32 v79, v79\n"
"v_mfma_f32_16x16x32_bf16 a[80:83], v[200:203], v[104:107], a[80:83]\n"
"ds_read_b128 v[164:167], v2 offset:9216\n"
"v_cvt_pk_bf16_f32 v111, v78, v79\n"
"v_mfma_f32_16x16x32_bf16 a[88:91], v[204:207], v[104:107], a[88:91]\n"
"ds_read_b128 v[168:171], v2 offset:10240\n"
"v_mfma_f32_16x16x32_bf16 a[96:99], v[208:211], v[104:107], a[96:99]\n"
"v_exp_f32 v48, v48\n"
"v_mfma_f32_16x16x32_bf16 a[104:107], v[212:215], v[104:107], a[104:107]\n"
"ds_read_b128 v[172:175], v2 offset:11264\n"
"v_mfma_f32_16x16x32_bf16 a[112:115], v[216:219], v[104:107], a[112:115]\n"
"v_exp_f32 v49, v49\n"
"v_mfma_f32_16x16x32_bf16 a[120:123], v[220:223], v[104:107], a[120:123]\n"
"ds_read_b128 v[176:179], v2 offset:12288\n"
"v_cvt_pk_bf16_f32 v96, v48, v49\n"
"v_mfma_f32_16x16x32_bf16 v[140:143], v[224:227], v[108:111], v[140:143]\n"
"v_exp_f32 v50, v50\n"
"v_mfma_f32_16x16x32_bf16 a[68:71], v[192:195], v[108:111], a[68:71]\n"
"ds_read_b128 v[180:183], v2 offset:13312\n"
"v_mfma_f32_16x16x32_bf16 a[76:79], v[196:199], v[108:111], a[76:79]\n"
"v_exp_f32 v51, v51\n"
"v_mfma_f32_16x16x32_bf16 a[84:87], v[200:203], v[108:111], a[84:87]\n"
"ds_read_b128 v[184:187], v2 offset:14336\n"
"v_cvt_pk_bf16_f32 v97, v50, v51\n"
"v_mfma_f32_16x16x32_bf16 a[92:95], v[204:207], v[108:111], a[92:95]\n"
"v_exp_f32 v56, v56\n"
"v_mfma_f32_16x16x32_bf16 a[100:103], v[208:211], v[108:111], a[100:103]\n"
"ds_read_b128 v[188:191], v2 offset:15360\n"
"v_mfma_f32_16x16x32_bf16 a[108:111], v[212:215], v[108:111], a[108:111]\n"
"v_exp_f32 v57, v57\n"
"v_mfma_f32_16x16x32_bf16 a[116:119], v[216:219], v[108:111], a[116:119]\n"
"v_cvt_pk_bf16_f32 v98, v56, v57\n"
"v_mfma_f32_16x16x32_bf16 a[124:127], v[220:223], v[108:111], a[124:127]\n"
"v_add_u32 v2, 0x4000, v2\n"
"v_and_b32 v2, 0x1ffff, v2\n"
"s_waitcnt lgkmcnt(8)\n"
"v_mfma_f32_16x16x32_bf16 v[64:67], a[224:227], a[160:163], v[120:123]\n"
"v_exp_f32 v58, v58\n"
"v_mfma_f32_16x16x32_bf16 v[64:67], a[228:231], a[164:167], v[64:67]\n"
"v_exp_f32 v59, v59\n"
"v_mfma_f32_16x16x32_bf16 v[64:67], a[232:235], a[168:171], v[64:67]\n"
"ds_read_b128 a[192:195], v2 offset:0\n"
"v_cvt_pk_bf16_f32 v99, v58, v59\n"
"v_mfma_f32_16x16x32_bf16 v[64:67], a[236:239], a[172:175], v[64:67]\n"
"ds_read_b128 a[196:199], v2 offset:1024\n"
"ds_read_b128 a[200:203], v2 offset:2048\n"
"v_mfma_f32_16x16x32_bf16 v[68:71], a[224:227], a[176:179], v[124:127]\n"
"v_exp_f32 v52, v52\n"
"v_mfma_f32_16x16x32_bf16 v[68:71], a[228:231], a[180:183], v[68:71]\n"
"ds_read_b128 a[204:207], v2 offset:3072\n"
"v_mfma_f32_16x16x32_bf16 v[68:71], a[232:235], a[184:187], v[68:71]\n"
"v_exp_f32 v53, v53\n"
"v_mfma_f32_16x16x32_bf16 v[68:71], a[236:239], a[188:191], v[68:71]\n"
"ds_read_b128 a[208:211], v2 offset:4096\n"
"v_cvt_pk_bf16_f32 v100, v52, v53\n"
"v_mfma_f32_16x16x32_bf16 v[72:75], a[240:243], a[160:163], v[120:123]\n"
"v_exp_f32 v54, v54\n"
"v_mfma_f32_16x16x32_bf16 v[72:75], a[244:247], a[164:167], v[72:75]\n"
"ds_read_b128 a[212:215], v2 offset:5120\n"
"v_mfma_f32_16x16x32_bf16 v[72:75], a[248:251], a[168:171], v[72:75]\n"
"v_exp_f32 v55, v55\n"
"v_mfma_f32_16x16x32_bf16 v[72:75], a[252:255], a[172:175], v[72:75]\n"
"ds_read_b128 a[216:219], v2 offset:6144\n"
"v_cvt_pk_bf16_f32 v101, v54, v55\n"
"v_mfma_f32_16x16x32_bf16 v[76:79], a[240:243], a[176:179], v[124:127]\n"
"ds_read_b128 a[220:223], v2 offset:7168\n"
"v_mfma_f32_16x16x32_bf16 v[76:79], a[244:247], a[180:183], v[76:79]\n"
"v_exp_f32 v60, v60\n"
"v_mfma_f32_16x16x32_bf16 v[76:79], a[248:251], a[184:187], v[76:79]\n"
"v_exp_f32 v61, v61\n"
"v_mfma_f32_16x16x32_bf16 v[76:79], a[252:255], a[188:191], v[76:79]\n"
"s_waitcnt lgkmcnt(8)\n"
"v_cvt_pk_bf16_f32 v102, v60, v61\n"
"v_mfma_f32_16x16x32_bf16 v[136:139], v[224:227], v[96:99], v[136:139]\n"
"ds_read_b128 v[192:195], v2 offset:8192\n"
"v_mfma_f32_16x16x32_bf16 a[64:67], v[160:163], v[96:99], a[64:67]\n"
"v_exp_f32 v62, v62\n"
"v_mfma_f32_16x16x32_bf16 a[72:75], v[164:167], v[96:99], a[72:75]\n"
"v_exp_f32 v63, v63\n"
"v_mfma_f32_16x16x32_bf16 a[80:83], v[168:171], v[96:99], a[80:83]\n"
"ds_read_b128 v[196:199], v2 offset:9216\n"
"v_cvt_pk_bf16_f32 v103, v62, v63\n"
"v_mfma_f32_16x16x32_bf16 a[88:91], v[172:175], v[96:99], a[88:91]\n"
"ds_read_b128 v[200:203], v2 offset:10240\n"
"v_mfma_f32_16x16x32_bf16 a[96:99], v[176:179], v[96:99], a[96:99]\n"
"v_exp_f32 v64, v64\n"
"v_mfma_f32_16x16x32_bf16 a[104:107], v[180:183], v[96:99], a[104:107]\n"
"ds_read_b128 v[204:207], v2 offset:11264\n"
"v_mfma_f32_16x16x32_bf16 a[112:115], v[184:187], v[96:99], a[112:115]\n"
"v_exp_f32 v65, v65\n"
"v_mfma_f32_16x16x32_bf16 a[120:123], v[188:191], v[96:99], a[120:123]\n"
"ds_read_b128 v[208:211], v2 offset:12288\n"
"v_cvt_pk_bf16_f32 v104, v64, v65\n"
"v_mfma_f32_16x16x32_bf16 v[140:143], v[224:227], v[100:103], v[140:143]\n"
"v_exp_f32 v66, v66\n"
"v_mfma_f32_16x16x32_bf16 a[68:71], v[160:163], v[100:103], a[68:71]\n"
"ds_read_b128 v[212:215], v2 offset:13312\n"
"v_mfma_f32_16x16x32_bf16 a[76:79], v[164:167], v[100:103], a[76:79]\n"
"v_exp_f32 v67, v67\n"
"v_mfma_f32_16x16x32_bf16 a[84:87], v[168:171], v[100:103], a[84:87]\n"
"ds_read_b128 v[216:219], v2 offset:14336\n"
"v_cvt_pk_bf16_f32 v105, v66, v67\n"
"v_mfma_f32_16x16x32_bf16 a[92:95], v[172:175], v[100:103], a[92:95]\n"
"v_exp_f32 v72, v72\n"
"v_mfma_f32_16x16x32_bf16 a[100:103], v[176:179], v[100:103], a[100:103]\n"
"ds_read_b128 v[220:223], v2 offset:15360\n"
"v_mfma_f32_16x16x32_bf16 a[108:111], v[180:183], v[100:103], a[108:111]\n"
"v_exp_f32 v73, v73\n"
"v_mfma_f32_16x16x32_bf16 a[116:119], v[184:187], v[100:103], a[116:119]\n"
"v_cvt_pk_bf16_f32 v106, v72, v73\n"
"v_mfma_f32_16x16x32_bf16 a[124:127], v[188:191], v[100:103], a[124:127]\n"
"v_add_u32 v2, 0x4000, v2\n"
"v_and_b32 v2, 0x1ffff, v2\n"
"s_waitcnt lgkmcnt(8)\n"
"v_mfma_f32_16x16x32_bf16 v[48:51], a[192:195], a[160:163], v[120:123]\n"
"v_exp_f32 v74, v74\n"
"v_mfma_f32_16x16x32_bf16 v[48:51], a[196:199], a[164:167], v[48:51]\n"
"v_exp_f32 v75, v75\n"
"v_mfma_f32_16x16x32_bf16 v[48:51], a[200:203], a[168:171], v[48:51]\n"
"ds_read_b128 a[224:227], v2 offset:0\n"
"v_cvt_pk_bf16_f32 v107, v74, v75\n"
"v_mfma_f32_16x16x32_bf16 v[48:51], a[204:207], a[172:175], v[48:51]\n"
"ds_read_b128 a[228:231], v2 offset:1024\n"
"ds_read_b128 a[232:235], v2 offset:2048\n"
"v_mfma_f32_16x16x32_bf16 v[52:55], a[192:195], a[176:179], v[124:127]\n"
"v_exp_f32 v68, v68\n"
"v_mfma_f32_16x16x32_bf16 v[52:55], a[196:199], a[180:183], v[52:55]\n"
"ds_read_b128 a[236:239], v2 offset:3072\n"
"v_mfma_f32_16x16x32_bf16 v[52:55], a[200:203], a[184:187], v[52:55]\n"
"v_exp_f32 v69, v69\n"
"v_mfma_f32_16x16x32_bf16 v[52:55], a[204:207], a[188:191], v[52:55]\n"
"ds_read_b128 a[240:243], v2 offset:4096\n"
"v_cvt_pk_bf16_f32 v108, v68, v69\n"
"v_mfma_f32_16x16x32_bf16 v[56:59], a[208:211], a[160:163], v[120:123]\n"
"v_exp_f32 v70, v70\n"
"v_mfma_f32_16x16x32_bf16 v[56:59], a[212:215], a[164:167], v[56:59]\n"
"ds_read_b128 a[244:247], v2 offset:5120\n"
"v_mfma_f32_16x16x32_bf16 v[56:59], a[216:219], a[168:171], v[56:59]\n"
"v_exp_f32 v71, v71\n"
"v_mfma_f32_16x16x32_bf16 v[56:59], a[220:223], a[172:175], v[56:59]\n"
"ds_read_b128 a[248:251], v2 offset:6144\n"
"v_cvt_pk_bf16_f32 v109, v70, v71\n"
"v_mfma_f32_16x16x32_bf16 v[60:63], a[208:211], a[176:179], v[124:127]\n"
"ds_read_b128 a[252:255], v2 offset:7168\n"
"v_mfma_f32_16x16x32_bf16 v[60:63], a[212:215], a[180:183], v[60:63]\n"
"v_exp_f32 v76, v76\n"
"v_mfma_f32_16x16x32_bf16 v[60:63], a[216:219], a[184:187], v[60:63]\n"
"v_exp_f32 v77, v77\n"
"v_mfma_f32_16x16x32_bf16 v[60:63], a[220:223], a[188:191], v[60:63]\n"
"s_waitcnt lgkmcnt(8)\n"
"v_cvt_pk_bf16_f32 v110, v76, v77\n"
"v_mfma_f32_16x16x32_bf16 v[136:139], v[224:227], v[104:107], v[136:139]\n"
"ds_read_b128 v[160:163], v2 offset:8192\n"
"v_mfma_f32_16x16x32_bf16 a[64:67], v[192:195], v[104:107], a[64:67]\n"
"v_exp_f32 v78, v78\n"
"v_mfma_f32_16x16x32_bf16 a[72:75], v[196:199], v[104:107], a[72:75]\n"
"v_exp_f32 v79, v79\n"
"v_mfma_f32_16x16x32_bf16 a[80:83], v[200:203], v[104:107], a[80:83]\n"
"ds_read_b128 v[164:167], v2 offset:9216\n"
"v_cvt_pk_bf16_f32 v111, v78, v79\n"
"v_mfma_f32_16x16x32_bf16 a[88:91], v[204:207], v[104:107], a[88:91]\n"
"ds_read_b128 v[168:171], v2 offset:10240\n"
"v_mfma_f32_16x16x32_bf16 a[96:99], v[208:211], v[104:107], a[96:99]\n"
"v_exp_f32 v48, v48\n"
"v_mfma_f32_16x16x32_bf16 a[104:107], v[212:215], v[104:107], a[104:107]\n"
"ds_read_b128 v[172:175], v2 offset:11264\n"
"v_mfma_f32_16x16x32_bf16 a[112:115], v[216:219], v[104:107], a[112:115]\n"
"v_exp_f32 v49, v49\n"
"v_mfma_f32_16x16x32_bf16 a[120:123], v[220:223], v[104:107], a[120:123]\n"
"ds_read_b128 v[176:179], v2 offset:12288\n"
"v_cvt_pk_bf16_f32 v96, v48, v49\n"
"v_mfma_f32_16x16x32_bf16 v[140:143], v[224:227], v[108:111], v[140:143]\n"
"v_exp_f32 v50, v50\n"
"v_mfma_f32_16x16x32_bf16 a[68:71], v[192:195], v[108:111], a[68:71]\n"
"ds_read_b128 v[180:183], v2 offset:13312\n"
"v_mfma_f32_16x16x32_bf16 a[76:79], v[196:199], v[108:111], a[76:79]\n"
"v_exp_f32 v51, v51\n"
"v_mfma_f32_16x16x32_bf16 a[84:87], v[200:203], v[108:111], a[84:87]\n"
"ds_read_b128 v[184:187], v2 offset:14336\n"
"v_cvt_pk_bf16_f32 v97, v50, v51\n"
"v_mfma_f32_16x16x32_bf16 a[92:95], v[204:207], v[108:111], a[92:95]\n"
"v_exp_f32 v56, v56\n"
"v_mfma_f32_16x16x32_bf16 a[100:103], v[208:211], v[108:111], a[100:103]\n"
"ds_read_b128 v[188:191], v2 offset:15360\n"
"v_mfma_f32_16x16x32_bf16 a[108:111], v[212:215], v[108:111], a[108:111]\n"
"v_exp_f32 v57, v57\n"
"v_mfma_f32_16x16x32_bf16 a[116:119], v[216:219], v[108:111], a[116:119]\n"
"v_cvt_pk_bf16_f32 v98, v56, v57\n"
"v_mfma_f32_16x16x32_bf16 a[124:127], v[220:223], v[108:111], a[124:127]\n"
"v_add_u32 v2, 0x4000, v2\n"
"v_and_b32 v2, 0x1ffff, v2\n"
"s_waitcnt lgkmcnt(8)\n"
"v_mfma_f32_16x16x32_bf16 v[64:67], a[224:227], a[160:163], v[144:147]\n"
"v_exp_f32 v58, v58\n"
"v_mfma_f32_16x16x32_bf16 v[64:67], a[228:231], a[164:167], v[64:67]\n"
"v_exp_f32 v59, v59\n"
"v_mfma_f32_16x16x32_bf16 v[64:67], a[232:235], a[168:171], v[64:67]\n"
"ds_read_b128 a[192:195], v2 offset:0\n"
"v_cvt_pk_bf16_f32 v99, v58, v59\n"
"v_mfma_f32_16x16x32_bf16 v[64:67], a[236:239], a[172:175], v[64:67]\n"
"ds_read_b128 a[196:199], v2 offset:1024\n"
"ds_read_b128 a[200:203], v2 offset:2048\n"
"v_mfma_f32_16x16x32_bf16 v[68:71], a[224:227], a[176:179], v[148:151]\n"
"v_exp_f32 v52, v52\n"
"v_mfma_f32_16x16x32_bf16 v[68:71], a[228:231], a[180:183], v[68:71]\n"
"ds_read_b128 a[204:207], v2 offset:3072\n"
"v_mfma_f32_16x16x32_bf16 v[68:71], a[232:235], a[184:187], v[68:71]\n"
"v_exp_f32 v53, v53\n"
"v_mfma_f32_16x16x32_bf16 v[68:71], a[236:239], a[188:191], v[68:71]\n"
"ds_read_b128 a[208:211], v2 offset:4096\n"
"v_cvt_pk_bf16_f32 v100, v52, v53\n"
"v_mfma_f32_16x16x32_bf16 v[72:75], a[240:243], a[160:163], v[152:155]\n"
"v_exp_f32 v54, v54\n"
"v_mfma_f32_16x16x32_bf16 v[72:75], a[244:247], a[164:167], v[72:75]\n"
"ds_read_b128 a[212:215], v2 offset:5120\n"
"v_mfma_f32_16x16x32_bf16 v[72:75], a[248:251], a[168:171], v[72:75]\n"
"v_exp_f32 v55, v55\n"
"v_mfma_f32_16x16x32_bf16 v[72:75], a[252:255], a[172:175], v[72:75]\n"
"ds_read_b128 a[216:219], v2 offset:6144\n"
"v_cvt_pk_bf16_f32 v101, v54, v55\n"
"v_mfma_f32_16x16x32_bf16 v[76:79], a[240:243], a[176:179], v[156:159]\n"
"ds_read_b128 a[220:223], v2 offset:7168\n"
"v_mfma_f32_16x16x32_bf16 v[76:79], a[244:247], a[180:183], v[76:79]\n"
"v_exp_f32 v60, v60\n"
"v_mfma_f32_16x16x32_bf16 v[76:79], a[248:251], a[184:187], v[76:79]\n"
"v_exp_f32 v61, v61\n"
"v_mfma_f32_16x16x32_bf16 v[76:79], a[252:255], a[188:191], v[76:79]\n"
"s_waitcnt lgkmcnt(8)\n"
"v_cvt_pk_bf16_f32 v102, v60, v61\n"
"v_mfma_f32_16x16x32_bf16 v[136:139], v[224:227], v[96:99], v[136:139]\n"
"ds_read_b128 v[192:195], v2 offset:8192\n"
"v_mfma_f32_16x16x32_bf16 a[64:67], v[160:163], v[96:99], a[64:67]\n"
"v_exp_f32 v62, v62\n"
"v_mfma_f32_16x16x32_bf16 a[72:75], v[164:167], v[96:99], a[72:75]\n"
"v_exp_f32 v63, v63\n"
"v_mfma_f32_16x16x32_bf16 a[80:83], v[168:171], v[96:99], a[80:83]\n"
"ds_read_b128 v[196:199], v2 offset:9216\n"
"v_cvt_pk_bf16_f32 v103, v62, v63\n"
"v_mfma_f32_16x16x32_bf16 a[88:91], v[172:175], v[96:99], a[88:91]\n"
"ds_read_b128 v[200:203], v2 offset:10240\n"
"v_mfma_f32_16x16x32_bf16 a[96:99], v[176:179], v[96:99], a[96:99]\n"
"v_exp_f32 v64, v64\n"
"v_mfma_f32_16x16x32_bf16 a[104:107], v[180:183], v[96:99], a[104:107]\n"
"ds_read_b128 v[204:207], v2 offset:11264\n"
"v_mfma_f32_16x16x32_bf16 a[112:115], v[184:187], v[96:99], a[112:115]\n"
"v_exp_f32 v65, v65\n"
"v_mfma_f32_16x16x32_bf16 a[120:123], v[188:191], v[96:99], a[120:123]\n"
"ds_read_b128 v[208:211], v2 offset:12288\n"
"v_cvt_pk_bf16_f32 v104, v64, v65\n"
"v_mfma_f32_16x16x32_bf16 v[140:143], v[224:227], v[100:103], v[140:143]\n"
"v_exp_f32 v66, v66\n"
"v_mfma_f32_16x16x32_bf16 a[68:71], v[160:163], v[100:103], a[68:71]\n"
"ds_read_b128 v[212:215], v2 offset:13312\n"
"v_mfma_f32_16x16x32_bf16 a[76:79], v[164:167], v[100:103], a[76:79]\n"
"v_exp_f32 v67, v67\n"
"v_mfma_f32_16x16x32_bf16 a[84:87], v[168:171], v[100:103], a[84:87]\n"
"ds_read_b128 v[216:219], v2 offset:14336\n"
"v_cvt_pk_bf16_f32 v105, v66, v67\n"
"v_mfma_f32_16x16x32_bf16 a[92:95], v[172:175], v[100:103], a[92:95]\n"
"v_exp_f32 v72, v72\n"
"v_mfma_f32_16x16x32_bf16 a[100:103], v[176:179], v[100:103], a[100:103]\n"
"ds_read_b128 v[220:223], v2 offset:15360\n"
"v_mfma_f32_16x16x32_bf16 a[108:111], v[180:183], v[100:103], a[108:111]\n"
"v_exp_f32 v73, v73\n"
"v_mfma_f32_16x16x32_bf16 a[116:119], v[184:187], v[100:103], a[116:119]\n"
"v_cvt_pk_bf16_f32 v106, v72, v73\n"
"v_mfma_f32_16x16x32_bf16 a[124:127], v[188:191], v[100:103], a[124:127]\n"
"v_add_u32 v2, 0x4000, v2\n"
"v_and_b32 v2, 0x1ffff, v2\n"
"v_exp_f32 v74, v74\n"
"v_exp_f32 v75, v75\n"
"s_nop 0\n"
"v_cvt_pk_bf16_f32 v107, v74, v75\n"
"v_exp_f32 v68, v68\n"
"v_exp_f32 v69, v69\n"
"s_nop 0\n"
"v_cvt_pk_bf16_f32 v108, v68, v69\n"
"v_exp_f32 v70, v70\n"
"v_exp_f32 v71, v71\n"
"s_nop 0\n"
"v_cvt_pk_bf16_f32 v109, v70, v71\n"
"v_exp_f32 v76, v76\n"
"v_exp_f32 v77, v77\n"
"s_nop 0\n"
"v_cvt_pk_bf16_f32 v110, v76, v77\n"
"v_exp_f32 v78, v78\n"
"v_exp_f32 v79, v79\n"
"s_nop 0\n"
"v_cvt_pk_bf16_f32 v111, v78, v79\n"
"s_waitcnt lgkmcnt(0)\n"
"s_nop 0\n"
"v_mfma_f32_16x16x32_bf16 v[136:139], v[224:227], v[104:107], v[136:139]\n"
"v_mfma_f32_16x16x32_bf16 a[64:67], v[192:195], v[104:107], a[64:67]\n"
"v_mfma_f32_16x16x32_bf16 a[72:75], v[196:199], v[104:107], a[72:75]\n"
"v_mfma_f32_16x16x32_bf16 a[80:83], v[200:203], v[104:107], a[80:83]\n"
"v_mfma_f32_16x16x32_bf16 a[88:91], v[204:207], v[104:107], a[88:91]\n"
"v_mfma_f32_16x16x32_bf16 a[96:99], v[208:211], v[104:107], a[96:99]\n"
"v_mfma_f32_16x16x32_bf16 a[104:107], v[212:215], v[104:107], a[104:107]\n"
"v_mfma_f32_16x16x32_bf16 a[112:115], v[216:219], v[104:107], a[112:115]\n"
"v_mfma_f32_16x16x32_bf16 a[120:123], v[220:223], v[104:107], a[120:123]\n"
"v_mfma_f32_16x16x32_bf16 v[140:143], v[224:227], v[108:111], v[140:143]\n"
"v_mfma_f32_16x16x32_bf16 a[68:71], v[192:195], v[108:111], a[68:71]\n"
"v_mfma_f32_16x16x32_bf16 a[76:79], v[196:199], v[108:111], a[76:79]\n"
"v_mfma_f32_16x16x32_bf16 a[84:87], v[200:203], v[108:111], a[84:87]\n"
"v_mfma_f32_16x16x32_bf16 a[92:95], v[204:207], v[108:111], a[92:95]\n"
"v_mfma_f32_16x16x32_bf16 a[100:103], v[208:211], v[108:111], a[100:103]\n"
"v_mfma_f32_16x16x32_bf16 a[108:111], v[212:215], v[108:111], a[108:111]\n"
"v_mfma_f32_16x16x32_bf16 a[116:119], v[216:219], v[108:111], a[116:119]\n"
"v_mfma_f32_16x16x32_bf16 a[124:127], v[220:223], v[108:111], a[124:127]\n"
"s_branch LB_end\n"
"LB_w0:\n"
"s_waitcnt lgkmcnt(8)\n"
"v_mfma_f32_16x16x32_bf16 v[48:51], a[192:195], a[160:163], v[144:147]\n"
"v_exp_f32 v74, v74\n"
"v_mfma_f32_16x16x32_bf16 v[48:51], a[196:199], a[164:167], v[48:51]\n"
"v_exp_f32 v75, v75\n"
"v_mfma_f32_16x16x32_bf16 v[48:51], a[200:203], a[168:171], v[48:51]\n"
"ds_read_b128 a[224:227], v2 offset:0\n"
"v_cvt_pk_bf16_f32 v107, v74, v75\n"
"v_mfma_f32_16x16x32_bf16 v[48:51], a[204:207], a[172:175], v[48:51]\n"
"ds_read_b128 a[228:231], v2 offset:1024\n"
"ds_read_b128 a[232:235], v2 offset:2048\n"
"v_mfma_f32_16x16x32_bf16 v[52:55], a[192:195], a[176:179], v[148:151]\n"
"v_exp_f32 v68, v68\n"
"v_mfma_f32_16x16x32_bf16 v[52:55], a[196:199], a[180:183], v[52:55]\n"
"ds_read_b128 a[236:239], v2 offset:3072\n"
"v_mfma_f32_16x16x32_bf16 v[52:55], a[200:203], a[184:187], v[52:55]\n"
"v_exp_f32 v69, v69\n"
"v_mfma_f32_16x16x32_bf16 v[52:55], a[204:207], a[188:191], v[52:55]\n"
"ds_read_b128 a[240:243], v2 offset:4096\n"
"v_cvt_pk_bf16_f32 v108, v68, v69\n"
"v_mfma_f32_16x16x32_bf16 v[56:59], a[208:211], a[160:163], v[152:155]\n"
"v_exp_f32 v70, v70\n"
"v_mfma_f32_16x16x32_bf16 v[56:59], a[212:215], a[164:167], v[56:59]\n"
"ds_read_b128 a[244:247], v2 offset:5120\n"
"v_mfma_f32_16x16x32_bf16 v[56:59], a[216:219], a[168:171], v[56:59]\n"
"v_exp_f32 v71, v71\n"
"v_mfma_f32_16x16x32_bf16 v[56:59], a[220:223], a[172:175], v[56:59]\n"
"ds_read_b128 a[248:251], v2 offset:6144\n"
"v_cvt_pk_bf16_f32 v109, v70, v71\n"
"v_mfma_f32_16x16x32_bf16 v[60:63], a[208:211], a[176:179], v[156:159]\n"
"ds_read_b128 a[252:255], v2 offset:7168\n"
"v_mfma_f32_16x16x32_bf16 v[60:63], a[212:215], a[180:183], v[60:63]\n"
"v_exp_f32 v76, v76\n"
"v_mfma_f32_16x16x32_bf16 v[60:63], a[216:219], a[184:187], v[60:63]\n"
"v_exp_f32 v77, v77\n"
"v_mfma_f32_16x16x32_bf16 v[60:63], a[220:223], a[188:191], v[60:63]\n"
"s_waitcnt lgkmcnt(8)\n"
"v_cvt_pk_bf16_f32 v110, v76, v77\n"
"v_mfma_f32_16x16x32_bf16 v[136:139], v[224:227], v[104:107], v[136:139]\n"
"ds_read_b128 v[160:163], v2 offset:8192\n"
"v_mfma_f32_16x16x32_bf16 a[64:67], v[192:195], v[104:107], a[64:67]\n"
"v_exp_f32 v78, v78\n"
"v_mfma_f32_16x16x32_bf16 a[72:75], v[196:199], v[104:107], a[72:75]\n"
"v_exp_f32 v79, v79\n"
"v_mfma_f32_16x16x32_bf16 a[80:83], v[200:203], v[104:107], a[80:83]\n"
"ds_read_b128 v[164:167], v2 offset:9216\n"
"v_cvt_pk_bf16_f32 v111, v78, v79\n"
"v_mfma_f32_16x16x32_bf16 a[88:91], v[204:207], v[104:107], a[88:91]\n"
"ds_read_b128 v[168:171], v2 offset:10240\n"
"v_mfma_f32_16x16x32_bf16 a[96:99], v[208:211], v[104:107], a[96:99]\n"
"v_exp_f32 v48, v48\n"
"v_mfma_f32_16x16x32_bf16 a[104:107], v[212:215], v[104:107], a[104:107]\n"
"ds_read_b128 v[172:175], v2 offset:11264\n"
"v_mfma_f32_16x16x32_bf16 a[112:115], v[216:219], v[104:107], a[112:115]\n"
"v_exp_f32 v49, v49\n"
"v_mfma_f32_16x16x32_bf16 a[120:123], v[220:223], v[104:107], a[120:123]\n"
"ds_read_b128 v[176:179], v2 offset:12288\n"
"v_cvt_pk_bf16_f32 v96, v48, v49\n"
"v_mfma_f32_16x16x32_bf16 v[140:143], v[224:227], v[108:111], v[140:143]\n"
"v_exp_f32 v50, v50\n"
"v_mfma_f32_16x16x32_bf16 a[68:71], v[192:195], v[108:111], a[68:71]\n"
"ds_read_b128 v[180:183], v2 offset:13312\n"
"v_mfma_f32_16x16x32_bf16 a[76:79], v[196:199], v[108:111], a[76:79]\n"
"v_exp_f32 v51, v51\n"
"v_mfma_f32_16x16x32_bf16 a[84:87], v[200:203], v[108:111], a[84:87]\n"
"ds_read_b128 v[184:187], v2 offset:14336\n"
"v_cvt_pk_bf16_f32 v97, v50, v51\n"
"v_mfma_f32_16x16x32_bf16 a[92:95], v[204:207], v[108:111], a[92:95]\n"
"v_exp_f32 v56, v56\n"
"v_mfma_f32_16x16x32_bf16 a[100:103], v[208:211], v[108:111], a[100:103]\n"
"ds_read_b128 v[188:191], v2 offset:15360\n"
"v_mfma_f32_16x16x32_bf16 a[108:111], v[212:215], v[108:111], a[108:111]\n"
"v_exp_f32 v57, v57\n"
"v_mfma_f32_16x16x32_bf16 a[116:119], v[216:219], v[108:111], a[116:119]\n"
"v_cvt_pk_bf16_f32 v98, v56, v57\n"
"v_mfma_f32_16x16x32_bf16 a[124:127], v[220:223], v[108:111], a[124:127]\n"
"v_add_u32 v2, 0x4000, v2\n"
"v_and_b32 v2, 0x1ffff, v2\n"
"v_exp_f32 v58, v58\n"
"v_exp_f32 v59, v59\n"
"s_nop 0\n"
"v_cvt_pk_bf16_f32 v99, v58, v59\n"
"v_exp_f32 v52, v52\n"
"v_exp_f32 v53, v53\n"
"s_nop 0\n"
"v_cvt_pk_bf16_f32 v100, v52, v53\n"
"v_exp_f32 v54, v54\n"
"v_exp_f32 v55, v55\n"
"s_nop 0\n"
"v_cvt_pk_bf16_f32 v101, v54, v55\n"
"v_exp_f32 v60, v60\n"
"v_exp_f32 v61, v61\n"
"s_nop 0\n"
"v_cvt_pk_bf16_f32 v102, v60, v61\n"
"v_exp_f32 v62, v62\n"
"v_exp_f32 v63, v63\n"
"s_nop 0\n"
"v_cvt_pk_bf16_f32 v103, v62, v63\n"
"s_waitcnt lgkmcnt(0)\n"
"s_nop 0\n"
"v_mfma_f32_16x16x32_bf16 v[136:139], v[224:227], v[96:99], v[136:139]\n"
"v_mfma_f32_16x16x32_bf16 a[64:67], v[160:163], v[96:99], a[64:67]\n"
"v_mfma_f32_16x16x32_bf16 a[72:75], v[164:167], v[96:99], a[72:75]\n"
"v_mfma_f32_16x16x32_bf16 a[80:83], v[168:171], v[96:99], a[80:83]\n"
"v_mfma_f32_16x16x32_bf16 a[88:91], v[172:175], v[96:99], a[88:91]\n"
"v_mfma_f32_16x16x32_bf16 a[96:99], v[176:179], v[96:99], a[96:99]\n"
"v_mfma_f32_16x16x32_bf16 a[104:107], v[180:183], v[96:99], a[104:107]\n"
"v_mfma_f32_16x16x32_bf16 a[112:115], v[184:187], v[96:99], a[112:115]\n"
"v_mfma_f32_16x16x32_bf16 a[120:123], v[188:191], v[96:99], a[120:123]\n"
"v_mfma_f32_16x16x32_bf16 v[140:143], v[224:227], v[100:103], v[140:143]\n"
"v_mfma_f32_16x16x32_bf16 a[68:71], v[160:163], v[100:103], a[68:71]\n"
"v_mfma_f32_16x16x32_bf16 a[76:79], v[164:167], v[100:103], a[76:79]\n"
"v_mfma_f32_16x16x32_bf16 a[84:87], v[168:171], v[100:103], a[84:87]\n"
"v_mfma_f32_16x16x32_bf16 a[92:95], v[172:175], v[100:103], a[92:95]\n"
"v_mfma_f32_16x16x32_bf16 a[100:103], v[176:179], v[100:103], a[100:103]\n"
"v_mfma_f32_16x16x32_bf16 a[108:111], v[180:183], v[100:103], a[108:111]\n"
"v_mfma_f32_16x16x32_bf16 a[116:119], v[184:187], v[100:103], a[116:119]\n"
"v_mfma_f32_16x16x32_bf16 a[124:127], v[188:191], v[100:103], a[124:127]\n"
"s_branch LB_end\n"
"LB_w1:\n"
"s_waitcnt lgkmcnt(8)\n"
"v_mfma_f32_16x16x32_bf16 v[48:51], a[192:195], a[160:163], v[120:123]\n"
"v_exp_f32 v74, v74\n"
"v_mfma_f32_16x16x32_bf16 v[48:51], a[196:199], a[164:167], v[48:51]\n"
"v_exp_f32 v75, v75\n"
"v_mfma_f32_16x16x32_bf16 v[48:51], a[200:203], a[168:171], v[48:51]\n"
"ds_read_b128 a[224:227], v2 offset:0\n"
"v_cvt_pk_bf16_f32 v107, v74, v75\n"
"v_mfma_f32_16x16x32_bf16 v[48:51], a[204:207], a[172:175], v[48:51]\n"
"ds_read_b128 a[228:231], v2 offset:1024\n"
"ds_read_b128 a[232:235], v2 offset:2048\n"
"v_mfma_f32_16x16x32_bf16 v[52:55], a[192:195], a[176:179], v[124:127]\n"
"v_exp_f32 v68, v68\n"
"v_mfma_f32_16x16x32_bf16 v[52:55], a[196:199], a[180:183], v[52:55]\n"
"ds_read_b128 a[236:239], v2 offset:3072\n"
"v_mfma_f32_16x16x32_bf16 v[52:55], a[200:203], a[184:187], v[52:55]\n"
"v_exp_f32 v69, v69\n"
"v_mfma_f32_16x16x32_bf16 v[52:55], a[204:207], a[188:191], v[52:55]\n"
"ds_read_b128 a[240:243], v2 offset:4096\n"
"v_cvt_pk_bf16_f32 v108, v68, v69\n"
"v_mfma_f32_16x16x32_bf16 v[56:59], a[208:211], a[160:163], v[120:123]\n"
"v_exp_f32 v70, v70\n"
"v_mfma_f32_16x16x32_bf16 v[56:59], a[212:215], a[164:167], v[56:59]\n"
"ds_read_b128 a[244:247], v2 offset:5120\n"
"v_mfma_f32_16x16x32_bf16 v[56:59], a[216:219], a[168:171], v[56:59]\n"
"v_exp_f32 v71, v71\n"
"v_mfma_f32_16x16x32_bf16 v[56:59], a[220:223], a[172:175], v[56:59]\n"
"ds_read_b128 a[248:251], v2 offset:6144\n"
"v_cvt_pk_bf16_f32 v109, v70, v71\n"
"v_mfma_f32_16x16x32_bf16 v[60:63], a[208:211], a[176:179], v[124:127]\n"
"ds_read_b128 a[252:255], v2 offset:7168\n"
"v_mfma_f32_16x16x32_bf16 v[60:63], a[212:215], a[180:183], v[60:63]\n"
"v_exp_f32 v76, v76\n"
"v_mfma_f32_16x16x32_bf16 v[60:63], a[216:219], a[184:187], v[60:63]\n"
"v_exp_f32 v77, v77\n"
"v_mfma_f32_16x16x32_bf16 v[60:63], a[220:223], a[188:191], v[60:63]\n"
"s_waitcnt lgkmcnt(8)\n"
"v_cvt_pk_bf16_f32 v110, v76, v77\n"
"v_mfma_f32_16x16x32_bf16 v[136:139], v[224:227], v[104:107], v[136:139]\n"
"ds_read_b128 v[160:163], v2 offset:8192\n"
"v_mfma_f32_16x16x32_bf16 a[64:67], v[192:195], v[104:107], a[64:67]\n"
"v_exp_f32 v78, v78\n"
"v_mfma_f32_16x16x32_bf16 a[72:75], v[196:199], v[104:107], a[72:75]\n"
"v_exp_f32 v79, v79\n"
"v_mfma_f32_16x16x32_bf16 a[80:83], v[200:203], v[104:107], a[80:83]\n"
"ds_read_b128 v[164:167], v2 offset:9216\n"
"v_cvt_pk_bf16_f32 v111, v78, v79\n"
"v_mfma_f32_16x16x32_bf16 a[88:91], v[204:207], v[104:107], a[88:91]\n"
"ds_read_b128 v[168:171], v2 offset:10240\n"
"v_mfma_f32_16x16x32_bf16 a[96:99], v[208:211], v[104:107], a[96:99]\n"
"v_exp_f32 v48, v48\n"
"v_mfma_f32_16x16x32_bf16 a[104:107], v[212:215], v[104:107], a[104:107]\n"
"ds_read_b128 v[172:175], v2 offset:11264\n"
"v_mfma_f32_16x16x32_bf16 a[112:115], v[216:219], v[104:107], a[112:115]\n"
"v_exp_f32 v49, v49\n"
"v_mfma_f32_16x16x32_bf16 a[120:123], v[220:223], v[104:107], a[120:123]\n"
"ds_read_b128 v[176:179], v2 offset:12288\n"
"v_cvt_pk_bf16_f32 v96, v48, v49\n"
"v_mfma_f32_16x16x32_bf16 v[140:143], v[224:227], v[108:111], v[140:143]\n"
"v_exp_f32 v50, v50\n"
"v_mfma_f32_16x16x32_bf16 a[68:71], v[192:195], v[108:111], a[68:71]\n"
"ds_read_b128 v[180:183], v2 offset:13312\n"
"v_mfma_f32_16x16x32_bf16 a[76:79], v[196:199], v[108:111], a[76:79]\n"
"v_exp_f32 v51, v51\n"
"v_mfma_f32_16x16x32_bf16 a[84:87], v[200:203], v[108:111], a[84:87]\n"
"ds_read_b128 v[184:187], v2 offset:14336\n"
"v_cvt_pk_bf16_f32 v97, v50, v51\n"
"v_mfma_f32_16x16x32_bf16 a[92:95], v[204:207], v[108:111], a[92:95]\n"
"v_exp_f32 v56, v56\n"
"v_mfma_f32_16x16x32_bf16 a[100:103], v[208:211], v[108:111], a[100:103]\n"
"ds_read_b128 v[188:191], v2 offset:15360\n"
"v_mfma_f32_16x16x32_bf16 a[108:111], v[212:215], v[108:111], a[108:111]\n"
"v_exp_f32 v57, v57\n"
"v_mfma_f32_16x16x32_bf16 a[116:119], v[216:219], v[108:111], a[116:119]\n"
"v_cvt_pk_bf16_f32 v98, v56, v57\n"
"v_mfma_f32_16x16x32_bf16 a[124:127], v[220:223], v[108:111], a[124:127]\n"
"v_add_u32 v2, 0x4000, v2\n"
"v_and_b32 v2, 0x1ffff, v2\n"
"s_waitcnt lgkmcnt(8)\n"
"v_mfma_f32_16x16x32_bf16 v[64:67], a[224:227], a[160:163], v[144:147]\n"
"v_exp_f32 v58, v58\n"
"v_mfma_f32_16x16x32_bf16 v[64:67], a[228:231], a[164:167], v[64:67]\n"
"v_exp_f32 v59, v59\n"
"v_mfma_f32_16x16x32_bf16 v[64:67], a[232:235], a[168:171], v[64:67]\n"
"ds_read_b128 a[192:195], v2 offset:0\n"
"v_cvt_pk_bf16_f32 v99, v58, v59\n"
"v_mfma_f32_16x16x32_bf16 v[64:67], a[236:239], a[172:175], v[64:67]\n"
"ds_read_b128 a[196:199], v2 offset:1024\n"
"ds_read_b128 a[200:203], v2 offset:2048\n"
"v_mfma_f32_16x16x32_bf16 v[68:71], a[224:227], a[176:179], v[148:151]\n"
"v_exp_f32 v52, v52\n"
"v_mfma_f32_16x16x32_bf16 v[68:71], a[228:231], a[180:183], v[68:71]\n"
"ds_read_b128 a[204:207], v2 offset:3072\n"
"v_mfma_f32_16x16x32_bf16 v[68:71], a[232:235], a[184:187], v[68:71]\n"
"v_exp_f32 v53, v53\n"
"v_mfma_f32_16x16x32_bf16 v[68:71], a[236:239], a[188:191], v[68:71]\n"
"ds_read_b128 a[208:211], v2 offset:4096\n"
"v_cvt_pk_bf16_f32 v100, v52, v53\n"
"v_mfma_f32_16x16x32_bf16 v[72:75], a[240:243], a[160:163], v[152:155]\n"
"v_exp_f32 v54, v54\n"
"v_mfma_f32_16x16x32_bf16 v[72:75], a[244:247], a[164:167], v[72:75]\n"
"ds_read_b128 a[212:215], v2 offset:5120\n"
"v_mfma_f32_16x16x32_bf16 v[72:75], a[248:251], a[168:171], v[72:75]\n"
"v_exp_f32 v55, v55\n"
"v_mfma_f32_16x16x32_bf16 v[72:75], a[252:255], a[172:175], v[72:75]\n"
"ds_read_b128 a[216:219], v2 offset:6144\n"
"v_cvt_pk_bf16_f32 v101, v54, v55\n"
"v_mfma_f32_16x16x32_bf16 v[76:79], a[240:243], a[176:179], v[156:159]\n"
"ds_read_b128 a[220:223], v2 offset:7168\n"
"v_mfma_f32_16x16x32_bf16 v[76:79], a[244:247], a[180:183], v[76:79]\n"
"v_exp_f32 v60, v60\n"
"v_mfma_f32_16x16x32_bf16 v[76:79], a[248:251], a[184:187], v[76:79]\n"
"v_exp_f32 v61, v61\n"
"v_mfma_f32_16x16x32_bf16 v[76:79], a[252:255], a[188:191], v[76:79]\n"
"s_waitcnt lgkmcnt(8)\n"
"v_cvt_pk_bf16_f32 v102, v60, v61\n"
"v_mfma_f32_16x16x32_bf16 v[136:139], v[224:227], v[96:99], v[136:139]\n"
"ds_read_b128 v[192:195], v2 offset:8192\n"
"v_mfma_f32_16x16x32_bf16 a[64:67], v[160:163], v[96:99], a[64:67]\n"
"v_exp_f32 v62, v62\n"
"v_mfma_f32_16x16x32_bf16 a[72:75], v[164:167], v[96:99], a[72:75]\n"
"v_exp_f32 v63, v63\n"
"v_mfma_f32_16x16x32_bf16 a[80:83], v[168:171], v[96:99], a[80:83]\n"
"ds_read_b128 v[196:199], v2 offset:9216\n"
"v_cvt_pk_bf16_f32 v103, v62, v63\n"
"v_mfma_f32_16x16x32_bf16 a[88:91], v[172:175], v[96:99], a[88:91]\n"
"ds_read_b128 v[200:203], v2 offset:10240\n"
"v_mfma_f32_16x16x32_bf16 a[96:99], v[176:179], v[96:99], a[96:99]\n"
"v_exp_f32 v64, v64\n"
"v_mfma_f32_16x16x32_bf16 a[104:107], v[180:183], v[96:99], a[104:107]\n"
"ds_read_b128 v[204:207], v2 offset:11264\n"
"v_mfma_f32_16x16x32_bf16 a[112:115], v[184:187], v[96:99], a[112:115]\n"
"v_exp_f32 v65, v65\n"
"v_mfma_f32_16x16x32_bf16 a[120:123], v[188:191], v[96:99], a[120:123]\n"
"ds_read_b128 v[208:211], v2 offset:12288\n"
"v_cvt_pk_bf16_f32 v104, v64, v65\n"
"v_mfma_f32_16x16x32_bf16 v[140:143], v[224:227], v[100:103], v[140:143]\n"
"v_exp_f32 v66, v66\n"
"v_mfma_f32_16x16x32_bf16 a[68:71], v[160:163], v[100:103], a[68:71]\n"
"ds_read_b128 v[212:215], v2 offset:13312\n"
"v_mfma_f32_16x16x32_bf16 a[76:79], v[164:167], v[100:103], a[76:79]\n"
"v_exp_f32 v67, v67\n"
"v_mfma_f32_16x16x32_bf16 a[84:87], v[168:171], v[100:103], a[84:87]\n"
"ds_read_b128 v[216:219], v2 offset:14336\n"
"v_cvt_pk_bf16_f32 v105, v66, v67\n"
"v_mfma_f32_16x16x32_bf16 a[92:95], v[172:175], v[100:103], a[92:95]\n"
"v_exp_f32 v72, v72\n"
"v_mfma_f32_16x16x32_bf16 a[100:103], v[176:179], v[100:103], a[100:103]\n"
"ds_read_b128 v[220:223], v2 offset:15360\n"
"v_mfma_f32_16x16x32_bf16 a[108:111], v[180:183], v[100:103], a[108:111]\n"
"v_exp_f32 v73, v73\n"
"v_mfma_f32_16x16x32_bf16 a[116:119], v[184:187], v[100:103], a[116:119]\n"
"v_cvt_pk_bf16_f32 v106, v72, v73\n"
"v_mfma_f32_16x16x32_bf16 a[124:127], v[188:191], v[100:103], a[124:127]\n"
"v_add_u32 v2, 0x4000, v2\n"
"v_and_b32 v2, 0x1ffff, v2\n"
"v_exp_f32 v74, v74\n"
"v_exp_f32 v75, v75\n"
"s_nop 0\n"
"v_cvt_pk_bf16_f32 v107, v74, v75\n"
"v_exp_f32 v68, v68\n"
"v_exp_f32 v69, v69\n"
"s_nop 0\n"
"v_cvt_pk_bf16_f32 v108, v68, v69\n"
"v_exp_f32 v70, v70\n"
"v_exp_f32 v71, v71\n"
"s_nop 0\n"
"v_cvt_pk_bf16_f32 v109, v70, v71\n"
"v_exp_f32 v76, v76\n"
"v_exp_f32 v77, v77\n"
"s_nop 0\n"
"v_cvt_pk_bf16_f32 v110, v76, v77\n"
"v_exp_f32 v78, v78\n"
"v_exp_f32 v79, v79\n"
"s_nop 0\n"
"v_cvt_pk_bf16_f32 v111, v78, v79\n"
"s_waitcnt lgkmcnt(0)\n"
"s_nop 0\n"
"v_mfma_f32_16x16x32_bf16 v[136:139], v[224:227], v[104:107], v[136:139]\n"
"v_mfma_f32_16x16x32_bf16 a[64:67], v[192:195], v[104:107], a[64:67]\n"
"v_mfma_f32_16x16x32_bf16 a[72:75], v[196:199], v[104:107], a[72:75]\n"
"v_mfma_f32_16x16x32_bf16 a[80:83], v[200:203], v[104:107], a[80:83]\n"
"v_mfma_f32_16x16x32_bf16 a[88:91], v[204:207], v[104:107], a[88:91]\n"
"v_mfma_f32_16x16x32_bf16 a[96:99], v[208:211], v[104:107], a[96:99]\n"
"v_mfma_f32_16x16x32_bf16 a[104:107], v[212:215], v[104:107], a[104:107]\n"
"v_mfma_f32_16x16x32_bf16 a[112:115], v[216:219], v[104:107], a[112:115]\n"
"v_mfma_f32_16x16x32_bf16 a[120:123], v[220:223], v[104:107], a[120:123]\n"
"v_mfma_f32_16x16x32_bf16 v[140:143], v[224:227], v[108:111], v[140:143]\n"
"v_mfma_f32_16x16x32_bf16 a[68:71], v[192:195], v[108:111], a[68:71]\n"
"v_mfma_f32_16x16x32_bf16 a[76:79], v[196:199], v[108:111], a[76:79]\n"
"v_mfma_f32_16x16x32_bf16 a[84:87], v[200:203], v[108:111], a[84:87]\n"
"v_mfma_f32_16x16x32_bf16 a[92:95], v[204:207], v[108:111], a[92:95]\n"
"v_mfma_f32_16x16x32_bf16 a[100:103], v[208:211], v[108:111], a[100:103]\n"
"v_mfma_f32_16x16x32_bf16 a[108:111], v[212:215], v[108:111], a[108:111]\n"
"v_mfma_f32_16x16x32_bf16 a[116:119], v[216:219], v[108:111], a[116:119]\n"
"v_mfma_f32_16x16x32_bf16 a[124:127], v[220:223], v[108:111], a[124:127]\n"
"s_branch LB_end\n"
"LB_w2:\n"
"s_waitcnt lgkmcnt(8)\n"
"v_mfma_f32_16x16x32_bf16 v[48:51], a[192:195], a[160:163], v[120:123]\n"
"v_exp_f32 v74, v74\n"
"v_mfma_f32_16x16x32_bf16 v[48:51], a[196:199], a[164:167], v[48:51]\n"
"v_exp_f32 v75, v75\n"
"v_mfma_f32_16x16x32_bf16 v[48:51], a[200:203], a[168:171], v[48:51]\n"
"ds_read_b128 a[224:227], v2 offset:0\n"
"v_cvt_pk_bf16_f32 v107, v74, v75\n"
"v_mfma_f32_16x16x32_bf16 v[48:51], a[204:207], a[172:175], v[48:51]\n"
"ds_read_b128 a[228:231], v2 offset:1024\n"
"ds_read_b128 a[232:235], v2 offset:2048\n"
"v_mfma_f32_16x16x32_bf16 v[52:55], a[192:195], a[176:179], v[124:127]\n"
"v_exp_f32 v68, v68\n"
"v_mfma_f32_16x16x32_bf16 v[52:55], a[196:199], a[180:183], v[52:55]\n"
"ds_read_b128 a[236:239], v2 offset:3072\n"
"v_mfma_f32_16x16x32_bf16 v[52:55], a[200:203], a[184:187], v[52:55]\n"
"v_exp_f32 v69, v69\n"
"v_mfma_f32_16x16x32_bf16 v[52:55], a[204:207], a[188:191], v[52:55]\n"
"ds_read_b128 a[240:243], v2 offset:4096\n"
"v_cvt_pk_bf16_f32 v108, v68, v69\n"
"v_mfma_f32_16x16x32_bf16 v[56:59], a[208:211], a[160:163], v[120:123]\n"
"v_exp_f32 v70, v70\n"
"v_mfma_f32_16x16x32_bf16 v[56:59], a[212:215], a[164:167], v[56:59]\n"
"ds_read_b128 a[244:247], v2 offset:5120\n"
"v_mfma_f32_16x16x32_bf16 v[56:59], a[216:219], a[168:171], v[56:59]\n"
"v_exp_f32 v71, v71\n"
"v_mfma_f32_16x16x32_bf16 v[56:59], a[220:223], a[172:175], v[56:59]\n"
"ds_read_b128 a[248:251], v2 offset:6144\n"
"v_cvt_pk_bf16_f32 v109, v70, v71\n"
"v_mfma_f32_16x16x32_bf16 v[60:63], a[208:211], a[176:179], v[124:127]\n"
"ds_read_b128 a[252:255], v2 offset:7168\n"
"v_mfma_f32_16x16x32_bf16 v[60:63], a[212:215], a[180:183], v[60:63]\n"
"v_exp_f32 v76, v76\n"
"v_mfma_f32_16x16x32_bf16 v[60:63], a[216:219], a[184:187], v[60:63]\n"
"v_exp_f32 v77, v77\n"
"v_mfma_f32_16x16x32_bf16 v[60:63], a[220:223], a[188:191], v[60:63]\n"
"s_waitcnt lgkmcnt(8)\n"
"v_cvt_pk_bf16_f32 v110, v76, v77\n"
"v_mfma_f32_16x16x32_bf16 v[136:139], v[224:227], v[104:107], v[136:139]\n"
"ds_read_b128 v[160:163], v2 offset:8192\n"
"v_mfma_f32_16x16x32_bf16 a[64:67], v[192:195], v[104:107], a[64:67]\n"
"v_exp_f32 v78, v78\n"
"v_mfma_f32_16x16x32_bf16 a[72:75], v[196:199], v[104:107], a[72:75]\n"
"v_exp_f32 v79, v79\n"
"v_mfma_f32_16x16x32_bf16 a[80:83], v[200:203], v[104:107], a[80:83]\n"
"ds_read_b128 v[164:167], v2 offset:9216\n"
"v_cvt_pk_bf16_f32 v111, v78, v79\n"
"v_mfma_f32_16x16x32_bf16 a[88:91], v[204:207], v[104:107], a[88:91]\n"
"ds_read_b128 v[168:171], v2 offset:10240\n"
"v_mfma_f32_16x16x32_bf16 a[96:99], v[208:211], v[104:107], a[96:99]\n"
"v_exp_f32 v48, v48\n"
"v_mfma_f32_16x16x32_bf16 a[104:107], v[212:215], v[104:107], a[104:107]\n"
"ds_read_b128 v[172:175], v2 offset:11264\n"
"v_mfma_f32_16x16x32_bf16 a[112:115], v[216:219], v[104:107], a[112:115]\n"
"v_exp_f32 v49, v49\n"
"v_mfma_f32_16x16x32_bf16 a[120:123], v[220:223], v[104:107], a[120:123]\n"
"ds_read_b128 v[176:179], v2 offset:12288\n"
"v_cvt_pk_bf16_f32 v96, v48, v49\n"
"v_mfma_f32_16x16x32_bf16 v[140:143], v[224:227], v[108:111], v[140:143]\n"
"v_exp_f32 v50, v50\n"
"v_mfma_f32_16x16x32_bf16 a[68:71], v[192:195], v[108:111], a[68:71]\n"
"ds_read_b128 v[180:183], v2 offset:13312\n"
"v_mfma_f32_16x16x32_bf16 a[76:79], v[196:199], v[108:111], a[76:79]\n"
"v_exp_f32 v51, v51\n"
"v_mfma_f32_16x16x32_bf16 a[84:87], v[200:203], v[108:111], a[84:87]\n"
"ds_read_b128 v[184:187], v2 offset:14336\n"
"v_cvt_pk_bf16_f32 v97, v50, v51\n"
"v_mfma_f32_16x16x32_bf16 a[92:95], v[204:207], v[108:111], a[92:95]\n"
"v_exp_f32 v56, v56\n"
"v_mfma_f32_16x16x32_bf16 a[100:103], v[208:211], v[108:111], a[100:103]\n"
"ds_read_b128 v[188:191], v2 offset:15360\n"
"v_mfma_f32_16x16x32_bf16 a[108:111], v[212:215], v[108:111], a[108:111]\n"
"v_exp_f32 v57, v57\n"
"v_mfma_f32_16x16x32_bf16 a[116:119], v[216:219], v[108:111], a[116:119]\n"
"v_cvt_pk_bf16_f32 v98, v56, v57\n"
"v_mfma_f32_16x16x32_bf16 a[124:127], v[220:223], v[108:111], a[124:127]\n"
"v_add_u32 v2, 0x4000, v2\n"
"v_and_b32 v2, 0x1ffff, v2\n"
"s_waitcnt lgkmcnt(8)\n"
"v_mfma_f32_16x16x32_bf16 v[64:67], a[224:227], a[160:163], v[120:123]\n"
"v_exp_f32 v58, v58\n"
"v_mfma_f32_16x16x32_bf16 v[64:67], a[228:231], a[164:167], v[64:67]\n"
"v_exp_f32 v59, v59\n"
"v_mfma_f32_16x16x32_bf16 v[64:67], a[232:235], a[168:171], v[64:67]\n"
"ds_read_b128 a[192:195], v2 offset:0\n"
"v_cvt_pk_bf16_f32 v99, v58, v59\n"
"v_mfma_f32_16x16x32_bf16 v[64:67], a[236:239], a[172:175], v[64:67]\n"
"ds_read_b128 a[196:199], v2 offset:1024\n"
"ds_read_b128 a[200:203], v2 offset:2048\n"
"v_mfma_f32_16x16x32_bf16 v[68:71], a[224:227], a[176:179], v[124:127]\n"
"v_exp_f32 v52, v52\n"
"v_mfma_f32_16x16x32_bf16 v[68:71], a[228:231], a[180:183], v[68:71]\n"
"ds_read_b128 a[204:207], v2 offset:3072\n"
"v_mfma_f32_16x16x32_bf16 v[68:71], a[232:235], a[184:187], v[68:71]\n"
"v_exp_f32 v53, v53\n"
"v_mfma_f32_16x16x32_bf16 v[68:71], a[236:239], a[188:191], v[68:71]\n"
"ds_read_b128 a[208:211], v2 offset:4096\n"
"v_cvt_pk_bf16_f32 v100, v52, v53\n"
"v_mfma_f32_16x16x32_bf16 v[72:75], a[240:243], a[160:163], v[120:123]\n"
"v_exp_f32 v54, v54\n"
"v_mfma_f32_16x16x32_bf16 v[72:75], a[244:247], a[164:167], v[72:75]\n"
"ds_read_b128 a[212:215], v2 offset:5120\n"
"v_mfma_f32_16x16x32_bf16 v[72:75], a[248:251], a[168:171], v[72:75]\n"
"v_exp_f32 v55, v55\n"
"v_mfma_f32_16x16x32_bf16 v[72:75], a[252:255], a[172:175], v[72:75]\n"
"ds_read_b128 a[216:219], v2 offset:6144\n"
"v_cvt_pk_bf16_f32 v101, v54, v55\n"
"v_mfma_f32_16x16x32_bf16 v[76:79], a[240:243], a[176:179], v[124:127]\n"
"ds_read_b128 a[220:223], v2 offset:7168\n"
"v_mfma_f32_16x16x32_bf16 v[76:79], a[244:247], a[180:183], v[76:79]\n"
"v_exp_f32 v60, v60\n"
"v_mfma_f32_16x16x32_bf16 v[76:79], a[248:251], a[184:187], v[76:79]\n"
"v_exp_f32 v61, v61\n"
"v_mfma_f32_16x16x32_bf16 v[76:79], a[252:255], a[188:191], v[76:79]\n"
"s_waitcnt lgkmcnt(8)\n"
"v_cvt_pk_bf16_f32 v102, v60, v61\n"
"v_mfma_f32_16x16x32_bf16 v[136:139], v[224:227], v[96:99], v[136:139]\n"
"ds_read_b128 v[192:195], v2 offset:8192\n"
"v_mfma_f32_16x16x32_bf16 a[64:67], v[160:163], v[96:99], a[64:67]\n"
"v_exp_f32 v62, v62\n"
"v_mfma_f32_16x16x32_bf16 a[72:75], v[164:167], v[96:99], a[72:75]\n"
"v_exp_f32 v63, v63\n"
"v_mfma_f32_16x16x32_bf16 a[80:83], v[168:171], v[96:99], a[80:83]\n"
"ds_read_b128 v[196:199], v2 offset:9216\n"
"v_cvt_pk_bf16_f32 v103, v62, v63\n"
"v_mfma_f32_16x16x32_bf16 a[88:91], v[172:175], v[96:99], a[88:91]\n"
"ds_read_b128 v[200:203], v2 offset:10240\n"
"v_mfma_f32_16x16x32_bf16 a[96:99], v[176:179], v[96:99], a[96:99]\n"
"v_exp_f32 v64, v64\n"
"v_mfma_f32_16x16x32_bf16 a[104:107], v[180:183], v[96:99], a[104:107]\n"
"ds_read_b128 v[204:207], v2 offset:11264\n"
"v_mfma_f32_16x16x32_bf16 a[112:115], v[184:187], v[96:99], a[112:115]\n"
"v_exp_f32 v65, v65\n"
"v_mfma_f32_16x16x32_bf16 a[120:123], v[188:191], v[96:99], a[120:123]\n"
"ds_read_b128 v[208:211], v2 offset:12288\n"
"v_cvt_pk_bf16_f32 v104, v64, v65\n"
"v_mfma_f32_16x16x32_bf16 v[140:143], v[224:227], v[100:103], v[140:143]\n"
"v_exp_f32 v66, v66\n"
"v_mfma_f32_16x16x32_bf16 a[68:71], v[160:163], v[100:103], a[68:71]\n"
"ds_read_b128 v[212:215], v2 offset:13312\n"
"v_mfma_f32_16x16x32_bf16 a[76:79], v[164:167], v[100:103], a[76:79]\n"
"v_exp_f32 v67, v67\n"
"v_mfma_f32_16x16x32_bf16 a[84:87], v[168:171], v[100:103], a[84:87]\n"
"ds_read_b128 v[216:219], v2 offset:14336\n"
"v_cvt_pk_bf16_f32 v105, v66, v67\n"
"v_mfma_f32_16x16x32_bf16 a[92:95], v[172:175], v[100:103], a[92:95]\n"
"v_exp_f32 v72, v72\n"
"v_mfma_f32_16x16x32_bf16 a[100:103], v[176:179], v[100:103], a[100:103]\n"
"ds_read_b128 v[220:223], v2 offset:15360\n"
"v_mfma_f32_16x16x32_bf16 a[108:111], v[180:183], v[100:103], a[108:111]\n"
"v_exp_f32 v73, v73\n"
"v_mfma_f32_16x16x32_bf16 a[116:119], v[184:187], v[100:103], a[116:119]\n"
"v_cvt_pk_bf16_f32 v106, v72, v73\n"
"v_mfma_f32_16x16x32_bf16 a[124:127], v[188:191], v[100:103], a[124:127]\n"
"v_add_u32 v2, 0x4000, v2\n"
"v_and_b32 v2, 0x1ffff, v2\n"
"s_waitcnt lgkmcnt(8)\n"
"v_mfma_f32_16x16x32_bf16 v[48:51], a[192:195], a[160:163], v[144:147]\n"
"v_exp_f32 v74, v74\n"
"v_mfma_f32_16x16x32_bf16 v[48:51], a[196:199], a[164:167], v[48:51]\n"
"v_exp_f32 v75, v75\n"
"v_mfma_f32_16x16x32_bf16 v[48:51], a[200:203], a[168:171], v[48:51]\n"
"ds_read_b128 a[224:227], v2 offset:0\n"
"v_cvt_pk_bf16_f32 v107, v74, v75\n"
"v_mfma_f32_16x16x32_bf16 v[48:51], a[204:207], a[172:175], v[48:51]\n"
"ds_read_b128 a[228:231], v2 offset:1024\n"
"ds_read_b128 a[232:235], v2 offset:2048\n"
"v_mfma_f32_16x16x32_bf16 v[52:55], a[192:195], a[176:179], v[148:151]\n"
"v_exp_f32 v68, v68\n"
"v_mfma_f32_16x16x32_bf16 v[52:55], a[196:199], a[180:183], v[52:55]\n"
"ds_read_b128 a[236:239], v2 offset:3072\n"
"v_mfma_f32_16x16x32_bf16 v[52:55], a[200:203], a[184:187], v[52:55]\n"
"v_exp_f32 v69, v69\n"
"v_mfma_f32_16x16x32_bf16 v[52:55], a[204:207], a[188:191], v[52:55]\n"
"ds_read_b128 a[240:243], v2 offset:4096\n"
"v_cvt_pk_bf16_f32 v108, v68, v69\n"
"v_mfma_f32_16x16x32_bf16 v[56:59], a[208:211], a[160:163], v[152:155]\n"
"v_exp_f32 v70, v70\n"
"v_mfma_f32_16x16x32_bf16 v[56:59], a[212:215], a[164:167], v[56:59]\n"
"ds_read_b128 a[244:247], v2 offset:5120\n"
"v_mfma_f32_16x16x32_bf16 v[56:59], a[216:219], a[168:171], v[56:59]\n"
"v_exp_f32 v71, v71\n"
"v_mfma_f32_16x16x32_bf16 v[56:59], a[220:223], a[172:175], v[56:59]\n"
"ds_read_b128 a[248:251], v2 offset:6144\n"
"v_cvt_pk_bf16_f32 v109, v70, v71\n"
"v_mfma_f32_16x16x32_bf16 v[60:63], a[208:211], a[176:179], v[156:159]\n"
"ds_read_b128 a[252:255], v2 offset:7168\n"
"v_mfma_f32_16x16x32_bf16 v[60:63], a[212:215], a[180:183], v[60:63]\n"
"v_exp_f32 v76, v76\n"
"v_mfma_f32_16x16x32_bf16 v[60:63], a[216:219], a[184:187], v[60:63]\n"
"v_exp_f32 v77, v77\n"
"v_mfma_f32_16x16x32_bf16 v[60:63], a[220:223], a[188:191], v[60:63]\n"
"s_waitcnt lgkmcnt(8)\n"
"v_cvt_pk_bf16_f32 v110, v76, v77\n"
"v_mfma_f32_16x16x32_bf16 v[136:139], v[224:227], v[104:107], v[136:139]\n"
"ds_read_b128 v[160:163], v2 offset:8192\n"
"v_mfma_f32_16x16x32_bf16 a[64:67], v[192:195], v[104:107], a[64:67]\n"
"v_exp_f32 v78, v78\n"
"v_mfma_f32_16x16x32_bf16 a[72:75], v[196:199], v[104:107], a[72:75]\n"
"v_exp_f32 v79, v79\n"
"v_mfma_f32_16x16x32_bf16 a[80:83], v[200:203], v[104:107], a[80:83]\n"
"ds_read_b128 v[164:167], v2 offset:9216\n"
"v_cvt_pk_bf16_f32 v111, v78, v79\n"
"v_mfma_f32_16x16x32_bf16 a[88:91], v[204:207], v[104:107], a[88:91]\n"
"ds_read_b128 v[168:171], v2 offset:10240\n"
"v_mfma_f32_16x16x32_bf16 a[96:99], v[208:211], v[104:107], a[96:99]\n"
"v_exp_f32 v48, v48\n"
"v_mfma_f32_16x16x32_bf16 a[104:107], v[212:215], v[104:107], a[104:107]\n"
"ds_read_b128 v[172:175], v2 offset:11264\n"
"v_mfma_f32_16x16x32_bf16 a[112:115], v[216:219], v[104:107], a[112:115]\n"
"v_exp_f32 v49, v49\n"
"v_mfma_f32_16x16x32_bf16 a[120:123], v[220:223], v[104:107], a[120:123]\n"
"ds_read_b128 v[176:179], v2 offset:12288\n"
"v_cvt_pk_bf16_f32 v96, v48, v49\n"
"v_mfma_f32_16x16x32_bf16 v[140:143], v[224:227], v[108:111], v[140:143]\n"
"v_exp_f32 v50, v50\n"
"v_mfma_f32_16x16x32_bf16 a[68:71], v[192:195], v[108:111], a[68:71]\n"
"ds_read_b128 v[180:183], v2 offset:13312\n"
"v_mfma_f32_16x16x32_bf16 a[76:79], v[196:199], v[108:111], a[76:79]\n"
"v_exp_f32 v51, v51\n"
"v_mfma_f32_16x16x32_bf16 a[84:87], v[200:203], v[108:111], a[84:87]\n"
"ds_read_b128 v[184:187], v2 offset:14336\n"
"v_cvt_pk_bf16_f32 v97, v50, v51\n"
"v_mfma_f32_16x16x32_bf16 a[92:95], v[204:207], v[108:111], a[92:95]\n"
"v_exp_f32 v56, v56\n"
"v_mfma_f32_16x16x32_bf16 a[100:103], v[208:211], v[108:111], a[100:103]\n"
"ds_read_b128 v[188:191], v2 offset:15360\n"
"v_mfma_f32_16x16x32_bf16 a[108:111], v[212:215], v[108:111], a[108:111]\n"
"v_exp_f32 v57, v57\n"
"v_mfma_f32_16x16x32_bf16 a[116:119], v[216:219], v[108:111], a[116:119]\n"
"v_cvt_pk_bf16_f32 v98, v56, v57\n"
"v_mfma_f32_16x16x32_bf16 a[124:127], v[220:223], v[108:111], a[124:127]\n"
"v_add_u32 v2, 0x4000, v2\n"
"v_and_b32 v2, 0x1ffff, v2\n"
"v_exp_f32 v58, v58\n"
"v_exp_f32 v59, v59\n"
"s_nop 0\n"
"v_cvt_pk_bf16_f32 v99, v58, v59\n"
"v_exp_f32 v52, v52\n"
"v_exp_f32 v53, v53\n"
"s_nop 0\n"
"v_cvt_pk_bf16_f32 v100, v52, v53\n"
"v_exp_f32 v54, v54\n"
"v_exp_f32 v55, v55\n"
"s_nop 0\n"
"v_cvt_pk_bf16_f32 v101, v54, v55\n"
"v_exp_f32 v60, v60\n"
"v_exp_f32 v61, v61\n"
"s_nop 0\n"
"v_cvt_pk_bf16_f32 v102, v60, v61\n"
"v_exp_f32 v62, v62\n"
"v_exp_f32 v63, v63\n"
"s_nop 0\n"
"v_cvt_pk_bf16_f32 v103, v62, v63\n"
"s_waitcnt lgkmcnt(0)\n"
"s_nop 0\n"
"v_mfma_f32_16x16x32_bf16 v[136:139], v[224:227], v[96:99], v[136:139]\n"
"v_mfma_f32_16x16x32_bf16 a[64:67], v[160:163], v[96:99], a[64:67]\n"
"v_mfma_f32_16x16x32_bf16 a[72:75], v[164:167], v[96:99], a[72:75]\n"
"v_mfma_f32_16x16x32_bf16 a[80:83], v[168:171], v[96:99], a[80:83]\n"
"v_mfma_f32_16x16x32_bf16 a[88:91], v[172:175], v[96:99], a[88:91]\n"
"v_mfma_f32_16x16x32_bf16 a[96:99], v[176:179], v[96:99], a[96:99]\n"
"v_mfma_f32_16x16x32_bf16 a[104:107], v[180:183], v[96:99], a[104:107]\n"
"v_mfma_f32_16x16x32_bf16 a[112:115], v[184:187], v[96:99], a[112:115]\n"
"v_mfma_f32_16x16x32_bf16 a[120:123], v[188:191], v[96:99], a[120:123]\n"
"v_mfma_f32_16x16x32_bf16 v[140:143], v[224:227], v[100:103], v[140:143]\n"
"v_mfma_f32_16x16x32_bf16 a[68:71], v[160:163], v[100:103], a[68:71]\n"
"v_mfma_f32_16x16x32_bf16 a[76:79], v[164:167], v[100:103], a[76:79]\n"
"v_mfma_f32_16x16x32_bf16 a[84:87], v[168:171], v[100:103], a[84:87]\n"
"v_mfma_f32_16x16x32_bf16 a[92:95], v[172:175], v[100:103], a[92:95]\n"
"v_mfma_f32_16x16x32_bf16 a[100:103], v[176:179], v[100:103], a[100:103]\n"
"v_mfma_f32_16x16x32_bf16 a[108:111], v[180:183], v[100:103], a[108:111]\n"
"v_mfma_f32_16x16x32_bf16 a[116:119], v[184:187], v[100:103], a[116:119]\n"
"v_mfma_f32_16x16x32_bf16 a[124:127], v[188:191], v[100:103], a[124:127]\n"
"s_branch LB_end\n"
"LA_j7:\n"
"v_add_f32 v9, 0, v11\n"
"v_add_f32 v10, 0x6e800000, v11\n"
"v_add_f32 v144, 0, v9\n"
"v_min_f32 v144, v144, v7\n"
"v_add_f32 v145, 0xec800000, v9\n"
"v_min_f32 v145, v145, v7\n"
"v_add_f32 v146, 0xed000000, v9\n"
"v_min_f32 v146, v146, v7\n"
"v_add_f32 v147, 0xed400000, v9\n"
"v_min_f32 v147, v147, v7\n"
"v_add_f32 v148, 0, v10\n"
"v_min_f32 v148, v148, v8\n"
"v_add_f32 v149, 0xec800000, v10\n"
"v_min_f32 v149, v149, v8\n"
"v_add_f32 v150, 0xed000000, v10\n"
"v_min_f32 v150, v150, v8\n"
"v_add_f32 v151, 0xed400000, v10\n"
"v_min_f32 v151, v151, v8\n"
"v_add_f32 v152, 0xee800000, v9\n"
"v_min_f32 v152, v152, v7\n"
"v_add_f32 v153, 0xee880000, v9\n"
"v_min_f32 v153, v153, v7\n"
"v_add_f32 v154, 0xee900000, v9\n"
"v_min_f32 v154, v154, v7\n"
"v_add_f32 v155, 0xee980000, v9\n"
"v_min_f32 v155, v155, v7\n"
"v_add_f32 v156, 0xee800000, v10\n"
"v_min_f32 v156, v156, v8\n"
"v_add_f32 v157, 0xee880000, v10\n"
"v_min_f32 v157, v157, v8\n"
"v_add_f32 v158, 0xee900000, v10\n"
"v_min_f32 v158, v158, v8\n"
"v_add_f32 v159, 0xee980000, v10\n"
"v_min_f32 v159, v159, v8\n"
"v_add_f32 v9, 0xef000000, v9\n"
"v_add_f32 v10, 0xef000000, v10\n"
"s_nop 1\n"
"s_waitcnt lgkmcnt(8)\n"
"v_mfma_f32_16x16x32_bf16 v[48:51], a[192:195], a[160:163], v[144:147]\n"
"ds_read_b128 a[224:227], v2 offset:0\n"
"v_exp_f32 v44, v44\n"
"v_mfma_f32_16x16x32_bf16 v[48:51], a[196:199], a[164:167], v[48:51]\n"
"ds_read_b128 a[228:231], v2 offset:1024\n"
"v_exp_f32 v74, v74\n"
"v_mfma_f32_16x16x32_bf16 v[48:51], a[200:203], a[168:171], v[48:51]\n"
"v_exp_f32 v75, v75\n"
"v_mfma_f32_16x16x32_bf16 v[48:51], a[204:207], a[172:175], v[48:51]\n"
"ds_read_b128 a[232:235], v2 offset:2048\n"
"v_exp_f32 v45, v45\n"
"v_mfma_f32_16x16x32_bf16 v[52:55], a[192:195], a[176:179], v[148:151]\n"
"v_cvt_pk_bf16_f32 v107, v74, v75\n"
"v_mfma_f32_16x16x32_bf16 v[52:55], a[196:199], a[180:183], v[52:55]\n"
"ds_read_b128 a[236:239], v2 offset:3072\n"
"v_mfma_f32_16x16x32_bf16 v[52:55], a[200:203], a[184:187], v[52:55]\n"
"v_exp_f32 v68, v68\n"
"v_mfma_f32_16x16x32_bf16 v[52:55], a[204:207], a[188:191], v[52:55]\n"
"ds_read_b128 a[240:243], v2 offset:4096\n"
"v_cvt_pk_bf16_f32 v94, v44, v45\n"
"v_mfma_f32_16x16x32_bf16 v[56:59], a[208:211], a[160:163], v[152:155]\n"
"v_exp_f32 v69, v69\n"
"v_mfma_f32_16x16x32_bf16 v[56:59], a[212:215], a[164:167], v[56:59]\n"
"ds_read_b128 a[244:247], v2 offset:5120\n"
"v_mfma_f32_16x16x32_bf16 v[56:59], a[216:219], a[168:171], v[56:59]\n"
"v_cvt_pk_bf16_f32 v108, v68, v69\n"
"v_mfma_f32_16x16x32_bf16 v[56:59], a[220:223], a[172:175], v[56:59]\n"
"ds_read_b128 a[248:251], v2 offset:6144\n"
"v_exp_f32 v46, v46\n"
"v_mfma_f32_16x16x32_bf16 v[60:63], a[208:211], a[176:179], v[156:159]\n"
"ds_read_b128 a[252:255], v2 offset:7168\n"
"v_mfma_f32_16x16x32_bf16 v[60:63], a[212:215], a[180:183], v[60:63]\n"
"v_exp_f32 v70, v70\n"
"v_mfma_f32_16x16x32_bf16 v[60:63], a[216:219], a[184:187], v[60:63]\n"
"v_mfma_f32_16x16x32_bf16 v[60:63], a[220:223], a[188:191], v[60:63]\n"
"s_waitcnt lgkmcnt(8)\n"
"v_exp_f32 v47, v47\n"
"v_mfma_f32_16x16x32_bf16 v[128:131], v[224:227], v[88:91], v[128:131]\n"
"ds_read_b128 v[160:163], v2 offset:8192\n"
"v_exp_f32 v71, v71\n"
"v_mfma_f32_16x16x32_bf16 a[0:3], v[192:195], v[88:91], a[0:3]\n"
"v_cvt_pk_bf16_f32 v109, v70, v71\n"
"v_add_f32 v144, 0, v9\n"
"v_min_f32 v144, v144, v7\n"
"v_mfma_f32_16x16x32_bf16 a[8:11], v[196:199], v[88:91], a[8:11]\n"
"v_add_f32 v145, 0xec800000, v9\n"
"v_mfma_f32_16x16x32_bf16 a[16:19], v[200:203], v[88:91], a[16:19]\n"
"v_cvt_pk_bf16_f32 v95, v46, v47\n"
"v_exp_f32 v76, v76\n"
"v_mfma_f32_16x16x32_bf16 a[24:27], v[204:207], v[88:91], a[24:27]\n"
"ds_read_b128 v[164:167], v2 offset:9216\n"
"v_min_f32 v145, v145, v7\n"
"v_add_f32 v146, 0xed000000, v9\n"
"v_mfma_f32_16x16x32_bf16 a[32:35], v[208:211], v[88:91], a[32:35]\n"
"v_exp_f32 v77, v77\n"
"v_min_f32 v146, v146, v7\n"
"v_mfma_f32_16x16x32_bf16 a[40:43], v[212:215], v[88:91], a[40:43]\n"
"v_add_f32 v147, 0xed400000, v9\n"
"v_mfma_f32_16x16x32_bf16 a[48:51], v[216:219], v[88:91], a[48:51]\n"
"v_cvt_pk_bf16_f32 v110, v76, v77\n"
"v_min_f32 v147, v147, v7\n"
"v_mfma_f32_16x16x32_bf16 a[56:59], v[220:223], v[88:91], a[56:59]\n"
"ds_read_b128 v[168:171], v2 offset:10240\n"
"v_add_f32 v148, 0, v10\n"
"v_mfma_f32_16x16x32_bf16 v[132:135], v[224:227], v[92:95], v[132:135]\n"
"v_min_f32 v148, v148, v8\n"
"v_mfma_f32_16x16x32_bf16 a[4:7], v[192:195], v[92:95], a[4:7]\n"
"v_exp_f32 v78, v78\n"
"v_add_f32 v149, 0xec800000, v10\n"
"v_mfma_f32_16x16x32_bf16 a[12:15], v[196:199], v[92:95], a[12:15]\n"
"v_min_f32 v149, v149, v8\n"
"v_mfma_f32_16x16x32_bf16 a[20:23], v[200:203], v[92:95], a[20:23]\n"
"ds_read_b128 v[172:175], v2 offset:11264\n"
"v_exp_f32 v79, v79\n"
"v_mfma_f32_16x16x32_bf16 a[28:31], v[204:207], v[92:95], a[28:31]\n"
"v_add_f32 v150, 0xed000000, v10\n"
"v_min_f32 v150, v150, v8\n"
"v_mfma_f32_16x16x32_bf16 a[36:39], v[208:211], v[92:95], a[36:39]\n"
"v_cvt_pk_bf16_f32 v111, v78, v79\n"
"v_add_f32 v151, 0xed400000, v10\n"
"v_mfma_f32_16x16x32_bf16 a[44:47], v[212:215], v[92:95], a[44:47]\n"
"v_min_f32 v151, v151, v8\n"
"v_mfma_f32_16x16x32_bf16 a[52:55], v[216:219], v[92:95], a[52:55]\n"
"v_exp_f32 v48, v48\n"
"v_add_f32 v152, 0xee800000, v9\n"
"v_mfma_f32_16x16x32_bf16 a[60:63], v[220:223], v[92:95], a[60:63]\n"
"ds_read_b128 v[176:179], v2 offset:12288\n"
"v_mfma_f32_16x16x32_bf16 v[136:139], v[224:227], v[104:107], v[136:139]\n"
"v_exp_f32 v49, v49\n"
"v_min_f32 v152, v152, v7\n"
"v_mfma_f32_16x16x32_bf16 a[64:67], v[192:195], v[104:107], a[64:67]\n"
"v_add_f32 v153, 0xee880000, v9\n"
"v_mfma_f32_16x16x32_bf16 a[72:75], v[196:199], v[104:107], a[72:75]\n"
"v_min_f32 v153, v153, v7\n"
"v_cvt_pk_bf16_f32 v96, v48, v49\n"
"v_mfma_f32_16x16x32_bf16 a[80:83], v[200:203], v[104:107], a[80:83]\n"
"ds_read_b128 v[180:183], v2 offset:13312\n"
"v_add_f32 v154, 0xee900000, v9\n"
"v_mfma_f32_16x16x32_bf16 a[88:91], v[204:207], v[104:107], a[88:91]\n"
"v_min_f32 v154, v154, v7\n"
"v_mfma_f32_16x16x32_bf16 a[96:99], v[208:211], v[104:107], a[96:99]\n"
"v_exp_f32 v50, v50\n"
"v_add_f32 v155, 0xee980000, v9\n"
"v_mfma_f32_16x16x32_bf16 a[104:107], v[212:215], v[104:107], a[104:107]\n"
"v_min_f32 v155, v155, v7\n"
"v_mfma_f32_16x16x32_bf16 a[112:115], v[216:219], v[104:107], a[112:115]\n"
"ds_read_b128 v[184:187], v2 offset:14336\n"
"v_exp_f32 v51, v51\n"
"v_mfma_f32_16x16x32_bf16 a[120:123], v[220:223], v[104:107], a[120:123]\n"
"v_add_f32 v156, 0xee800000, v10\n"
"v_min_f32 v156, v156, v8\n"
"v_mfma_f32_16x16x32_bf16 v[140:143], v[224:227], v[108:111], v[140:143]\n"
"v_add_f32 v157, 0xee880000, v10\n"
"v_cvt_pk_bf16_f32 v97, v50, v51\n"
"v_mfma_f32_16x16x32_bf16 a[68:71], v[192:195], v[108:111], a[68:71]\n"
"v_min_f32 v157, v157, v8\n"
"v_mfma_f32_16x16x32_bf16 a[76:79], v[196:199], v[108:111], a[76:79]\n"
"ds_read_b128 v[188:191], v2 offset:15360\n"
"v_exp_f32 v56, v56\n"
"v_mfma_f32_16x16x32_bf16 a[84:87], v[200:203], v[108:111], a[84:87]\n"
"v_add_f32 v158, 0xee900000, v10\n"
"v_min_f32 v158, v158, v8\n"
"v_mfma_f32_16x16x32_bf16 a[92:95], v[204:207], v[108:111], a[92:95]\n"
"v_exp_f32 v57, v57\n"
"v_add_f32 v159, 0xee980000, v10\n"
"v_mfma_f32_16x16x32_bf16 a[100:103], v[208:211], v[108:111], a[100:103]\n"
"v_min_f32 v159, v159, v8\n"
"v_mfma_f32_16x16x32_bf16 a[108:111], v[212:215], v[108:111], a[108:111]\n"
"v_add_f32 v9, 0xef000000, v9\n"
"v_cvt_pk_bf16_f32 v98, v56, v57\n"
"v_add_f32 v10, 0xef000000, v10\n"
"v_mfma_f32_16x16x32_bf16 a[116:119], v[216:219], v[108:111], a[116:119]\n"
"v_mfma_f32_16x16x32_bf16 a[124:127], v[220:223], v[108:111], a[124:127]\n"
"v_add_u32 v2, 0x4000, v2\n"
"v_and_b32 v2, 0x1ffff, v2\n"
"s_waitcnt lgkmcnt(8)\n"
"v_mfma_f32_16x16x32_bf16 v[64:67], a[224:227], a[160:163], v[144:147]\n"
"s_waitcnt vmcnt(0)\n"
"s_barrier\n"
"v_mfma_f32_16x16x32_bf16 v[64:67], a[228:231], a[164:167], v[64:67]\n"
"ds_read_b128 a[192:195], v2 offset:0\n"
"v_exp_f32 v58, v58\n"
"v_mfma_f32_16x16x32_bf16 v[64:67], a[232:235], a[168:171], v[64:67]\n"
"ds_read_b128 a[196:199], v2 offset:1024\n"
"v_exp_f32 v59, v59\n"
"v_mfma_f32_16x16x32_bf16 v[64:67], a[236:239], a[172:175], v[64:67]\n"
"v_cvt_pk_bf16_f32 v99, v58, v59\n"
"v_mfma_f32_16x16x32_bf16 v[68:71], a[224:227], a[176:179], v[148:151]\n"
"ds_read_b128 a[200:203], v2 offset:2048\n"
"v_exp_f32 v52, v52\n"
"v_mfma_f32_16x16x32_bf16 v[68:71], a[228:231], a[180:183], v[68:71]\n"
"v_exp_f32 v53, v53\n"
"v_mfma_f32_16x16x32_bf16 v[68:71], a[232:235], a[184:187], v[68:71]\n"
"ds_read_b128 a[204:207], v2 offset:3072\n"
"v_mfma_f32_16x16x32_bf16 v[68:71], a[236:239], a[188:191], v[68:71]\n"
"v_cvt_pk_bf16_f32 v100, v52, v53\n"
"v_mfma_f32_16x16x32_bf16 v[72:75], a[240:243], a[160:163], v[152:155]\n"
"ds_read_b128 a[208:211], v2 offset:4096\n"
"v_exp_f32 v54, v54\n"
"v_mfma_f32_16x16x32_bf16 v[72:75], a[244:247], a[164:167], v[72:75]\n"
"ds_read_b128 a[212:215], v2 offset:5120\n"
"v_exp_f32 v55, v55\n"
"v_mfma_f32_16x16x32_bf16 v[72:75], a[248:251], a[168:171], v[72:75]\n"
"v_mfma_f32_16x16x32_bf16 v[72:75], a[252:255], a[172:175], v[72:75]\n"
"ds_read_b128 a[216:219], v2 offset:6144\n"
"v_cvt_pk_bf16_f32 v101, v54, v55\n"
"v_mfma_f32_16x16x32_bf16 v[76:79], a[240:243], a[176:179], v[156:159]\n"
"ds_read_b128 a[220:223], v2 offset:7168\n"
"v_exp_f32 v60, v60\n"
"v_mfma_f32_16x16x32_bf16 v[76:79], a[244:247], a[180:183], v[76:79]\n"
"v_mfma_f32_16x16x32_bf16 v[76:79], a[248:251], a[184:187], v[76:79]\n"
"v_exp_f32 v61, v61\n"
"v_mfma_f32_16x16x32_bf16 v[76:79], a[252:255], a[188:191], v[76:79]\n"
"s_waitcnt lgkmcnt(8)\n"
"v_cvt_pk_bf16_f32 v102, v60, v61\n"
"v_mfma_f32_16x16x32_bf16 v[136:139], v[224:227], v[96:99], v[136:139]\n"
"ds_read_b128 v[192:195], v2 offset:8192\n"
"v_add_f32 v144, 0, v9\n"
"v_min_f32 v144, v144, v7\n"
"v_mfma_f32_16x16x32_bf16 a[64:67], v[160:163], v[96:99], a[64:67]\n"
"v_exp_f32 v62, v62\n"
"v_add_f32 v145, 0xec800000, v9\n"
"v_mfma_f32_16x16x32_bf16 a[72:75], v[164:167], v[96:99], a[72:75]\n"
"v_min_f32 v145, v145, v7\n"
"v_exp_f32 v63, v63\n"
"v_mfma_f32_16x16x32_bf16 a[80:83], v[168:171], v[96:99], a[80:83]\n"
"v_add_f32 v146, 0xed000000, v9\n"
"v_min_f32 v146, v146, v7\n"
"v_cvt_pk_bf16_f32 v103, v62, v63\n"
"v_mfma_f32_16x16x32_bf16 a[88:91], v[172:175], v[96:99], a[88:91]\n"
"ds_read_b128 v[196:199], v2 offset:9216\n"
"v_add_f32 v147, 0xed400000, v9\n"
"v_min_f32 v147, v147, v7\n"
"v_mfma_f32_16x16x32_bf16 a[96:99], v[176:179], v[96:99], a[96:99]\n"
"ds_read_b128 v[200:203], v2 offset:10240\n"
"v_add_f32 v148, 0, v10\n"
"v_min_f32 v148, v148, v8\n"
"v_mfma_f32_16x16x32_bf16 a[104:107], v[180:183], v[96:99], a[104:107]\n"
"ds_read_b128 v[204:207], v2 offset:11264\n"
"v_add_f32 v149, 0xec800000, v10\n"
"v_min_f32 v149, v149, v8\n"
"v_mfma_f32_16x16x32_bf16 a[112:115], v[184:187], v[96:99], a[112:115]\n"
"v_add_f32 v150, 0xed000000, v10\n"
"v_min_f32 v150, v150, v8\n"
"v_add_f32 v151, 0xed400000, v10\n"
"v_mfma_f32_16x16x32_bf16 a[120:123], v[188:191], v[96:99], a[120:123]\n"
"v_min_f32 v151, v151, v8\n"
"v_exp_f32 v64, v64\n"
"v_mfma_f32_16x16x32_bf16 v[140:143], v[224:227], v[100:103], v[140:143]\n"
"ds_read_b128 v[208:211], v2 offset:12288\n"
"v_add_f32 v152, 0xee800000, v9\n"
"v_min_f32 v152, v152, v7\n"
"v_mfma_f32_16x16x32_bf16 a[68:71], v[160:163], v[100:103], a[68:71]\n"
"ds_read_b128 v[212:215], v2 offset:13312\n"
"v_add_f32 v153, 0xee880000, v9\n"
"v_min_f32 v153, v153, v7\n"
"v_mfma_f32_16x16x32_bf16 a[76:79], v[164:167], v[100:103], a[76:79]\n"
"v_exp_f32 v65, v65\n"
"v_add_f32 v154, 0xee900000, v9\n"
"v_mfma_f32_16x16x32_bf16 a[84:87], v[168:171], v[100:103], a[84:87]\n"
"v_min_f32 v154, v154, v7\n"
"v_cvt_pk_bf16_f32 v104, v64, v65\n"
"v_add_f32 v155, 0xee980000, v9\n"
"v_mfma_f32_16x16x32_bf16 a[92:95], v[172:175], v[100:103], a[92:95]\n"
"v_min_f32 v155, v155, v7\n"
"v_exp_f32 v66, v66\n"
"v_mfma_f32_16x16x32_bf16 a[100:103], v[176:179], v[100:103], a[100:103]\n"
"v_add_f32 v156, 0xee800000, v10\n"
"v_min_f32 v156, v156, v8\n"
"v_exp_f32 v67, v67\n"
"v_mfma_f32_16x16x32_bf16 a[108:111], v[180:183], v[100:103], a[108:111]\n"
"ds_read_b128 v[216:219], v2 offset:14336\n"
"ds_read_b128 v[220:223], v2 offset:15360\n"
"v_add_f32 v157, 0xee880000, v10\n"
"v_min_f32 v157, v157, v8\n"
"v_cvt_pk_bf16_f32 v105, v66, v67\n"
"v_add_f32 v158, 0xee900000, v10\n"
"v_exp_f32 v72, v72\n"
"v_mfma_f32_16x16x32_bf16 a[116:119], v[184:187], v[100:103], a[116:119]\n"
"v_min_f32 v158, v158, v8\n"
"v_add_f32 v159, 0xee980000, v10\n"
"v_exp_f32 v73, v73\n"
"v_mfma_f32_16x16x32_bf16 a[124:127], v[188:191], v[100:103], a[124:127]\n"
"v_min_f32 v159, v159, v8\n"
"v_add_f32 v9, 0xef000000, v9\n"
"v_cvt_pk_bf16_f32 v106, v72, v73\n"
"v_add_f32 v10, 0xef000000, v10\n"
"v_add_u32 v2, 0x4000, v2\n"
"v_and_b32 v2, 0x1ffff, v2\n"
"s_mul_i32 s44, s86, 376832\n"
"s_lshl_b32 s45, s37, 12\n"
"s_add_u32 s44, s44, s45\n"
"s_add_u32 s45, s44, 188416\n"
"v_cmp_nlt_f32 vcc, v128, v228\n"
"v_rcp_f32 v13, v128\n"
"s_nop 0\n"
"s_or_b32 s31, s31, vcc_lo\n"
"s_or_b32 s31, s31, vcc_hi\n"
"v_mov_b32 v12, v13\n"
"v_accvgpr_read_b32 v230, a0\n"
"v_accvgpr_read_b32 v231, a1\n"
"v_accvgpr_read_b32 v232, a2\n"
"v_accvgpr_read_b32 v233, a3\n"
"v_accvgpr_read_b32 v234, a8\n"
"v_accvgpr_read_b32 v235, a9\n"
"v_accvgpr_read_b32 v236, a10\n"
"v_accvgpr_read_b32 v237, a11\n"
"v_pk_mul_f32 v[230:231], v[230:231], v[12:13]\n"
"v_pk_mul_f32 v[232:233], v[232:233], v[12:13]\n"
"v_pk_mul_f32 v[234:235], v[234:235], v[12:13]\n"
"v_pk_mul_f32 v[236:237], v[236:237], v[12:13]\n"
"v_cvt_pk_bf16_f32 v246, v230, v231\n"
"v_cvt_pk_bf16_f32 v247, v232, v233\n"
"v_cvt_pk_bf16_f32 v248, v234, v235\n"
"v_cvt_pk_bf16_f32 v249, v236, v237\n"
"buffer_store_dwordx4 v[246:249], v1, s[60:63], s44 offen offset:0 sc1\n"
"v_accvgpr_read_b32 v238, a16\n"
"v_accvgpr_read_b32 v239, a17\n"
"v_accvgpr_read_b32 v240, a18\n"
"v_accvgpr_read_b32 v241, a19\n"
"v_accvgpr_read_b32 v242, a24\n"
"v_accvgpr_read_b32 v243, a25\n"
"v_accvgpr_read_b32 v244, a26\n"
"v_accvgpr_read_b32 v245, a27\n"
"v_pk_mul_f32 v[238:239], v[238:239], v[12:13]\n"
"v_pk_mul_f32 v[240:241], v[240:241], v[12:13]\n"
"v_pk_mul_f32 v[242:243], v[242:243], v[12:13]\n"
"v_pk_mul_f32 v[244:245], v[244:245], v[12:13]\n"
"v_cvt_pk_bf16_f32 v250, v238, v239\n"
"v_cvt_pk_bf16_f32 v251, v240, v241\n"
"v_cvt_pk_bf16_f32 v252, v242, v243\n"
"v_cvt_pk_bf16_f32 v253, v244, v245\n"
"buffer_store_dwordx4 v[250:253], v1, s[60:63], s44 offen offset:1024 sc1\n"
"v_accvgpr_read_b32 v230, a32\n"
"v_accvgpr_read_b32 v231, a33\n"
"v_accvgpr_read_b32 v232, a34\n"
"v_accvgpr_read_b32 v233, a35\n"
"v_accvgpr_read_b32 v234, a40\n"
"v_accvgpr_read_b32 v235, a41\n"
"v_accvgpr_read_b32 v236, a42\n"
"v_accvgpr_read_b32 v237, a43\n"
"v_pk_mul_f32 v[230:231], v[230:231], v[12:13]\n"
"v_pk_mul_f32 v[232:233], v[232:233], v[12:13]\n"
"v_pk_mul_f32 v[234:235], v[234:235], v[12:13]\n"
"v_pk_mul_f32 v[236:237], v[236:237], v[12:13]\n"
"v_cvt_pk_bf16_f32 v246, v230, v231\n"
"v_cvt_pk_bf16_f32 v247, v232, v233\n"
"v_cvt_pk_bf16_f32 v248, v234, v235\n"
"v_cvt_pk_bf16_f32 v249, v236, v237\n"
"buffer_store_dwordx4 v[246:249], v1, s[60:63], s44 offen offset:2048 sc1\n"
"v_accvgpr_read_b32 v238, a48\n"
"v_accvgpr_read_b32 v239, a49\n"
"v_accvgpr_read_b32 v240, a50\n"
"v_accvgpr_read_b32 v241, a51\n"
"v_accvgpr_read_b32 v242, a56\n"
"v_accvgpr_read_b32 v243, a57\n"
"v_accvgpr_read_b32 v244, a58\n"
"v_accvgpr_read_b32 v245, a59\n"
"v_pk_mul_f32 v[238:239], v[238:239], v[12:13]\n"
"v_pk_mul_f32 v[240:241], v[240:241], v[12:13]\n"
"v_pk_mul_f32 v[242:243], v[242:243], v[12:13]\n"
"v_pk_mul_f32 v[244:245], v[244:245], v[12:13]\n"
"v_cvt_pk_bf16_f32 v250, v238, v239\n"
"v_cvt_pk_bf16_f32 v251, v240, v241\n"
"v_cvt_pk_bf16_f32 v252, v242, v243\n"
"v_cvt_pk_bf16_f32 v253, v244, v245\n"
"buffer_store_dwordx4 v[250:253], v1, s[60:63], s44 offen offset:3072 sc1\n"
"v_cmp_nlt_f32 vcc, v132, v228\n"
"v_rcp_f32 v13, v132\n"
"s_nop 0\n"
"s_or_b32 s31, s31, vcc_lo\n"
"s_or_b32 s31, s31, vcc_hi\n"
"v_mov_b32 v12, v13\n"
"v_accvgpr_read_b32 v230, a4\n"
"v_accvgpr_read_b32 v231, a5\n"
"v_accvgpr_read_b32 v232, a6\n"
"v_accvgpr_read_b32 v233, a7\n"
"v_accvgpr_read_b32 v234, a12\n"
"v_accvgpr_read_b32 v235, a13\n"
"v_accvgpr_read_b32 v236, a14\n"
"v_accvgpr_read_b32 v237, a15\n"
"v_pk_mul_f32 v[230:231], v[230:231], v[12:13]\n"
"v_pk_mul_f32 v[232:233], v[232:233], v[12:13]\n"
"v_pk_mul_f32 v[234:235], v[234:235], v[12:13]\n"
"v_pk_mul_f32 v[236:237], v[236:237], v[12:13]\n"
"v_cvt_pk_bf16_f32 v246, v230, v231\n"
"v_cvt_pk_bf16_f32 v247, v232, v233\n"
"v_cvt_pk_bf16_f32 v248, v234, v235\n"
"v_cvt_pk_bf16_f32 v249, v236, v237\n"
"buffer_store_dwordx4 v[246:249], v1, s[60:63], s45 offen offset:0 sc1\n"
"v_accvgpr_read_b32 v238, a20\n"
"v_accvgpr_read_b32 v239, a21\n"
"v_accvgpr_read_b32 v240, a22\n"
"v_accvgpr_read_b32 v241, a23\n"
"v_accvgpr_read_b32 v242, a28\n"
"v_accvgpr_read_b32 v243, a29\n"
"v_accvgpr_read_b32 v244, a30\n"
"v_accvgpr_read_b32 v245, a31\n"
"v_pk_mul_f32 v[238:239], v[238:239], v[12:13]\n"
"v_pk_mul_f32 v[240:241], v[240:241], v[12:13]\n"
"v_pk_mul_f32 v[242:243], v[242:243], v[12:13]\n"
"v_pk_mul_f32 v[244:245], v[244:245], v[12:13]\n"
"v_cvt_pk_bf16_f32 v250, v238, v239\n"
"v_cvt_pk_bf16_f32 v251, v240, v241\n"
"v_cvt_pk_bf16_f32 v252, v242, v243\n"
"v_cvt_pk_bf16_f32 v253, v244, v245\n"
"buffer_store_dwordx4 v[250:253], v1, s[60:63], s45 offen offset:1024 sc1\n"
"v_accvgpr_read_b32 v230, a36\n"
"v_accvgpr_read_b32 v231, a37\n"
"v_accvgpr_read_b32 v232, a38\n"
"v_accvgpr_read_b32 v233, a39\n"
"v_accvgpr_read_b32 v234, a44\n"
"v_accvgpr_read_b32 v235, a45\n"
"v_accvgpr_read_b32 v236, a46\n"
"v_accvgpr_read_b32 v237, a47\n"
"v_pk_mul_f32 v[230:231], v[230:231], v[12:13]\n"
"v_pk_mul_f32 v[232:233], v[232:233], v[12:13]\n"
"v_pk_mul_f32 v[234:235], v[234:235], v[12:13]\n"
"v_pk_mul_f32 v[236:237], v[236:237], v[12:13]\n"
"v_cvt_pk_bf16_f32 v246, v230, v231\n"
"v_cvt_pk_bf16_f32 v247, v232, v233\n"
"v_cvt_pk_bf16_f32 v248, v234, v235\n"
"v_cvt_pk_bf16_f32 v249, v236, v237\n"
"buffer_store_dwordx4 v[246:249], v1, s[60:63], s45 offen offset:2048 sc1\n"
"v_accvgpr_read_b32 v238, a52\n"
"v_accvgpr_read_b32 v239, a53\n"
"v_accvgpr_read_b32 v240, a54\n"
"v_accvgpr_read_b32 v241, a55\n"
"v_accvgpr_read_b32 v242, a60\n"
"v_accvgpr_read_b32 v243, a61\n"
"v_accvgpr_read_b32 v244, a62\n"
"v_accvgpr_read_b32 v245, a63\n"
"v_pk_mul_f32 v[238:239], v[238:239], v[12:13]\n"
"v_pk_mul_f32 v[240:241], v[240:241], v[12:13]\n"
"v_pk_mul_f32 v[242:243], v[242:243], v[12:13]\n"
"v_pk_mul_f32 v[244:245], v[244:245], v[12:13]\n"
"v_cvt_pk_bf16_f32 v250, v238, v239\n"
"v_cvt_pk_bf16_f32 v251, v240, v241\n"
"v_cvt_pk_bf16_f32 v252, v242, v243\n"
"v_cvt_pk_bf16_f32 v253, v244, v245\n"
"buffer_store_dwordx4 v[250:253], v1, s[60:63], s45 offen offset:3072 sc1\n"
"v_and_b32 v52, 63, v0\n"
"v_lshlrev_b32 v38, 4, v52\n"
"v_and_b32 v39, 15, v52\n"
"v_lshrrev_b32 v40, 4, v52\n"
"s_and_b32 s80, s30, 3\n"
"s_lshr_b32 s44, s30, 2\n"
"s_and_b32 s44, s44, 1\n"
"s_lshl_b32 s44, s44, 3\n"
"s_lshr_b32 s45, s30, 5\n"
"s_add_u32 s81, s44, s45\n"
"s_lshr_b32 s45, s30, 3\n"
"s_and_b32 s84, s45, 3\n"
"s_lshl_b32 s88, s81, 2\n"
"s_add_u32 s88, s88, s84\n"
"s_mov_b32 s52, s18\n"
"s_and_b32 s53, s19, 0xffff\n"
"s_mov_b32 s54, 0x400000\n"
"s_mov_b32 s55, 0x20000\n"
"s_mov_b32 s56, s24\n"
"s_and_b32 s57, s25, 0xffff\n"
"s_mov_b32 s58, 0x1c0000\n"
"s_mov_b32 s59, 0x20000\n"
"s_add_u32 s60, s26, 0x2000000\n"
"s_addc_u32 s61, s27, 0\n"
"s_and_b32 s61, s61, 0xffff\n"
"s_mov_b32 s62, 0x1700000\n"
"s_mov_b32 s63, 0x20000\n"
"s_lshl_b32 s44, s34, 1\n"
"s_add_u32 s44, s44, 0\n"
"s_and_b32 s45, s44, 4\n"
"s_add_u32 s44, s44, s80\n"
"s_and_b32 s44, s44, 3\n"
"s_add_u32 s44, s44, s45\n"
"s_lshl_b32 s45, s81, 3\n"
"s_add_u32 s44, s44, s45\n"
"s_mul_i32 s44, s44, 188416\n"
"s_mul_i32 s45, s80, 14336\n"
"s_add_u32 s68, s44, s45\n"
"s_add_u32 s68, s68, 131072\n"
"s_lshl_b32 s45, s80, 15\n"
"s_add_u32 s70, s44, s45\n"
"s_lshl_b32 s44, s34, 1\n"
"s_add_u32 s44, s44, 1\n"
"s_and_b32 s45, s44, 4\n"
"s_add_u32 s44, s44, s80\n"
"s_and_b32 s44, s44, 3\n"
"s_add_u32 s44, s44, s45\n"
"s_lshl_b32 s45, s81, 3\n"
"s_add_u32 s44, s44, s45\n"
"s_mul_i32 s44, s44, 188416\n"
"s_mul_i32 s45, s80, 14336\n"
"s_add_u32 s69, s44, s45\n"
"s_add_u32 s69, s69, 131072\n"
"s_lshl_b32 s45, s80, 15\n"
"s_add_u32 s71, s44, s45\n"
"s_lshl_b32 s82, s34, 12\n"
"s_lshl_b32 s83, s34, 12\n"
"s_lshl_b32 s46, s84, 8\n"
"s_lshl_b32 s44, s39, 5\n"
"s_lshl_b32 s45, s40, 3\n"
"s_add_u32 s44, s44, s45\n"
"s_add_u32 s44, s44, 0\n"
"s_lshl_b32 s45, s80, 10\n"
"s_add_u32 s44, s44, s45\n"
"s_lshl_b32 s44, s44, 10\n"
"s_add_u32 s76, s44, s46\n"
"s_lshl_b32 s44, s39, 5\n"
"s_lshl_b32 s45, s40, 3\n"
"s_add_u32 s44, s44, s45\n"
"s_add_u32 s44, s44, 0\n"
"s_mul_i32 s45, s80, 448\n"
"s_add_u32 s44, s44, s45\n"
"s_lshl_b32 s44, s44, 10\n"
"s_add_u32 s72, s44, s46\n"
"s_lshl_b32 s44, s39, 5\n"
"s_lshl_b32 s45, s40, 3\n"
"s_add_u32 s44, s44, s45\n"
"s_add_u32 s44, s44, 16\n"
"s_lshl_b32 s45, s80, 10\n"
"s_add_u32 s44, s44, s45\n"
"s_lshl_b32 s44, s44, 10\n"
"s_add_u32 s77, s44, s46\n"
"s_lshl_b32 s44, s39, 5\n"
"s_lshl_b32 s45, s40, 3\n"
"s_add_u32 s44, s44, s45\n"
"s_add_u32 s44, s44, 16\n"
"s_mul_i32 s45, s80, 448\n"
"s_add_u32 s44, s44, s45\n"
"s_lshl_b32 s44, s44, 10\n"
"s_add_u32 s73, s44, s46\n"
"s_lshl_b32 s44, s39, 5\n"
"s_lshl_b32 s45, s40, 3\n"
"s_add_u32 s44, s44, s45\n"
"s_add_u32 s44, s44, 4\n"
"s_lshl_b32 s45, s80, 10\n"
"s_add_u32 s44, s44, s45\n"
"s_lshl_b32 s44, s44, 10\n"
"s_add_u32 s78, s44, s46\n"
"s_lshl_b32 s44, s39, 5\n"
"s_lshl_b32 s45, s40, 3\n"
"s_add_u32 s44, s44, s45\n"
"s_add_u32 s44, s44, 4\n"
"s_mul_i32 s45, s80, 448\n"
"s_add_u32 s44, s44, s45\n"
"s_lshl_b32 s44, s44, 10\n"
"s_add_u32 s74, s44, s46\n"
"s_lshl_b32 s44, s39, 5\n"
"s_lshl_b32 s45, s40, 3\n"
"s_add_u32 s44, s44, s45\n"
"s_add_u32 s44, s44, 20\n"
"s_lshl_b32 s45, s80, 10\n"
"s_add_u32 s44, s44, s45\n"
"s_lshl_b32 s44, s44, 10\n"
"s_add_u32 s79, s44, s46\n"
"s_lshl_b32 s44, s39, 5\n"
"s_lshl_b32 s45, s40, 3\n"
"s_add_u32 s44, s44, s45\n"
"s_add_u32 s44, s44, 20\n"
"s_mul_i32 s45, s80, 448\n"
"s_add_u32 s44, s44, s45\n"
"s_lshl_b32 s44, s44, 10\n"
"s_add_u32 s75, s44, s46\n"
"v_add_u32 v34, 0x0, v38\n"
"v_mov_b32 v35, v38\n"
"v_lshrrev_b32 v52, 1, v39\n"
"v_xor_b32 v52, v52, v40\n"
"v_lshlrev_b32 v52, 5, v52\n"
"v_and_b32 v53, 1, v39\n"
"v_lshl_add_u32 v52, v53, 4, v52\n"
"v_lshl_add_u32 v37, v40, 10, v52\n"
"v_xor_b32 v42, 0x80, v37\n"
"v_add_u32 v36, 0x0, v37\n"
"v_add_u32 v41, 0x0, v42\n"
"s_lshl_b32 s44, s39, 13\n"
"s_add_u32 s44, s44, 0x0\n"
"v_add_u32 v24, s44, v38\n"
"v_add_u32 v25, 0x400, v24\n"
"v_lshrrev_b32 v52, 2, v39\n"
"v_and_b32 v53, 3, v39\n"
"v_lshlrev_b32 v54, 11, v40\n"
"v_lshl_add_u32 v54, v52, 8, v54\n"
"v_lshl_add_u32 v54, v53, 3, v54\n"
"v_add_u32 v54, 0x4000, v54\n"
"v_and_b32 v53, 1, v40\n"
"v_lshl_or_b32 v52, v53, 2, v52\n"
"s_lshl_b32 s45, s40, 2\n"
"s_add_u32 s44, s45, 0\n"
"v_xor_b32 v55, s44, v52\n"
"v_lshl_add_u32 v26, v55, 5, v54\n"
"v_add_u32 v30, 0x2000, v26\n"
"s_add_u32 s44, s45, 1\n"
"v_xor_b32 v55, s44, v52\n"
"v_lshl_add_u32 v27, v55, 5, v54\n"
"v_add_u32 v31, 0x2000, v27\n"
"s_add_u32 s44, s45, 2\n"
"v_xor_b32 v55, s44, v52\n"
"v_lshl_add_u32 v28, v55, 5, v54\n"
"v_add_u32 v32, 0x2000, v28\n"
"s_add_u32 s44, s45, 3\n"
"v_xor_b32 v55, s44, v52\n"
"v_lshl_add_u32 v29, v55, 5, v54\n"
"v_add_u32 v33, 0x2000, v29\n"
"s_getpc_b64 s[48:49]\n"
"LM_pc3:\n"
"s_add_u32 s48, s48, LB_tailcode-LM_pc3\n"
"s_addc_u32 s49, s49, 0\n"
"s_and_b32 s49, s49, 0xffff\n"
"s_mov_b32 s50, LM_codeend-LB_tailcode\n"
"s_mov_b32 s51, 0x20000\n"
"s_lshl_b32 s44, s36, 2\n"
"s_add_u32 s44, s44, s34\n"
"s_mul_i32 s44, s44, 256\n"
"v_and_b32 v19, 1, v0\n"
"v_lshlrev_b32 v19, 7, v19\n"
"buffer_load_dword v18, v19, s[48:51], s44 offen\n"
"s_cmp_lg_u32 s34, 0\n"
"s_cbranch_scc1 LM_pact_iss_1\n"
"v_and_b32 v43, 63, v0\n"
"s_sub_u32 s45, 15, s81\n"
"s_min_u32 s46, s81, s45\n"
"s_mul_i32 s44, s46, 28\n"
"s_mul_i32 s45, s80, 7\n"
"s_add_u32 s44, s44, s45\n"
"s_lshl_b32 s44, s44, 7\n"
"s_add_u32 s44, s44, 36864\n"
"v_lshlrev_b32 v44, 7, v43\n"
"v_add_u32 v44, s44, v44\n"
"s_lshl_b32 s45, s46, 7\n"
"s_add_u32 s45, s45, 69632\n"
"v_mov_b32 v45, s45\n"
"v_cmp_gt_u32 vcc, 7, v43\n"
"s_nop 1\n"
"v_cndmask_b32 v44, v45, v44, vcc\n"
"buffer_load_dwordx2 v[46:47], v44, s[64:67], 0 offen sc1 ;@cond\n"
"LM_pact_iss_1:\n"
"s_waitcnt lgkmcnt(8)\n"
"v_mfma_f32_16x16x32_bf16 v[48:51], a[192:195], a[160:163], v[144:147]\n"
"ds_read_b128 a[224:227], v2 offset:0\n"
"v_exp_f32 v74, v74\n"
"v_mfma_f32_16x16x32_bf16 v[48:51], a[196:199], a[164:167], v[48:51]\n"
"ds_read_b128 a[228:231], v2 offset:1024\n"
"v_exp_f32 v75, v75\n"
"v_mfma_f32_16x16x32_bf16 v[48:51], a[200:203], a[168:171], v[48:51]\n"
"v_cvt_pk_bf16_f32 v107, v74, v75\n"
"v_mfma_f32_16x16x32_bf16 v[48:51], a[204:207], a[172:175], v[48:51]\n"
"ds_read_b128 a[232:235], v2 offset:2048\n"
"v_mfma_f32_16x16x32_bf16 v[52:55], a[192:195], a[176:179], v[148:151]\n"
"v_exp_f32 v68, v68\n"
"v_mfma_f32_16x16x32_bf16 v[52:55], a[196:199], a[180:183], v[52:55]\n"
"ds_read_b128 a[236:239], v2 offset:3072\n"
"v_exp_f32 v69, v69\n"
"v_mfma_f32_16x16x32_bf16 v[52:55], a[200:203], a[184:187], v[52:55]\n"
"v_mfma_f32_16x16x32_bf16 v[52:55], a[204:207], a[188:191], v[52:55]\n"
"ds_read_b128 a[240:243], v2 offset:4096\n"
"v_cvt_pk_bf16_f32 v108, v68, v69\n"
"v_mfma_f32_16x16x32_bf16 v[56:59], a[208:211], a[160:163], v[152:155]\n"
"v_exp_f32 v70, v70\n"
"v_mfma_f32_16x16x32_bf16 v[56:59], a[212:215], a[164:167], v[56:59]\n"
"ds_read_b128 a[244:247], v2 offset:5120\n"
"v_exp_f32 v71, v71\n"
"v_mfma_f32_16x16x32_bf16 v[56:59], a[216:219], a[168:171], v[56:59]\n"
"v_mfma_f32_16x16x32_bf16 v[56:59], a[220:223], a[172:175], v[56:59]\n"
"ds_read_b128 a[248:251], v2 offset:6144\n"
"v_cvt_pk_bf16_f32 v109, v70, v71\n"
"v_mfma_f32_16x16x32_bf16 v[60:63], a[208:211], a[176:179], v[156:159]\n"
"ds_read_b128 a[252:255], v2 offset:7168\n"
"v_exp_f32 v76, v76\n"
"v_mfma_f32_16x16x32_bf16 v[60:63], a[212:215], a[180:183], v[60:63]\n"
"v_mfma_f32_16x16x32_bf16 v[60:63], a[216:219], a[184:187], v[60:63]\n"
"v_exp_f32 v77, v77\n"
"v_mfma_f32_16x16x32_bf16 v[60:63], a[220:223], a[188:191], v[60:63]\n"
"s_waitcnt lgkmcnt(8)\n"
"v_cvt_pk_bf16_f32 v110, v76, v77\n"
"v_mfma_f32_16x16x32_bf16 v[136:139], v[224:227], v[104:107], v[136:139]\n"
"ds_read_b128 v[160:163], v2 offset:8192\n"
"v_add_f32 v144, 0, v9\n"
"v_min_f32 v144, v144, v7\n"
"v_mfma_f32_16x16x32_bf16 a[64:67], v[192:195], v[104:107], a[64:67]\n"
"v_exp_f32 v78, v78\n"
"v_add_f32 v145, 0xec800000, v9\n"
"v_mfma_f32_16x16x32_bf16 a[72:75], v[196:199], v[104:107], a[72:75]\n"
"v_min_f32 v145, v145, v7\n"
"v_exp_f32 v79, v79\n"
"v_mfma_f32_16x16x32_bf16 a[80:83], v[200:203], v[104:107], a[80:83]\n"
"v_add_f32 v146, 0xed000000, v9\n"
"v_min_f32 v146, v146, v7\n"
"v_cvt_pk_bf16_f32 v111, v78, v79\n"
"v_mfma_f32_16x16x32_bf16 a[88:91], v[204:207], v[104:107], a[88:91]\n"
"ds_read_b128 v[164:167], v2 offset:9216\n"
"v_add_f32 v147, 0xed400000, v9\n"
"v_min_f32 v147, v147, v7\n"
"v_mfma_f32_16x16x32_bf16 a[96:99], v[208:211], v[104:107], a[96:99]\n"
"ds_read_b128 v[168:171], v2 offset:10240\n"
"v_add_f32 v148, 0, v10\n"
"v_min_f32 v148, v148, v8\n"
"v_mfma_f32_16x16x32_bf16 a[104:107], v[212:215], v[104:107], a[104:107]\n"
"ds_read_b128 v[172:175], v2 offset:11264\n"
"v_add_f32 v149, 0xec800000, v10\n"
"v_min_f32 v149, v149, v8\n"
"v_mfma_f32_16x16x32_bf16 a[112:115], v[216:219], v[104:107], a[112:115]\n"
"v_add_f32 v150, 0xed000000, v10\n"
"v_min_f32 v150, v150, v8\n"
"v_add_f32 v151, 0xed400000, v10\n"
"v_mfma_f32_16x16x32_bf16 a[120:123], v[220:223], v[104:107], a[120:123]\n"
"v_min_f32 v151, v151, v8\n"
"v_exp_f32 v48, v48\n"
"v_mfma_f32_16x16x32_bf16 v[140:143], v[224:227], v[108:111], v[140:143]\n"
"ds_read_b128 v[176:179], v2 offset:12288\n"
"v_add_f32 v152, 0xee800000, v9\n"
"v_min_f32 v152, v152, v7\n"
"v_mfma_f32_16x16x32_bf16 a[68:71], v[192:195], v[108:111], a[68:71]\n"
"ds_read_b128 v[180:183], v2 offset:13312\n"
"v_add_f32 v153, 0xee880000, v9\n"
"v_min_f32 v153, v153, v7\n"
"v_mfma_f32_16x16x32_bf16 a[76:79], v[196:199], v[108:111], a[76:79]\n"
"v_exp_f32 v49, v49\n"
"v_add_f32 v154, 0xee900000, v9\n"
"v_mfma_f32_16x16x32_bf16 a[84:87], v[200:203], v[108:111], a[84:87]\n"
"v_min_f32 v154, v154, v7\n"
"v_cvt_pk_bf16_f32 v96, v48, v49\n"
"v_add_f32 v155, 0xee980000, v9\n"
"v_mfma_f32_16x16x32_bf16 a[92:95], v[204:207], v[108:111], a[92:95]\n"
"v_min_f32 v155, v155, v7\n"
"v_exp_f32 v50, v50\n"
"v_mfma_f32_16x16x32_bf16 a[100:103], v[208:211], v[108:111], a[100:103]\n"
"v_add_f32 v156, 0xee800000, v10\n"
"v_min_f32 v156, v156, v8\n"
"v_exp_f32 v51, v51\n"
"v_mfma_f32_16x16x32_bf16 a[108:111], v[212:215], v[108:111], a[108:111]\n"
"ds_read_b128 v[184:187], v2 offset:14336\n"
"ds_read_b128 v[188:191], v2 offset:15360\n"
"v_add_f32 v157, 0xee880000, v10\n"
"v_min_f32 v157, v157, v8\n"
"v_cvt_pk_bf16_f32 v97, v50, v51\n"
"v_add_f32 v158, 0xee900000, v10\n"
"v_exp_f32 v56, v56\n"
"v_mfma_f32_16x16x32_bf16 a[116:119], v[216:219], v[108:111], a[116:119]\n"
"v_min_f32 v158, v158, v8\n"
"v_add_f32 v159, 0xee980000, v10\n"
"v_exp_f32 v57, v57\n"
"v_mfma_f32_16x16x32_bf16 a[124:127], v[220:223], v[108:111], a[124:127]\n"
"v_min_f32 v159, v159, v8\n"
"v_add_f32 v9, 0xef000000, v9\n"
"v_cvt_pk_bf16_f32 v98, v56, v57\n"
"v_add_f32 v10, 0xef000000, v10\n"
"v_add_u32 v2, 0x4000, v2\n"
"v_and_b32 v2, 0x1ffff, v2\n"
"s_waitcnt lgkmcnt(8)\n"
"v_mfma_f32_16x16x32_bf16 v[64:67], a[224:227], a[160:163], v[144:147]\n"
"s_waitcnt vmcnt(0)\n"
"s_barrier\n"
"v_mfma_f32_16x16x32_bf16 v[64:67], a[228:231], a[164:167], v[64:67]\n"
"ds_read_b128 a[192:195], v2 offset:0\n"
"v_exp_f32 v58, v58\n"
"v_mfma_f32_16x16x32_bf16 v[64:67], a[232:235], a[168:171], v[64:67]\n"
"ds_read_b128 a[196:199], v2 offset:1024\n"
"v_exp_f32 v59, v59\n"
"v_mfma_f32_16x16x32_bf16 v[64:67], a[236:239], a[172:175], v[64:67]\n"
"v_cvt_pk_bf16_f32 v99, v58, v59\n"
"v_mfma_f32_16x16x32_bf16 v[68:71], a[224:227], a[176:179], v[148:151]\n"
"ds_read_b128 a[200:203], v2 offset:2048\n"
"v_exp_f32 v52, v52\n"
"v_mfma_f32_16x16x32_bf16 v[68:71], a[228:231], a[180:183], v[68:71]\n"
"v_exp_f32 v53, v53\n"
"v_mfma_f32_16x16x32_bf16 v[68:71], a[232:235], a[184:187], v[68:71]\n"
"ds_read_b128 a[204:207], v2 offset:3072\n"
"v_mfma_f32_16x16x32_bf16 v[68:71], a[236:239], a[188:191], v[68:71]\n"
"v_cvt_pk_bf16_f32 v100, v52, v53\n"
"v_mfma_f32_16x16x32_bf16 v[72:75], a[240:243], a[160:163], v[152:155]\n"
"ds_read_b128 a[208:211], v2 offset:4096\n"
"v_exp_f32 v54, v54\n"
"v_mfma_f32_16x16x32_bf16 v[72:75], a[244:247], a[164:167], v[72:75]\n"
"ds_read_b128 a[212:215], v2 offset:5120\n"
"v_exp_f32 v55, v55\n"
"v_mfma_f32_16x16x32_bf16 v[72:75], a[248:251], a[168:171], v[72:75]\n"
"v_mfma_f32_16x16x32_bf16 v[72:75], a[252:255], a[172:175], v[72:75]\n"
"ds_read_b128 a[216:219], v2 offset:6144\n"
"v_cvt_pk_bf16_f32 v101, v54, v55\n"
"v_mfma_f32_16x16x32_bf16 v[76:79], a[240:243], a[176:179], v[156:159]\n"
"ds_read_b128 a[220:223], v2 offset:7168\n"
"v_exp_f32 v60, v60\n"
"v_mfma_f32_16x16x32_bf16 v[76:79], a[244:247], a[180:183], v[76:79]\n"
"v_mfma_f32_16x16x32_bf16 v[76:79], a[248:251], a[184:187], v[76:79]\n"
"v_exp_f32 v61, v61\n"
"v_mfma_f32_16x16x32_bf16 v[76:79], a[252:255], a[188:191], v[76:79]\n"
"s_waitcnt lgkmcnt(8)\n"
"v_cvt_pk_bf16_f32 v102, v60, v61\n"
"v_mfma_f32_16x16x32_bf16 v[136:139], v[224:227], v[96:99], v[136:139]\n"
"ds_read_b128 v[192:195], v2 offset:8192\n"
"v_add_f32 v144, 0, v9\n"
"v_min_f32 v144, v144, v7\n"
"v_mfma_f32_16x16x32_bf16 a[64:67], v[160:163], v[96:99], a[64:67]\n"
"v_exp_f32 v62, v62\n"
"v_add_f32 v145, 0xec800000, v9\n"
"v_mfma_f32_16x16x32_bf16 a[72:75], v[164:167], v[96:99], a[72:75]\n"
"v_min_f32 v145, v145, v7\n"
"v_exp_f32 v63, v63\n"
"v_mfma_f32_16x16x32_bf16 a[80:83], v[168:171], v[96:99], a[80:83]\n"
"v_add_f32 v146, 0xed000000, v9\n"
"v_min_f32 v146, v146, v7\n"
"v_cvt_pk_bf16_f32 v103, v62, v63\n"
"v_mfma_f32_16x16x32_bf16 a[88:91], v[172:175], v[96:99], a[88:91]\n"
"ds_read_b128 v[196:199], v2 offset:9216\n"
"v_add_f32 v147, 0xed400000, v9\n"
"v_min_f32 v147, v147, v7\n"
"v_mfma_f32_16x16x32_bf16 a[96:99], v[176:179], v[96:99], a[96:99]\n"
"ds_read_b128 v[200:203], v2 offset:10240\n"
"v_add_f32 v148, 0, v10\n"
"v_min_f32 v148, v148, v8\n"
"v_mfma_f32_16x16x32_bf16 a[104:107], v[180:183], v[96:99], a[104:107]\n"
"ds_read_b128 v[204:207], v2 offset:11264\n"
"v_add_f32 v149, 0xec800000, v10\n"
"v_min_f32 v149, v149, v8\n"
"v_mfma_f32_16x16x32_bf16 a[112:115], v[184:187], v[96:99], a[112:115]\n"
"v_add_f32 v150, 0xed000000, v10\n"
"v_min_f32 v150, v150, v8\n"
"v_add_f32 v151, 0xed400000, v10\n"
"v_mfma_f32_16x16x32_bf16 a[120:123], v[188:191], v[96:99], a[120:123]\n"
"v_min_f32 v151, v151, v8\n"
"v_exp_f32 v64, v64\n"
"v_mfma_f32_16x16x32_bf16 v[140:143], v[224:227], v[100:103], v[140:143]\n"
"ds_read_b128 v[208:211], v2 offset:12288\n"
"v_add_f32 v152, 0xee800000, v9\n"
"v_min_f32 v152, v152, v7\n"
"v_mfma_f32_16x16x32_bf16 a[68:71], v[160:163], v[100:103], a[68:71]\n"
"ds_read_b128 v[212:215], v2 offset:13312\n"
"v_add_f32 v153, 0xee880000, v9\n"
"v_min_f32 v153, v153, v7\n"
"v_mfma_f32_16x16x32_bf16 a[76:79], v[164:167], v[100:103], a[76:79]\n"
"v_exp_f32 v65, v65\n"
"v_add_f32 v154, 0xee900000, v9\n"
"v_mfma_f32_16x16x32_bf16 a[84:87], v[168:171], v[100:103], a[84:87]\n"
"v_min_f32 v154, v154, v7\n"
"v_cvt_pk_bf16_f32 v104, v64, v65\n"
"v_add_f32 v155, 0xee980000, v9\n"
"v_mfma_f32_16x16x32_bf16 a[92:95], v[172:175], v[100:103], a[92:95]\n"
"v_min_f32 v155, v155, v7\n"
"v_exp_f32 v66, v66\n"
"v_mfma_f32_16x16x32_bf16 a[100:103], v[176:179], v[100:103], a[100:103]\n"
"v_add_f32 v156, 0xee800000, v10\n"
"v_min_f32 v156, v156, v8\n"
"v_exp_f32 v67, v67\n"
"v_mfma_f32_16x16x32_bf16 a[108:111], v[180:183], v[100:103], a[108:111]\n"
"ds_read_b128 v[216:219], v2 offset:14336\n"
"ds_read_b128 v[220:223], v2 offset:15360\n"
"v_add_f32 v157, 0xee880000, v10\n"
"v_min_f32 v157, v157, v8\n"
"v_cvt_pk_bf16_f32 v105, v66, v67\n"
"v_add_f32 v158, 0xee900000, v10\n"
"v_exp_f32 v72, v72\n"
"v_mfma_f32_16x16x32_bf16 a[116:119], v[184:187], v[100:103], a[116:119]\n"
"v_min_f32 v158, v158, v8\n"
"v_add_f32 v159, 0xee980000, v10\n"
"v_exp_f32 v73, v73\n"
"v_mfma_f32_16x16x32_bf16 a[124:127], v[188:191], v[100:103], a[124:127]\n"
"v_min_f32 v159, v159, v8\n"
"v_add_f32 v9, 0xef000000, v9\n"
"v_cvt_pk_bf16_f32 v106, v72, v73\n"
"v_add_f32 v10, 0xef000000, v10\n"
"v_add_u32 v2, 0x4000, v2\n"
"v_and_b32 v2, 0x1ffff, v2\n"
"v_exp_f32 v74, v74\n"
"v_exp_f32 v75, v75\n"
"s_nop 0\n"
"v_cvt_pk_bf16_f32 v107, v74, v75\n"
"v_exp_f32 v68, v68\n"
"v_exp_f32 v69, v69\n"
"s_nop 0\n"
"v_cvt_pk_bf16_f32 v108, v68, v69\n"
"v_exp_f32 v70, v70\n"
"v_exp_f32 v71, v71\n"
"s_nop 0\n"
"v_cvt_pk_bf16_f32 v109, v70, v71\n"
"v_exp_f32 v76, v76\n"
"v_exp_f32 v77, v77\n"
"s_nop 0\n"
"v_cvt_pk_bf16_f32 v110, v76, v77\n"
"v_exp_f32 v78, v78\n"
"v_exp_f32 v79, v79\n"
"s_nop 0\n"
"v_cvt_pk_bf16_f32 v111, v78, v79\n"
"s_waitcnt lgkmcnt(0)\n"
"s_nop 0\n"
"v_mfma_f32_16x16x32_bf16 v[136:139], v[224:227], v[104:107], v[136:139]\n"
"v_mfma_f32_16x16x32_bf16 a[64:67], v[192:195], v[104:107], a[64:67]\n"
"v_mfma_f32_16x16x32_bf16 a[72:75], v[196:199], v[104:107], a[72:75]\n"
"v_mfma_f32_16x16x32_bf16 a[80:83], v[200:203], v[104:107], a[80:83]\n"
"v_mfma_f32_16x16x32_bf16 a[88:91], v[204:207], v[104:107], a[88:91]\n"
"v_mfma_f32_16x16x32_bf16 a[96:99], v[208:211], v[104:107], a[96:99]\n"
"v_mfma_f32_16x16x32_bf16 a[104:107], v[212:215], v[104:107], a[104:107]\n"
"v_mfma_f32_16x16x32_bf16 a[112:115], v[216:219], v[104:107], a[112:115]\n"
"v_mfma_f32_16x16x32_bf16 a[120:123], v[220:223], v[104:107], a[120:123]\n"
"v_mfma_f32_16x16x32_bf16 v[140:143], v[224:227], v[108:111], v[140:143]\n"
"v_mfma_f32_16x16x32_bf16 a[68:71], v[192:195], v[108:111], a[68:71]\n"
"v_mfma_f32_16x16x32_bf16 a[76:79], v[196:199], v[108:111], a[76:79]\n"
"v_mfma_f32_16x16x32_bf16 a[84:87], v[200:203], v[108:111], a[84:87]\n"
"v_mfma_f32_16x16x32_bf16 a[92:95], v[204:207], v[108:111], a[92:95]\n"
"v_mfma_f32_16x16x32_bf16 a[100:103], v[208:211], v[108:111], a[100:103]\n"
"v_mfma_f32_16x16x32_bf16 a[108:111], v[212:215], v[108:111], a[108:111]\n"
"v_mfma_f32_16x16x32_bf16 a[116:119], v[216:219], v[108:111], a[116:119]\n"
"v_mfma_f32_16x16x32_bf16 a[124:127], v[220:223], v[108:111], a[124:127]\n"
"LB_end:\n"
"s_nop 7\n"
"s_nop 3\n"
"LB_tailcode:\n"
"s_waitcnt vmcnt(0) lgkmcnt(0)\n"
"s_cmp_lg_u32 s34, 0\n"
"s_cbranch_scc1 LM_pact_done\n"
"s_mov_b32 s85, -40000\n"
"s_branch LM_pact_chk\n"
"LM_pact_spin:\n"
"s_sleep 1\n"
"buffer_load_dwordx2 v[46:47], v44, s[64:67], 0 offen sc1 ;@cond\n"
"LM_pact_chk:\n"
"s_waitcnt vmcnt(0)\n"
"v_cmp_ne_u32 s[46:47], s42, v46\n"
"v_cmp_ne_u32 vcc, s43, v47\n"
"s_nop 1\n"
"s_or_b64 vcc, vcc, s[46:47]\n"
"s_cbranch_scc0 LM_pact_done\n"
"s_add_u32 s85, s85, 1\n"
"s_cbranch_scc0 LM_pact_spin\n"
"LM_pact_done:\n"
"s_barrier\n"
"s_add_u32 s48, s26, 0x3d00000\n"
"s_addc_u32 s49, s27, 0\n"
"s_and_b32 s49, s49, 0xffff\n"
"s_mov_b32 s50, 0x2000\n"
"s_mov_b32 s51, 0x20000\n"
"v_lshlrev_b32 v53, 2, v39\n"
"s_add_u32 s44, s80, 0\n"
"s_and_b32 s44, s44, 3\n"
"s_lshl_b32 s44, s44, 4\n"
"s_lshl_b32 s45, s39, 6\n"
"s_add_u32 s44, s44, s45\n"
"s_lshl_b32 s45, s81, 7\n"
"s_add_u32 s44, s44, s45\n"
"s_lshl_b32 s44, s44, 2\n"
"buffer_load_dword v20, v53, s[48:51], s44 offen sc1\n"
"s_add_u32 s44, s80, 1\n"
"s_and_b32 s44, s44, 3\n"
"s_lshl_b32 s44, s44, 4\n"
"s_lshl_b32 s45, s39, 6\n"
"s_add_u32 s44, s44, s45\n"
"s_lshl_b32 s45, s81, 7\n"
"s_add_u32 s44, s44, s45\n"
"s_lshl_b32 s44, s44, 2\n"
"buffer_load_dword v21, v53, s[48:51], s44 offen sc1\n"
"s_add_u32 s44, s80, 2\n"
"s_and_b32 s44, s44, 3\n"
"s_lshl_b32 s44, s44, 4\n"
"s_lshl_b32 s45, s39, 6\n"
"s_add_u32 s44, s44, s45\n"
"s_lshl_b32 s45, s81, 7\n"
"s_add_u32 s44, s44, s45\n"
"s_lshl_b32 s44, s44, 2\n"
"buffer_load_dword v22, v53, s[48:51], s44 offen sc1\n"
"s_add_u32 s44, s80, 3\n"
"s_and_b32 s44, s44, 3\n"
"s_lshl_b32 s44, s44, 4\n"
"s_lshl_b32 s45, s39, 6\n"
"s_add_u32 s44, s44, s45\n"
"s_lshl_b32 s45, s81, 7\n"
"s_add_u32 s44, s44, s45\n"
"s_lshl_b32 s44, s44, 2\n"
"buffer_load_dword v23, v53, s[48:51], s44 offen sc1\n"
"v_cmp_nlt_f32 vcc, v136, v228\n"
"v_rcp_f32 v13, v136\n"
"s_nop 0\n"
"s_or_b32 s31, s31, vcc_lo\n"
"s_or_b32 s31, s31, vcc_hi\n"
"v_mov_b32 v12, v13\n"
"v_accvgpr_read_b32 v230, a64\n"
"v_accvgpr_read_b32 v231, a65\n"
"v_accvgpr_read_b32 v232, a66\n"
"v_accvgpr_read_b32 v233, a67\n"
"v_accvgpr_read_b32 v234, a72\n"
"v_accvgpr_read_b32 v235, a73\n"
"v_accvgpr_read_b32 v236, a74\n"
"v_accvgpr_read_b32 v237, a75\n"
"s_add_u32 m0, s83, 0x4000\n"
"s_nop 0\n"
"buffer_load_dwordx4 v36, s[56:59], s72 offen lds ;@F0\n"
"s_add_u32 m0, s83, 0x4400\n"
"s_nop 0\n"
"buffer_load_dwordx4 v36, s[56:59], s73 offen lds ;@F0\n"
"v_pk_mul_f32 v[230:231], v[230:231], v[12:13]\n"
"v_pk_mul_f32 v[232:233], v[232:233], v[12:13]\n"
"v_pk_mul_f32 v[234:235], v[234:235], v[12:13]\n"
"v_pk_mul_f32 v[236:237], v[236:237], v[12:13]\n"
"v_cvt_pk_bf16_f32 v192, v230, v231\n"
"v_cvt_pk_bf16_f32 v193, v232, v233\n"
"v_cvt_pk_bf16_f32 v194, v234, v235\n"
"v_cvt_pk_bf16_f32 v195, v236, v237\n"
"v_accvgpr_read_b32 v238, a80\n"
"v_accvgpr_read_b32 v239, a81\n"
"v_accvgpr_read_b32 v240, a82\n"
"v_accvgpr_read_b32 v241, a83\n"
"v_accvgpr_read_b32 v242, a88\n"
"v_accvgpr_read_b32 v243, a89\n"
"v_accvgpr_read_b32 v244, a90\n"
"v_accvgpr_read_b32 v245, a91\n"
"s_add_u32 m0, s83, 0x4800\n"
"s_nop 0\n"
"buffer_load_dwordx4 v41, s[56:59], s74 offen lds ;@F0\n"
"s_add_u32 m0, s83, 0x4c00\n"
"s_nop 0\n"
"buffer_load_dwordx4 v41, s[56:59], s75 offen lds ;@F0\n"
"v_add_u32 v36, 0x10000, v36\n"
"v_add_u32 v41, 0x10000, v41\n"
"v_pk_mul_f32 v[238:239], v[238:239], v[12:13]\n"
"v_pk_mul_f32 v[240:241], v[240:241], v[12:13]\n"
"v_pk_mul_f32 v[242:243], v[242:243], v[12:13]\n"
"v_pk_mul_f32 v[244:245], v[244:245], v[12:13]\n"
"v_cvt_pk_bf16_f32 v196, v238, v239\n"
"v_cvt_pk_bf16_f32 v197, v240, v241\n"
"v_cvt_pk_bf16_f32 v198, v242, v243\n"
"v_cvt_pk_bf16_f32 v199, v244, v245\n"
"v_accvgpr_read_b32 v230, a96\n"
"v_accvgpr_read_b32 v231, a97\n"
"v_accvgpr_read_b32 v232, a98\n"
"v_accvgpr_read_b32 v233, a99\n"
"v_accvgpr_read_b32 v234, a104\n"
"v_accvgpr_read_b32 v235, a105\n"
"v_accvgpr_read_b32 v236, a106\n"
"v_accvgpr_read_b32 v237, a107\n"
"s_add_u32 m0, s82, 0x0\n"
"s_nop 0\n"
"buffer_load_dwordx4 v34, s[60:63], s68 offen sc1 lds ;@F0\n"
"buffer_load_dwordx4 v34, s[60:63], s68 offen offset:1024 sc1 lds ;@F0\n"
"s_add_u32 m0, s82, 0x800\n"
"s_nop 0\n"
"buffer_load_dwordx4 v34, s[60:63], s69 offen sc1 lds ;@F0\n"
"buffer_load_dwordx4 v34, s[60:63], s69 offen offset:1024 sc1 lds ;@F0\n"
"v_add_u32 v34, 0x800, v34\n"
"v_pk_mul_f32 v[230:231], v[230:231], v[12:13]\n"
"v_pk_mul_f32 v[232:233], v[232:233], v[12:13]\n"
"v_pk_mul_f32 v[234:235], v[234:235], v[12:13]\n"
"v_pk_mul_f32 v[236:237], v[236:237], v[12:13]\n"
"v_cvt_pk_bf16_f32 v200, v230, v231\n"
"v_cvt_pk_bf16_f32 v201, v232, v233\n"
"v_cvt_pk_bf16_f32 v202, v234, v235\n"
"v_cvt_pk_bf16_f32 v203, v236, v237\n"
"v_accvgpr_read_b32 v238, a112\n"
"v_accvgpr_read_b32 v239, a113\n"
"v_accvgpr_read_b32 v240, a114\n"
"v_accvgpr_read_b32 v241, a115\n"
"v_accvgpr_read_b32 v242, a120\n"
"v_accvgpr_read_b32 v243, a121\n"
"v_accvgpr_read_b32 v244, a122\n"
"v_accvgpr_read_b32 v245, a123\n"
"s_add_u32 m0, s83, 0xc000\n"
"s_nop 0\n"
"buffer_load_dwordx4 v36, s[56:59], s72 offen lds ;@F1\n"
"s_add_u32 m0, s83, 0xc400\n"
"s_nop 0\n"
"buffer_load_dwordx4 v36, s[56:59], s73 offen lds ;@F1\n"
"v_pk_mul_f32 v[238:239], v[238:239], v[12:13]\n"
"v_pk_mul_f32 v[240:241], v[240:241], v[12:13]\n"
"v_pk_mul_f32 v[242:243], v[242:243], v[12:13]\n"
"v_pk_mul_f32 v[244:245], v[244:245], v[12:13]\n"
"v_cvt_pk_bf16_f32 v204, v238, v239\n"
"v_cvt_pk_bf16_f32 v205, v240, v241\n"
"v_cvt_pk_bf16_f32 v206, v242, v243\n"
"v_cvt_pk_bf16_f32 v207, v244, v245\n"
"v_cmp_nlt_f32 vcc, v140, v228\n"
"v_rcp_f32 v13, v140\n"
"s_nop 0\n"
"s_or_b32 s31, s31, vcc_lo\n"
"s_or_b32 s31, s31, vcc_hi\n"
"v_mov_b32 v12, v13\n"
"v_accvgpr_read_b32 v230, a68\n"
"v_accvgpr_read_b32 v231, a69\n"
"v_accvgpr_read_b32 v232, a70\n"
"v_accvgpr_read_b32 v233, a71\n"
"v_accvgpr_read_b32 v234, a76\n"
"v_accvgpr_read_b32 v235, a77\n"
"v_accvgpr_read_b32 v236, a78\n"
"v_accvgpr_read_b32 v237, a79\n"
"s_add_u32 m0, s83, 0xc800\n"
"s_nop 0\n"
"buffer_load_dwordx4 v41, s[56:59], s74 offen lds ;@F1\n"
"v_pk_mul_f32 v[230:231], v[230:231], v[12:13]\n"
"v_pk_mul_f32 v[232:233], v[232:233], v[12:13]\n"
"v_pk_mul_f32 v[234:235], v[234:235], v[12:13]\n"
"v_pk_mul_f32 v[236:237], v[236:237], v[12:13]\n"
"v_cvt_pk_bf16_f32 v208, v230, v231\n"
"v_cvt_pk_bf16_f32 v209, v232, v233\n"
"v_cvt_pk_bf16_f32 v210, v234, v235\n"
"v_cvt_pk_bf16_f32 v211, v236, v237\n"
"v_accvgpr_read_b32 v238, a84\n"
"v_accvgpr_read_b32 v239, a85\n"
"v_accvgpr_read_b32 v240, a86\n"
"v_accvgpr_read_b32 v241, a87\n"
"v_accvgpr_read_b32 v242, a92\n"
"v_accvgpr_read_b32 v243, a93\n"
"v_accvgpr_read_b32 v244, a94\n"
"v_accvgpr_read_b32 v245, a95\n"
"s_add_u32 m0, s83, 0xcc00\n"
"s_nop 0\n"
"buffer_load_dwordx4 v41, s[56:59], s75 offen lds ;@F1\n"
"v_add_u32 v36, 0x10000, v36\n"
"v_add_u32 v41, 0x10000, v41\n"
"v_pk_mul_f32 v[238:239], v[238:239], v[12:13]\n"
"v_pk_mul_f32 v[240:241], v[240:241], v[12:13]\n"
"v_pk_mul_f32 v[242:243], v[242:243], v[12:13]\n"
"v_pk_mul_f32 v[244:245], v[244:245], v[12:13]\n"
"v_cvt_pk_bf16_f32 v212, v238, v239\n"
"v_cvt_pk_bf16_f32 v213, v240, v241\n"
"v_cvt_pk_bf16_f32 v214, v242, v243\n"
"v_cvt_pk_bf16_f32 v215, v244, v245\n"
"v_accvgpr_read_b32 v230, a100\n"
"v_accvgpr_read_b32 v231, a101\n"
"v_accvgpr_read_b32 v232, a102\n"
"v_accvgpr_read_b32 v233, a103\n"
"v_accvgpr_read_b32 v234, a108\n"
"v_accvgpr_read_b32 v235, a109\n"
"v_accvgpr_read_b32 v236, a110\n"
"v_accvgpr_read_b32 v237, a111\n"
"s_add_u32 m0, s82, 0x8000\n"
"s_nop 0\n"
"buffer_load_dwordx4 v34, s[60:63], s68 offen sc1 lds ;@F1\n"
"buffer_load_dwordx4 v34, s[60:63], s68 offen offset:1024 sc1 lds ;@F1\n"
"v_pk_mul_f32 v[230:231], v[230:231], v[12:13]\n"
"v_pk_mul_f32 v[232:233], v[232:233], v[12:13]\n"
"v_pk_mul_f32 v[234:235], v[234:235], v[12:13]\n"
"v_pk_mul_f32 v[236:237], v[236:237], v[12:13]\n"
"v_cvt_pk_bf16_f32 v216, v230, v231\n"
"v_cvt_pk_bf16_f32 v217, v232, v233\n"
"v_cvt_pk_bf16_f32 v218, v234, v235\n"
"v_cvt_pk_bf16_f32 v219, v236, v237\n"
"v_accvgpr_read_b32 v238, a116\n"
"v_accvgpr_read_b32 v239, a117\n"
"v_accvgpr_read_b32 v240, a118\n"
"v_accvgpr_read_b32 v241, a119\n"
"v_accvgpr_read_b32 v242, a124\n"
"v_accvgpr_read_b32 v243, a125\n"
"v_accvgpr_read_b32 v244, a126\n"
"v_accvgpr_read_b32 v245, a127\n"
"s_add_u32 m0, s82, 0x8800\n"
"s_nop 0\n"
"buffer_load_dwordx4 v34, s[60:63], s69 offen sc1 lds ;@F1\n"
"buffer_load_dwordx4 v34, s[60:63], s69 offen offset:1024 sc1 lds ;@F1\n"
"v_add_u32 v34, 0x800, v34\n"
"v_pk_mul_f32 v[238:239], v[238:239], v[12:13]\n"
"v_pk_mul_f32 v[240:241], v[240:241], v[12:13]\n"
"v_pk_mul_f32 v[242:243], v[242:243], v[12:13]\n"
"v_pk_mul_f32 v[244:245], v[244:245], v[12:13]\n"
"v_cvt_pk_bf16_f32 v220, v238, v239\n"
"v_cvt_pk_bf16_f32 v221, v240, v241\n"
"v_cvt_pk_bf16_f32 v222, v242, v243\n"
"v_cvt_pk_bf16_f32 v223, v244, v245\n"
"s_mul_i32 s44, s87, 376832\n"
"s_lshl_b32 s45, s37, 12\n"
"s_add_u32 s44, s44, s45\n"
"s_add_u32 s45, s44, 188416\n"
"buffer_store_dwordx4 v[192:195], v1, s[60:63], s44 offen offset:0 sc1\n"
"buffer_store_dwordx4 v[196:199], v1, s[60:63], s44 offen offset:1024 sc1\n"
"buffer_store_dwordx4 v[200:203], v1, s[60:63], s44 offen offset:2048 sc1\n"
"buffer_store_dwordx4 v[204:207], v1, s[60:63], s44 offen offset:3072 sc1\n"
"buffer_store_dwordx4 v[208:211], v1, s[60:63], s45 offen offset:0 sc1\n"
"buffer_store_dwordx4 v[212:215], v1, s[60:63], s45 offen offset:1024 sc1\n"
"buffer_store_dwordx4 v[216:219], v1, s[60:63], s45 offen offset:2048 sc1\n"
"buffer_store_dwordx4 v[220:223], v1, s[60:63], s45 offen offset:3072 sc1\n"
  : "={s31}"(flag), "={s[42:43]}"(tag), "+{s[8:9]}"(px), "+{s[12:13]}"(pq), "+{s[18:19]}"(po), "+{s[24:25]}"(p2), "+{s[26:27]}"(pw), "+{s[28:29]}"(pout), "+{s30}"(blk), "+{v0}"(tid)
  : "{s[10:11]}"(ckv), "{s[14:15]}"(wuk), "{s[16:17]}"(wuv), "{s[20:21]}"(w1), "{s[22:23]}"(b1), "{s[4:5]}"(gw), "{s7}"(ldsb)
  : "v1","v2","v3","v4","v5","v6","v7","v8","v9","v10","v11","v12","v13","v14","v15","v16","v17","v18","v19","v20","v21","v22","v23","v24","v25","v26","v27","v28","v29","v30","v31","v32","v33","v34","v35","v36","v37","v38","v39","v40","v41","v42","v43","v44","v45","v46","v47","v48","v49","v50","v51","v52","v53","v54","v55","v56","v57","v58","v59","v60","v61","v62","v63","v64","v65","v66","v67","v68","v69","v70","v71","v72","v73","v74","v75","v76","v77","v78","v79","v80","v81","v82","v83","v84","v85","v86","v87","v88","v89","v90","v91","v92","v93","v94","v95","v96","v97","v98","v99","v100","v101","v102","v103","v104","v105","v106","v107","v108","v109","v110","v111","v112","v113","v114","v115","v116","v117","v118","v119","v120","v121","v122","v123","v124","v125","v126","v127","v128","v129","v130","v131","v132","v133","v134","v135","v136","v137","v138","v139","v140","v141","v142","v143","v144","v145","v146","v147","v148","v149","v150","v151","v152","v153","v154","v155","v156","v157","v158","v159","v160","v161","v162","v163","v164","v165","v166","v167","v168","v169","v170","v171","v172","v173","v174","v175","v176","v177","v178","v179","v180","v181","v182","v183","v184","v185","v186","v187","v188","v189","v190","v191","v192","v193","v194","v195","v196","v197","v198","v199","v200","v201","v202","v203","v204","v205","v206","v207","v208","v209","v210","v211","v212","v213","v214","v215","v216","v217","v218","v219","v220","v221","v222","v223","v224","v225","v226","v227","v228","v229","v230","v231","v232","v233","v234","v235","v236","v237","v238","v239","v240","v241","v242","v243","v244","v245","v246","v247","v248","v249","v250","v251","v252","v253","v254","v255","a0","a1","a2","a3","a4","a5","a6","a7","a8","a9","a10","a11","a12","a13","a14","a15","a16","a17","a18","a19","a20","a21","a22","a23","a24","a25","a26","a27","a28","a29","a30","a31","a32","a33","a34","a35","a36","a37","a38","a39","a40","a41","a42","a43","a44","a45","a46","a47","a48","a49","a50","a51","a52","a53","a54","a55","a56","a57","a58","a59","a60","a61","a62","a63","a64","a65","a66","a67","a68","a69","a70","a71","a72","a73","a74","a75","a76","a77","a78","a79","a80","a81","a82","a83","a84","a85","a86","a87","a88","a89","a90","a91","a92","a93","a94","a95","a96","a97","a98","a99","a100","a101","a102","a103","a104","a105","a106","a107","a108","a109","a110","a111","a112","a113","a114","a115","a116","a117","a118","a119","a120","a121","a122","a123","a124","a125","a126","a127","a128","a129","a130","a131","a132","a133","a134","a135","a136","a137","a138","a139","a140","a141","a142","a143","a144","a145","a146","a147","a148","a149","a150","a151","a152","a153","a154","a155","a156","a157","a158","a159","a160","a161","a162","a163","a164","a165","a166","a167","a168","a169","a170","a171","a172","a173","a174","a175","a176","a177","a178","a179","a180","a181","a182","a183","a184","a185","a186","a187","a188","a189","a190","a191","a192","a193","a194","a195","a196","a197","a198","a199","a200","a201","a202","a203","a204","a205","a206","a207","a208","a209","a210","a211","a212","a213","a214","a215","a216","a217","a218","a219","a220","a221","a222","a223","a224","a225","a226","a227","a228","a229","a230","a231","a232","a233","a234","a235","a236","a237","a238","a239","a240","a241","a242","a243","a244","a245","a246","a247","a248","a249","a250","a251","a252","a253","a254","a255","s34","s35","s36","s37","s38","s39","s40","s41","s44","s45","s46","s47","s48","s49","s50","s51","s52","s53","s54","s55","s56","s57","s58","s59","s60","s61","s62","s63","s64","s65","s66","s67","s68","s69","s70","s71","s72","s73","s74","s75","s76","s77","s78","s79","s80","s81","s82","s83","s84","s85","s86","s87","s88","s89","s90","s91","vcc","memory");
  if(flag){
    asm volatile("s_waitcnt vmcnt(0)" ::: "memory");
    __builtin_amdgcn_fence(__ATOMIC_ACQUIRE,"agent");
    asm volatile("s_waitcnt vmcnt(0)" ::: "memory");
    attn_true_max_rows((const gu16*)px,(const gu16*)pq,(const gu16*)(pw+WS_KV),(gu16*)(pw+WS_AF),(u16*)(lds+65536+(tid>>6)*4096),(blk&7)*4+(blk>>6),(blk>>3)&7,__builtin_amdgcn_readfirstlane(tid>>6),tid&63);
  }
  asm volatile(
"s_cmp_eq_u32 s31, 0\n"
"s_cbranch_scc1 LM_f0\n"
"v_lshrrev_b32 v52, 6, v0\n"
"s_and_b32 s35, s30, 7\n"
"s_lshr_b32 s36, s30, 3\n"
"v_readfirstlane_b32 s34, v52\n"
"s_nop 0\n"
"s_lshr_b32 s39, s34, 1\n"
"s_and_b32 s40, s34, 1\n"
"s_lshl_b32 s44, s35, 2\n"
"s_lshr_b32 s45, s36, 3\n"
"s_add_u32 s37, s44, s45\n"
"s_and_b32 s38, s36, 7\n"
"s_add_u32 s64, s26, 0x3c00000\n"
"s_addc_u32 s65, s27, 0\n"
"s_and_b32 s65, s65, 0xffff\n"
"s_mov_b32 s66, 0xa2000\n"
"s_mov_b32 s67, 0x20000\n"
"v_and_b32 v52, 63, v0\n"
"v_lshlrev_b32 v38, 4, v52\n"
"v_and_b32 v39, 15, v52\n"
"v_lshrrev_b32 v40, 4, v52\n"
"s_and_b32 s80, s30, 3\n"
"s_lshr_b32 s44, s30, 2\n"
"s_and_b32 s44, s44, 1\n"
"s_lshl_b32 s44, s44, 3\n"
"s_lshr_b32 s45, s30, 5\n"
"s_add_u32 s81, s44, s45\n"
"s_lshr_b32 s45, s30, 3\n"
"s_and_b32 s84, s45, 3\n"
"s_lshl_b32 s88, s81, 2\n"
"s_add_u32 s88, s88, s84\n"
"s_mov_b32 s52, s18\n"
"s_and_b32 s53, s19, 0xffff\n"
"s_mov_b32 s54, 0x400000\n"
"s_mov_b32 s55, 0x20000\n"
"s_mov_b32 s56, s24\n"
"s_and_b32 s57, s25, 0xffff\n"
"s_mov_b32 s58, 0x1c0000\n"
"s_mov_b32 s59, 0x20000\n"
"s_add_u32 s60, s26, 0x2000000\n"
"s_addc_u32 s61, s27, 0\n"
"s_and_b32 s61, s61, 0xffff\n"
"s_mov_b32 s62, 0x1700000\n"
"s_mov_b32 s63, 0x20000\n"
"s_lshl_b32 s44, s34, 1\n"
"s_add_u32 s44, s44, 0\n"
"s_and_b32 s45, s44, 4\n"
"s_add_u32 s44, s44, s80\n"
"s_and_b32 s44, s44, 3\n"
"s_add_u32 s44, s44, s45\n"
"s_lshl_b32 s45, s81, 3\n"
"s_add_u32 s44, s44, s45\n"
"s_mul_i32 s44, s44, 188416\n"
"s_mul_i32 s45, s80, 14336\n"
"s_add_u32 s68, s44, s45\n"
"s_add_u32 s68, s68, 131072\n"
"s_lshl_b32 s45, s80, 15\n"
"s_add_u32 s70, s44, s45\n"
"s_lshl_b32 s44, s34, 1\n"
"s_add_u32 s44, s44, 1\n"
"s_and_b32 s45, s44, 4\n"
"s_add_u32 s44, s44, s80\n"
"s_and_b32 s44, s44, 3\n"
"s_add_u32 s44, s44, s45\n"
"s_lshl_b32 s45, s81, 3\n"
"s_add_u32 s44, s44, s45\n"
"s_mul_i32 s44, s44, 188416\n"
"s_mul_i32 s45, s80, 14336\n"
"s_add_u32 s69, s44, s45\n"
"s_add_u32 s69, s69, 131072\n"
"s_lshl_b32 s45, s80, 15\n"
"s_add_u32 s71, s44, s45\n"
"s_lshl_b32 s82, s34, 12\n"
"s_lshl_b32 s83, s34, 12\n"
"s_lshl_b32 s46, s84, 8\n"
"s_lshl_b32 s44, s39, 5\n"
"s_lshl_b32 s45, s40, 3\n"
"s_add_u32 s44, s44, s45\n"
"s_add_u32 s44, s44, 0\n"
"s_lshl_b32 s45, s80, 10\n"
"s_add_u32 s44, s44, s45\n"
"s_lshl_b32 s44, s44, 10\n"
"s_add_u32 s76, s44, s46\n"
"s_lshl_b32 s44, s39, 5\n"
"s_lshl_b32 s45, s40, 3\n"
"s_add_u32 s44, s44, s45\n"
"s_add_u32 s44, s44, 0\n"
"s_mul_i32 s45, s80, 448\n"
"s_add_u32 s44, s44, s45\n"
"s_lshl_b32 s44, s44, 10\n"
"s_add_u32 s72, s44, s46\n"
"s_lshl_b32 s44, s39, 5\n"
"s_lshl_b32 s45, s40, 3\n"
"s_add_u32 s44, s44, s45\n"
"s_add_u32 s44, s44, 16\n"
"s_lshl_b32 s45, s80, 10\n"
"s_add_u32 s44, s44, s45\n"
"s_lshl_b32 s44, s44, 10\n"
"s_add_u32 s77, s44, s46\n"
"s_lshl_b32 s44, s39, 5\n"
"s_lshl_b32 s45, s40, 3\n"
"s_add_u32 s44, s44, s45\n"
"s_add_u32 s44, s44, 16\n"
"s_mul_i32 s45, s80, 448\n"
"s_add_u32 s44, s44, s45\n"
"s_lshl_b32 s44, s44, 10\n"
"s_add_u32 s73, s44, s46\n"
"s_lshl_b32 s44, s39, 5\n"
"s_lshl_b32 s45, s40, 3\n"
"s_add_u32 s44, s44, s45\n"
"s_add_u32 s44, s44, 4\n"
"s_lshl_b32 s45, s80, 10\n"
"s_add_u32 s44, s44, s45\n"
"s_lshl_b32 s44, s44, 10\n"
"s_add_u32 s78, s44, s46\n"
"s_lshl_b32 s44, s39, 5\n"
"s_lshl_b32 s45, s40, 3\n"
"s_add_u32 s44, s44, s45\n"
"s_add_u32 s44, s44, 4\n"
"s_mul_i32 s45, s80, 448\n"
"s_add_u32 s44, s44, s45\n"
"s_lshl_b32 s44, s44, 10\n"
"s_add_u32 s74, s44, s46\n"
"s_lshl_b32 s44, s39, 5\n"
"s_lshl_b32 s45, s40, 3\n"
"s_add_u32 s44, s44, s45\n"
"s_add_u32 s44, s44, 20\n"
"s_lshl_b32 s45, s80, 10\n"
"s_add_u32 s44, s44, s45\n"
"s_lshl_b32 s44, s44, 10\n"
"s_add_u32 s79, s44, s46\n"
"s_lshl_b32 s44, s39, 5\n"
"s_lshl_b32 s45, s40, 3\n"
"s_add_u32 s44, s44, s45\n"
"s_add_u32 s44, s44, 20\n"
"s_mul_i32 s45, s80, 448\n"
"s_add_u32 s44, s44, s45\n"
"s_lshl_b32 s44, s44, 10\n"
"s_add_u32 s75, s44, s46\n"
"v_add_u32 v34, 0x1000, v38\n"
"v_mov_b32 v35, v38\n"
"v_lshrrev_b32 v52, 1, v39\n"
"v_xor_b32 v52, v52, v40\n"
"v_lshlrev_b32 v52, 5, v52\n"
"v_and_b32 v53, 1, v39\n"
"v_lshl_add_u32 v52, v53, 4, v52\n"
"v_lshl_add_u32 v37, v40, 10, v52\n"
"v_xor_b32 v42, 0x80, v37\n"
"v_add_u32 v36, 0x20000, v37\n"
"v_add_u32 v41, 0x20000, v42\n"
"s_lshl_b32 s44, s39, 13\n"
"s_add_u32 s44, s44, 0x0\n"
"v_add_u32 v24, s44, v38\n"
"v_add_u32 v25, 0x400, v24\n"
"v_lshrrev_b32 v52, 2, v39\n"
"v_and_b32 v53, 3, v39\n"
"v_lshlrev_b32 v54, 11, v40\n"
"v_lshl_add_u32 v54, v52, 8, v54\n"
"v_lshl_add_u32 v54, v53, 3, v54\n"
"v_add_u32 v54, 0x4000, v54\n"
"v_and_b32 v53, 1, v40\n"
"v_lshl_or_b32 v52, v53, 2, v52\n"
"s_lshl_b32 s45, s40, 2\n"
"s_add_u32 s44, s45, 0\n"
"v_xor_b32 v55, s44, v52\n"
"v_lshl_add_u32 v26, v55, 5, v54\n"
"v_add_u32 v30, 0x2000, v26\n"
"s_add_u32 s44, s45, 1\n"
"v_xor_b32 v55, s44, v52\n"
"v_lshl_add_u32 v27, v55, 5, v54\n"
"v_add_u32 v31, 0x2000, v27\n"
"s_add_u32 s44, s45, 2\n"
"v_xor_b32 v55, s44, v52\n"
"v_lshl_add_u32 v28, v55, 5, v54\n"
"v_add_u32 v32, 0x2000, v28\n"
"s_add_u32 s44, s45, 3\n"
"v_xor_b32 v55, s44, v52\n"
"v_lshl_add_u32 v29, v55, 5, v54\n"
"v_add_u32 v33, 0x2000, v29\n"
"s_add_u32 s48, s26, 0x3d00000\n"
"s_addc_u32 s49, s27, 0\n"
"s_and_b32 s49, s49, 0xffff\n"
"s_mov_b32 s50, 0x2000\n"
"s_mov_b32 s51, 0x20000\n"
"v_lshlrev_b32 v53, 2, v39\n"
"s_add_u32 s44, s80, 0\n"
"s_and_b32 s44, s44, 3\n"
"s_lshl_b32 s44, s44, 4\n"
"s_lshl_b32 s45, s39, 6\n"
"s_add_u32 s44, s44, s45\n"
"s_lshl_b32 s45, s81, 7\n"
"s_add_u32 s44, s44, s45\n"
"s_lshl_b32 s44, s44, 2\n"
"buffer_load_dword v20, v53, s[48:51], s44 offen sc1 ;@cond\n"
"s_add_u32 s44, s80, 1\n"
"s_and_b32 s44, s44, 3\n"
"s_lshl_b32 s44, s44, 4\n"
"s_lshl_b32 s45, s39, 6\n"
"s_add_u32 s44, s44, s45\n"
"s_lshl_b32 s45, s81, 7\n"
"s_add_u32 s44, s44, s45\n"
"s_lshl_b32 s44, s44, 2\n"
"buffer_load_dword v21, v53, s[48:51], s44 offen sc1 ;@cond\n"
"s_add_u32 s44, s80, 2\n"
"s_and_b32 s44, s44, 3\n"
"s_lshl_b32 s44, s44, 4\n"
"s_lshl_b32 s45, s39, 6\n"
"s_add_u32 s44, s44, s45\n"
"s_lshl_b32 s45, s81, 7\n"
"s_add_u32 s44, s44, s45\n"
"s_lshl_b32 s44, s44, 2\n"
"buffer_load_dword v22, v53, s[48:51], s44 offen sc1 ;@cond\n"
"s_add_u32 s44, s80, 3\n"
"s_and_b32 s44, s44, 3\n"
"s_lshl_b32 s44, s44, 4\n"
"s_lshl_b32 s45, s39, 6\n"
"s_add_u32 s44, s44, s45\n"
"s_lshl_b32 s45, s81, 7\n"
"s_add_u32 s44, s44, s45\n"
"s_lshl_b32 s44, s44, 2\n"
"buffer_load_dword v23, v53, s[48:51], s44 offen sc1 ;@cond\n"
"s_waitcnt vmcnt(0)\n"
"LM_f0:\n"
"s_lshl_b32 s44, s38, 5\n"
"s_add_u32 s44, s44, s37\n"
"s_lshl_b32 s44, s44, 7\n"
"s_add_u32 s41, s44, 73728\n"
"s_sub_u32 s44, 15, s38\n"
"s_lshl_b32 s44, s44, 5\n"
"s_add_u32 s44, s44, s37\n"
"s_lshl_b32 s44, s44, 7\n"
"s_add_u32 s91, s44, 73728\n"
"v_mov_b32 v44, s41\n"
"v_mov_b32 v46, s42\n"
"v_mov_b32 v47, s43\n"
"v_mov_b32 v45, s91\n"
"v_and_b32 v43, 63, v0\n"
"v_cmp_eq_u32 vcc, 1, v43\n"
"s_nop 1\n"
"v_cndmask_b32 v44, v44, v45, vcc\n"
"s_waitcnt vmcnt(16)\n"
"s_barrier\n"
"ds_read_b64_tr_b16 a[160:161], v26 offset:0\n"
"ds_read_b64_tr_b16 a[162:163], v26 offset:1024\n"
"ds_read_b128 a[128:131], v24 offset:0\n"
"ds_read_b128 a[132:135], v24 offset:2048\n"
"ds_read_b128 a[136:139], v24 offset:4096\n"
"ds_read_b128 a[140:143], v24 offset:6144\n"
"ds_read_b64_tr_b16 a[164:165], v27 offset:0\n"
"ds_read_b64_tr_b16 a[166:167], v27 offset:1024\n"
"ds_read_b64_tr_b16 a[168:169], v28 offset:0\n"
"ds_read_b64_tr_b16 a[170:171], v28 offset:1024\n"
"ds_read_b64_tr_b16 a[172:173], v29 offset:0\n"
"ds_read_b64_tr_b16 a[174:175], v29 offset:1024\n"
"s_waitcnt lgkmcnt(0)\n"
"v_mfma_f32_16x16x32_bf16 v[64:67], a[160:163], a[128:131], 0\n"
"ds_read_b64_tr_b16 a[224:225], v30 offset:0\n"
"ds_read_b64_tr_b16 a[226:227], v30 offset:1024\n"
"v_mfma_f32_16x16x32_bf16 v[68:71], a[160:163], a[132:135], 0\n"
"ds_read_b128 a[192:195], v25 offset:0\n"
"ds_read_b128 a[196:199], v25 offset:2048\n"
"s_add_u32 m0, s83, 0x14000\n"
"v_add_u32 v24, 0x8000, v24\n"
"v_mfma_f32_16x16x32_bf16 v[72:75], a[160:163], a[136:139], 0\n"
"ds_read_b128 a[200:203], v25 offset:4096\n"
"ds_read_b128 a[204:207], v25 offset:6144\n"
"buffer_load_dwordx4 v36, s[56:59], s72 offen lds ;@F2\n"
"v_mfma_f32_16x16x32_bf16 v[76:79], a[160:163], a[140:143], 0\n"
"ds_read_b64_tr_b16 a[228:229], v31 offset:0\n"
"ds_read_b64_tr_b16 a[230:231], v31 offset:1024\n"
"s_add_u32 m0, s83, 0x14400\n"
"v_add_u32 v26, 0x8000, v26\n"
"v_mfma_f32_16x16x32_bf16 v[80:83], a[164:167], a[128:131], 0\n"
"ds_read_b64_tr_b16 a[232:233], v32 offset:0\n"
"ds_read_b64_tr_b16 a[234:235], v32 offset:1024\n"
"buffer_load_dwordx4 v36, s[56:59], s73 offen lds ;@F2\n"
"v_mfma_f32_16x16x32_bf16 v[84:87], a[164:167], a[132:135], 0\n"
"ds_read_b64_tr_b16 a[236:237], v33 offset:0\n"
"ds_read_b64_tr_b16 a[238:239], v33 offset:1024\n"
"s_add_u32 m0, s83, 0x14800\n"
"v_add_u32 v27, 0x8000, v27\n"
"v_mfma_f32_16x16x32_bf16 v[88:91], a[164:167], a[136:139], 0\n"
"buffer_load_dwordx4 v41, s[56:59], s74 offen lds ;@F2\n"
"v_mfma_f32_16x16x32_bf16 v[92:95], a[164:167], a[140:143], 0\n"
"s_add_u32 m0, s83, 0x14c00\n"
"v_add_u32 v28, 0x8000, v28\n"
"v_mfma_f32_16x16x32_bf16 v[96:99], a[168:171], a[128:131], 0\n"
"buffer_load_dwordx4 v41, s[56:59], s75 offen lds ;@F2\n"
"v_mfma_f32_16x16x32_bf16 v[100:103], a[168:171], a[132:135], 0\n"
"s_add_u32 m0, s82, 0x10000\n"
"v_add_u32 v29, 0x8000, v29\n"
"v_mfma_f32_16x16x32_bf16 v[104:107], a[168:171], a[136:139], 0\n"
"buffer_load_dwordx4 v34, s[60:63], s68 offen sc1 lds ;@F2\n"
"v_mfma_f32_16x16x32_bf16 v[108:111], a[168:171], a[140:143], 0\n"
"buffer_load_dwordx4 v34, s[60:63], s68 offen offset:1024 sc1 lds ;@F2\n"
"v_mfma_f32_16x16x32_bf16 v[112:115], a[172:175], a[128:131], 0\n"
"s_add_u32 m0, s82, 0x10800\n"
"v_mfma_f32_16x16x32_bf16 v[116:119], a[172:175], a[132:135], 0\n"
"buffer_load_dwordx4 v34, s[60:63], s69 offen sc1 lds ;@F2\n"
"v_mfma_f32_16x16x32_bf16 v[120:123], a[172:175], a[136:139], 0\n"
"buffer_load_dwordx4 v34, s[60:63], s69 offen offset:1024 sc1 lds ;@F2\n"
"v_mfma_f32_16x16x32_bf16 v[124:127], a[172:175], a[140:143], 0\n"
"v_add_u32 v36, 0x10000, v36\n"
"v_add_u32 v41, 0x10000, v41\n"
"v_add_u32 v34, 0x800, v34\n"
"s_waitcnt lgkmcnt(0)\n"
"v_mfma_f32_16x16x32_bf16 v[64:67], a[224:227], a[192:195], v[64:67]\n"
"s_waitcnt vmcnt(16)\n"
"s_barrier\n"
"v_mfma_f32_16x16x32_bf16 v[68:71], a[224:227], a[196:199], v[68:71]\n"
"ds_read_b64_tr_b16 a[160:161], v26 offset:0\n"
"ds_read_b64_tr_b16 a[162:163], v26 offset:1024\n"
"s_add_u32 m0, s83, 0x1c000\n"
"v_add_u32 v25, 0x8000, v25\n"
"v_mfma_f32_16x16x32_bf16 v[72:75], a[224:227], a[200:203], v[72:75]\n"
"ds_read_b128 a[128:131], v24 offset:0\n"
"ds_read_b128 a[132:135], v24 offset:2048\n"
"buffer_load_dwordx4 v36, s[56:59], s72 offen lds ;@F3\n"
"v_mfma_f32_16x16x32_bf16 v[76:79], a[224:227], a[204:207], v[76:79]\n"
"ds_read_b128 a[136:139], v24 offset:4096\n"
"ds_read_b128 a[140:143], v24 offset:6144\n"
"s_add_u32 m0, s83, 0x1c400\n"
"v_add_u32 v30, 0x8000, v30\n"
"v_mfma_f32_16x16x32_bf16 v[80:83], a[228:231], a[192:195], v[80:83]\n"
"ds_read_b64_tr_b16 a[164:165], v27 offset:0\n"
"ds_read_b64_tr_b16 a[166:167], v27 offset:1024\n"
"buffer_load_dwordx4 v36, s[56:59], s73 offen lds ;@F3\n"
"v_mfma_f32_16x16x32_bf16 v[84:87], a[228:231], a[196:199], v[84:87]\n"
"ds_read_b64_tr_b16 a[168:169], v28 offset:0\n"
"ds_read_b64_tr_b16 a[170:171], v28 offset:1024\n"
"s_add_u32 m0, s83, 0x1c800\n"
"v_add_u32 v31, 0x8000, v31\n"
"v_mfma_f32_16x16x32_bf16 v[88:91], a[228:231], a[200:203], v[88:91]\n"
"ds_read_b64_tr_b16 a[172:173], v29 offset:0\n"
"ds_read_b64_tr_b16 a[174:175], v29 offset:1024\n"
"buffer_load_dwordx4 v41, s[56:59], s74 offen lds ;@F3\n"
"v_mfma_f32_16x16x32_bf16 v[92:95], a[228:231], a[204:207], v[92:95]\n"
"s_add_u32 m0, s83, 0x1cc00\n"
"v_add_u32 v32, 0x8000, v32\n"
"v_mfma_f32_16x16x32_bf16 v[96:99], a[232:235], a[192:195], v[96:99]\n"
"buffer_load_dwordx4 v41, s[56:59], s75 offen lds ;@F3\n"
"v_mfma_f32_16x16x32_bf16 v[100:103], a[232:235], a[196:199], v[100:103]\n"
"s_add_u32 m0, s82, 0x18000\n"
"v_add_u32 v33, 0x8000, v33\n"
"v_mfma_f32_16x16x32_bf16 v[104:107], a[232:235], a[200:203], v[104:107]\n"
"buffer_load_dwordx4 v34, s[60:63], s68 offen sc1 lds ;@F3\n"
"v_mfma_f32_16x16x32_bf16 v[108:111], a[232:235], a[204:207], v[108:111]\n"
"buffer_load_dwordx4 v34, s[60:63], s68 offen offset:1024 sc1 lds ;@F3\n"
"v_mfma_f32_16x16x32_bf16 v[112:115], a[236:239], a[192:195], v[112:115]\n"
"s_add_u32 m0, s82, 0x18800\n"
"v_mfma_f32_16x16x32_bf16 v[116:119], a[236:239], a[196:199], v[116:119]\n"
"buffer_load_dwordx4 v34, s[60:63], s69 offen sc1 lds ;@F3\n"
"v_mfma_f32_16x16x32_bf16 v[120:123], a[236:239], a[200:203], v[120:123]\n"
"buffer_load_dwordx4 v34, s[60:63], s69 offen offset:1024 sc1 lds ;@F3\n"
"v_mfma_f32_16x16x32_bf16 v[124:127], a[236:239], a[204:207], v[124:127]\n"
"v_add_u32 v36, 0x10000, v36\n"
"v_add_u32 v41, 0x10000, v41\n"
"v_add_u32 v34, 0x800, v34\n"
"s_waitcnt lgkmcnt(0)\n"
"v_mfma_f32_16x16x32_bf16 v[64:67], a[160:163], a[128:131], v[64:67]\n"
"ds_read_b64_tr_b16 a[224:225], v30 offset:0\n"
"ds_read_b64_tr_b16 a[226:227], v30 offset:1024\n"
"v_mfma_f32_16x16x32_bf16 v[68:71], a[160:163], a[132:135], v[68:71]\n"
"ds_read_b128 a[192:195], v25 offset:0\n"
"ds_read_b128 a[196:199], v25 offset:2048\n"
"s_add_u32 m0, s83, 0x24000\n"
"v_add_u32 v24, 0x8000, v24\n"
"v_mfma_f32_16x16x32_bf16 v[72:75], a[160:163], a[136:139], v[72:75]\n"
"ds_read_b128 a[200:203], v25 offset:4096\n"
"ds_read_b128 a[204:207], v25 offset:6144\n"
"buffer_load_dwordx4 v36, s[56:59], s72 offen lds ;@F4\n"
"v_mfma_f32_16x16x32_bf16 v[76:79], a[160:163], a[140:143], v[76:79]\n"
"ds_read_b64_tr_b16 a[228:229], v31 offset:0\n"
"ds_read_b64_tr_b16 a[230:231], v31 offset:1024\n"
"s_add_u32 m0, s83, 0x24400\n"
"v_add_u32 v26, 0x8000, v26\n"
"v_mfma_f32_16x16x32_bf16 v[80:83], a[164:167], a[128:131], v[80:83]\n"
"ds_read_b64_tr_b16 a[232:233], v32 offset:0\n"
"ds_read_b64_tr_b16 a[234:235], v32 offset:1024\n"
"buffer_load_dwordx4 v36, s[56:59], s73 offen lds ;@F4\n"
"v_mfma_f32_16x16x32_bf16 v[84:87], a[164:167], a[132:135], v[84:87]\n"
"ds_read_b64_tr_b16 a[236:237], v33 offset:0\n"
"ds_read_b64_tr_b16 a[238:239], v33 offset:1024\n"
"s_add_u32 m0, s83, 0x24800\n"
"v_add_u32 v27, 0x8000, v27\n"
"v_mfma_f32_16x16x32_bf16 v[88:91], a[164:167], a[136:139], v[88:91]\n"
"buffer_load_dwordx4 v41, s[56:59], s74 offen lds ;@F4\n"
"v_mfma_f32_16x16x32_bf16 v[92:95], a[164:167], a[140:143], v[92:95]\n"
"s_add_u32 m0, s83, 0x24c00\n"
"v_add_u32 v28, 0x8000, v28\n"
"v_mfma_f32_16x16x32_bf16 v[96:99], a[168:171], a[128:131], v[96:99]\n"
"buffer_load_dwordx4 v41, s[56:59], s75 offen lds ;@F4\n"
"v_mfma_f32_16x16x32_bf16 v[100:103], a[168:171], a[132:135], v[100:103]\n"
"s_add_u32 m0, s82, 0x20000\n"
"v_add_u32 v29, 0x8000, v29\n"
"v_mfma_f32_16x16x32_bf16 v[104:107], a[168:171], a[136:139], v[104:107]\n"
"buffer_load_dwordx4 v34, s[60:63], s68 offen sc1 lds ;@F4\n"
"v_mfma_f32_16x16x32_bf16 v[108:111], a[168:171], a[140:143], v[108:111]\n"
"buffer_load_dwordx4 v34, s[60:63], s68 offen offset:1024 sc1 lds ;@F4\n"
"v_mfma_f32_16x16x32_bf16 v[112:115], a[172:175], a[128:131], v[112:115]\n"
"s_add_u32 m0, s82, 0x20800\n"
"v_mfma_f32_16x16x32_bf16 v[116:119], a[172:175], a[132:135], v[116:119]\n"
"buffer_load_dwordx4 v34, s[60:63], s69 offen sc1 lds ;@F4\n"
"v_mfma_f32_16x16x32_bf16 v[120:123], a[172:175], a[136:139], v[120:123]\n"
"buffer_load_dwordx4 v34, s[60:63], s69 offen offset:1024 sc1 lds ;@F4\n"
"v_mfma_f32_16x16x32_bf16 v[124:127], a[172:175], a[140:143], v[124:127]\n"
"v_add_u32 v36, 0x10000, v36\n"
"v_add_u32 v41, 0x10000, v41\n"
"v_add_u32 v34, 0x800, v34\n"
"s_waitcnt lgkmcnt(0)\n"
"v_mfma_f32_16x16x32_bf16 v[64:67], a[224:227], a[192:195], v[64:67]\n"
"s_waitcnt vmcnt(16)\n"
"s_barrier\n"
"s_cmp_lg_u32 s34, 0\n"
"s_cbranch_scc1 LP3\n"
"s_mov_b64 exec, 3\n"
"buffer_store_dwordx2 v[46:47], v44, s[64:67], 0 offen sc1 ;@cond\n"
"s_mov_b64 exec, -1\n"
"LP3:\n"
"v_mfma_f32_16x16x32_bf16 v[68:71], a[224:227], a[196:199], v[68:71]\n"
"ds_read_b64_tr_b16 a[160:161], v26 offset:0\n"
"ds_read_b64_tr_b16 a[162:163], v26 offset:1024\n"
"s_add_u32 m0, s83, 0x4000\n"
"v_add_u32 v25, 0x8000, v25\n"
"v_mfma_f32_16x16x32_bf16 v[72:75], a[224:227], a[200:203], v[72:75]\n"
"ds_read_b128 a[128:131], v24 offset:0\n"
"ds_read_b128 a[132:135], v24 offset:2048\n"
"buffer_load_dwordx4 v36, s[56:59], s72 offen lds ;@F5\n"
"v_mfma_f32_16x16x32_bf16 v[76:79], a[224:227], a[204:207], v[76:79]\n"
"ds_read_b128 a[136:139], v24 offset:4096\n"
"ds_read_b128 a[140:143], v24 offset:6144\n"
"s_add_u32 m0, s83, 0x4400\n"
"v_add_u32 v30, 0x8000, v30\n"
"v_mfma_f32_16x16x32_bf16 v[80:83], a[228:231], a[192:195], v[80:83]\n"
"ds_read_b64_tr_b16 a[164:165], v27 offset:0\n"
"ds_read_b64_tr_b16 a[166:167], v27 offset:1024\n"
"buffer_load_dwordx4 v36, s[56:59], s73 offen lds ;@F5\n"
"v_mfma_f32_16x16x32_bf16 v[84:87], a[228:231], a[196:199], v[84:87]\n"
"ds_read_b64_tr_b16 a[168:169], v28 offset:0\n"
"ds_read_b64_tr_b16 a[170:171], v28 offset:1024\n"
"s_add_u32 m0, s83, 0x4800\n"
"v_add_u32 v31, 0x8000, v31\n"
"v_mfma_f32_16x16x32_bf16 v[88:91], a[228:231], a[200:203], v[88:91]\n"
"ds_read_b64_tr_b16 a[172:173], v29 offset:0\n"
"ds_read_b64_tr_b16 a[174:175], v29 offset:1024\n"
"buffer_load_dwordx4 v41, s[56:59], s74 offen lds ;@F5\n"
"v_mfma_f32_16x16x32_bf16 v[92:95], a[228:231], a[204:207], v[92:95]\n"
"s_add_u32 m0, s83, 0x4c00\n"
"v_add_u32 v32, 0x8000, v32\n"
"v_mfma_f32_16x16x32_bf16 v[96:99], a[232:235], a[192:195], v[96:99]\n"
"buffer_load_dwordx4 v41, s[56:59], s75 offen lds ;@F5\n"
"v_mfma_f32_16x16x32_bf16 v[100:103], a[232:235], a[196:199], v[100:103]\n"
"s_add_u32 m0, s82, 0x0\n"
"v_add_u32 v33, 0x8000, v33\n"
"v_mfma_f32_16x16x32_bf16 v[104:107], a[232:235], a[200:203], v[104:107]\n"
"buffer_load_dwordx4 v34, s[60:63], s68 offen sc1 lds ;@F5\n"
"v_mfma_f32_16x16x32_bf16 v[108:111], a[232:235], a[204:207], v[108:111]\n"
"buffer_load_dwordx4 v34, s[60:63], s68 offen offset:1024 sc1 lds ;@F5\n"
"v_mfma_f32_16x16x32_bf16 v[112:115], a[236:239], a[192:195], v[112:115]\n"
"s_add_u32 m0, s82, 0x800\n"
"v_mfma_f32_16x16x32_bf16 v[116:119], a[236:239], a[196:199], v[116:119]\n"
"buffer_load_dwordx4 v34, s[60:63], s69 offen sc1 lds ;@F5\n"
"v_mfma_f32_16x16x32_bf16 v[120:123], a[236:239], a[200:203], v[120:123]\n"
"buffer_load_dwordx4 v34, s[60:63], s69 offen offset:1024 sc1 lds ;@F5\n"
"v_mfma_f32_16x16x32_bf16 v[124:127], a[236:239], a[204:207], v[124:127]\n"
"v_add_u32 v36, 0x10000, v36\n"
"v_add_u32 v41, 0x10000, v41\n"
"v_add_u32 v34, 0x800, v34\n"
"s_waitcnt lgkmcnt(0)\n"
"v_mfma_f32_16x16x32_bf16 v[64:67], a[160:163], a[128:131], v[64:67]\n"
"ds_read_b64_tr_b16 a[224:225], v30 offset:0\n"
"ds_read_b64_tr_b16 a[226:227], v30 offset:1024\n"
"v_mfma_f32_16x16x32_bf16 v[68:71], a[160:163], a[132:135], v[68:71]\n"
"ds_read_b128 a[192:195], v25 offset:0\n"
"ds_read_b128 a[196:199], v25 offset:2048\n"
"s_add_u32 m0, s83, 0xc000\n"
"v_add_u32 v24, 0x8000, v24\n"
"v_mfma_f32_16x16x32_bf16 v[72:75], a[160:163], a[136:139], v[72:75]\n"
"ds_read_b128 a[200:203], v25 offset:4096\n"
"ds_read_b128 a[204:207], v25 offset:6144\n"
"buffer_load_dwordx4 v36, s[56:59], s72 offen lds ;@F6\n"
"v_mfma_f32_16x16x32_bf16 v[76:79], a[160:163], a[140:143], v[76:79]\n"
"ds_read_b64_tr_b16 a[228:229], v31 offset:0\n"
"ds_read_b64_tr_b16 a[230:231], v31 offset:1024\n"
"s_add_u32 m0, s83, 0xc400\n"
"v_add_u32 v26, 0x8000, v26\n"
"v_mfma_f32_16x16x32_bf16 v[80:83], a[164:167], a[128:131], v[80:83]\n"
"ds_read_b64_tr_b16 a[232:233], v32 offset:0\n"
"ds_read_b64_tr_b16 a[234:235], v32 offset:1024\n"
"buffer_load_dwordx4 v36, s[56:59], s73 offen lds ;@F6\n"
"v_mfma_f32_16x16x32_bf16 v[84:87], a[164:167], a[132:135], v[84:87]\n"
"ds_read_b64_tr_b16 a[236:237], v33 offset:0\n"
"ds_read_b64_tr_b16 a[238:239], v33 offset:1024\n"
"s_add_u32 m0, s83, 0xc800\n"
"v_add_u32 v27, 0x8000, v27\n"
"v_mfma_f32_16x16x32_bf16 v[88:91], a[164:167], a[136:139], v[88:91]\n"
"buffer_load_dwordx4 v41, s[56:59], s74 offen lds ;@F6\n"
"v_mfma_f32_16x16x32_bf16 v[92:95], a[164:167], a[140:143], v[92:95]\n"
"s_add_u32 m0, s83, 0xcc00\n"
"v_add_u32 v28, 0x8000, v28\n"
"v_mfma_f32_16x16x32_bf16 v[96:99], a[168:171], a[128:131], v[96:99]\n"
"buffer_load_dwordx4 v41, s[56:59], s75 offen lds ;@F6\n"
"v_mfma_f32_16x16x32_bf16 v[100:103], a[168:171], a[132:135], v[100:103]\n"
"s_add_u32 m0, s82, 0x8000\n"
"v_add_u32 v29, 0x8000, v29\n"
"v_mfma_f32_16x16x32_bf16 v[104:107], a[168:171], a[136:139], v[104:107]\n"
"buffer_load_dwordx4 v34, s[60:63], s68 offen sc1 lds ;@F6\n"
"v_mfma_f32_16x16x32_bf16 v[108:111], a[168:171], a[140:143], v[108:111]\n"
"buffer_load_dwordx4 v34, s[60:63], s68 offen offset:1024 sc1 lds ;@F6\n"
"v_mfma_f32_16x16x32_bf16 v[112:115], a[172:175], a[128:131], v[112:115]\n"
"s_add_u32 m0, s82, 0x8800\n"
"v_mfma_f32_16x16x32_bf16 v[116:119], a[172:175], a[132:135], v[116:119]\n"
"buffer_load_dwordx4 v34, s[60:63], s69 offen sc1 lds ;@F6\n"
"v_mfma_f32_16x16x32_bf16 v[120:123], a[172:175], a[136:139], v[120:123]\n"
"buffer_load_dwordx4 v34, s[60:63], s69 offen offset:1024 sc1 lds ;@F6\n"
"v_mfma_f32_16x16x32_bf16 v[124:127], a[172:175], a[140:143], v[124:127]\n"
"v_add_u32 v36, 0x10000, v36\n"
"v_add_u32 v41, 0x10000, v41\n"
"v_add_u32 v34, 0x800, v34\n"
"s_waitcnt lgkmcnt(0)\n"
"v_mfma_f32_16x16x32_bf16 v[64:67], a[224:227], a[192:195], v[64:67]\n"
"s_waitcnt vmcnt(24)\n"
"s_barrier\n"
"v_mfma_f32_16x16x32_bf16 v[68:71], a[224:227], a[196:199], v[68:71]\n"
"ds_read_b64_tr_b16 a[160:161], v26 offset:0\n"
"ds_read_b64_tr_b16 a[162:163], v26 offset:1024\n"
"v_mfma_f32_16x16x32_bf16 v[72:75], a[224:227], a[200:203], v[72:75]\n"
"ds_read_b128 a[128:131], v24 offset:0\n"
"ds_read_b128 a[132:135], v24 offset:2048\n"
"v_mfma_f32_16x16x32_bf16 v[76:79], a[224:227], a[204:207], v[76:79]\n"
"ds_read_b128 a[136:139], v24 offset:4096\n"
"ds_read_b128 a[140:143], v24 offset:6144\n"
"v_mfma_f32_16x16x32_bf16 v[80:83], a[228:231], a[192:195], v[80:83]\n"
"ds_read_b64_tr_b16 a[164:165], v27 offset:0\n"
"ds_read_b64_tr_b16 a[166:167], v27 offset:1024\n"
"v_mfma_f32_16x16x32_bf16 v[84:87], a[228:231], a[196:199], v[84:87]\n"
"ds_read_b64_tr_b16 a[168:169], v28 offset:0\n"
"ds_read_b64_tr_b16 a[170:171], v28 offset:1024\n"
"v_mfma_f32_16x16x32_bf16 v[88:91], a[228:231], a[200:203], v[88:91]\n"
"ds_read_b64_tr_b16 a[172:173], v29 offset:0\n"
"ds_read_b64_tr_b16 a[174:175], v29 offset:1024\n"
"v_mfma_f32_16x16x32_bf16 v[92:95], a[228:231], a[204:207], v[92:95]\n"
"s_add_u32 m0, s83, 0x14000\n"
"v_add_u32 v25, 0x8000, v25\n"
"v_mfma_f32_16x16x32_bf16 v[96:99], a[232:235], a[192:195], v[96:99]\n"
"buffer_load_dwordx4 v37, s[52:55], s76 offen lds ;@F7\n"
"v_mfma_f32_16x16x32_bf16 v[100:103], a[232:235], a[196:199], v[100:103]\n"
"s_add_u32 m0, s83, 0x14400\n"
"v_add_u32 v30, 0x8000, v30\n"
"v_mfma_f32_16x16x32_bf16 v[104:107], a[232:235], a[200:203], v[104:107]\n"
"buffer_load_dwordx4 v37, s[52:55], s77 offen lds ;@F7\n"
"v_mfma_f32_16x16x32_bf16 v[108:111], a[232:235], a[204:207], v[108:111]\n"
"s_add_u32 m0, s83, 0x14800\n"
"v_add_u32 v31, 0x8000, v31\n"
"v_mfma_f32_16x16x32_bf16 v[112:115], a[236:239], a[192:195], v[112:115]\n"
"buffer_load_dwordx4 v42, s[52:55], s78 offen lds ;@F7\n"
"v_mfma_f32_16x16x32_bf16 v[116:119], a[236:239], a[196:199], v[116:119]\n"
"s_add_u32 m0, s83, 0x14c00\n"
"v_add_u32 v32, 0x8000, v32\n"
"v_mfma_f32_16x16x32_bf16 v[120:123], a[236:239], a[200:203], v[120:123]\n"
"buffer_load_dwordx4 v42, s[52:55], s79 offen lds ;@F7\n"
"v_mfma_f32_16x16x32_bf16 v[124:127], a[236:239], a[204:207], v[124:127]\n"
"v_add_u32 v33, 0x8000, v33\n"
"v_add_u32 v37, 0x10000, v37\n"
"v_add_u32 v42, 0x10000, v42\n"
"s_waitcnt lgkmcnt(0)\n"
"v_mfma_f32_16x16x32_bf16 v[64:67], a[160:163], a[128:131], v[64:67]\n"
"ds_read_b64_tr_b16 a[224:225], v30 offset:0\n"
"ds_read_b64_tr_b16 a[226:227], v30 offset:1024\n"
"v_mfma_f32_16x16x32_bf16 v[68:71], a[160:163], a[132:135], v[68:71]\n"
"ds_read_b128 a[192:195], v25 offset:0\n"
"ds_read_b128 a[196:199], v25 offset:2048\n"
"v_mfma_f32_16x16x32_bf16 v[72:75], a[160:163], a[136:139], v[72:75]\n"
"ds_read_b128 a[200:203], v25 offset:4096\n"
"ds_read_b128 a[204:207], v25 offset:6144\n"
"v_mfma_f32_16x16x32_bf16 v[76:79], a[160:163], a[140:143], v[76:79]\n"
"ds_read_b64_tr_b16 a[228:229], v31 offset:0\n"
"ds_read_b64_tr_b16 a[230:231], v31 offset:1024\n"
"v_mfma_f32_16x16x32_bf16 v[80:83], a[164:167], a[128:131], v[80:83]\n"
"ds_read_b64_tr_b16 a[232:233], v32 offset:0\n"
"ds_read_b64_tr_b16 a[234:235], v32 offset:1024\n"
"v_mfma_f32_16x16x32_bf16 v[84:87], a[164:167], a[132:135], v[84:87]\n"
"ds_read_b64_tr_b16 a[236:237], v33 offset:0\n"
"ds_read_b64_tr_b16 a[238:239], v33 offset:1024\n"
"v_mfma_f32_16x16x32_bf16 v[88:91], a[164:167], a[136:139], v[88:91]\n"
"v_mfma_f32_16x16x32_bf16 v[92:95], a[164:167], a[140:143], v[92:95]\n"
"v_mfma_f32_16x16x32_bf16 v[96:99], a[168:171], a[128:131], v[96:99]\n"
"v_mfma_f32_16x16x32_bf16 v[100:103], a[168:171], a[132:135], v[100:103]\n"
"v_mfma_f32_16x16x32_bf16 v[104:107], a[168:171], a[136:139], v[104:107]\n"
"v_mfma_f32_16x16x32_bf16 v[108:111], a[168:171], a[140:143], v[108:111]\n"
"v_mfma_f32_16x16x32_bf16 v[112:115], a[172:175], a[128:131], v[112:115]\n"
"v_mfma_f32_16x16x32_bf16 v[116:119], a[172:175], a[132:135], v[116:119]\n"
"v_add_u32 v29, 0x8000, v29\n"
"v_mfma_f32_16x16x32_bf16 v[120:123], a[172:175], a[136:139], v[120:123]\n"
"v_add_u32 v27, 0x8000, v27\n"
"v_add_u32 v28, 0x8000, v28\n"
"v_mfma_f32_16x16x32_bf16 v[124:127], a[172:175], a[140:143], v[124:127]\n"
"v_add_u32 v24, 0x8000, v24\n"
"v_add_u32 v26, 0x8000, v26\n"
"s_waitcnt lgkmcnt(0)\n"
"v_mfma_f32_16x16x32_bf16 v[64:67], a[224:227], a[192:195], v[64:67]\n"
"s_waitcnt vmcnt(20)\n"
"s_barrier\n"
"v_mfma_f32_16x16x32_bf16 v[68:71], a[224:227], a[196:199], v[68:71]\n"
"ds_read_b64_tr_b16 a[160:161], v26 offset:0\n"
"ds_read_b64_tr_b16 a[162:163], v26 offset:1024\n"
"v_mfma_f32_16x16x32_bf16 v[72:75], a[224:227], a[200:203], v[72:75]\n"
"ds_read_b128 a[128:131], v24 offset:0\n"
"ds_read_b128 a[132:135], v24 offset:2048\n"
"v_mfma_f32_16x16x32_bf16 v[76:79], a[224:227], a[204:207], v[76:79]\n"
"ds_read_b128 a[136:139], v24 offset:4096\n"
"ds_read_b128 a[140:143], v24 offset:6144\n"
"v_mfma_f32_16x16x32_bf16 v[80:83], a[228:231], a[192:195], v[80:83]\n"
"ds_read_b64_tr_b16 a[164:165], v27 offset:0\n"
"ds_read_b64_tr_b16 a[166:167], v27 offset:1024\n"
"v_mfma_f32_16x16x32_bf16 v[84:87], a[228:231], a[196:199], v[84:87]\n"
"ds_read_b64_tr_b16 a[168:169], v28 offset:0\n"
"ds_read_b64_tr_b16 a[170:171], v28 offset:1024\n"
"v_mfma_f32_16x16x32_bf16 v[88:91], a[228:231], a[200:203], v[88:91]\n"
"ds_read_b64_tr_b16 a[172:173], v29 offset:0\n"
"ds_read_b64_tr_b16 a[174:175], v29 offset:1024\n"
"v_mfma_f32_16x16x32_bf16 v[92:95], a[228:231], a[204:207], v[92:95]\n"
"s_add_u32 m0, s83, 0x1c000\n"
"v_add_u32 v25, 0x8000, v25\n"
"v_mfma_f32_16x16x32_bf16 v[96:99], a[232:235], a[192:195], v[96:99]\n"
"buffer_load_dwordx4 v37, s[52:55], s76 offen lds ;@F8\n"
"v_mfma_f32_16x16x32_bf16 v[100:103], a[232:235], a[196:199], v[100:103]\n"
"s_add_u32 m0, s83, 0x1c400\n"
"v_add_u32 v30, 0x8000, v30\n"
"v_mfma_f32_16x16x32_bf16 v[104:107], a[232:235], a[200:203], v[104:107]\n"
"buffer_load_dwordx4 v37, s[52:55], s77 offen lds ;@F8\n"
"v_mfma_f32_16x16x32_bf16 v[108:111], a[232:235], a[204:207], v[108:111]\n"
"s_add_u32 m0, s83, 0x1c800\n"
"v_add_u32 v31, 0x8000, v31\n"
"v_mfma_f32_16x16x32_bf16 v[112:115], a[236:239], a[192:195], v[112:115]\n"
"buffer_load_dwordx4 v42, s[52:55], s78 offen lds ;@F8\n"
"v_mfma_f32_16x16x32_bf16 v[116:119], a[236:239], a[196:199], v[116:119]\n"
"s_add_u32 m0, s83, 0x1cc00\n"
"v_add_u32 v32, 0x8000, v32\n"
"v_mfma_f32_16x16x32_bf16 v[120:123], a[236:239], a[200:203], v[120:123]\n"
"buffer_load_dwordx4 v42, s[52:55], s79 offen lds ;@F8\n"
"v_mfma_f32_16x16x32_bf16 v[124:127], a[236:239], a[204:207], v[124:127]\n"
"v_add_u32 v33, 0x8000, v33\n"
"v_add_u32 v37, 0x10000, v37\n"
"v_add_u32 v42, 0x10000, v42\n"
"s_waitcnt lgkmcnt(0)\n"
"s_cmp_lg_u32 s34, 0\n"
"s_cbranch_scc1 LM_pctx_iss\n"
"v_and_b32 v44, 7, v0\n"
"v_lshlrev_b32 v44, 7, v44\n"
"s_lshl_b32 s44, s81, 5\n"
"s_lshl_b32 s45, s80, 3\n"
"s_add_u32 s44, s44, s45\n"
"s_lshl_b32 s44, s44, 7\n"
"s_add_u32 s44, s44, 73728\n"
"v_add_u32 v44, s44, v44\n"
"buffer_load_dwordx2 v[46:47], v44, s[64:67], 0 offen sc1 ;@cond\n"
"LM_pctx_iss:\n"
"s_cmp_lg_u32 s34, 0\n"
"s_cbranch_scc1 LM_pctx_done\n"
"s_mov_b32 s85, -40000\n"
"s_branch LM_pctx_chk\n"
"LM_pctx_spin:\n"
"s_sleep 1\n"
"buffer_load_dwordx2 v[46:47], v44, s[64:67], 0 offen sc1 ;@cond\n"
"LM_pctx_chk:\n"
"s_waitcnt vmcnt(0)\n"
"v_cmp_ne_u32 s[46:47], s42, v46\n"
"v_cmp_ne_u32 vcc, s43, v47\n"
"s_nop 1\n"
"s_or_b64 vcc, vcc, s[46:47]\n"
"s_cbranch_scc0 LM_pctx_done\n"
"s_add_u32 s85, s85, 1\n"
"s_cbranch_scc0 LM_pctx_spin\n"
"LM_pctx_done:\n"
"s_barrier\n"
"s_waitcnt lgkmcnt(0)\n"
"v_mfma_f32_16x16x32_bf16 v[64:67], a[160:163], a[128:131], v[64:67]\n"
"ds_read_b64_tr_b16 a[224:225], v30 offset:0\n"
"ds_read_b64_tr_b16 a[226:227], v30 offset:1024\n"
"v_mfma_f32_16x16x32_bf16 v[68:71], a[160:163], a[132:135], v[68:71]\n"
"ds_read_b128 a[192:195], v25 offset:0\n"
"ds_read_b128 a[196:199], v25 offset:2048\n"
"v_mfma_f32_16x16x32_bf16 v[72:75], a[160:163], a[136:139], v[72:75]\n"
"ds_read_b128 a[200:203], v25 offset:4096\n"
"ds_read_b128 a[204:207], v25 offset:6144\n"
"v_mfma_f32_16x16x32_bf16 v[76:79], a[160:163], a[140:143], v[76:79]\n"
"ds_read_b64_tr_b16 a[228:229], v31 offset:0\n"
"ds_read_b64_tr_b16 a[230:231], v31 offset:1024\n"
"v_mfma_f32_16x16x32_bf16 v[80:83], a[164:167], a[128:131], v[80:83]\n"
"ds_read_b64_tr_b16 a[232:233], v32 offset:0\n"
"ds_read_b64_tr_b16 a[234:235], v32 offset:1024\n"
"v_mfma_f32_16x16x32_bf16 v[84:87], a[164:167], a[132:135], v[84:87]\n"
"ds_read_b64_tr_b16 a[236:237], v33 offset:0\n"
"ds_read_b64_tr_b16 a[238:239], v33 offset:1024\n"
"v_mfma_f32_16x16x32_bf16 v[88:91], a[164:167], a[136:139], v[88:91]\n"
"s_add_u32 m0, s82, 0x10000\n"
"v_add_u32 v24, 0xfffe0000, v24\n"
"v_mfma_f32_16x16x32_bf16 v[92:95], a[164:167], a[140:143], v[92:95]\n"
"buffer_load_dwordx4 v35, s[60:63], s70 offen sc1 lds ;@F7\n"
"v_mfma_f32_16x16x32_bf16 v[96:99], a[168:171], a[128:131], v[96:99]\n"
"buffer_load_dwordx4 v35, s[60:63], s70 offen offset:1024 sc1 lds ;@F7\n"
"v_mfma_f32_16x16x32_bf16 v[100:103], a[168:171], a[132:135], v[100:103]\n"
"s_add_u32 m0, s82, 0x10800\n"
"v_add_u32 v26, 0xfffe0000, v26\n"
"v_mfma_f32_16x16x32_bf16 v[104:107], a[168:171], a[136:139], v[104:107]\n"
"buffer_load_dwordx4 v35, s[60:63], s71 offen sc1 lds ;@F7\n"
"v_mfma_f32_16x16x32_bf16 v[108:111], a[168:171], a[140:143], v[108:111]\n"
"buffer_load_dwordx4 v35, s[60:63], s71 offen offset:1024 sc1 lds ;@F7\n"
"v_mfma_f32_16x16x32_bf16 v[112:115], a[172:175], a[128:131], v[112:115]\n"
"v_add_u32 v35, 0x800, v35\n"
"v_mfma_f32_16x16x32_bf16 v[116:119], a[172:175], a[132:135], v[116:119]\n"
"v_mfma_f32_16x16x32_bf16 v[120:123], a[172:175], a[136:139], v[120:123]\n"
"v_add_u32 v29, 0xfffe0000, v29\n"
"v_mfma_f32_16x16x32_bf16 v[124:127], a[172:175], a[140:143], v[124:127]\n"
"v_add_u32 v27, 0xfffe0000, v27\n"
"v_add_u32 v28, 0xfffe0000, v28\n"
"s_waitcnt lgkmcnt(0)\n"
"v_mfma_f32_16x16x32_bf16 v[64:67], a[224:227], a[192:195], v[64:67]\n"
"s_waitcnt vmcnt(20)\n"
"s_barrier\n"
"v_mfma_f32_16x16x32_bf16 v[68:71], a[224:227], a[196:199], v[68:71]\n"
"ds_read_b64_tr_b16 a[160:161], v26 offset:0\n"
"ds_read_b64_tr_b16 a[162:163], v26 offset:1024\n"
"s_add_u32 m0, s83, 0x24000\n"
"v_add_u32 v25, 0xfffe0000, v25\n"
"v_mfma_f32_16x16x32_bf16 v[72:75], a[224:227], a[200:203], v[72:75]\n"
"ds_read_b128 a[128:131], v24 offset:0\n"
"ds_read_b128 a[132:135], v24 offset:2048\n"
"buffer_load_dwordx4 v37, s[52:55], s76 offen lds ;@F9\n"
"v_mfma_f32_16x16x32_bf16 v[76:79], a[224:227], a[204:207], v[76:79]\n"
"ds_read_b128 a[136:139], v24 offset:4096\n"
"ds_read_b128 a[140:143], v24 offset:6144\n"
"s_add_u32 m0, s83, 0x24400\n"
"v_add_u32 v30, 0xfffe0000, v30\n"
"v_mfma_f32_16x16x32_bf16 v[80:83], a[228:231], a[192:195], v[80:83]\n"
"ds_read_b64_tr_b16 a[164:165], v27 offset:0\n"
"ds_read_b64_tr_b16 a[166:167], v27 offset:1024\n"
"buffer_load_dwordx4 v37, s[52:55], s77 offen lds ;@F9\n"
"v_mfma_f32_16x16x32_bf16 v[84:87], a[228:231], a[196:199], v[84:87]\n"
"ds_read_b64_tr_b16 a[168:169], v28 offset:0\n"
"ds_read_b64_tr_b16 a[170:171], v28 offset:1024\n"
"s_add_u32 m0, s83, 0x24800\n"
"v_add_u32 v31, 0xfffe0000, v31\n"
"v_mfma_f32_16x16x32_bf16 v[88:91], a[228:231], a[200:203], v[88:91]\n"
"ds_read_b64_tr_b16 a[172:173], v29 offset:0\n"
"ds_read_b64_tr_b16 a[174:175], v29 offset:1024\n"
"buffer_load_dwordx4 v42, s[52:55], s78 offen lds ;@F9\n"
"v_mfma_f32_16x16x32_bf16 v[92:95], a[228:231], a[204:207], v[92:95]\n"
"s_add_u32 m0, s83, 0x24c00\n"
"v_add_u32 v32, 0xfffe0000, v32\n"
"v_mfma_f32_16x16x32_bf16 v[96:99], a[232:235], a[192:195], v[96:99]\n"
"buffer_load_dwordx4 v42, s[52:55], s79 offen lds ;@F9\n"
"v_mfma_f32_16x16x32_bf16 v[100:103], a[232:235], a[196:199], v[100:103]\n"
"s_add_u32 m0, s82, 0x18000\n"
"v_add_u32 v33, 0xfffe0000, v33\n"
"v_mfma_f32_16x16x32_bf16 v[104:107], a[232:235], a[200:203], v[104:107]\n"
"buffer_load_dwordx4 v35, s[60:63], s70 offen sc1 lds ;@F8\n"
"v_mfma_f32_16x16x32_bf16 v[108:111], a[232:235], a[204:207], v[108:111]\n"
"buffer_load_dwordx4 v35, s[60:63], s70 offen offset:1024 sc1 lds ;@F8\n"
"v_mfma_f32_16x16x32_bf16 v[112:115], a[236:239], a[192:195], v[112:115]\n"
"s_add_u32 m0, s82, 0x18800\n"
"v_mfma_f32_16x16x32_bf16 v[116:119], a[236:239], a[196:199], v[116:119]\n"
"buffer_load_dwordx4 v35, s[60:63], s71 offen sc1 lds ;@F8\n"
"v_mfma_f32_16x16x32_bf16 v[120:123], a[236:239], a[200:203], v[120:123]\n"
"buffer_load_dwordx4 v35, s[60:63], s71 offen offset:1024 sc1 lds ;@F8\n"
"v_mfma_f32_16x16x32_bf16 v[124:127], a[236:239], a[204:207], v[124:127]\n"
"v_add_u32 v37, 0x10000, v37\n"
"v_add_u32 v42, 0x10000, v42\n"
"v_add_u32 v35, 0x800, v35\n"
"s_waitcnt lgkmcnt(0)\n"
"v_mfma_f32_16x16x32_bf16 v[64:67], a[160:163], a[128:131], v[64:67]\n"
"ds_read_b64_tr_b16 a[224:225], v30 offset:0\n"
"ds_read_b64_tr_b16 a[226:227], v30 offset:1024\n"
"v_mfma_f32_16x16x32_bf16 v[68:71], a[160:163], a[132:135], v[68:71]\n"
"ds_read_b128 a[192:195], v25 offset:0\n"
"ds_read_b128 a[196:199], v25 offset:2048\n"
"v_mfma_f32_16x16x32_bf16 v[72:75], a[160:163], a[136:139], v[72:75]\n"
"ds_read_b128 a[200:203], v25 offset:4096\n"
"ds_read_b128 a[204:207], v25 offset:6144\n"
"v_mfma_f32_16x16x32_bf16 v[76:79], a[160:163], a[140:143], v[76:79]\n"
"ds_read_b64_tr_b16 a[228:229], v31 offset:0\n"
"ds_read_b64_tr_b16 a[230:231], v31 offset:1024\n"
"v_mfma_f32_16x16x32_bf16 v[80:83], a[164:167], a[128:131], v[80:83]\n"
"ds_read_b64_tr_b16 a[232:233], v32 offset:0\n"
"ds_read_b64_tr_b16 a[234:235], v32 offset:1024\n"
"v_mfma_f32_16x16x32_bf16 v[84:87], a[164:167], a[132:135], v[84:87]\n"
"ds_read_b64_tr_b16 a[236:237], v33 offset:0\n"
"ds_read_b64_tr_b16 a[238:239], v33 offset:1024\n"
"v_mfma_f32_16x16x32_bf16 v[88:91], a[164:167], a[136:139], v[88:91]\n"
"s_add_u32 m0, s82, 0x20000\n"
"v_add_u32 v24, 0x8000, v24\n"
"v_mfma_f32_16x16x32_bf16 v[92:95], a[164:167], a[140:143], v[92:95]\n"
"buffer_load_dwordx4 v35, s[60:63], s70 offen sc1 lds ;@F9\n"
"v_mfma_f32_16x16x32_bf16 v[96:99], a[168:171], a[128:131], v[96:99]\n"
"buffer_load_dwordx4 v35, s[60:63], s70 offen offset:1024 sc1 lds ;@F9\n"
"v_mfma_f32_16x16x32_bf16 v[100:103], a[168:171], a[132:135], v[100:103]\n"
"s_add_u32 m0, s82, 0x20800\n"
"v_add_u32 v26, 0x8000, v26\n"
"v_mfma_f32_16x16x32_bf16 v[104:107], a[168:171], a[136:139], v[104:107]\n"
"buffer_load_dwordx4 v35, s[60:63], s71 offen sc1 lds ;@F9\n"
"v_mfma_f32_16x16x32_bf16 v[108:111], a[168:171], a[140:143], v[108:111]\n"
"buffer_load_dwordx4 v35, s[60:63], s71 offen offset:1024 sc1 lds ;@F9\n"
"v_mfma_f32_16x16x32_bf16 v[112:115], a[172:175], a[128:131], v[112:115]\n"
"v_add_u32 v35, 0x800, v35\n"
"v_mfma_f32_16x16x32_bf16 v[116:119], a[172:175], a[132:135], v[116:119]\n"
"v_mfma_f32_16x16x32_bf16 v[120:123], a[172:175], a[136:139], v[120:123]\n"
"v_add_u32 v29, 0x8000, v29\n"
"v_mfma_f32_16x16x32_bf16 v[124:127], a[172:175], a[140:143], v[124:127]\n"
"v_add_u32 v27, 0x8000, v27\n"
"v_add_u32 v28, 0x8000, v28\n"
"s_waitcnt lgkmcnt(0)\n"
"v_mfma_f32_16x16x32_bf16 v[64:67], a[224:227], a[192:195], v[64:67]\n"
"s_waitcnt vmcnt(24)\n"
"s_barrier\n"
"v_mfma_f32_16x16x32_bf16 v[68:71], a[224:227], a[196:199], v[68:71]\n"
"ds_read_b64_tr_b16 a[160:161], v26 offset:0\n"
"ds_read_b64_tr_b16 a[162:163], v26 offset:1024\n"
"s_add_u32 m0, s83, 0x4000\n"
"v_add_u32 v25, 0x8000, v25\n"
"v_mfma_f32_16x16x32_bf16 v[72:75], a[224:227], a[200:203], v[72:75]\n"
"ds_read_b128 a[128:131], v24 offset:0\n"
"ds_read_b128 a[132:135], v24 offset:2048\n"
"buffer_load_dwordx4 v37, s[52:55], s76 offen lds ;@F10\n"
"v_mfma_f32_16x16x32_bf16 v[76:79], a[224:227], a[204:207], v[76:79]\n"
"ds_read_b128 a[136:139], v24 offset:4096\n"
"ds_read_b128 a[140:143], v24 offset:6144\n"
"s_add_u32 m0, s83, 0x4400\n"
"v_add_u32 v30, 0x8000, v30\n"
"v_mfma_f32_16x16x32_bf16 v[80:83], a[228:231], a[192:195], v[80:83]\n"
"ds_read_b64_tr_b16 a[164:165], v27 offset:0\n"
"ds_read_b64_tr_b16 a[166:167], v27 offset:1024\n"
"buffer_load_dwordx4 v37, s[52:55], s77 offen lds ;@F10\n"
"v_mfma_f32_16x16x32_bf16 v[84:87], a[228:231], a[196:199], v[84:87]\n"
"ds_read_b64_tr_b16 a[168:169], v28 offset:0\n"
"ds_read_b64_tr_b16 a[170:171], v28 offset:1024\n"
"s_add_u32 m0, s83, 0x4800\n"
"v_add_u32 v31, 0x8000, v31\n"
"v_mfma_f32_16x16x32_bf16 v[88:91], a[228:231], a[200:203], v[88:91]\n"
"ds_read_b64_tr_b16 a[172:173], v29 offset:0\n"
"ds_read_b64_tr_b16 a[174:175], v29 offset:1024\n"
"buffer_load_dwordx4 v42, s[52:55], s78 offen lds ;@F10\n"
"v_mfma_f32_16x16x32_bf16 v[92:95], a[228:231], a[204:207], v[92:95]\n"
"s_add_u32 m0, s83, 0x4c00\n"
"v_add_u32 v32, 0x8000, v32\n"
"v_mfma_f32_16x16x32_bf16 v[96:99], a[232:235], a[192:195], v[96:99]\n"
"buffer_load_dwordx4 v42, s[52:55], s79 offen lds ;@F10\n"
"v_mfma_f32_16x16x32_bf16 v[100:103], a[232:235], a[196:199], v[100:103]\n"
"s_add_u32 m0, s82, 0x0\n"
"v_add_u32 v33, 0x8000, v33\n"
"v_mfma_f32_16x16x32_bf16 v[104:107], a[232:235], a[200:203], v[104:107]\n"
"buffer_load_dwordx4 v35, s[60:63], s70 offen sc1 lds ;@F10\n"
"v_mfma_f32_16x16x32_bf16 v[108:111], a[232:235], a[204:207], v[108:111]\n"
"buffer_load_dwordx4 v35, s[60:63], s70 offen offset:1024 sc1 lds ;@F10\n"
"v_mfma_f32_16x16x32_bf16 v[112:115], a[236:239], a[192:195], v[112:115]\n"
"s_add_u32 m0, s82, 0x800\n"
"v_mfma_f32_16x16x32_bf16 v[116:119], a[236:239], a[196:199], v[116:119]\n"
"buffer_load_dwordx4 v35, s[60:63], s71 offen sc1 lds ;@F10\n"
"v_mfma_f32_16x16x32_bf16 v[120:123], a[236:239], a[200:203], v[120:123]\n"
"buffer_load_dwordx4 v35, s[60:63], s71 offen offset:1024 sc1 lds ;@F10\n"
"v_mfma_f32_16x16x32_bf16 v[124:127], a[236:239], a[204:207], v[124:127]\n"
"v_add_u32 v37, 0x10000, v37\n"
"v_add_u32 v42, 0x10000, v42\n"
"v_add_u32 v35, 0x800, v35\n"
"s_waitcnt lgkmcnt(0)\n"
"v_mfma_f32_16x16x32_bf16 v[64:67], a[160:163], a[128:131], v[64:67]\n"
"ds_read_b64_tr_b16 a[224:225], v30 offset:0\n"
"ds_read_b64_tr_b16 a[226:227], v30 offset:1024\n"
"v_mfma_f32_16x16x32_bf16 v[68:71], a[160:163], a[132:135], v[68:71]\n"
"ds_read_b128 a[192:195], v25 offset:0\n"
"ds_read_b128 a[196:199], v25 offset:2048\n"
"v_mfma_f32_16x16x32_bf16 v[72:75], a[160:163], a[136:139], v[72:75]\n"
"ds_read_b128 a[200:203], v25 offset:4096\n"
"ds_read_b128 a[204:207], v25 offset:6144\n"
"v_mfma_f32_16x16x32_bf16 v[76:79], a[160:163], a[140:143], v[76:79]\n"
"ds_read_b64_tr_b16 a[228:229], v31 offset:0\n"
"ds_read_b64_tr_b16 a[230:231], v31 offset:1024\n"
"v_mfma_f32_16x16x32_bf16 v[80:83], a[164:167], a[128:131], v[80:83]\n"
"ds_read_b64_tr_b16 a[232:233], v32 offset:0\n"
"ds_read_b64_tr_b16 a[234:235], v32 offset:1024\n"
"v_mfma_f32_16x16x32_bf16 v[84:87], a[164:167], a[132:135], v[84:87]\n"
"ds_read_b64_tr_b16 a[236:237], v33 offset:0\n"
"ds_read_b64_tr_b16 a[238:239], v33 offset:1024\n"
"v_mfma_f32_16x16x32_bf16 v[88:91], a[164:167], a[136:139], v[88:91]\n"
"v_mfma_f32_16x16x32_bf16 v[92:95], a[164:167], a[140:143], v[92:95]\n"
"v_mfma_f32_16x16x32_bf16 v[96:99], a[168:171], a[128:131], v[96:99]\n"
"v_mfma_f32_16x16x32_bf16 v[100:103], a[168:171], a[132:135], v[100:103]\n"
"v_mfma_f32_16x16x32_bf16 v[104:107], a[168:171], a[136:139], v[104:107]\n"
"v_mfma_f32_16x16x32_bf16 v[108:111], a[168:171], a[140:143], v[108:111]\n"
"v_mfma_f32_16x16x32_bf16 v[112:115], a[172:175], a[128:131], v[112:115]\n"
"v_mfma_f32_16x16x32_bf16 v[116:119], a[172:175], a[132:135], v[116:119]\n"
"v_add_u32 v29, 0x8000, v29\n"
"v_mfma_f32_16x16x32_bf16 v[120:123], a[172:175], a[136:139], v[120:123]\n"
"v_add_u32 v27, 0x8000, v27\n"
"v_add_u32 v28, 0x8000, v28\n"
"v_mfma_f32_16x16x32_bf16 v[124:127], a[172:175], a[140:143], v[124:127]\n"
"v_add_u32 v24, 0x8000, v24\n"
"v_add_u32 v26, 0x8000, v26\n"
"s_waitcnt lgkmcnt(0)\n"
"v_mfma_f32_16x16x32_bf16 v[64:67], a[224:227], a[192:195], v[64:67]\n"
"s_waitcnt vmcnt(20)\n"
"s_barrier\n"
"v_mfma_f32_16x16x32_bf16 v[68:71], a[224:227], a[196:199], v[68:71]\n"
"ds_read_b64_tr_b16 a[160:161], v26 offset:0\n"
"ds_read_b64_tr_b16 a[162:163], v26 offset:1024\n"
"s_add_u32 m0, s83, 0xc000\n"
"v_add_u32 v25, 0x8000, v25\n"
"v_mfma_f32_16x16x32_bf16 v[72:75], a[224:227], a[200:203], v[72:75]\n"
"ds_read_b128 a[128:131], v24 offset:0\n"
"ds_read_b128 a[132:135], v24 offset:2048\n"
"buffer_load_dwordx4 v37, s[52:55], s76 offen lds ;@F11\n"
"v_mfma_f32_16x16x32_bf16 v[76:79], a[224:227], a[204:207], v[76:79]\n"
"ds_read_b128 a[136:139], v24 offset:4096\n"
"ds_read_b128 a[140:143], v24 offset:6144\n"
"s_add_u32 m0, s83, 0xc400\n"
"v_add_u32 v30, 0x8000, v30\n"
"v_mfma_f32_16x16x32_bf16 v[80:83], a[228:231], a[192:195], v[80:83]\n"
"ds_read_b64_tr_b16 a[164:165], v27 offset:0\n"
"ds_read_b64_tr_b16 a[166:167], v27 offset:1024\n"
"buffer_load_dwordx4 v37, s[52:55], s77 offen lds ;@F11\n"
"v_mfma_f32_16x16x32_bf16 v[84:87], a[228:231], a[196:199], v[84:87]\n"
"ds_read_b64_tr_b16 a[168:169], v28 offset:0\n"
"ds_read_b64_tr_b16 a[170:171], v28 offset:1024\n"
"s_add_u32 m0, s83, 0xc800\n"
"v_add_u32 v31, 0x8000, v31\n"
"v_mfma_f32_16x16x32_bf16 v[88:91], a[228:231], a[200:203], v[88:91]\n"
"ds_read_b64_tr_b16 a[172:173], v29 offset:0\n"
"ds_read_b64_tr_b16 a[174:175], v29 offset:1024\n"
"buffer_load_dwordx4 v42, s[52:55], s78 offen lds ;@F11\n"
"v_mfma_f32_16x16x32_bf16 v[92:95], a[228:231], a[204:207], v[92:95]\n"
"s_add_u32 m0, s83, 0xcc00\n"
"v_add_u32 v32, 0x8000, v32\n"
"v_mfma_f32_16x16x32_bf16 v[96:99], a[232:235], a[192:195], v[96:99]\n"
"buffer_load_dwordx4 v42, s[52:55], s79 offen lds ;@F11\n"
"v_mfma_f32_16x16x32_bf16 v[100:103], a[232:235], a[196:199], v[100:103]\n"
"s_add_u32 m0, s82, 0x8000\n"
"v_add_u32 v33, 0x8000, v33\n"
"v_mfma_f32_16x16x32_bf16 v[104:107], a[232:235], a[200:203], v[104:107]\n"
"buffer_load_dwordx4 v35, s[60:63], s70 offen sc1 lds ;@F11\n"
"v_mfma_f32_16x16x32_bf16 v[108:111], a[232:235], a[204:207], v[108:111]\n"
"buffer_load_dwordx4 v35, s[60:63], s70 offen offset:1024 sc1 lds ;@F11\n"
"v_mfma_f32_16x16x32_bf16 v[112:115], a[236:239], a[192:195], v[112:115]\n"
"s_add_u32 m0, s82, 0x8800\n"
"v_mfma_f32_16x16x32_bf16 v[116:119], a[236:239], a[196:199], v[116:119]\n"
"buffer_load_dwordx4 v35, s[60:63], s71 offen sc1 lds ;@F11\n"
"v_mfma_f32_16x16x32_bf16 v[120:123], a[236:239], a[200:203], v[120:123]\n"
"buffer_load_dwordx4 v35, s[60:63], s71 offen offset:1024 sc1 lds ;@F11\n"
"v_mfma_f32_16x16x32_bf16 v[124:127], a[236:239], a[204:207], v[124:127]\n"
"v_add_u32 v37, 0x10000, v37\n"
"v_add_u32 v42, 0x10000, v42\n"
"v_add_u32 v35, 0x800, v35\n"
"s_waitcnt lgkmcnt(0)\n"
"v_mfma_f32_16x16x32_bf16 v[128:131], a[160:163], a[128:131], 0\n"
"ds_read_b64_tr_b16 a[224:225], v30 offset:0\n"
"ds_read_b64_tr_b16 a[226:227], v30 offset:1024\n"
"v_mfma_f32_16x16x32_bf16 v[132:135], a[160:163], a[132:135], 0\n"
"ds_read_b128 a[192:195], v25 offset:0\n"
"ds_read_b128 a[196:199], v25 offset:2048\n"
"v_mfma_f32_16x16x32_bf16 v[136:139], a[160:163], a[136:139], 0\n"
"ds_read_b128 a[200:203], v25 offset:4096\n"
"ds_read_b128 a[204:207], v25 offset:6144\n"
"v_mfma_f32_16x16x32_bf16 v[140:143], a[160:163], a[140:143], 0\n"
"ds_read_b64_tr_b16 a[228:229], v31 offset:0\n"
"ds_read_b64_tr_b16 a[230:231], v31 offset:1024\n"
"v_mfma_f32_16x16x32_bf16 v[144:147], a[164:167], a[128:131], 0\n"
"ds_read_b64_tr_b16 a[232:233], v32 offset:0\n"
"ds_read_b64_tr_b16 a[234:235], v32 offset:1024\n"
"v_mfma_f32_16x16x32_bf16 v[148:151], a[164:167], a[132:135], 0\n"
"ds_read_b64_tr_b16 a[236:237], v33 offset:0\n"
"ds_read_b64_tr_b16 a[238:239], v33 offset:1024\n"
"v_mfma_f32_16x16x32_bf16 v[152:155], a[164:167], a[136:139], 0\n"
"v_mfma_f32_16x16x32_bf16 v[156:159], a[164:167], a[140:143], 0\n"
"v_mfma_f32_16x16x32_bf16 v[160:163], a[168:171], a[128:131], 0\n"
"v_mfma_f32_16x16x32_bf16 v[164:167], a[168:171], a[132:135], 0\n"
"v_mfma_f32_16x16x32_bf16 v[168:171], a[168:171], a[136:139], 0\n"
"v_mfma_f32_16x16x32_bf16 v[172:175], a[168:171], a[140:143], 0\n"
"v_mfma_f32_16x16x32_bf16 v[176:179], a[172:175], a[128:131], 0\n"
"v_mfma_f32_16x16x32_bf16 v[180:183], a[172:175], a[132:135], 0\n"
"v_add_u32 v29, 0x8000, v29\n"
"v_mfma_f32_16x16x32_bf16 v[184:187], a[172:175], a[136:139], 0\n"
"v_add_u32 v27, 0x8000, v27\n"
"v_add_u32 v28, 0x8000, v28\n"
"v_mfma_f32_16x16x32_bf16 v[188:191], a[172:175], a[140:143], 0\n"
"v_add_u32 v24, 0x8000, v24\n"
"v_add_u32 v26, 0x8000, v26\n"
"s_waitcnt lgkmcnt(0)\n"
"v_mfma_f32_16x16x32_bf16 v[128:131], a[224:227], a[192:195], v[128:131]\n"
"s_waitcnt vmcnt(20)\n"
"s_barrier\n"
"v_mfma_f32_16x16x32_bf16 v[132:135], a[224:227], a[196:199], v[132:135]\n"
"ds_read_b64_tr_b16 a[160:161], v26 offset:0\n"
"ds_read_b64_tr_b16 a[162:163], v26 offset:1024\n"
"v_mfma_f32_16x16x32_bf16 v[136:139], a[224:227], a[200:203], v[136:139]\n"
"ds_read_b128 a[128:131], v24 offset:0\n"
"ds_read_b128 a[132:135], v24 offset:2048\n"
"v_mfma_f32_16x16x32_bf16 v[140:143], a[224:227], a[204:207], v[140:143]\n"
"ds_read_b128 a[136:139], v24 offset:4096\n"
"ds_read_b128 a[140:143], v24 offset:6144\n"
"v_mfma_f32_16x16x32_bf16 v[144:147], a[228:231], a[192:195], v[144:147]\n"
"ds_read_b64_tr_b16 a[164:165], v27 offset:0\n"
"ds_read_b64_tr_b16 a[166:167], v27 offset:1024\n"
"v_mfma_f32_16x16x32_bf16 v[148:151], a[228:231], a[196:199], v[148:151]\n"
"ds_read_b64_tr_b16 a[168:169], v28 offset:0\n"
"ds_read_b64_tr_b16 a[170:171], v28 offset:1024\n"
"v_mfma_f32_16x16x32_bf16 v[152:155], a[228:231], a[200:203], v[152:155]\n"
"ds_read_b64_tr_b16 a[172:173], v29 offset:0\n"
"ds_read_b64_tr_b16 a[174:175], v29 offset:1024\n"
"v_mfma_f32_16x16x32_bf16 v[156:159], a[228:231], a[204:207], v[156:159]\n"
"s_add_u32 m0, s83, 0x14000\n"
"v_add_u32 v25, 0x8000, v25\n"
"v_mfma_f32_16x16x32_bf16 v[160:163], a[232:235], a[192:195], v[160:163]\n"
"buffer_load_dwordx4 v37, s[52:55], s76 offen lds ;@F12\n"
"v_mfma_f32_16x16x32_bf16 v[164:167], a[232:235], a[196:199], v[164:167]\n"
"s_add_u32 m0, s83, 0x14400\n"
"v_add_u32 v30, 0x8000, v30\n"
"v_mfma_f32_16x16x32_bf16 v[168:171], a[232:235], a[200:203], v[168:171]\n"
"buffer_load_dwordx4 v37, s[52:55], s77 offen lds ;@F12\n"
"v_mfma_f32_16x16x32_bf16 v[172:175], a[232:235], a[204:207], v[172:175]\n"
"s_add_u32 m0, s83, 0x14800\n"
"v_add_u32 v31, 0x8000, v31\n"
"v_mfma_f32_16x16x32_bf16 v[176:179], a[236:239], a[192:195], v[176:179]\n"
"buffer_load_dwordx4 v42, s[52:55], s78 offen lds ;@F12\n"
"v_mfma_f32_16x16x32_bf16 v[180:183], a[236:239], a[196:199], v[180:183]\n"
"s_add_u32 m0, s83, 0x14c00\n"
"v_add_u32 v32, 0x8000, v32\n"
"v_mfma_f32_16x16x32_bf16 v[184:187], a[236:239], a[200:203], v[184:187]\n"
"buffer_load_dwordx4 v42, s[52:55], s79 offen lds ;@F12\n"
"v_mfma_f32_16x16x32_bf16 v[188:191], a[236:239], a[204:207], v[188:191]\n"
"v_add_u32 v33, 0x8000, v33\n"
"v_add_u32 v37, 0x10000, v37\n"
"v_add_u32 v42, 0x10000, v42\n"
"s_waitcnt lgkmcnt(0)\n"
"v_mfma_f32_16x16x32_bf16 v[128:131], a[160:163], a[128:131], v[128:131]\n"
"ds_read_b64_tr_b16 a[224:225], v30 offset:0\n"
"ds_read_b64_tr_b16 a[226:227], v30 offset:1024\n"
"v_mfma_f32_16x16x32_bf16 v[132:135], a[160:163], a[132:135], v[132:135]\n"
"ds_read_b128 a[192:195], v25 offset:0\n"
"ds_read_b128 a[196:199], v25 offset:2048\n"
"v_mfma_f32_16x16x32_bf16 v[136:139], a[160:163], a[136:139], v[136:139]\n"
"ds_read_b128 a[200:203], v25 offset:4096\n"
"ds_read_b128 a[204:207], v25 offset:6144\n"
"v_mfma_f32_16x16x32_bf16 v[140:143], a[160:163], a[140:143], v[140:143]\n"
"ds_read_b64_tr_b16 a[228:229], v31 offset:0\n"
"ds_read_b64_tr_b16 a[230:231], v31 offset:1024\n"
"v_mfma_f32_16x16x32_bf16 v[144:147], a[164:167], a[128:131], v[144:147]\n"
"ds_read_b64_tr_b16 a[232:233], v32 offset:0\n"
"ds_read_b64_tr_b16 a[234:235], v32 offset:1024\n"
"v_mfma_f32_16x16x32_bf16 v[148:151], a[164:167], a[132:135], v[148:151]\n"
"ds_read_b64_tr_b16 a[236:237], v33 offset:0\n"
"ds_read_b64_tr_b16 a[238:239], v33 offset:1024\n"
"v_mfma_f32_16x16x32_bf16 v[152:155], a[164:167], a[136:139], v[152:155]\n"
"s_add_u32 m0, s82, 0x10000\n"
"v_add_u32 v24, 0x8000, v24\n"
"v_mfma_f32_16x16x32_bf16 v[156:159], a[164:167], a[140:143], v[156:159]\n"
"buffer_load_dwordx4 v35, s[60:63], s70 offen sc1 lds ;@F12\n"
"v_mfma_f32_16x16x32_bf16 v[160:163], a[168:171], a[128:131], v[160:163]\n"
"buffer_load_dwordx4 v35, s[60:63], s70 offen offset:1024 sc1 lds ;@F12\n"
"v_mfma_f32_16x16x32_bf16 v[164:167], a[168:171], a[132:135], v[164:167]\n"
"s_add_u32 m0, s82, 0x10800\n"
"v_add_u32 v26, 0x8000, v26\n"
"v_mfma_f32_16x16x32_bf16 v[168:171], a[168:171], a[136:139], v[168:171]\n"
"buffer_load_dwordx4 v35, s[60:63], s71 offen sc1 lds ;@F12\n"
"v_mfma_f32_16x16x32_bf16 v[172:175], a[168:171], a[140:143], v[172:175]\n"
"buffer_load_dwordx4 v35, s[60:63], s71 offen offset:1024 sc1 lds ;@F12\n"
"v_mfma_f32_16x16x32_bf16 v[176:179], a[172:175], a[128:131], v[176:179]\n"
"v_add_u32 v35, 0x800, v35\n"
"v_mfma_f32_16x16x32_bf16 v[180:183], a[172:175], a[132:135], v[180:183]\n"
"v_mfma_f32_16x16x32_bf16 v[184:187], a[172:175], a[136:139], v[184:187]\n"
"v_add_u32 v29, 0x8000, v29\n"
"v_mfma_f32_16x16x32_bf16 v[188:191], a[172:175], a[140:143], v[188:191]\n"
"v_add_u32 v27, 0x8000, v27\n"
"v_add_u32 v28, 0x8000, v28\n"
"s_waitcnt lgkmcnt(0)\n"
"v_mfma_f32_16x16x32_bf16 v[128:131], a[224:227], a[192:195], v[128:131]\n"
"s_waitcnt vmcnt(24)\n"
"s_barrier\n"
"v_mfma_f32_16x16x32_bf16 v[132:135], a[224:227], a[196:199], v[132:135]\n"
"ds_read_b64_tr_b16 a[160:161], v26 offset:0\n"
"ds_read_b64_tr_b16 a[162:163], v26 offset:1024\n"
"v_mfma_f32_16x16x32_bf16 v[136:139], a[224:227], a[200:203], v[136:139]\n"
"ds_read_b128 a[128:131], v24 offset:0\n"
"ds_read_b128 a[132:135], v24 offset:2048\n"
"v_mfma_f32_16x16x32_bf16 v[140:143], a[224:227], a[204:207], v[140:143]\n"
"ds_read_b128 a[136:139], v24 offset:4096\n"
"ds_read_b128 a[140:143], v24 offset:6144\n"
"v_mfma_f32_16x16x32_bf16 v[144:147], a[228:231], a[192:195], v[144:147]\n"
"ds_read_b64_tr_b16 a[164:165], v27 offset:0\n"
"ds_read_b64_tr_b16 a[166:167], v27 offset:1024\n"
"v_mfma_f32_16x16x32_bf16 v[148:151], a[228:231], a[196:199], v[148:151]\n"
"ds_read_b64_tr_b16 a[168:169], v28 offset:0\n"
"ds_read_b64_tr_b16 a[170:171], v28 offset:1024\n"
"v_mfma_f32_16x16x32_bf16 v[152:155], a[228:231], a[200:203], v[152:155]\n"
"ds_read_b64_tr_b16 a[172:173], v29 offset:0\n"
"ds_read_b64_tr_b16 a[174:175], v29 offset:1024\n"
"v_mfma_f32_16x16x32_bf16 v[156:159], a[228:231], a[204:207], v[156:159]\n"
"s_add_u32 m0, s83, 0x1c000\n"
"v_add_u32 v25, 0x8000, v25\n"
"v_mfma_f32_16x16x32_bf16 v[160:163], a[232:235], a[192:195], v[160:163]\n"
"buffer_load_dwordx4 v37, s[52:55], s76 offen lds ;@F13\n"
"v_mfma_f32_16x16x32_bf16 v[164:167], a[232:235], a[196:199], v[164:167]\n"
"s_add_u32 m0, s83, 0x1c400\n"
"v_add_u32 v30, 0x8000, v30\n"
"v_mfma_f32_16x16x32_bf16 v[168:171], a[232:235], a[200:203], v[168:171]\n"
"buffer_load_dwordx4 v37, s[52:55], s77 offen lds ;@F13\n"
"v_mfma_f32_16x16x32_bf16 v[172:175], a[232:235], a[204:207], v[172:175]\n"
"s_add_u32 m0, s83, 0x1c800\n"
"v_add_u32 v31, 0x8000, v31\n"
"v_mfma_f32_16x16x32_bf16 v[176:179], a[236:239], a[192:195], v[176:179]\n"
"buffer_load_dwordx4 v42, s[52:55], s78 offen lds ;@F13\n"
"v_mfma_f32_16x16x32_bf16 v[180:183], a[236:239], a[196:199], v[180:183]\n"
"s_add_u32 m0, s83, 0x1cc00\n"
"v_add_u32 v32, 0x8000, v32\n"
"v_mfma_f32_16x16x32_bf16 v[184:187], a[236:239], a[200:203], v[184:187]\n"
"buffer_load_dwordx4 v42, s[52:55], s79 offen lds ;@F13\n"
"v_mfma_f32_16x16x32_bf16 v[188:191], a[236:239], a[204:207], v[188:191]\n"
"v_add_u32 v33, 0x8000, v33\n"
"v_add_u32 v37, 0x10000, v37\n"
"v_add_u32 v42, 0x10000, v42\n"
"s_waitcnt lgkmcnt(0)\n"
"v_mfma_f32_16x16x32_bf16 v[128:131], a[160:163], a[128:131], v[128:131]\n"
"ds_read_b64_tr_b16 a[224:225], v30 offset:0\n"
"ds_read_b64_tr_b16 a[226:227], v30 offset:1024\n"
"v_mfma_f32_16x16x32_bf16 v[132:135], a[160:163], a[132:135], v[132:135]\n"
"ds_read_b128 a[192:195], v25 offset:0\n"
"ds_read_b128 a[196:199], v25 offset:2048\n"
"v_mfma_f32_16x16x32_bf16 v[136:139], a[160:163], a[136:139], v[136:139]\n"
"ds_read_b128 a[200:203], v25 offset:4096\n"
"ds_read_b128 a[204:207], v25 offset:6144\n"
"v_mfma_f32_16x16x32_bf16 v[140:143], a[160:163], a[140:143], v[140:143]\n"
"ds_read_b64_tr_b16 a[228:229], v31 offset:0\n"
"ds_read_b64_tr_b16 a[230:231], v31 offset:1024\n"
"v_mfma_f32_16x16x32_bf16 v[144:147], a[164:167], a[128:131], v[144:147]\n"
"ds_read_b64_tr_b16 a[232:233], v32 offset:0\n"
"ds_read_b64_tr_b16 a[234:235], v32 offset:1024\n"
"v_mfma_f32_16x16x32_bf16 v[148:151], a[164:167], a[132:135], v[148:151]\n"
"ds_read_b64_tr_b16 a[236:237], v33 offset:0\n"
"ds_read_b64_tr_b16 a[238:239], v33 offset:1024\n"
"v_mfma_f32_16x16x32_bf16 v[152:155], a[164:167], a[136:139], v[152:155]\n"
"s_add_u32 m0, s82, 0x18000\n"
"v_add_u32 v24, 0xfffe0000, v24\n"
"v_mfma_f32_16x16x32_bf16 v[156:159], a[164:167], a[140:143], v[156:159]\n"
"buffer_load_dwordx4 v35, s[60:63], s70 offen sc1 lds ;@F13\n"
"v_mfma_f32_16x16x32_bf16 v[160:163], a[168:171], a[128:131], v[160:163]\n"
"buffer_load_dwordx4 v35, s[60:63], s70 offen offset:1024 sc1 lds ;@F13\n"
"v_mfma_f32_16x16x32_bf16 v[164:167], a[168:171], a[132:135], v[164:167]\n"
"s_add_u32 m0, s82, 0x18800\n"
"v_add_u32 v26, 0xfffe0000, v26\n"
"v_mfma_f32_16x16x32_bf16 v[168:171], a[168:171], a[136:139], v[168:171]\n"
"buffer_load_dwordx4 v35, s[60:63], s71 offen sc1 lds ;@F13\n"
"v_mfma_f32_16x16x32_bf16 v[172:175], a[168:171], a[140:143], v[172:175]\n"
"buffer_load_dwordx4 v35, s[60:63], s71 offen offset:1024 sc1 lds ;@F13\n"
"v_mfma_f32_16x16x32_bf16 v[176:179], a[172:175], a[128:131], v[176:179]\n"
"v_add_u32 v35, 0x800, v35\n"
"v_mfma_f32_16x16x32_bf16 v[180:183], a[172:175], a[132:135], v[180:183]\n"
"v_mfma_f32_16x16x32_bf16 v[184:187], a[172:175], a[136:139], v[184:187]\n"
"v_add_u32 v29, 0xfffe0000, v29\n"
"v_mfma_f32_16x16x32_bf16 v[188:191], a[172:175], a[140:143], v[188:191]\n"
"v_add_u32 v27, 0xfffe0000, v27\n"
"v_add_u32 v28, 0xfffe0000, v28\n"
"s_waitcnt lgkmcnt(0)\n"
"v_mfma_f32_16x16x32_bf16 v[128:131], a[224:227], a[192:195], v[128:131]\n"
"s_waitcnt vmcnt(24)\n"
"s_barrier\n"
"v_mfma_f32_16x16x32_bf16 v[132:135], a[224:227], a[196:199], v[132:135]\n"
"ds_read_b64_tr_b16 a[160:161], v26 offset:0\n"
"ds_read_b64_tr_b16 a[162:163], v26 offset:1024\n"
"v_mfma_f32_16x16x32_bf16 v[136:139], a[224:227], a[200:203], v[136:139]\n"
"ds_read_b128 a[128:131], v24 offset:0\n"
"ds_read_b128 a[132:135], v24 offset:2048\n"
"v_mfma_f32_16x16x32_bf16 v[140:143], a[224:227], a[204:207], v[140:143]\n"
"ds_read_b128 a[136:139], v24 offset:4096\n"
"ds_read_b128 a[140:143], v24 offset:6144\n"
"v_mfma_f32_16x16x32_bf16 v[144:147], a[228:231], a[192:195], v[144:147]\n"
"ds_read_b64_tr_b16 a[164:165], v27 offset:0\n"
"ds_read_b64_tr_b16 a[166:167], v27 offset:1024\n"
"v_mfma_f32_16x16x32_bf16 v[148:151], a[228:231], a[196:199], v[148:151]\n"
"ds_read_b64_tr_b16 a[168:169], v28 offset:0\n"
"ds_read_b64_tr_b16 a[170:171], v28 offset:1024\n"
"v_mfma_f32_16x16x32_bf16 v[152:155], a[228:231], a[200:203], v[152:155]\n"
"ds_read_b64_tr_b16 a[172:173], v29 offset:0\n"
"ds_read_b64_tr_b16 a[174:175], v29 offset:1024\n"
"v_mfma_f32_16x16x32_bf16 v[156:159], a[228:231], a[204:207], v[156:159]\n"
"s_add_u32 m0, s83, 0x24000\n"
"v_add_u32 v25, 0xfffe0000, v25\n"
"v_mfma_f32_16x16x32_bf16 v[160:163], a[232:235], a[192:195], v[160:163]\n"
"buffer_load_dwordx4 v37, s[52:55], s76 offen lds ;@F14\n"
"v_mfma_f32_16x16x32_bf16 v[164:167], a[232:235], a[196:199], v[164:167]\n"
"s_add_u32 m0, s83, 0x24400\n"
"v_add_u32 v30, 0xfffe0000, v30\n"
"v_mfma_f32_16x16x32_bf16 v[168:171], a[232:235], a[200:203], v[168:171]\n"
"buffer_load_dwordx4 v37, s[52:55], s77 offen lds ;@F14\n"
"v_mfma_f32_16x16x32_bf16 v[172:175], a[232:235], a[204:207], v[172:175]\n"
"s_add_u32 m0, s83, 0x24800\n"
"v_add_u32 v31, 0xfffe0000, v31\n"
"v_mfma_f32_16x16x32_bf16 v[176:179], a[236:239], a[192:195], v[176:179]\n"
"buffer_load_dwordx4 v42, s[52:55], s78 offen lds ;@F14\n"
"v_mfma_f32_16x16x32_bf16 v[180:183], a[236:239], a[196:199], v[180:183]\n"
"s_add_u32 m0, s83, 0x24c00\n"
"v_add_u32 v32, 0xfffe0000, v32\n"
"v_mfma_f32_16x16x32_bf16 v[184:187], a[236:239], a[200:203], v[184:187]\n"
"buffer_load_dwordx4 v42, s[52:55], s79 offen lds ;@F14\n"
"v_mfma_f32_16x16x32_bf16 v[188:191], a[236:239], a[204:207], v[188:191]\n"
"v_add_u32 v33, 0xfffe0000, v33\n"
"v_add_u32 v37, 0x10000, v37\n"
"v_add_u32 v42, 0x10000, v42\n"
"s_waitcnt lgkmcnt(0)\n"
"v_mfma_f32_16x16x32_bf16 v[128:131], a[160:163], a[128:131], v[128:131]\n"
"ds_read_b64_tr_b16 a[224:225], v30 offset:0\n"
"ds_read_b64_tr_b16 a[226:227], v30 offset:1024\n"
"v_mfma_f32_16x16x32_bf16 v[132:135], a[160:163], a[132:135], v[132:135]\n"
"ds_read_b128 a[192:195], v25 offset:0\n"
"ds_read_b128 a[196:199], v25 offset:2048\n"
"v_mfma_f32_16x16x32_bf16 v[136:139], a[160:163], a[136:139], v[136:139]\n"
"ds_read_b128 a[200:203], v25 offset:4096\n"
"ds_read_b128 a[204:207], v25 offset:6144\n"
"v_mfma_f32_16x16x32_bf16 v[140:143], a[160:163], a[140:143], v[140:143]\n"
"ds_read_b64_tr_b16 a[228:229], v31 offset:0\n"
"ds_read_b64_tr_b16 a[230:231], v31 offset:1024\n"
"v_mfma_f32_16x16x32_bf16 v[144:147], a[164:167], a[128:131], v[144:147]\n"
"ds_read_b64_tr_b16 a[232:233], v32 offset:0\n"
"ds_read_b64_tr_b16 a[234:235], v32 offset:1024\n"
"v_mfma_f32_16x16x32_bf16 v[148:151], a[164:167], a[132:135], v[148:151]\n"
"ds_read_b64_tr_b16 a[236:237], v33 offset:0\n"
"ds_read_b64_tr_b16 a[238:239], v33 offset:1024\n"
"v_mfma_f32_16x16x32_bf16 v[152:155], a[164:167], a[136:139], v[152:155]\n"
"s_add_u32 m0, s82, 0x20000\n"
"v_add_u32 v24, 0x8000, v24\n"
"v_mfma_f32_16x16x32_bf16 v[156:159], a[164:167], a[140:143], v[156:159]\n"
"buffer_load_dwordx4 v35, s[60:63], s70 offen sc1 lds ;@F14\n"
"v_mfma_f32_16x16x32_bf16 v[160:163], a[168:171], a[128:131], v[160:163]\n"
"buffer_load_dwordx4 v35, s[60:63], s70 offen offset:1024 sc1 lds ;@F14\n"
"v_mfma_f32_16x16x32_bf16 v[164:167], a[168:171], a[132:135], v[164:167]\n"
"s_add_u32 m0, s82, 0x20800\n"
"v_add_u32 v26, 0x8000, v26\n"
"v_mfma_f32_16x16x32_bf16 v[168:171], a[168:171], a[136:139], v[168:171]\n"
"buffer_load_dwordx4 v35, s[60:63], s71 offen sc1 lds ;@F14\n"
"v_mfma_f32_16x16x32_bf16 v[172:175], a[168:171], a[140:143], v[172:175]\n"
"buffer_load_dwordx4 v35, s[60:63], s71 offen offset:1024 sc1 lds ;@F14\n"
"v_mfma_f32_16x16x32_bf16 v[176:179], a[172:175], a[128:131], v[176:179]\n"
"v_add_u32 v35, 0x800, v35\n"
"v_mfma_f32_16x16x32_bf16 v[180:183], a[172:175], a[132:135], v[180:183]\n"
"v_mfma_f32_16x16x32_bf16 v[184:187], a[172:175], a[136:139], v[184:187]\n"
"v_add_u32 v29, 0x8000, v29\n"
"v_mfma_f32_16x16x32_bf16 v[188:191], a[172:175], a[140:143], v[188:191]\n"
"v_add_u32 v27, 0x8000, v27\n"
"v_add_u32 v28, 0x8000, v28\n"
"s_waitcnt lgkmcnt(0)\n"
"v_mfma_f32_16x16x32_bf16 v[128:131], a[224:227], a[192:195], v[128:131]\n"
"s_waitcnt vmcnt(24)\n"
"s_barrier\n"
"v_mfma_f32_16x16x32_bf16 v[132:135], a[224:227], a[196:199], v[132:135]\n"
"ds_read_b64_tr_b16 a[160:161], v26 offset:0\n"
"ds_read_b64_tr_b16 a[162:163], v26 offset:1024\n"
"v_mfma_f32_16x16x32_bf16 v[136:139], a[224:227], a[200:203], v[136:139]\n"
"ds_read_b128 a[128:131], v24 offset:0\n"
"ds_read_b128 a[132:135], v24 offset:2048\n"
"v_mfma_f32_16x16x32_bf16 v[140:143], a[224:227], a[204:207], v[140:143]\n"
"ds_read_b128 a[136:139], v24 offset:4096\n"
"ds_read_b128 a[140:143], v24 offset:6144\n"
"v_mfma_f32_16x16x32_bf16 v[144:147], a[228:231], a[192:195], v[144:147]\n"
"ds_read_b64_tr_b16 a[164:165], v27 offset:0\n"
"ds_read_b64_tr_b16 a[166:167], v27 offset:1024\n"
"v_mfma_f32_16x16x32_bf16 v[148:151], a[228:231], a[196:199], v[148:151]\n"
"ds_read_b64_tr_b16 a[168:169], v28 offset:0\n"
"ds_read_b64_tr_b16 a[170:171], v28 offset:1024\n"
"v_mfma_f32_16x16x32_bf16 v[152:155], a[228:231], a[200:203], v[152:155]\n"
"ds_read_b64_tr_b16 a[172:173], v29 offset:0\n"
"ds_read_b64_tr_b16 a[174:175], v29 offset:1024\n"
"v_mfma_f32_16x16x32_bf16 v[156:159], a[228:231], a[204:207], v[156:159]\n"
"s_add_u32 m0, s83, 0x4000\n"
"v_add_u32 v25, 0x8000, v25\n"
"v_mfma_f32_16x16x32_bf16 v[160:163], a[232:235], a[192:195], v[160:163]\n"
"buffer_load_dwordx4 v37, s[52:55], s76 offen lds ;@F15\n"
"v_mfma_f32_16x16x32_bf16 v[164:167], a[232:235], a[196:199], v[164:167]\n"
"s_add_u32 m0, s83, 0x4400\n"
"v_add_u32 v30, 0x8000, v30\n"
"v_mfma_f32_16x16x32_bf16 v[168:171], a[232:235], a[200:203], v[168:171]\n"
"buffer_load_dwordx4 v37, s[52:55], s77 offen lds ;@F15\n"
"v_mfma_f32_16x16x32_bf16 v[172:175], a[232:235], a[204:207], v[172:175]\n"
"s_add_u32 m0, s83, 0x4800\n"
"v_add_u32 v31, 0x8000, v31\n"
"v_mfma_f32_16x16x32_bf16 v[176:179], a[236:239], a[192:195], v[176:179]\n"
"buffer_load_dwordx4 v42, s[52:55], s78 offen lds ;@F15\n"
"v_mfma_f32_16x16x32_bf16 v[180:183], a[236:239], a[196:199], v[180:183]\n"
"s_add_u32 m0, s83, 0x4c00\n"
"v_add_u32 v32, 0x8000, v32\n"
"v_mfma_f32_16x16x32_bf16 v[184:187], a[236:239], a[200:203], v[184:187]\n"
"buffer_load_dwordx4 v42, s[52:55], s79 offen lds ;@F15\n"
"v_mfma_f32_16x16x32_bf16 v[188:191], a[236:239], a[204:207], v[188:191]\n"
"v_add_u32 v33, 0x8000, v33\n"
"v_add_u32 v37, 0x10000, v37\n"
"v_add_u32 v42, 0x10000, v42\n"
"s_waitcnt lgkmcnt(0)\n"
"v_mfma_f32_16x16x32_bf16 v[128:131], a[160:163], a[128:131], v[128:131]\n"
"ds_read_b64_tr_b16 a[224:225], v30 offset:0\n"
"ds_read_b64_tr_b16 a[226:227], v30 offset:1024\n"
"v_mfma_f32_16x16x32_bf16 v[132:135], a[160:163], a[132:135], v[132:135]\n"
"ds_read_b128 a[192:195], v25 offset:0\n"
"ds_read_b128 a[196:199], v25 offset:2048\n"
"v_mfma_f32_16x16x32_bf16 v[136:139], a[160:163], a[136:139], v[136:139]\n"
"ds_read_b128 a[200:203], v25 offset:4096\n"
"ds_read_b128 a[204:207], v25 offset:6144\n"
"v_mfma_f32_16x16x32_bf16 v[140:143], a[160:163], a[140:143], v[140:143]\n"
"ds_read_b64_tr_b16 a[228:229], v31 offset:0\n"
"ds_read_b64_tr_b16 a[230:231], v31 offset:1024\n"
"v_mfma_f32_16x16x32_bf16 v[144:147], a[164:167], a[128:131], v[144:147]\n"
"ds_read_b64_tr_b16 a[232:233], v32 offset:0\n"
"ds_read_b64_tr_b16 a[234:235], v32 offset:1024\n"
"v_mfma_f32_16x16x32_bf16 v[148:151], a[164:167], a[132:135], v[148:151]\n"
"ds_read_b64_tr_b16 a[236:237], v33 offset:0\n"
"ds_read_b64_tr_b16 a[238:239], v33 offset:1024\n"
"v_mfma_f32_16x16x32_bf16 v[152:155], a[164:167], a[136:139], v[152:155]\n"
"s_add_u32 m0, s82, 0x0\n"
"v_add_u32 v24, 0x8000, v24\n"
"v_mfma_f32_16x16x32_bf16 v[156:159], a[164:167], a[140:143], v[156:159]\n"
"buffer_load_dwordx4 v35, s[60:63], s70 offen sc1 lds ;@F15\n"
"v_mfma_f32_16x16x32_bf16 v[160:163], a[168:171], a[128:131], v[160:163]\n"
"buffer_load_dwordx4 v35, s[60:63], s70 offen offset:1024 sc1 lds ;@F15\n"
"v_mfma_f32_16x16x32_bf16 v[164:167], a[168:171], a[132:135], v[164:167]\n"
"s_add_u32 m0, s82, 0x800\n"
"v_add_u32 v26, 0x8000, v26\n"
"v_mfma_f32_16x16x32_bf16 v[168:171], a[168:171], a[136:139], v[168:171]\n"
"buffer_load_dwordx4 v35, s[60:63], s71 offen sc1 lds ;@F15\n"
"v_mfma_f32_16x16x32_bf16 v[172:175], a[168:171], a[140:143], v[172:175]\n"
"buffer_load_dwordx4 v35, s[60:63], s71 offen offset:1024 sc1 lds ;@F15\n"
"v_mfma_f32_16x16x32_bf16 v[176:179], a[172:175], a[128:131], v[176:179]\n"
"v_add_u32 v35, 0x800, v35\n"
"v_mfma_f32_16x16x32_bf16 v[180:183], a[172:175], a[132:135], v[180:183]\n"
"v_mfma_f32_16x16x32_bf16 v[184:187], a[172:175], a[136:139], v[184:187]\n"
"v_add_u32 v29, 0x8000, v29\n"
"v_mfma_f32_16x16x32_bf16 v[188:191], a[172:175], a[140:143], v[188:191]\n"
"v_add_u32 v27, 0x8000, v27\n"
"v_add_u32 v28, 0x8000, v28\n"
"s_waitcnt lgkmcnt(0)\n"
"v_mfma_f32_16x16x32_bf16 v[128:131], a[224:227], a[192:195], v[128:131]\n"
"s_waitcnt vmcnt(24)\n"
"s_barrier\n"
"v_mfma_f32_16x16x32_bf16 v[132:135], a[224:227], a[196:199], v[132:135]\n"
"ds_read_b64_tr_b16 a[160:161], v26 offset:0\n"
"ds_read_b64_tr_b16 a[162:163], v26 offset:1024\n"
"v_mfma_f32_16x16x32_bf16 v[136:139], a[224:227], a[200:203], v[136:139]\n"
"ds_read_b128 a[128:131], v24 offset:0\n"
"ds_read_b128 a[132:135], v24 offset:2048\n"
"v_mfma_f32_16x16x32_bf16 v[140:143], a[224:227], a[204:207], v[140:143]\n"
"ds_read_b128 a[136:139], v24 offset:4096\n"
"ds_read_b128 a[140:143], v24 offset:6144\n"
"v_mfma_f32_16x16x32_bf16 v[144:147], a[228:231], a[192:195], v[144:147]\n"
"ds_read_b64_tr_b16 a[164:165], v27 offset:0\n"
"ds_read_b64_tr_b16 a[166:167], v27 offset:1024\n"
"v_mfma_f32_16x16x32_bf16 v[148:151], a[228:231], a[196:199], v[148:151]\n"
"ds_read_b64_tr_b16 a[168:169], v28 offset:0\n"
"ds_read_b64_tr_b16 a[170:171], v28 offset:1024\n"
"v_mfma_f32_16x16x32_bf16 v[152:155], a[228:231], a[200:203], v[152:155]\n"
"ds_read_b64_tr_b16 a[172:173], v29 offset:0\n"
"ds_read_b64_tr_b16 a[174:175], v29 offset:1024\n"
"v_mfma_f32_16x16x32_bf16 v[156:159], a[228:231], a[204:207], v[156:159]\n"
"s_add_u32 m0, s83, 0xc000\n"
"v_add_u32 v25, 0x8000, v25\n"
"v_mfma_f32_16x16x32_bf16 v[160:163], a[232:235], a[192:195], v[160:163]\n"
"buffer_load_dwordx4 v37, s[52:55], s76 offen lds ;@F16\n"
"v_mfma_f32_16x16x32_bf16 v[164:167], a[232:235], a[196:199], v[164:167]\n"
"s_add_u32 m0, s83, 0xc400\n"
"v_add_u32 v30, 0x8000, v30\n"
"v_mfma_f32_16x16x32_bf16 v[168:171], a[232:235], a[200:203], v[168:171]\n"
"buffer_load_dwordx4 v37, s[52:55], s77 offen lds ;@F16\n"
"v_mfma_f32_16x16x32_bf16 v[172:175], a[232:235], a[204:207], v[172:175]\n"
"s_add_u32 m0, s83, 0xc800\n"
"v_add_u32 v31, 0x8000, v31\n"
"v_mfma_f32_16x16x32_bf16 v[176:179], a[236:239], a[192:195], v[176:179]\n"
"buffer_load_dwordx4 v42, s[52:55], s78 offen lds ;@F16\n"
"v_mfma_f32_16x16x32_bf16 v[180:183], a[236:239], a[196:199], v[180:183]\n"
"s_add_u32 m0, s83, 0xcc00\n"
"v_add_u32 v32, 0x8000, v32\n"
"v_mfma_f32_16x16x32_bf16 v[184:187], a[236:239], a[200:203], v[184:187]\n"
"buffer_load_dwordx4 v42, s[52:55], s79 offen lds ;@F16\n"
"v_mfma_f32_16x16x32_bf16 v[188:191], a[236:239], a[204:207], v[188:191]\n"
"v_add_u32 v33, 0x8000, v33\n"
"v_add_u32 v37, 0x10000, v37\n"
"v_add_u32 v42, 0x10000, v42\n"
"s_waitcnt lgkmcnt(0)\n"
"v_mfma_f32_16x16x32_bf16 v[128:131], a[160:163], a[128:131], v[128:131]\n"
"ds_read_b64_tr_b16 a[224:225], v30 offset:0\n"
"ds_read_b64_tr_b16 a[226:227], v30 offset:1024\n"
"v_mfma_f32_16x16x32_bf16 v[132:135], a[160:163], a[132:135], v[132:135]\n"
"ds_read_b128 a[192:195], v25 offset:0\n"
"ds_read_b128 a[196:199], v25 offset:2048\n"
"v_mfma_f32_16x16x32_bf16 v[136:139], a[160:163], a[136:139], v[136:139]\n"
"ds_read_b128 a[200:203], v25 offset:4096\n"
"ds_read_b128 a[204:207], v25 offset:6144\n"
"v_mfma_f32_16x16x32_bf16 v[140:143], a[160:163], a[140:143], v[140:143]\n"
"ds_read_b64_tr_b16 a[228:229], v31 offset:0\n"
"ds_read_b64_tr_b16 a[230:231], v31 offset:1024\n"
"v_mfma_f32_16x16x32_bf16 v[144:147], a[164:167], a[128:131], v[144:147]\n"
"ds_read_b64_tr_b16 a[232:233], v32 offset:0\n"
"ds_read_b64_tr_b16 a[234:235], v32 offset:1024\n"
"v_mfma_f32_16x16x32_bf16 v[148:151], a[164:167], a[132:135], v[148:151]\n"
"ds_read_b64_tr_b16 a[236:237], v33 offset:0\n"
"ds_read_b64_tr_b16 a[238:239], v33 offset:1024\n"
"v_mfma_f32_16x16x32_bf16 v[152:155], a[164:167], a[136:139], v[152:155]\n"
"s_add_u32 m0, s82, 0x8000\n"
"v_add_u32 v24, 0x8000, v24\n"
"v_mfma_f32_16x16x32_bf16 v[156:159], a[164:167], a[140:143], v[156:159]\n"
"buffer_load_dwordx4 v35, s[60:63], s70 offen sc1 lds ;@F16\n"
"v_mfma_f32_16x16x32_bf16 v[160:163], a[168:171], a[128:131], v[160:163]\n"
"buffer_load_dwordx4 v35, s[60:63], s70 offen offset:1024 sc1 lds ;@F16\n"
"v_mfma_f32_16x16x32_bf16 v[164:167], a[168:171], a[132:135], v[164:167]\n"
"s_add_u32 m0, s82, 0x8800\n"
"v_add_u32 v26, 0x8000, v26\n"
"v_mfma_f32_16x16x32_bf16 v[168:171], a[168:171], a[136:139], v[168:171]\n"
"buffer_load_dwordx4 v35, s[60:63], s71 offen sc1 lds ;@F16\n"
"v_mfma_f32_16x16x32_bf16 v[172:175], a[168:171], a[140:143], v[172:175]\n"
"buffer_load_dwordx4 v35, s[60:63], s71 offen offset:1024 sc1 lds ;@F16\n"
"v_mfma_f32_16x16x32_bf16 v[176:179], a[172:175], a[128:131], v[176:179]\n"
"v_add_u32 v35, 0x800, v35\n"
"v_mfma_f32_16x16x32_bf16 v[180:183], a[172:175], a[132:135], v[180:183]\n"
"v_mfma_f32_16x16x32_bf16 v[184:187], a[172:175], a[136:139], v[184:187]\n"
"v_add_u32 v29, 0x8000, v29\n"
"v_mfma_f32_16x16x32_bf16 v[188:191], a[172:175], a[140:143], v[188:191]\n"
"v_add_u32 v27, 0x8000, v27\n"
"v_add_u32 v28, 0x8000, v28\n"
"s_waitcnt lgkmcnt(0)\n"
"v_mfma_f32_16x16x32_bf16 v[128:131], a[224:227], a[192:195], v[128:131]\n"
"s_waitcnt vmcnt(24)\n"
"s_barrier\n"
"v_mfma_f32_16x16x32_bf16 v[132:135], a[224:227], a[196:199], v[132:135]\n"
"ds_read_b64_tr_b16 a[160:161], v26 offset:0\n"
"ds_read_b64_tr_b16 a[162:163], v26 offset:1024\n"
"v_mfma_f32_16x16x32_bf16 v[136:139], a[224:227], a[200:203], v[136:139]\n"
"ds_read_b128 a[128:131], v24 offset:0\n"
"ds_read_b128 a[132:135], v24 offset:2048\n"
"v_mfma_f32_16x16x32_bf16 v[140:143], a[224:227], a[204:207], v[140:143]\n"
"ds_read_b128 a[136:139], v24 offset:4096\n"
"ds_read_b128 a[140:143], v24 offset:6144\n"
"v_mfma_f32_16x16x32_bf16 v[144:147], a[228:231], a[192:195], v[144:147]\n"
"ds_read_b64_tr_b16 a[164:165], v27 offset:0\n"
"ds_read_b64_tr_b16 a[166:167], v27 offset:1024\n"
"v_mfma_f32_16x16x32_bf16 v[148:151], a[228:231], a[196:199], v[148:151]\n"
"ds_read_b64_tr_b16 a[168:169], v28 offset:0\n"
"ds_read_b64_tr_b16 a[170:171], v28 offset:1024\n"
"v_mfma_f32_16x16x32_bf16 v[152:155], a[228:231], a[200:203], v[152:155]\n"
"ds_read_b64_tr_b16 a[172:173], v29 offset:0\n"
"ds_read_b64_tr_b16 a[174:175], v29 offset:1024\n"
"v_mfma_f32_16x16x32_bf16 v[156:159], a[228:231], a[204:207], v[156:159]\n"
"s_add_u32 m0, s83, 0x14000\n"
"v_add_u32 v25, 0x8000, v25\n"
"v_mfma_f32_16x16x32_bf16 v[160:163], a[232:235], a[192:195], v[160:163]\n"
"buffer_load_dwordx4 v37, s[52:55], s76 offen lds ;@F17\n"
"v_mfma_f32_16x16x32_bf16 v[164:167], a[232:235], a[196:199], v[164:167]\n"
"s_add_u32 m0, s83, 0x14400\n"
"v_add_u32 v30, 0x8000, v30\n"
"v_mfma_f32_16x16x32_bf16 v[168:171], a[232:235], a[200:203], v[168:171]\n"
"buffer_load_dwordx4 v37, s[52:55], s77 offen lds ;@F17\n"
"v_mfma_f32_16x16x32_bf16 v[172:175], a[232:235], a[204:207], v[172:175]\n"
"s_add_u32 m0, s83, 0x14800\n"
"v_add_u32 v31, 0x8000, v31\n"
"v_mfma_f32_16x16x32_bf16 v[176:179], a[236:239], a[192:195], v[176:179]\n"
"buffer_load_dwordx4 v42, s[52:55], s78 offen lds ;@F17\n"
"v_mfma_f32_16x16x32_bf16 v[180:183], a[236:239], a[196:199], v[180:183]\n"
"s_add_u32 m0, s83, 0x14c00\n"
"v_add_u32 v32, 0x8000, v32\n"
"v_mfma_f32_16x16x32_bf16 v[184:187], a[236:239], a[200:203], v[184:187]\n"
"buffer_load_dwordx4 v42, s[52:55], s79 offen lds ;@F17\n"
"v_mfma_f32_16x16x32_bf16 v[188:191], a[236:239], a[204:207], v[188:191]\n"
"v_add_u32 v33, 0x8000, v33\n"
"v_add_u32 v37, 0x10000, v37\n"
"v_add_u32 v42, 0x10000, v42\n"
"s_waitcnt lgkmcnt(0)\n"
"v_mfma_f32_16x16x32_bf16 v[128:131], a[160:163], a[128:131], v[128:131]\n"
"ds_read_b64_tr_b16 a[224:225], v30 offset:0\n"
"ds_read_b64_tr_b16 a[226:227], v30 offset:1024\n"
"v_mfma_f32_16x16x32_bf16 v[132:135], a[160:163], a[132:135], v[132:135]\n"
"ds_read_b128 a[192:195], v25 offset:0\n"
"ds_read_b128 a[196:199], v25 offset:2048\n"
"v_mfma_f32_16x16x32_bf16 v[136:139], a[160:163], a[136:139], v[136:139]\n"
"ds_read_b128 a[200:203], v25 offset:4096\n"
"ds_read_b128 a[204:207], v25 offset:6144\n"
"v_mfma_f32_16x16x32_bf16 v[140:143], a[160:163], a[140:143], v[140:143]\n"
"ds_read_b64_tr_b16 a[228:229], v31 offset:0\n"
"ds_read_b64_tr_b16 a[230:231], v31 offset:1024\n"
"v_mfma_f32_16x16x32_bf16 v[144:147], a[164:167], a[128:131], v[144:147]\n"
"ds_read_b64_tr_b16 a[232:233], v32 offset:0\n"
"ds_read_b64_tr_b16 a[234:235], v32 offset:1024\n"
"v_mfma_f32_16x16x32_bf16 v[148:151], a[164:167], a[132:135], v[148:151]\n"
"ds_read_b64_tr_b16 a[236:237], v33 offset:0\n"
"ds_read_b64_tr_b16 a[238:239], v33 offset:1024\n"
"v_mfma_f32_16x16x32_bf16 v[152:155], a[164:167], a[136:139], v[152:155]\n"
"s_add_u32 m0, s82, 0x10000\n"
"v_add_u32 v24, 0x8000, v24\n"
"v_mfma_f32_16x16x32_bf16 v[156:159], a[164:167], a[140:143], v[156:159]\n"
"buffer_load_dwordx4 v35, s[60:63], s70 offen sc1 lds ;@F17\n"
"v_mfma_f32_16x16x32_bf16 v[160:163], a[168:171], a[128:131], v[160:163]\n"
"buffer_load_dwordx4 v35, s[60:63], s70 offen offset:1024 sc1 lds ;@F17\n"
"v_mfma_f32_16x16x32_bf16 v[164:167], a[168:171], a[132:135], v[164:167]\n"
"s_add_u32 m0, s82, 0x10800\n"
"v_add_u32 v26, 0x8000, v26\n"
"v_mfma_f32_16x16x32_bf16 v[168:171], a[168:171], a[136:139], v[168:171]\n"
"buffer_load_dwordx4 v35, s[60:63], s71 offen sc1 lds ;@F17\n"
"v_mfma_f32_16x16x32_bf16 v[172:175], a[168:171], a[140:143], v[172:175]\n"
"buffer_load_dwordx4 v35, s[60:63], s71 offen offset:1024 sc1 lds ;@F17\n"
"v_mfma_f32_16x16x32_bf16 v[176:179], a[172:175], a[128:131], v[176:179]\n"
"v_add_u32 v35, 0x800, v35\n"
"v_mfma_f32_16x16x32_bf16 v[180:183], a[172:175], a[132:135], v[180:183]\n"
"v_mfma_f32_16x16x32_bf16 v[184:187], a[172:175], a[136:139], v[184:187]\n"
"v_add_u32 v29, 0x8000, v29\n"
"v_mfma_f32_16x16x32_bf16 v[188:191], a[172:175], a[140:143], v[188:191]\n"
"v_add_u32 v27, 0x8000, v27\n"
"v_add_u32 v28, 0x8000, v28\n"
"s_waitcnt lgkmcnt(0)\n"
"v_mfma_f32_16x16x32_bf16 v[128:131], a[224:227], a[192:195], v[128:131]\n"
"s_waitcnt vmcnt(24)\n"
"s_barrier\n"
"v_mfma_f32_16x16x32_bf16 v[132:135], a[224:227], a[196:199], v[132:135]\n"
"ds_read_b64_tr_b16 a[160:161], v26 offset:0\n"
"ds_read_b64_tr_b16 a[162:163], v26 offset:1024\n"
"v_mfma_f32_16x16x32_bf16 v[136:139], a[224:227], a[200:203], v[136:139]\n"
"ds_read_b128 a[128:131], v24 offset:0\n"
"ds_read_b128 a[132:135], v24 offset:2048\n"
"v_mfma_f32_16x16x32_bf16 v[140:143], a[224:227], a[204:207], v[140:143]\n"
"ds_read_b128 a[136:139], v24 offset:4096\n"
"ds_read_b128 a[140:143], v24 offset:6144\n"
"v_mfma_f32_16x16x32_bf16 v[144:147], a[228:231], a[192:195], v[144:147]\n"
"ds_read_b64_tr_b16 a[164:165], v27 offset:0\n"
"ds_read_b64_tr_b16 a[166:167], v27 offset:1024\n"
"v_mfma_f32_16x16x32_bf16 v[148:151], a[228:231], a[196:199], v[148:151]\n"
"ds_read_b64_tr_b16 a[168:169], v28 offset:0\n"
"ds_read_b64_tr_b16 a[170:171], v28 offset:1024\n"
"v_mfma_f32_16x16x32_bf16 v[152:155], a[228:231], a[200:203], v[152:155]\n"
"ds_read_b64_tr_b16 a[172:173], v29 offset:0\n"
"ds_read_b64_tr_b16 a[174:175], v29 offset:1024\n"
"v_mfma_f32_16x16x32_bf16 v[156:159], a[228:231], a[204:207], v[156:159]\n"
"s_add_u32 m0, s83, 0x1c000\n"
"v_add_u32 v25, 0x8000, v25\n"
"v_mfma_f32_16x16x32_bf16 v[160:163], a[232:235], a[192:195], v[160:163]\n"
"buffer_load_dwordx4 v37, s[52:55], s76 offen lds ;@F18\n"
"v_mfma_f32_16x16x32_bf16 v[164:167], a[232:235], a[196:199], v[164:167]\n"
"s_add_u32 m0, s83, 0x1c400\n"
"v_add_u32 v30, 0x8000, v30\n"
"v_mfma_f32_16x16x32_bf16 v[168:171], a[232:235], a[200:203], v[168:171]\n"
"buffer_load_dwordx4 v37, s[52:55], s77 offen lds ;@F18\n"
"v_mfma_f32_16x16x32_bf16 v[172:175], a[232:235], a[204:207], v[172:175]\n"
"s_add_u32 m0, s83, 0x1c800\n"
"v_add_u32 v31, 0x8000, v31\n"
"v_mfma_f32_16x16x32_bf16 v[176:179], a[236:239], a[192:195], v[176:179]\n"
"buffer_load_dwordx4 v42, s[52:55], s78 offen lds ;@F18\n"
"v_mfma_f32_16x16x32_bf16 v[180:183], a[236:239], a[196:199], v[180:183]\n"
"s_add_u32 m0, s83, 0x1cc00\n"
"v_add_u32 v32, 0x8000, v32\n"
"v_mfma_f32_16x16x32_bf16 v[184:187], a[236:239], a[200:203], v[184:187]\n"
"buffer_load_dwordx4 v42, s[52:55], s79 offen lds ;@F18\n"
"v_mfma_f32_16x16x32_bf16 v[188:191], a[236:239], a[204:207], v[188:191]\n"
"v_add_u32 v33, 0x8000, v33\n"
"v_add_u32 v37, 0x10000, v37\n"
"v_add_u32 v42, 0x10000, v42\n"
"s_waitcnt lgkmcnt(0)\n"
"v_mfma_f32_16x16x32_bf16 v[128:131], a[160:163], a[128:131], v[128:131]\n"
"ds_read_b64_tr_b16 a[224:225], v30 offset:0\n"
"ds_read_b64_tr_b16 a[226:227], v30 offset:1024\n"
"v_mfma_f32_16x16x32_bf16 v[132:135], a[160:163], a[132:135], v[132:135]\n"
"ds_read_b128 a[192:195], v25 offset:0\n"
"ds_read_b128 a[196:199], v25 offset:2048\n"
"v_mfma_f32_16x16x32_bf16 v[136:139], a[160:163], a[136:139], v[136:139]\n"
"ds_read_b128 a[200:203], v25 offset:4096\n"
"ds_read_b128 a[204:207], v25 offset:6144\n"
"v_mfma_f32_16x16x32_bf16 v[140:143], a[160:163], a[140:143], v[140:143]\n"
"ds_read_b64_tr_b16 a[228:229], v31 offset:0\n"
"ds_read_b64_tr_b16 a[230:231], v31 offset:1024\n"
"v_mfma_f32_16x16x32_bf16 v[144:147], a[164:167], a[128:131], v[144:147]\n"
"ds_read_b64_tr_b16 a[232:233], v32 offset:0\n"
"ds_read_b64_tr_b16 a[234:235], v32 offset:1024\n"
"v_mfma_f32_16x16x32_bf16 v[148:151], a[164:167], a[132:135], v[148:151]\n"
"ds_read_b64_tr_b16 a[236:237], v33 offset:0\n"
"ds_read_b64_tr_b16 a[238:239], v33 offset:1024\n"
"v_mfma_f32_16x16x32_bf16 v[152:155], a[164:167], a[136:139], v[152:155]\n"
"s_add_u32 m0, s82, 0x18000\n"
"v_add_u32 v24, 0xfffe0000, v24\n"
"v_mfma_f32_16x16x32_bf16 v[156:159], a[164:167], a[140:143], v[156:159]\n"
"buffer_load_dwordx4 v35, s[60:63], s70 offen sc1 lds ;@F18\n"
"v_mfma_f32_16x16x32_bf16 v[160:163], a[168:171], a[128:131], v[160:163]\n"
"buffer_load_dwordx4 v35, s[60:63], s70 offen offset:1024 sc1 lds ;@F18\n"
"v_mfma_f32_16x16x32_bf16 v[164:167], a[168:171], a[132:135], v[164:167]\n"
"s_add_u32 m0, s82, 0x18800\n"
"v_add_u32 v26, 0xfffe0000, v26\n"
"v_mfma_f32_16x16x32_bf16 v[168:171], a[168:171], a[136:139], v[168:171]\n"
"buffer_load_dwordx4 v35, s[60:63], s71 offen sc1 lds ;@F18\n"
"v_mfma_f32_16x16x32_bf16 v[172:175], a[168:171], a[140:143], v[172:175]\n"
"buffer_load_dwordx4 v35, s[60:63], s71 offen offset:1024 sc1 lds ;@F18\n"
"v_mfma_f32_16x16x32_bf16 v[176:179], a[172:175], a[128:131], v[176:179]\n"
"v_add_u32 v35, 0x800, v35\n"
"v_mfma_f32_16x16x32_bf16 v[180:183], a[172:175], a[132:135], v[180:183]\n"
"v_mfma_f32_16x16x32_bf16 v[184:187], a[172:175], a[136:139], v[184:187]\n"
"v_add_u32 v29, 0xfffe0000, v29\n"
"v_mfma_f32_16x16x32_bf16 v[188:191], a[172:175], a[140:143], v[188:191]\n"
"v_add_u32 v27, 0xfffe0000, v27\n"
"v_add_u32 v28, 0xfffe0000, v28\n"
"s_waitcnt lgkmcnt(0)\n"
"v_mfma_f32_16x16x32_bf16 v[128:131], a[224:227], a[192:195], v[128:131]\n"
"s_waitcnt vmcnt(24)\n"
"s_barrier\n"
"v_mfma_f32_16x16x32_bf16 v[132:135], a[224:227], a[196:199], v[132:135]\n"
"ds_read_b64_tr_b16 a[160:161], v26 offset:0\n"
"ds_read_b64_tr_b16 a[162:163], v26 offset:1024\n"
"v_mfma_f32_16x16x32_bf16 v[136:139], a[224:227], a[200:203], v[136:139]\n"
"ds_read_b128 a[128:131], v24 offset:0\n"
"ds_read_b128 a[132:135], v24 offset:2048\n"
"v_mfma_f32_16x16x32_bf16 v[140:143], a[224:227], a[204:207], v[140:143]\n"
"ds_read_b128 a[136:139], v24 offset:4096\n"
"ds_read_b128 a[140:143], v24 offset:6144\n"
"v_mfma_f32_16x16x32_bf16 v[144:147], a[228:231], a[192:195], v[144:147]\n"
"ds_read_b64_tr_b16 a[164:165], v27 offset:0\n"
"ds_read_b64_tr_b16 a[166:167], v27 offset:1024\n"
"v_mfma_f32_16x16x32_bf16 v[148:151], a[228:231], a[196:199], v[148:151]\n"
"ds_read_b64_tr_b16 a[168:169], v28 offset:0\n"
"ds_read_b64_tr_b16 a[170:171], v28 offset:1024\n"
"v_mfma_f32_16x16x32_bf16 v[152:155], a[228:231], a[200:203], v[152:155]\n"
"ds_read_b64_tr_b16 a[172:173], v29 offset:0\n"
"ds_read_b64_tr_b16 a[174:175], v29 offset:1024\n"
"v_mfma_f32_16x16x32_bf16 v[156:159], a[228:231], a[204:207], v[156:159]\n"
"s_add_u32 m0, s83, 0x24000\n"
"v_add_u32 v25, 0xfffe0000, v25\n"
"v_mfma_f32_16x16x32_bf16 v[160:163], a[232:235], a[192:195], v[160:163]\n"
"buffer_load_dwordx4 v37, s[52:55], s76 offen lds ;@F19\n"
"v_mfma_f32_16x16x32_bf16 v[164:167], a[232:235], a[196:199], v[164:167]\n"
"s_add_u32 m0, s83, 0x24400\n"
"v_add_u32 v30, 0xfffe0000, v30\n"
"v_mfma_f32_16x16x32_bf16 v[168:171], a[232:235], a[200:203], v[168:171]\n"
"buffer_load_dwordx4 v37, s[52:55], s77 offen lds ;@F19\n"
"v_mfma_f32_16x16x32_bf16 v[172:175], a[232:235], a[204:207], v[172:175]\n"
"s_add_u32 m0, s83, 0x24800\n"
"v_add_u32 v31, 0xfffe0000, v31\n"
"v_mfma_f32_16x16x32_bf16 v[176:179], a[236:239], a[192:195], v[176:179]\n"
"buffer_load_dwordx4 v42, s[52:55], s78 offen lds ;@F19\n"
"v_mfma_f32_16x16x32_bf16 v[180:183], a[236:239], a[196:199], v[180:183]\n"
"s_add_u32 m0, s83, 0x24c00\n"
"v_add_u32 v32, 0xfffe0000, v32\n"
"v_mfma_f32_16x16x32_bf16 v[184:187], a[236:239], a[200:203], v[184:187]\n"
"buffer_load_dwordx4 v42, s[52:55], s79 offen lds ;@F19\n"
"v_mfma_f32_16x16x32_bf16 v[188:191], a[236:239], a[204:207], v[188:191]\n"
"v_add_u32 v33, 0xfffe0000, v33\n"
"v_add_u32 v37, 0x10000, v37\n"
"v_add_u32 v42, 0x10000, v42\n"
"s_waitcnt lgkmcnt(0)\n"
"v_mfma_f32_16x16x32_bf16 v[128:131], a[160:163], a[128:131], v[128:131]\n"
"ds_read_b64_tr_b16 a[224:225], v30 offset:0\n"
"ds_read_b64_tr_b16 a[226:227], v30 offset:1024\n"
"v_mfma_f32_16x16x32_bf16 v[132:135], a[160:163], a[132:135], v[132:135]\n"
"ds_read_b128 a[192:195], v25 offset:0\n"
"ds_read_b128 a[196:199], v25 offset:2048\n"
"v_mfma_f32_16x16x32_bf16 v[136:139], a[160:163], a[136:139], v[136:139]\n"
"ds_read_b128 a[200:203], v25 offset:4096\n"
"ds_read_b128 a[204:207], v25 offset:6144\n"
"v_mfma_f32_16x16x32_bf16 v[140:143], a[160:163], a[140:143], v[140:143]\n"
"ds_read_b64_tr_b16 a[228:229], v31 offset:0\n"
"ds_read_b64_tr_b16 a[230:231], v31 offset:1024\n"
"v_mfma_f32_16x16x32_bf16 v[144:147], a[164:167], a[128:131], v[144:147]\n"
"ds_read_b64_tr_b16 a[232:233], v32 offset:0\n"
"ds_read_b64_tr_b16 a[234:235], v32 offset:1024\n"
"v_mfma_f32_16x16x32_bf16 v[148:151], a[164:167], a[132:135], v[148:151]\n"
"ds_read_b64_tr_b16 a[236:237], v33 offset:0\n"
"ds_read_b64_tr_b16 a[238:239], v33 offset:1024\n"
"v_mfma_f32_16x16x32_bf16 v[152:155], a[164:167], a[136:139], v[152:155]\n"
"s_add_u32 m0, s82, 0x20000\n"
"v_add_u32 v24, 0x8000, v24\n"
"v_mfma_f32_16x16x32_bf16 v[156:159], a[164:167], a[140:143], v[156:159]\n"
"buffer_load_dwordx4 v35, s[60:63], s70 offen sc1 lds ;@F19\n"
"v_mfma_f32_16x16x32_bf16 v[160:163], a[168:171], a[128:131], v[160:163]\n"
"buffer_load_dwordx4 v35, s[60:63], s70 offen offset:1024 sc1 lds ;@F19\n"
"v_mfma_f32_16x16x32_bf16 v[164:167], a[168:171], a[132:135], v[164:167]\n"
"s_add_u32 m0, s82, 0x20800\n"
"v_add_u32 v26, 0x8000, v26\n"
"v_mfma_f32_16x16x32_bf16 v[168:171], a[168:171], a[136:139], v[168:171]\n"
"buffer_load_dwordx4 v35, s[60:63], s71 offen sc1 lds ;@F19\n"
"v_mfma_f32_16x16x32_bf16 v[172:175], a[168:171], a[140:143], v[172:175]\n"
"buffer_load_dwordx4 v35, s[60:63], s71 offen offset:1024 sc1 lds ;@F19\n"
"v_mfma_f32_16x16x32_bf16 v[176:179], a[172:175], a[128:131], v[176:179]\n"
"v_add_u32 v35, 0x800, v35\n"
"v_mfma_f32_16x16x32_bf16 v[180:183], a[172:175], a[132:135], v[180:183]\n"
"v_mfma_f32_16x16x32_bf16 v[184:187], a[172:175], a[136:139], v[184:187]\n"
"v_add_u32 v29, 0x8000, v29\n"
"v_mfma_f32_16x16x32_bf16 v[188:191], a[172:175], a[140:143], v[188:191]\n"
"v_add_u32 v27, 0x8000, v27\n"
"v_add_u32 v28, 0x8000, v28\n"
"s_waitcnt lgkmcnt(0)\n"
"v_mfma_f32_16x16x32_bf16 v[128:131], a[224:227], a[192:195], v[128:131]\n"
"s_waitcnt vmcnt(24)\n"
"s_barrier\n"
"v_mfma_f32_16x16x32_bf16 v[132:135], a[224:227], a[196:199], v[132:135]\n"
"ds_read_b64_tr_b16 a[160:161], v26 offset:0\n"
"ds_read_b64_tr_b16 a[162:163], v26 offset:1024\n"
"v_mfma_f32_16x16x32_bf16 v[136:139], a[224:227], a[200:203], v[136:139]\n"
"ds_read_b128 a[128:131], v24 offset:0\n"
"ds_read_b128 a[132:135], v24 offset:2048\n"
"v_mfma_f32_16x16x32_bf16 v[140:143], a[224:227], a[204:207], v[140:143]\n"
"ds_read_b128 a[136:139], v24 offset:4096\n"
"ds_read_b128 a[140:143], v24 offset:6144\n"
"v_mfma_f32_16x16x32_bf16 v[144:147], a[228:231], a[192:195], v[144:147]\n"
"ds_read_b64_tr_b16 a[164:165], v27 offset:0\n"
"ds_read_b64_tr_b16 a[166:167], v27 offset:1024\n"
"v_mfma_f32_16x16x32_bf16 v[148:151], a[228:231], a[196:199], v[148:151]\n"
"ds_read_b64_tr_b16 a[168:169], v28 offset:0\n"
"ds_read_b64_tr_b16 a[170:171], v28 offset:1024\n"
"v_mfma_f32_16x16x32_bf16 v[152:155], a[228:231], a[200:203], v[152:155]\n"
"ds_read_b64_tr_b16 a[172:173], v29 offset:0\n"
"ds_read_b64_tr_b16 a[174:175], v29 offset:1024\n"
"v_mfma_f32_16x16x32_bf16 v[156:159], a[228:231], a[204:207], v[156:159]\n"
"s_add_u32 m0, s83, 0x4000\n"
"v_add_u32 v25, 0x8000, v25\n"
"v_mfma_f32_16x16x32_bf16 v[160:163], a[232:235], a[192:195], v[160:163]\n"
"buffer_load_dwordx4 v37, s[52:55], s76 offen lds ;@F20\n"
"v_mfma_f32_16x16x32_bf16 v[164:167], a[232:235], a[196:199], v[164:167]\n"
"s_add_u32 m0, s83, 0x4400\n"
"v_add_u32 v30, 0x8000, v30\n"
"v_mfma_f32_16x16x32_bf16 v[168:171], a[232:235], a[200:203], v[168:171]\n"
"buffer_load_dwordx4 v37, s[52:55], s77 offen lds ;@F20\n"
"v_mfma_f32_16x16x32_bf16 v[172:175], a[232:235], a[204:207], v[172:175]\n"
"s_add_u32 m0, s83, 0x4800\n"
"v_add_u32 v31, 0x8000, v31\n"
"v_mfma_f32_16x16x32_bf16 v[176:179], a[236:239], a[192:195], v[176:179]\n"
"buffer_load_dwordx4 v42, s[52:55], s78 offen lds ;@F20\n"
"v_mfma_f32_16x16x32_bf16 v[180:183], a[236:239], a[196:199], v[180:183]\n"
"s_add_u32 m0, s83, 0x4c00\n"
"v_add_u32 v32, 0x8000, v32\n"
"v_mfma_f32_16x16x32_bf16 v[184:187], a[236:239], a[200:203], v[184:187]\n"
"buffer_load_dwordx4 v42, s[52:55], s79 offen lds ;@F20\n"
"v_mfma_f32_16x16x32_bf16 v[188:191], a[236:239], a[204:207], v[188:191]\n"
"v_add_u32 v33, 0x8000, v33\n"
"v_add_u32 v37, 0x10000, v37\n"
"v_add_u32 v42, 0x10000, v42\n"
"s_waitcnt lgkmcnt(0)\n"
"v_mfma_f32_16x16x32_bf16 v[128:131], a[160:163], a[128:131], v[128:131]\n"
"ds_read_b64_tr_b16 a[224:225], v30 offset:0\n"
"ds_read_b64_tr_b16 a[226:227], v30 offset:1024\n"
"v_mfma_f32_16x16x32_bf16 v[132:135], a[160:163], a[132:135], v[132:135]\n"
"ds_read_b128 a[192:195], v25 offset:0\n"
"ds_read_b128 a[196:199], v25 offset:2048\n"
"v_mfma_f32_16x16x32_bf16 v[136:139], a[160:163], a[136:139], v[136:139]\n"
"ds_read_b128 a[200:203], v25 offset:4096\n"
"ds_read_b128 a[204:207], v25 offset:6144\n"
"v_mfma_f32_16x16x32_bf16 v[140:143], a[160:163], a[140:143], v[140:143]\n"
"ds_read_b64_tr_b16 a[228:229], v31 offset:0\n"
"ds_read_b64_tr_b16 a[230:231], v31 offset:1024\n"
"v_mfma_f32_16x16x32_bf16 v[144:147], a[164:167], a[128:131], v[144:147]\n"
"ds_read_b64_tr_b16 a[232:233], v32 offset:0\n"
"ds_read_b64_tr_b16 a[234:235], v32 offset:1024\n"
"v_mfma_f32_16x16x32_bf16 v[148:151], a[164:167], a[132:135], v[148:151]\n"
"ds_read_b64_tr_b16 a[236:237], v33 offset:0\n"
"ds_read_b64_tr_b16 a[238:239], v33 offset:1024\n"
"v_mfma_f32_16x16x32_bf16 v[152:155], a[164:167], a[136:139], v[152:155]\n"
"s_add_u32 m0, s82, 0x0\n"
"v_add_u32 v24, 0x8000, v24\n"
"v_mfma_f32_16x16x32_bf16 v[156:159], a[164:167], a[140:143], v[156:159]\n"
"buffer_load_dwordx4 v35, s[60:63], s70 offen sc1 lds ;@F20\n"
"v_mfma_f32_16x16x32_bf16 v[160:163], a[168:171], a[128:131], v[160:163]\n"
"buffer_load_dwordx4 v35, s[60:63], s70 offen offset:1024 sc1 lds ;@F20\n"
"v_mfma_f32_16x16x32_bf16 v[164:167], a[168:171], a[132:135], v[164:167]\n"
"s_add_u32 m0, s82, 0x800\n"
"v_add_u32 v26, 0x8000, v26\n"
"v_mfma_f32_16x16x32_bf16 v[168:171], a[168:171], a[136:139], v[168:171]\n"
"buffer_load_dwordx4 v35, s[60:63], s71 offen sc1 lds ;@F20\n"
"v_mfma_f32_16x16x32_bf16 v[172:175], a[168:171], a[140:143], v[172:175]\n"
"buffer_load_dwordx4 v35, s[60:63], s71 offen offset:1024 sc1 lds ;@F20\n"
"v_mfma_f32_16x16x32_bf16 v[176:179], a[172:175], a[128:131], v[176:179]\n"
"v_add_u32 v35, 0x800, v35\n"
"v_mfma_f32_16x16x32_bf16 v[180:183], a[172:175], a[132:135], v[180:183]\n"
"v_mfma_f32_16x16x32_bf16 v[184:187], a[172:175], a[136:139], v[184:187]\n"
"v_add_u32 v29, 0x8000, v29\n"
"v_mfma_f32_16x16x32_bf16 v[188:191], a[172:175], a[140:143], v[188:191]\n"
"v_add_u32 v27, 0x8000, v27\n"
"v_add_u32 v28, 0x8000, v28\n"
"s_waitcnt lgkmcnt(0)\n"
"v_mfma_f32_16x16x32_bf16 v[128:131], a[224:227], a[192:195], v[128:131]\n"
"s_waitcnt vmcnt(24)\n"
"s_barrier\n"
"v_mfma_f32_16x16x32_bf16 v[132:135], a[224:227], a[196:199], v[132:135]\n"
"ds_read_b64_tr_b16 a[160:161], v26 offset:0\n"
"ds_read_b64_tr_b16 a[162:163], v26 offset:1024\n"
"v_mfma_f32_16x16x32_bf16 v[136:139], a[224:227], a[200:203], v[136:139]\n"
"ds_read_b128 a[128:131], v24 offset:0\n"
"ds_read_b128 a[132:135], v24 offset:2048\n"
"v_mfma_f32_16x16x32_bf16 v[140:143], a[224:227], a[204:207], v[140:143]\n"
"ds_read_b128 a[136:139], v24 offset:4096\n"
"ds_read_b128 a[140:143], v24 offset:6144\n"
"v_mfma_f32_16x16x32_bf16 v[144:147], a[228:231], a[192:195], v[144:147]\n"
"ds_read_b64_tr_b16 a[164:165], v27 offset:0\n"
"ds_read_b64_tr_b16 a[166:167], v27 offset:1024\n"
"v_mfma_f32_16x16x32_bf16 v[148:151], a[228:231], a[196:199], v[148:151]\n"
"ds_read_b64_tr_b16 a[168:169], v28 offset:0\n"
"ds_read_b64_tr_b16 a[170:171], v28 offset:1024\n"
"v_mfma_f32_16x16x32_bf16 v[152:155], a[228:231], a[200:203], v[152:155]\n"
"ds_read_b64_tr_b16 a[172:173], v29 offset:0\n"
"ds_read_b64_tr_b16 a[174:175], v29 offset:1024\n"
"v_mfma_f32_16x16x32_bf16 v[156:159], a[228:231], a[204:207], v[156:159]\n"
"s_add_u32 m0, s83, 0xc000\n"
"v_add_u32 v25, 0x8000, v25\n"
"v_mfma_f32_16x16x32_bf16 v[160:163], a[232:235], a[192:195], v[160:163]\n"
"buffer_load_dwordx4 v37, s[52:55], s76 offen lds ;@F21\n"
"v_mfma_f32_16x16x32_bf16 v[164:167], a[232:235], a[196:199], v[164:167]\n"
"s_add_u32 m0, s83, 0xc400\n"
"v_add_u32 v30, 0x8000, v30\n"
"v_mfma_f32_16x16x32_bf16 v[168:171], a[232:235], a[200:203], v[168:171]\n"
"buffer_load_dwordx4 v37, s[52:55], s77 offen lds ;@F21\n"
"v_mfma_f32_16x16x32_bf16 v[172:175], a[232:235], a[204:207], v[172:175]\n"
"s_add_u32 m0, s83, 0xc800\n"
"v_add_u32 v31, 0x8000, v31\n"
"v_mfma_f32_16x16x32_bf16 v[176:179], a[236:239], a[192:195], v[176:179]\n"
"buffer_load_dwordx4 v42, s[52:55], s78 offen lds ;@F21\n"
"v_mfma_f32_16x16x32_bf16 v[180:183], a[236:239], a[196:199], v[180:183]\n"
"s_add_u32 m0, s83, 0xcc00\n"
"v_add_u32 v32, 0x8000, v32\n"
"v_mfma_f32_16x16x32_bf16 v[184:187], a[236:239], a[200:203], v[184:187]\n"
"buffer_load_dwordx4 v42, s[52:55], s79 offen lds ;@F21\n"
"v_mfma_f32_16x16x32_bf16 v[188:191], a[236:239], a[204:207], v[188:191]\n"
"v_add_u32 v33, 0x8000, v33\n"
"v_add_u32 v37, 0x10000, v37\n"
"v_add_u32 v42, 0x10000, v42\n"
"s_waitcnt lgkmcnt(0)\n"
"v_mfma_f32_16x16x32_bf16 v[128:131], a[160:163], a[128:131], v[128:131]\n"
"ds_read_b64_tr_b16 a[224:225], v30 offset:0\n"
"ds_read_b64_tr_b16 a[226:227], v30 offset:1024\n"
"v_mfma_f32_16x16x32_bf16 v[132:135], a[160:163], a[132:135], v[132:135]\n"
"ds_read_b128 a[192:195], v25 offset:0\n"
"ds_read_b128 a[196:199], v25 offset:2048\n"
"v_mfma_f32_16x16x32_bf16 v[136:139], a[160:163], a[136:139], v[136:139]\n"
"ds_read_b128 a[200:203], v25 offset:4096\n"
"ds_read_b128 a[204:207], v25 offset:6144\n"
"v_mfma_f32_16x16x32_bf16 v[140:143], a[160:163], a[140:143], v[140:143]\n"
"ds_read_b64_tr_b16 a[228:229], v31 offset:0\n"
"ds_read_b64_tr_b16 a[230:231], v31 offset:1024\n"
"v_mfma_f32_16x16x32_bf16 v[144:147], a[164:167], a[128:131], v[144:147]\n"
"ds_read_b64_tr_b16 a[232:233], v32 offset:0\n"
"ds_read_b64_tr_b16 a[234:235], v32 offset:1024\n"
"v_mfma_f32_16x16x32_bf16 v[148:151], a[164:167], a[132:135], v[148:151]\n"
"ds_read_b64_tr_b16 a[236:237], v33 offset:0\n"
"ds_read_b64_tr_b16 a[238:239], v33 offset:1024\n"
"v_mfma_f32_16x16x32_bf16 v[152:155], a[164:167], a[136:139], v[152:155]\n"
"s_add_u32 m0, s82, 0x8000\n"
"v_add_u32 v24, 0x8000, v24\n"
"v_mfma_f32_16x16x32_bf16 v[156:159], a[164:167], a[140:143], v[156:159]\n"
"buffer_load_dwordx4 v35, s[60:63], s70 offen sc1 lds ;@F21\n"
"v_mfma_f32_16x16x32_bf16 v[160:163], a[168:171], a[128:131], v[160:163]\n"
"buffer_load_dwordx4 v35, s[60:63], s70 offen offset:1024 sc1 lds ;@F21\n"
"v_mfma_f32_16x16x32_bf16 v[164:167], a[168:171], a[132:135], v[164:167]\n"
"s_add_u32 m0, s82, 0x8800\n"
"v_add_u32 v26, 0x8000, v26\n"
"v_mfma_f32_16x16x32_bf16 v[168:171], a[168:171], a[136:139], v[168:171]\n"
"buffer_load_dwordx4 v35, s[60:63], s71 offen sc1 lds ;@F21\n"
"v_mfma_f32_16x16x32_bf16 v[172:175], a[168:171], a[140:143], v[172:175]\n"
"buffer_load_dwordx4 v35, s[60:63], s71 offen offset:1024 sc1 lds ;@F21\n"
"v_mfma_f32_16x16x32_bf16 v[176:179], a[172:175], a[128:131], v[176:179]\n"
"v_add_u32 v35, 0x800, v35\n"
"v_mfma_f32_16x16x32_bf16 v[180:183], a[172:175], a[132:135], v[180:183]\n"
"v_mfma_f32_16x16x32_bf16 v[184:187], a[172:175], a[136:139], v[184:187]\n"
"v_add_u32 v29, 0x8000, v29\n"
"v_mfma_f32_16x16x32_bf16 v[188:191], a[172:175], a[140:143], v[188:191]\n"
"v_add_u32 v27, 0x8000, v27\n"
"v_add_u32 v28, 0x8000, v28\n"
"s_waitcnt lgkmcnt(0)\n"
"v_mfma_f32_16x16x32_bf16 v[128:131], a[224:227], a[192:195], v[128:131]\n"
"s_waitcnt vmcnt(24)\n"
"s_barrier\n"
"v_mfma_f32_16x16x32_bf16 v[132:135], a[224:227], a[196:199], v[132:135]\n"
"ds_read_b64_tr_b16 a[160:161], v26 offset:0\n"
"ds_read_b64_tr_b16 a[162:163], v26 offset:1024\n"
"v_mfma_f32_16x16x32_bf16 v[136:139], a[224:227], a[200:203], v[136:139]\n"
"ds_read_b128 a[128:131], v24 offset:0\n"
"ds_read_b128 a[132:135], v24 offset:2048\n"
"v_mfma_f32_16x16x32_bf16 v[140:143], a[224:227], a[204:207], v[140:143]\n"
"ds_read_b128 a[136:139], v24 offset:4096\n"
"ds_read_b128 a[140:143], v24 offset:6144\n"
"v_mfma_f32_16x16x32_bf16 v[144:147], a[228:231], a[192:195], v[144:147]\n"
"ds_read_b64_tr_b16 a[164:165], v27 offset:0\n"
"ds_read_b64_tr_b16 a[166:167], v27 offset:1024\n"
"v_mfma_f32_16x16x32_bf16 v[148:151], a[228:231], a[196:199], v[148:151]\n"
"ds_read_b64_tr_b16 a[168:169], v28 offset:0\n"
"ds_read_b64_tr_b16 a[170:171], v28 offset:1024\n"
"v_mfma_f32_16x16x32_bf16 v[152:155], a[228:231], a[200:203], v[152:155]\n"
"ds_read_b64_tr_b16 a[172:173], v29 offset:0\n"
"ds_read_b64_tr_b16 a[174:175], v29 offset:1024\n"
"v_mfma_f32_16x16x32_bf16 v[156:159], a[228:231], a[204:207], v[156:159]\n"
"s_add_u32 m0, s83, 0x14000\n"
"v_add_u32 v25, 0x8000, v25\n"
"v_mfma_f32_16x16x32_bf16 v[160:163], a[232:235], a[192:195], v[160:163]\n"
"buffer_load_dwordx4 v37, s[52:55], s76 offen lds ;@F22\n"
"v_mfma_f32_16x16x32_bf16 v[164:167], a[232:235], a[196:199], v[164:167]\n"
"s_add_u32 m0, s83, 0x14400\n"
"v_add_u32 v30, 0x8000, v30\n"
"v_mfma_f32_16x16x32_bf16 v[168:171], a[232:235], a[200:203], v[168:171]\n"
"buffer_load_dwordx4 v37, s[52:55], s77 offen lds ;@F22\n"
"v_mfma_f32_16x16x32_bf16 v[172:175], a[232:235], a[204:207], v[172:175]\n"
"s_add_u32 m0, s83, 0x14800\n"
"v_add_u32 v31, 0x8000, v31\n"
"v_mfma_f32_16x16x32_bf16 v[176:179], a[236:239], a[192:195], v[176:179]\n"
"buffer_load_dwordx4 v42, s[52:55], s78 offen lds ;@F22\n"
"v_mfma_f32_16x16x32_bf16 v[180:183], a[236:239], a[196:199], v[180:183]\n"
"s_add_u32 m0, s83, 0x14c00\n"
"v_add_u32 v32, 0x8000, v32\n"
"v_mfma_f32_16x16x32_bf16 v[184:187], a[236:239], a[200:203], v[184:187]\n"
"buffer_load_dwordx4 v42, s[52:55], s79 offen lds ;@F22\n"
"v_mfma_f32_16x16x32_bf16 v[188:191], a[236:239], a[204:207], v[188:191]\n"
"v_add_u32 v33, 0x8000, v33\n"
"v_add_u32 v37, 0x10000, v37\n"
"v_add_u32 v42, 0x10000, v42\n"
"s_waitcnt lgkmcnt(0)\n"
"v_mfma_f32_16x16x32_bf16 v[128:131], a[160:163], a[128:131], v[128:131]\n"
"ds_read_b64_tr_b16 a[224:225], v30 offset:0\n"
"ds_read_b64_tr_b16 a[226:227], v30 offset:1024\n"
"v_mfma_f32_16x16x32_bf16 v[132:135], a[160:163], a[132:135], v[132:135]\n"
"ds_read_b128 a[192:195], v25 offset:0\n"
"ds_read_b128 a[196:199], v25 offset:2048\n"
"v_mfma_f32_16x16x32_bf16 v[136:139], a[160:163], a[136:139], v[136:139]\n"
"ds_read_b128 a[200:203], v25 offset:4096\n"
"ds_read_b128 a[204:207], v25 offset:6144\n"
"v_mfma_f32_16x16x32_bf16 v[140:143], a[160:163], a[140:143], v[140:143]\n"
"ds_read_b64_tr_b16 a[228:229], v31 offset:0\n"
"ds_read_b64_tr_b16 a[230:231], v31 offset:1024\n"
"v_mfma_f32_16x16x32_bf16 v[144:147], a[164:167], a[128:131], v[144:147]\n"
"ds_read_b64_tr_b16 a[232:233], v32 offset:0\n"
"ds_read_b64_tr_b16 a[234:235], v32 offset:1024\n"
"v_mfma_f32_16x16x32_bf16 v[148:151], a[164:167], a[132:135], v[148:151]\n"
"ds_read_b64_tr_b16 a[236:237], v33 offset:0\n"
"ds_read_b64_tr_b16 a[238:239], v33 offset:1024\n"
"v_mfma_f32_16x16x32_bf16 v[152:155], a[164:167], a[136:139], v[152:155]\n"
"s_add_u32 m0, s82, 0x10000\n"
"v_add_u32 v24, 0x8000, v24\n"
"v_mfma_f32_16x16x32_bf16 v[156:159], a[164:167], a[140:143], v[156:159]\n"
"buffer_load_dwordx4 v35, s[60:63], s70 offen sc1 lds ;@F22\n"
"v_mfma_f32_16x16x32_bf16 v[160:163], a[168:171], a[128:131], v[160:163]\n"
"buffer_load_dwordx4 v35, s[60:63], s70 offen offset:1024 sc1 lds ;@F22\n"
"v_mfma_f32_16x16x32_bf16 v[164:167], a[168:171], a[132:135], v[164:167]\n"
"s_add_u32 m0, s82, 0x10800\n"
"v_add_u32 v26, 0x8000, v26\n"
"v_mfma_f32_16x16x32_bf16 v[168:171], a[168:171], a[136:139], v[168:171]\n"
"buffer_load_dwordx4 v35, s[60:63], s71 offen sc1 lds ;@F22\n"
"v_mfma_f32_16x16x32_bf16 v[172:175], a[168:171], a[140:143], v[172:175]\n"
"buffer_load_dwordx4 v35, s[60:63], s71 offen offset:1024 sc1 lds ;@F22\n"
"v_mfma_f32_16x16x32_bf16 v[176:179], a[172:175], a[128:131], v[176:179]\n"
"v_add_u32 v35, 0x800, v35\n"
"v_mfma_f32_16x16x32_bf16 v[180:183], a[172:175], a[132:135], v[180:183]\n"
"v_mfma_f32_16x16x32_bf16 v[184:187], a[172:175], a[136:139], v[184:187]\n"
"v_add_u32 v29, 0x8000, v29\n"
"v_mfma_f32_16x16x32_bf16 v[188:191], a[172:175], a[140:143], v[188:191]\n"
"v_add_u32 v27, 0x8000, v27\n"
"v_add_u32 v28, 0x8000, v28\n"
"s_waitcnt lgkmcnt(0)\n"
"v_mfma_f32_16x16x32_bf16 v[128:131], a[224:227], a[192:195], v[128:131]\n"
"s_waitcnt vmcnt(24)\n"
"s_barrier\n"
"v_mfma_f32_16x16x32_bf16 v[132:135], a[224:227], a[196:199], v[132:135]\n"
"ds_read_b64_tr_b16 a[160:161], v26 offset:0\n"
"ds_read_b64_tr_b16 a[162:163], v26 offset:1024\n"
"v_mfma_f32_16x16x32_bf16 v[136:139], a[224:227], a[200:203], v[136:139]\n"
"ds_read_b128 a[128:131], v24 offset:0\n"
"ds_read_b128 a[132:135], v24 offset:2048\n"
"v_mfma_f32_16x16x32_bf16 v[140:143], a[224:227], a[204:207], v[140:143]\n"
"ds_read_b128 a[136:139], v24 offset:4096\n"
"ds_read_b128 a[140:143], v24 offset:6144\n"
"v_mfma_f32_16x16x32_bf16 v[144:147], a[228:231], a[192:195], v[144:147]\n"
"ds_read_b64_tr_b16 a[164:165], v27 offset:0\n"
"ds_read_b64_tr_b16 a[166:167], v27 offset:1024\n"
"v_mfma_f32_16x16x32_bf16 v[148:151], a[228:231], a[196:199], v[148:151]\n"
"ds_read_b64_tr_b16 a[168:169], v28 offset:0\n"
"ds_read_b64_tr_b16 a[170:171], v28 offset:1024\n"
"v_mfma_f32_16x16x32_bf16 v[152:155], a[228:231], a[200:203], v[152:155]\n"
"ds_read_b64_tr_b16 a[172:173], v29 offset:0\n"
"ds_read_b64_tr_b16 a[174:175], v29 offset:1024\n"
"v_mfma_f32_16x16x32_bf16 v[156:159], a[228:231], a[204:207], v[156:159]\n"
"v_mfma_f32_16x16x32_bf16 v[160:163], a[232:235], a[192:195], v[160:163]\n"
"v_mfma_f32_16x16x32_bf16 v[164:167], a[232:235], a[196:199], v[164:167]\n"
"v_mfma_f32_16x16x32_bf16 v[168:171], a[232:235], a[200:203], v[168:171]\n"
"v_mfma_f32_16x16x32_bf16 v[172:175], a[232:235], a[204:207], v[172:175]\n"
"v_mfma_f32_16x16x32_bf16 v[176:179], a[236:239], a[192:195], v[176:179]\n"
"v_mfma_f32_16x16x32_bf16 v[180:183], a[236:239], a[196:199], v[180:183]\n"
"v_add_u32 v33, 0x8000, v33\n"
"v_mfma_f32_16x16x32_bf16 v[184:187], a[236:239], a[200:203], v[184:187]\n"
"v_add_u32 v31, 0x8000, v31\n"
"v_add_u32 v32, 0x8000, v32\n"
"v_mfma_f32_16x16x32_bf16 v[188:191], a[236:239], a[204:207], v[188:191]\n"
"v_add_u32 v25, 0x8000, v25\n"
"v_add_u32 v30, 0x8000, v30\n"
"s_waitcnt lgkmcnt(0)\n"
"v_mfma_f32_16x16x32_bf16 v[128:131], a[160:163], a[128:131], v[128:131]\n"
"ds_read_b64_tr_b16 a[224:225], v30 offset:0\n"
"ds_read_b64_tr_b16 a[226:227], v30 offset:1024\n"
"v_mfma_f32_16x16x32_bf16 v[132:135], a[160:163], a[132:135], v[132:135]\n"
"ds_read_b128 a[192:195], v25 offset:0\n"
"ds_read_b128 a[196:199], v25 offset:2048\n"
"v_mfma_f32_16x16x32_bf16 v[136:139], a[160:163], a[136:139], v[136:139]\n"
"ds_read_b128 a[200:203], v25 offset:4096\n"
"ds_read_b128 a[204:207], v25 offset:6144\n"
"v_mfma_f32_16x16x32_bf16 v[140:143], a[160:163], a[140:143], v[140:143]\n"
"ds_read_b64_tr_b16 a[228:229], v31 offset:0\n"
"ds_read_b64_tr_b16 a[230:231], v31 offset:1024\n"
"v_mfma_f32_16x16x32_bf16 v[144:147], a[164:167], a[128:131], v[144:147]\n"
"ds_read_b64_tr_b16 a[232:233], v32 offset:0\n"
"ds_read_b64_tr_b16 a[234:235], v32 offset:1024\n"
"v_mfma_f32_16x16x32_bf16 v[148:151], a[164:167], a[132:135], v[148:151]\n"
"ds_read_b64_tr_b16 a[236:237], v33 offset:0\n"
"ds_read_b64_tr_b16 a[238:239], v33 offset:1024\n"
"v_mfma_f32_16x16x32_bf16 v[152:155], a[164:167], a[136:139], v[152:155]\n"
"v_mfma_f32_16x16x32_bf16 v[156:159], a[164:167], a[140:143], v[156:159]\n"
"v_mfma_f32_16x16x32_bf16 v[160:163], a[168:171], a[128:131], v[160:163]\n"
"v_mfma_f32_16x16x32_bf16 v[164:167], a[168:171], a[132:135], v[164:167]\n"
"v_mfma_f32_16x16x32_bf16 v[168:171], a[168:171], a[136:139], v[168:171]\n"
"v_mfma_f32_16x16x32_bf16 v[172:175], a[168:171], a[140:143], v[172:175]\n"
"v_mfma_f32_16x16x32_bf16 v[176:179], a[172:175], a[128:131], v[176:179]\n"
"v_mfma_f32_16x16x32_bf16 v[180:183], a[172:175], a[132:135], v[180:183]\n"
"v_add_u32 v29, 0xfffe0000, v29\n"
"v_mfma_f32_16x16x32_bf16 v[184:187], a[172:175], a[136:139], v[184:187]\n"
"v_add_u32 v27, 0xfffe0000, v27\n"
"v_add_u32 v28, 0xfffe0000, v28\n"
"v_mfma_f32_16x16x32_bf16 v[188:191], a[172:175], a[140:143], v[188:191]\n"
"v_add_u32 v24, 0xfffe0000, v24\n"
"v_add_u32 v26, 0xfffe0000, v26\n"
"s_waitcnt lgkmcnt(0)\n"
"v_mfma_f32_16x16x32_bf16 v[128:131], a[224:227], a[192:195], v[128:131]\n"
"s_waitcnt vmcnt(16)\n"
"s_barrier\n"
"v_mfma_f32_16x16x32_bf16 v[132:135], a[224:227], a[196:199], v[132:135]\n"
"ds_read_b64_tr_b16 a[160:161], v26 offset:0\n"
"ds_read_b64_tr_b16 a[162:163], v26 offset:1024\n"
"v_mfma_f32_16x16x32_bf16 v[136:139], a[224:227], a[200:203], v[136:139]\n"
"ds_read_b128 a[128:131], v24 offset:0\n"
"ds_read_b128 a[132:135], v24 offset:2048\n"
"v_mfma_f32_16x16x32_bf16 v[140:143], a[224:227], a[204:207], v[140:143]\n"
"ds_read_b128 a[136:139], v24 offset:4096\n"
"ds_read_b128 a[140:143], v24 offset:6144\n"
"v_mfma_f32_16x16x32_bf16 v[144:147], a[228:231], a[192:195], v[144:147]\n"
"ds_read_b64_tr_b16 a[164:165], v27 offset:0\n"
"ds_read_b64_tr_b16 a[166:167], v27 offset:1024\n"
"v_mfma_f32_16x16x32_bf16 v[148:151], a[228:231], a[196:199], v[148:151]\n"
"ds_read_b64_tr_b16 a[168:169], v28 offset:0\n"
"ds_read_b64_tr_b16 a[170:171], v28 offset:1024\n"
"v_mfma_f32_16x16x32_bf16 v[152:155], a[228:231], a[200:203], v[152:155]\n"
"ds_read_b64_tr_b16 a[172:173], v29 offset:0\n"
"ds_read_b64_tr_b16 a[174:175], v29 offset:1024\n"
"v_mfma_f32_16x16x32_bf16 v[156:159], a[228:231], a[204:207], v[156:159]\n"
"v_mfma_f32_16x16x32_bf16 v[160:163], a[232:235], a[192:195], v[160:163]\n"
"v_mfma_f32_16x16x32_bf16 v[164:167], a[232:235], a[196:199], v[164:167]\n"
"v_mfma_f32_16x16x32_bf16 v[168:171], a[232:235], a[200:203], v[168:171]\n"
"v_mfma_f32_16x16x32_bf16 v[172:175], a[232:235], a[204:207], v[172:175]\n"
"v_mfma_f32_16x16x32_bf16 v[176:179], a[236:239], a[192:195], v[176:179]\n"
"v_mfma_f32_16x16x32_bf16 v[180:183], a[236:239], a[196:199], v[180:183]\n"
"v_add_u32 v33, 0xfffe0000, v33\n"
"v_mfma_f32_16x16x32_bf16 v[184:187], a[236:239], a[200:203], v[184:187]\n"
"v_add_u32 v31, 0xfffe0000, v31\n"
"v_add_u32 v32, 0xfffe0000, v32\n"
"v_mfma_f32_16x16x32_bf16 v[188:191], a[236:239], a[204:207], v[188:191]\n"
"v_add_u32 v25, 0xfffe0000, v25\n"
"v_add_u32 v30, 0xfffe0000, v30\n"
"s_waitcnt lgkmcnt(0)\n"
"v_mfma_f32_16x16x32_bf16 v[128:131], a[160:163], a[128:131], v[128:131]\n"
"ds_read_b64_tr_b16 a[224:225], v30 offset:0\n"
"ds_read_b64_tr_b16 a[226:227], v30 offset:1024\n"
"v_mfma_f32_16x16x32_bf16 v[132:135], a[160:163], a[132:135], v[132:135]\n"
"ds_read_b128 a[192:195], v25 offset:0\n"
"ds_read_b128 a[196:199], v25 offset:2048\n"
"v_mfma_f32_16x16x32_bf16 v[136:139], a[160:163], a[136:139], v[136:139]\n"
"ds_read_b128 a[200:203], v25 offset:4096\n"
"ds_read_b128 a[204:207], v25 offset:6144\n"
"v_mfma_f32_16x16x32_bf16 v[140:143], a[160:163], a[140:143], v[140:143]\n"
"ds_read_b64_tr_b16 a[228:229], v31 offset:0\n"
"ds_read_b64_tr_b16 a[230:231], v31 offset:1024\n"
"v_mfma_f32_16x16x32_bf16 v[144:147], a[164:167], a[128:131], v[144:147]\n"
"ds_read_b64_tr_b16 a[232:233], v32 offset:0\n"
"ds_read_b64_tr_b16 a[234:235], v32 offset:1024\n"
"v_mfma_f32_16x16x32_bf16 v[148:151], a[164:167], a[132:135], v[148:151]\n"
"ds_read_b64_tr_b16 a[236:237], v33 offset:0\n"
"ds_read_b64_tr_b16 a[238:239], v33 offset:1024\n"
"v_mfma_f32_16x16x32_bf16 v[152:155], a[164:167], a[136:139], v[152:155]\n"
"v_mfma_f32_16x16x32_bf16 v[156:159], a[164:167], a[140:143], v[156:159]\n"
"v_mfma_f32_16x16x32_bf16 v[160:163], a[168:171], a[128:131], v[160:163]\n"
"v_mfma_f32_16x16x32_bf16 v[164:167], a[168:171], a[132:135], v[164:167]\n"
"v_mfma_f32_16x16x32_bf16 v[168:171], a[168:171], a[136:139], v[168:171]\n"
"v_mfma_f32_16x16x32_bf16 v[172:175], a[168:171], a[140:143], v[172:175]\n"
"v_mfma_f32_16x16x32_bf16 v[176:179], a[172:175], a[128:131], v[176:179]\n"
"v_mfma_f32_16x16x32_bf16 v[180:183], a[172:175], a[132:135], v[180:183]\n"
"v_add_u32 v29, 0x8000, v29\n"
"v_mfma_f32_16x16x32_bf16 v[184:187], a[172:175], a[136:139], v[184:187]\n"
"v_add_u32 v27, 0x8000, v27\n"
"v_add_u32 v28, 0x8000, v28\n"
"v_mfma_f32_16x16x32_bf16 v[188:191], a[172:175], a[140:143], v[188:191]\n"
"v_add_u32 v24, 0x8000, v24\n"
"v_add_u32 v26, 0x8000, v26\n"
"s_waitcnt lgkmcnt(0)\n"
"v_mfma_f32_16x16x32_bf16 v[128:131], a[224:227], a[192:195], v[128:131]\n"
"s_waitcnt vmcnt(8)\n"
"s_barrier\n"
"v_mfma_f32_16x16x32_bf16 v[132:135], a[224:227], a[196:199], v[132:135]\n"
"ds_read_b64_tr_b16 a[160:161], v26 offset:0\n"
"ds_read_b64_tr_b16 a[162:163], v26 offset:1024\n"
"v_mfma_f32_16x16x32_bf16 v[136:139], a[224:227], a[200:203], v[136:139]\n"
"ds_read_b128 a[128:131], v24 offset:0\n"
"ds_read_b128 a[132:135], v24 offset:2048\n"
"v_mfma_f32_16x16x32_bf16 v[140:143], a[224:227], a[204:207], v[140:143]\n"
"ds_read_b128 a[136:139], v24 offset:4096\n"
"ds_read_b128 a[140:143], v24 offset:6144\n"
"v_mfma_f32_16x16x32_bf16 v[144:147], a[228:231], a[192:195], v[144:147]\n"
"ds_read_b64_tr_b16 a[164:165], v27 offset:0\n"
"ds_read_b64_tr_b16 a[166:167], v27 offset:1024\n"
"v_mfma_f32_16x16x32_bf16 v[148:151], a[228:231], a[196:199], v[148:151]\n"
"ds_read_b64_tr_b16 a[168:169], v28 offset:0\n"
"ds_read_b64_tr_b16 a[170:171], v28 offset:1024\n"
"v_mfma_f32_16x16x32_bf16 v[152:155], a[228:231], a[200:203], v[152:155]\n"
"ds_read_b64_tr_b16 a[172:173], v29 offset:0\n"
"ds_read_b64_tr_b16 a[174:175], v29 offset:1024\n"
"v_mfma_f32_16x16x32_bf16 v[156:159], a[228:231], a[204:207], v[156:159]\n"
"v_mfma_f32_16x16x32_bf16 v[160:163], a[232:235], a[192:195], v[160:163]\n"
"v_mfma_f32_16x16x32_bf16 v[164:167], a[232:235], a[196:199], v[164:167]\n"
"v_mfma_f32_16x16x32_bf16 v[168:171], a[232:235], a[200:203], v[168:171]\n"
"v_mfma_f32_16x16x32_bf16 v[172:175], a[232:235], a[204:207], v[172:175]\n"
"v_mfma_f32_16x16x32_bf16 v[176:179], a[236:239], a[192:195], v[176:179]\n"
"v_mfma_f32_16x16x32_bf16 v[180:183], a[236:239], a[196:199], v[180:183]\n"
"v_add_u32 v33, 0x8000, v33\n"
"v_mfma_f32_16x16x32_bf16 v[184:187], a[236:239], a[200:203], v[184:187]\n"
"v_add_u32 v31, 0x8000, v31\n"
"v_add_u32 v32, 0x8000, v32\n"
"v_mfma_f32_16x16x32_bf16 v[188:191], a[236:239], a[204:207], v[188:191]\n"
"v_add_u32 v25, 0x8000, v25\n"
"v_add_u32 v30, 0x8000, v30\n"
"s_waitcnt lgkmcnt(0)\n"
"v_mfma_f32_16x16x32_bf16 v[128:131], a[160:163], a[128:131], v[128:131]\n"
"ds_read_b64_tr_b16 a[224:225], v30 offset:0\n"
"ds_read_b64_tr_b16 a[226:227], v30 offset:1024\n"
"v_mfma_f32_16x16x32_bf16 v[132:135], a[160:163], a[132:135], v[132:135]\n"
"ds_read_b128 a[192:195], v25 offset:0\n"
"ds_read_b128 a[196:199], v25 offset:2048\n"
"v_mfma_f32_16x16x32_bf16 v[136:139], a[160:163], a[136:139], v[136:139]\n"
"ds_read_b128 a[200:203], v25 offset:4096\n"
"ds_read_b128 a[204:207], v25 offset:6144\n"
"v_mfma_f32_16x16x32_bf16 v[140:143], a[160:163], a[140:143], v[140:143]\n"
"ds_read_b64_tr_b16 a[228:229], v31 offset:0\n"
"ds_read_b64_tr_b16 a[230:231], v31 offset:1024\n"
"v_mfma_f32_16x16x32_bf16 v[144:147], a[164:167], a[128:131], v[144:147]\n"
"ds_read_b64_tr_b16 a[232:233], v32 offset:0\n"
"ds_read_b64_tr_b16 a[234:235], v32 offset:1024\n"
"v_mfma_f32_16x16x32_bf16 v[148:151], a[164:167], a[132:135], v[148:151]\n"
"ds_read_b64_tr_b16 a[236:237], v33 offset:0\n"
"ds_read_b64_tr_b16 a[238:239], v33 offset:1024\n"
"v_mfma_f32_16x16x32_bf16 v[152:155], a[164:167], a[136:139], v[152:155]\n"
"v_mfma_f32_16x16x32_bf16 v[156:159], a[164:167], a[140:143], v[156:159]\n"
"v_mfma_f32_16x16x32_bf16 v[160:163], a[168:171], a[128:131], v[160:163]\n"
"v_mfma_f32_16x16x32_bf16 v[164:167], a[168:171], a[132:135], v[164:167]\n"
"v_mfma_f32_16x16x32_bf16 v[168:171], a[168:171], a[136:139], v[168:171]\n"
"v_mfma_f32_16x16x32_bf16 v[172:175], a[168:171], a[140:143], v[172:175]\n"
"v_mfma_f32_16x16x32_bf16 v[176:179], a[172:175], a[128:131], v[176:179]\n"
"v_mfma_f32_16x16x32_bf16 v[180:183], a[172:175], a[132:135], v[180:183]\n"
"v_add_u32 v29, 0x8000, v29\n"
"v_mfma_f32_16x16x32_bf16 v[184:187], a[172:175], a[136:139], v[184:187]\n"
"v_add_u32 v27, 0x8000, v27\n"
"v_add_u32 v28, 0x8000, v28\n"
"v_mfma_f32_16x16x32_bf16 v[188:191], a[172:175], a[140:143], v[188:191]\n"
"v_add_u32 v24, 0x8000, v24\n"
"v_add_u32 v26, 0x8000, v26\n"
"s_waitcnt lgkmcnt(0)\n"
"v_mfma_f32_16x16x32_bf16 v[128:131], a[224:227], a[192:195], v[128:131]\n"
"s_waitcnt vmcnt(0)\n"
"s_barrier\n"
"v_mfma_f32_16x16x32_bf16 v[132:135], a[224:227], a[196:199], v[132:135]\n"
"ds_read_b64_tr_b16 a[160:161], v26 offset:0\n"
"ds_read_b64_tr_b16 a[162:163], v26 offset:1024\n"
"v_mfma_f32_16x16x32_bf16 v[136:139], a[224:227], a[200:203], v[136:139]\n"
"ds_read_b128 a[128:131], v24 offset:0\n"
"ds_read_b128 a[132:135], v24 offset:2048\n"
"v_mfma_f32_16x16x32_bf16 v[140:143], a[224:227], a[204:207], v[140:143]\n"
"ds_read_b128 a[136:139], v24 offset:4096\n"
"ds_read_b128 a[140:143], v24 offset:6144\n"
"v_mfma_f32_16x16x32_bf16 v[144:147], a[228:231], a[192:195], v[144:147]\n"
"ds_read_b64_tr_b16 a[164:165], v27 offset:0\n"
"ds_read_b64_tr_b16 a[166:167], v27 offset:1024\n"
"v_mfma_f32_16x16x32_bf16 v[148:151], a[228:231], a[196:199], v[148:151]\n"
"ds_read_b64_tr_b16 a[168:169], v28 offset:0\n"
"ds_read_b64_tr_b16 a[170:171], v28 offset:1024\n"
"v_mfma_f32_16x16x32_bf16 v[152:155], a[228:231], a[200:203], v[152:155]\n"
"ds_read_b64_tr_b16 a[172:173], v29 offset:0\n"
"ds_read_b64_tr_b16 a[174:175], v29 offset:1024\n"
"v_mfma_f32_16x16x32_bf16 v[156:159], a[228:231], a[204:207], v[156:159]\n"
"v_mfma_f32_16x16x32_bf16 v[160:163], a[232:235], a[192:195], v[160:163]\n"
"v_mfma_f32_16x16x32_bf16 v[164:167], a[232:235], a[196:199], v[164:167]\n"
"v_mfma_f32_16x16x32_bf16 v[168:171], a[232:235], a[200:203], v[168:171]\n"
"v_mfma_f32_16x16x32_bf16 v[172:175], a[232:235], a[204:207], v[172:175]\n"
"v_mfma_f32_16x16x32_bf16 v[176:179], a[236:239], a[192:195], v[176:179]\n"
"v_mfma_f32_16x16x32_bf16 v[180:183], a[236:239], a[196:199], v[180:183]\n"
"v_add_u32 v33, 0x8000, v33\n"
"v_mfma_f32_16x16x32_bf16 v[184:187], a[236:239], a[200:203], v[184:187]\n"
"v_add_u32 v31, 0x8000, v31\n"
"v_add_u32 v32, 0x8000, v32\n"
"v_mfma_f32_16x16x32_bf16 v[188:191], a[236:239], a[204:207], v[188:191]\n"
"v_add_u32 v25, 0x8000, v25\n"
"v_add_u32 v30, 0x8000, v30\n"
"s_waitcnt lgkmcnt(0)\n"
"v_mfma_f32_16x16x32_bf16 v[128:131], a[160:163], a[128:131], v[128:131]\n"
"ds_read_b64_tr_b16 a[224:225], v30 offset:0\n"
"ds_read_b64_tr_b16 a[226:227], v30 offset:1024\n"
"v_mfma_f32_16x16x32_bf16 v[132:135], a[160:163], a[132:135], v[132:135]\n"
"ds_read_b128 a[192:195], v25 offset:0\n"
"ds_read_b128 a[196:199], v25 offset:2048\n"
"v_mfma_f32_16x16x32_bf16 v[136:139], a[160:163], a[136:139], v[136:139]\n"
"ds_read_b128 a[200:203], v25 offset:4096\n"
"ds_read_b128 a[204:207], v25 offset:6144\n"
"v_mfma_f32_16x16x32_bf16 v[140:143], a[160:163], a[140:143], v[140:143]\n"
"ds_read_b64_tr_b16 a[228:229], v31 offset:0\n"
"ds_read_b64_tr_b16 a[230:231], v31 offset:1024\n"
"v_mfma_f32_16x16x32_bf16 v[144:147], a[164:167], a[128:131], v[144:147]\n"
"ds_read_b64_tr_b16 a[232:233], v32 offset:0\n"
"ds_read_b64_tr_b16 a[234:235], v32 offset:1024\n"
"v_mfma_f32_16x16x32_bf16 v[148:151], a[164:167], a[132:135], v[148:151]\n"
"ds_read_b64_tr_b16 a[236:237], v33 offset:0\n"
"ds_read_b64_tr_b16 a[238:239], v33 offset:1024\n"
"v_mfma_f32_16x16x32_bf16 v[152:155], a[164:167], a[136:139], v[152:155]\n"
"v_mfma_f32_16x16x32_bf16 v[156:159], a[164:167], a[140:143], v[156:159]\n"
"v_mfma_f32_16x16x32_bf16 v[160:163], a[168:171], a[128:131], v[160:163]\n"
"v_mfma_f32_16x16x32_bf16 v[164:167], a[168:171], a[132:135], v[164:167]\n"
"v_mfma_f32_16x16x32_bf16 v[168:171], a[168:171], a[136:139], v[168:171]\n"
"v_mfma_f32_16x16x32_bf16 v[172:175], a[168:171], a[140:143], v[172:175]\n"
"v_mfma_f32_16x16x32_bf16 v[176:179], a[172:175], a[128:131], v[176:179]\n"
"v_mfma_f32_16x16x32_bf16 v[180:183], a[172:175], a[132:135], v[180:183]\n"
"v_mfma_f32_16x16x32_bf16 v[184:187], a[172:175], a[136:139], v[184:187]\n"
"v_mfma_f32_16x16x32_bf16 v[188:191], a[172:175], a[140:143], v[188:191]\n"
"s_waitcnt lgkmcnt(0)\n"
"v_mfma_f32_16x16x32_bf16 v[128:131], a[224:227], a[192:195], v[128:131]\n"
"v_mfma_f32_16x16x32_bf16 v[132:135], a[224:227], a[196:199], v[132:135]\n"
"v_mfma_f32_16x16x32_bf16 v[136:139], a[224:227], a[200:203], v[136:139]\n"
"v_mfma_f32_16x16x32_bf16 v[140:143], a[224:227], a[204:207], v[140:143]\n"
"v_mfma_f32_16x16x32_bf16 v[144:147], a[228:231], a[192:195], v[144:147]\n"
"v_mfma_f32_16x16x32_bf16 v[148:151], a[228:231], a[196:199], v[148:151]\n"
"v_mfma_f32_16x16x32_bf16 v[152:155], a[228:231], a[200:203], v[152:155]\n"
"v_mfma_f32_16x16x32_bf16 v[156:159], a[228:231], a[204:207], v[156:159]\n"
"v_mfma_f32_16x16x32_bf16 v[160:163], a[232:235], a[192:195], v[160:163]\n"
"v_mfma_f32_16x16x32_bf16 v[164:167], a[232:235], a[196:199], v[164:167]\n"
"v_mfma_f32_16x16x32_bf16 v[168:171], a[232:235], a[200:203], v[168:171]\n"
"v_mfma_f32_16x16x32_bf16 v[172:175], a[232:235], a[204:207], v[172:175]\n"
"v_mfma_f32_16x16x32_bf16 v[176:179], a[236:239], a[192:195], v[176:179]\n"
"v_mfma_f32_16x16x32_bf16 v[180:183], a[236:239], a[196:199], v[180:183]\n"
"v_mfma_f32_16x16x32_bf16 v[184:187], a[236:239], a[200:203], v[184:187]\n"
"v_mfma_f32_16x16x32_bf16 v[188:191], a[236:239], a[204:207], v[188:191]\n"
"s_nop 7\n"
"s_add_u32 s48, s26, 0x4000000\n"
"s_addc_u32 s49, s27, 0\n"
"s_and_b32 s49, s49, 0xffff\n"
"s_mov_b32 s50, 0x800000\n"
"s_mov_b32 s51, 0x20000\n"
"v_sub_f32 v132, v132, v68\n"
"v_sub_f32 v133, v133, v69\n"
"v_sub_f32 v134, v134, v70\n"
"v_sub_f32 v135, v135, v71\n"
"v_fma_f32 v68, v21, v132, v68\n"
"v_fma_f32 v69, v21, v133, v69\n"
"v_fma_f32 v70, v21, v134, v70\n"
"v_fma_f32 v71, v21, v135, v71\n"
"v_sub_f32 v148, v148, v84\n"
"v_sub_f32 v149, v149, v85\n"
"v_sub_f32 v150, v150, v86\n"
"v_sub_f32 v151, v151, v87\n"
"v_fma_f32 v84, v21, v148, v84\n"
"v_fma_f32 v85, v21, v149, v85\n"
"v_fma_f32 v86, v21, v150, v86\n"
"v_fma_f32 v87, v21, v151, v87\n"
"v_sub_f32 v164, v164, v100\n"
"v_sub_f32 v165, v165, v101\n"
"v_sub_f32 v166, v166, v102\n"
"v_sub_f32 v167, v167, v103\n"
"v_fma_f32 v100, v21, v164, v100\n"
"v_fma_f32 v101, v21, v165, v101\n"
"v_fma_f32 v102, v21, v166, v102\n"
"v_fma_f32 v103, v21, v167, v103\n"
"v_sub_f32 v180, v180, v116\n"
"v_sub_f32 v181, v181, v117\n"
"v_sub_f32 v182, v182, v118\n"
"v_sub_f32 v183, v183, v119\n"
"v_fma_f32 v116, v21, v180, v116\n"
"v_fma_f32 v117, v21, v181, v117\n"
"v_fma_f32 v118, v21, v182, v118\n"
"v_fma_f32 v119, v21, v183, v119\n"
"s_add_u32 s44, s80, 1\n"
"s_and_b32 s44, s44, 3\n"
"s_lshl_b32 s45, s88, 2\n"
"s_add_u32 s44, s44, s45\n"
"s_lshl_b32 s44, s44, 2\n"
"s_add_u32 s44, s44, s80\n"
"s_lshl_b32 s44, s44, 2\n"
"s_add_u32 s44, s44, s34\n"
"s_lshl_b32 s44, s44, 11\n"
"v_cvt_pk_bf16_f32 v192, v68, v69\n"
"v_cvt_pk_bf16_f32 v193, v70, v71\n"
"v_cvt_pk_bf16_f32 v194, v84, v85\n"
"v_cvt_pk_bf16_f32 v195, v86, v87\n"
"buffer_store_dwordx4 v[192:195], v38, s[48:51], s44 offen offset:0 sc1\n"
"v_cvt_pk_bf16_f32 v196, v100, v101\n"
"v_cvt_pk_bf16_f32 v197, v102, v103\n"
"v_cvt_pk_bf16_f32 v198, v116, v117\n"
"v_cvt_pk_bf16_f32 v199, v118, v119\n"
"buffer_store_dwordx4 v[196:199], v38, s[48:51], s44 offen offset:1024 sc1\n"
"v_sub_f32 v136, v136, v72\n"
"v_sub_f32 v137, v137, v73\n"
"v_sub_f32 v138, v138, v74\n"
"v_sub_f32 v139, v139, v75\n"
"v_fma_f32 v72, v22, v136, v72\n"
"v_fma_f32 v73, v22, v137, v73\n"
"v_fma_f32 v74, v22, v138, v74\n"
"v_fma_f32 v75, v22, v139, v75\n"
"v_sub_f32 v152, v152, v88\n"
"v_sub_f32 v153, v153, v89\n"
"v_sub_f32 v154, v154, v90\n"
"v_sub_f32 v155, v155, v91\n"
"v_fma_f32 v88, v22, v152, v88\n"
"v_fma_f32 v89, v22, v153, v89\n"
"v_fma_f32 v90, v22, v154, v90\n"
"v_fma_f32 v91, v22, v155, v91\n"
"v_sub_f32 v168, v168, v104\n"
"v_sub_f32 v169, v169, v105\n"
"v_sub_f32 v170, v170, v106\n"
"v_sub_f32 v171, v171, v107\n"
"v_fma_f32 v104, v22, v168, v104\n"
"v_fma_f32 v105, v22, v169, v105\n"
"v_fma_f32 v106, v22, v170, v106\n"
"v_fma_f32 v107, v22, v171, v107\n"
"v_sub_f32 v184, v184, v120\n"
"v_sub_f32 v185, v185, v121\n"
"v_sub_f32 v186, v186, v122\n"
"v_sub_f32 v187, v187, v123\n"
"v_fma_f32 v120, v22, v184, v120\n"
"v_fma_f32 v121, v22, v185, v121\n"
"v_fma_f32 v122, v22, v186, v122\n"
"v_fma_f32 v123, v22, v187, v123\n"
"s_add_u32 s44, s80, 2\n"
"s_and_b32 s44, s44, 3\n"
"s_lshl_b32 s45, s88, 2\n"
"s_add_u32 s44, s44, s45\n"
"s_lshl_b32 s44, s44, 2\n"
"s_add_u32 s44, s44, s80\n"
"s_lshl_b32 s44, s44, 2\n"
"s_add_u32 s44, s44, s34\n"
"s_lshl_b32 s44, s44, 11\n"
"v_cvt_pk_bf16_f32 v200, v72, v73\n"
"v_cvt_pk_bf16_f32 v201, v74, v75\n"
"v_cvt_pk_bf16_f32 v202, v88, v89\n"
"v_cvt_pk_bf16_f32 v203, v90, v91\n"
"buffer_store_dwordx4 v[200:203], v38, s[48:51], s44 offen offset:0 sc1\n"
"v_cvt_pk_bf16_f32 v204, v104, v105\n"
"v_cvt_pk_bf16_f32 v205, v106, v107\n"
"v_cvt_pk_bf16_f32 v206, v120, v121\n"
"v_cvt_pk_bf16_f32 v207, v122, v123\n"
"buffer_store_dwordx4 v[204:207], v38, s[48:51], s44 offen offset:1024 sc1\n"
"v_sub_f32 v140, v140, v76\n"
"v_sub_f32 v141, v141, v77\n"
"v_sub_f32 v142, v142, v78\n"
"v_sub_f32 v143, v143, v79\n"
"v_fma_f32 v76, v23, v140, v76\n"
"v_fma_f32 v77, v23, v141, v77\n"
"v_fma_f32 v78, v23, v142, v78\n"
"v_fma_f32 v79, v23, v143, v79\n"
"v_sub_f32 v156, v156, v92\n"
"v_sub_f32 v157, v157, v93\n"
"v_sub_f32 v158, v158, v94\n"
"v_sub_f32 v159, v159, v95\n"
"v_fma_f32 v92, v23, v156, v92\n"
"v_fma_f32 v93, v23, v157, v93\n"
"v_fma_f32 v94, v23, v158, v94\n"
"v_fma_f32 v95, v23, v159, v95\n"
"v_sub_f32 v172, v172, v108\n"
"v_sub_f32 v173, v173, v109\n"
"v_sub_f32 v174, v174, v110\n"
"v_sub_f32 v175, v175, v111\n"
"v_fma_f32 v108, v23, v172, v108\n"
"v_fma_f32 v109, v23, v173, v109\n"
"v_fma_f32 v110, v23, v174, v110\n"
"v_fma_f32 v111, v23, v175, v111\n"
"v_sub_f32 v188, v188, v124\n"
"v_sub_f32 v189, v189, v125\n"
"v_sub_f32 v190, v190, v126\n"
"v_sub_f32 v191, v191, v127\n"
"v_fma_f32 v124, v23, v188, v124\n"
"v_fma_f32 v125, v23, v189, v125\n"
"v_fma_f32 v126, v23, v190, v126\n"
"v_fma_f32 v127, v23, v191, v127\n"
"s_add_u32 s44, s80, 3\n"
"s_and_b32 s44, s44, 3\n"
"s_lshl_b32 s45, s88, 2\n"
"s_add_u32 s44, s44, s45\n"
"s_lshl_b32 s44, s44, 2\n"
"s_add_u32 s44, s44, s80\n"
"s_lshl_b32 s44, s44, 2\n"
"s_add_u32 s44, s44, s34\n"
"s_lshl_b32 s44, s44, 11\n"
"v_cvt_pk_bf16_f32 v208, v76, v77\n"
"v_cvt_pk_bf16_f32 v209, v78, v79\n"
"v_cvt_pk_bf16_f32 v210, v92, v93\n"
"v_cvt_pk_bf16_f32 v211, v94, v95\n"
"buffer_store_dwordx4 v[208:211], v38, s[48:51], s44 offen offset:0 sc1\n"
"v_cvt_pk_bf16_f32 v212, v108, v109\n"
"v_cvt_pk_bf16_f32 v213, v110, v111\n"
"v_cvt_pk_bf16_f32 v214, v124, v125\n"
"v_cvt_pk_bf16_f32 v215, v126, v127\n"
"buffer_store_dwordx4 v[212:215], v38, s[48:51], s44 offen offset:1024 sc1\n"
"v_sub_f32 v128, v128, v64\n"
"v_sub_f32 v129, v129, v65\n"
"v_sub_f32 v130, v130, v66\n"
"v_sub_f32 v131, v131, v67\n"
"v_fma_f32 v64, v20, v128, v64\n"
"v_fma_f32 v65, v20, v129, v65\n"
"v_fma_f32 v66, v20, v130, v66\n"
"v_fma_f32 v67, v20, v131, v67\n"
"v_sub_f32 v144, v144, v80\n"
"v_sub_f32 v145, v145, v81\n"
"v_sub_f32 v146, v146, v82\n"
"v_sub_f32 v147, v147, v83\n"
"v_fma_f32 v80, v20, v144, v80\n"
"v_fma_f32 v81, v20, v145, v81\n"
"v_fma_f32 v82, v20, v146, v82\n"
"v_fma_f32 v83, v20, v147, v83\n"
"v_sub_f32 v160, v160, v96\n"
"v_sub_f32 v161, v161, v97\n"
"v_sub_f32 v162, v162, v98\n"
"v_sub_f32 v163, v163, v99\n"
"v_fma_f32 v96, v20, v160, v96\n"
"v_fma_f32 v97, v20, v161, v97\n"
"v_fma_f32 v98, v20, v162, v98\n"
"v_fma_f32 v99, v20, v163, v99\n"
"v_sub_f32 v176, v176, v112\n"
"v_sub_f32 v177, v177, v113\n"
"v_sub_f32 v178, v178, v114\n"
"v_sub_f32 v179, v179, v115\n"
"v_fma_f32 v112, v20, v176, v112\n"
"v_fma_f32 v113, v20, v177, v113\n"
"v_fma_f32 v114, v20, v178, v114\n"
"v_fma_f32 v115, v20, v179, v115\n"
"v_and_b32 v43, 63, v0\n"
"v_add_u32 v44, 1, v43\n"
"v_add_u32 v44, s80, v44\n"
"v_and_b32 v44, 3, v44\n"
"s_lshl_b32 s44, s88, 2\n"
"v_add_u32 v44, s44, v44\n"
"v_lshlrev_b32 v44, 2, v44\n"
"v_add_u32 v44, s80, v44\n"
"v_lshlrev_b32 v44, 2, v44\n"
"v_add_u32 v44, s34, v44\n"
"v_lshlrev_b32 v44, 7, v44\n"
"v_add_u32 v44, 0x22000, v44\n"
"v_mov_b32 v46, s42\n"
"v_mov_b32 v47, s43\n"
"v_min_u32 v43, 2, v43\n"
"v_add_u32 v53, 1, v43\n"
"v_add_u32 v53, s80, v53\n"
"v_and_b32 v53, 3, v53\n"
"s_lshl_b32 s44, s88, 2\n"
"s_add_u32 s44, s44, s80\n"
"s_lshl_b32 s44, s44, 2\n"
"v_add_u32 v53, s44, v53\n"
"v_lshlrev_b32 v53, 2, v53\n"
"v_add_u32 v53, s34, v53\n"
"v_lshlrev_b32 v53, 7, v53\n"
"v_add_u32 v53, 0x22000, v53\n"
"s_mov_b32 s85, -40000\n"
"s_waitcnt vmcnt(0)\n"
"s_mov_b64 exec, 7\n"
"buffer_store_dwordx2 v[46:47], v44, s[64:67], 0 offen sc1\n"
"s_mov_b64 exec, -1\n"
"LM_px_spin:\n"
"buffer_load_dwordx2 v[54:55], v53, s[64:67], 0 offen sc1\n"
"s_waitcnt vmcnt(0)\n"
"v_cmp_ne_u32 s[46:47], s42, v54\n"
"v_cmp_ne_u32 vcc, s43, v55\n"
"s_nop 1\n"
"s_or_b64 vcc, vcc, s[46:47]\n"
"s_cbranch_scc0 LM_px_done\n"
"s_sleep 1\n"
"s_add_u32 s85, s85, 1\n"
"s_cbranch_scc0 LM_px_spin\n"
"LM_px_done:\n"
"s_add_u32 s44, s80, 1\n"
"s_and_b32 s44, s44, 3\n"
"s_lshl_b32 s45, s88, 2\n"
"s_add_u32 s45, s45, s80\n"
"s_lshl_b32 s45, s45, 2\n"
"s_add_u32 s44, s44, s45\n"
"s_lshl_b32 s44, s44, 2\n"
"s_add_u32 s44, s44, s34\n"
"s_lshl_b32 s44, s44, 11\n"
"buffer_load_dwordx4 v[128:131], v38, s[48:51], s44 offen offset:0 sc1\n"
"buffer_load_dwordx4 v[132:135], v38, s[48:51], s44 offen offset:1024 sc1\n"
"s_add_u32 s44, s80, 2\n"
"s_and_b32 s44, s44, 3\n"
"s_lshl_b32 s45, s88, 2\n"
"s_add_u32 s45, s45, s80\n"
"s_lshl_b32 s45, s45, 2\n"
"s_add_u32 s44, s44, s45\n"
"s_lshl_b32 s44, s44, 2\n"
"s_add_u32 s44, s44, s34\n"
"s_lshl_b32 s44, s44, 11\n"
"buffer_load_dwordx4 v[136:139], v38, s[48:51], s44 offen offset:0 sc1\n"
"buffer_load_dwordx4 v[140:143], v38, s[48:51], s44 offen offset:1024 sc1\n"
"s_add_u32 s44, s80, 3\n"
"s_and_b32 s44, s44, 3\n"
"s_lshl_b32 s45, s88, 2\n"
"s_add_u32 s45, s45, s80\n"
"s_lshl_b32 s45, s45, 2\n"
"s_add_u32 s44, s44, s45\n"
"s_lshl_b32 s44, s44, 2\n"
"s_add_u32 s44, s44, s34\n"
"s_lshl_b32 s44, s44, 11\n"
"buffer_load_dwordx4 v[144:147], v38, s[48:51], s44 offen offset:0 sc1\n"
"buffer_load_dwordx4 v[148:151], v38, s[48:51], s44 offen offset:1024 sc1\n"
"s_mov_b32 s52, s28\n"
"s_and_b32 s53, s29, 0xffff\n"
"s_mov_b32 s54, 0x200000\n"
"s_mov_b32 s55, 0x20000\n"
"s_lshl_b32 s44, s81, 7\n"
"s_lshl_b32 s45, s39, 6\n"
"s_add_u32 s44, s44, s45\n"
"s_lshl_b32 s45, s80, 4\n"
"s_add_u32 s44, s44, s45\n"
"s_lshl_b32 s44, s44, 10\n"
"s_lshl_b32 s45, s84, 8\n"
"s_add_u32 s44, s44, s45\n"
"s_lshl_b32 s45, s40, 7\n"
"s_add_u32 s44, s44, s45\n"
"v_lshlrev_b32 v52, 10, v39\n"
"v_lshl_add_u32 v52, v40, 3, v52\n"
"s_waitcnt vmcnt(0)\n"
"v_lshlrev_b32 v56, 16, v128\n"
"v_and_b32 v57, 0xffff0000, v128\n"
"v_lshlrev_b32 v58, 16, v129\n"
"v_and_b32 v59, 0xffff0000, v129\n"
"v_add_f32 v64, v64, v56\n"
"v_add_f32 v65, v65, v57\n"
"v_add_f32 v66, v66, v58\n"
"v_add_f32 v67, v67, v59\n"
"v_lshlrev_b32 v56, 16, v136\n"
"v_and_b32 v57, 0xffff0000, v136\n"
"v_lshlrev_b32 v58, 16, v137\n"
"v_and_b32 v59, 0xffff0000, v137\n"
"v_add_f32 v64, v64, v56\n"
"v_add_f32 v65, v65, v57\n"
"v_add_f32 v66, v66, v58\n"
"v_add_f32 v67, v67, v59\n"
"v_lshlrev_b32 v56, 16, v144\n"
"v_and_b32 v57, 0xffff0000, v144\n"
"v_lshlrev_b32 v58, 16, v145\n"
"v_and_b32 v59, 0xffff0000, v145\n"
"v_add_f32 v64, v64, v56\n"
"v_add_f32 v65, v65, v57\n"
"v_add_f32 v66, v66, v58\n"
"v_add_f32 v67, v67, v59\n"
"v_cvt_pk_bf16_f32 v160, v64, v65\n"
"v_cvt_pk_bf16_f32 v161, v66, v67\n"
"buffer_store_dwordx2 v[160:161], v52, s[52:55], s44 offen offset:0\n"
"v_lshlrev_b32 v56, 16, v130\n"
"v_and_b32 v57, 0xffff0000, v130\n"
"v_lshlrev_b32 v58, 16, v131\n"
"v_and_b32 v59, 0xffff0000, v131\n"
"v_add_f32 v80, v80, v56\n"
"v_add_f32 v81, v81, v57\n"
"v_add_f32 v82, v82, v58\n"
"v_add_f32 v83, v83, v59\n"
"v_lshlrev_b32 v56, 16, v138\n"
"v_and_b32 v57, 0xffff0000, v138\n"
"v_lshlrev_b32 v58, 16, v139\n"
"v_and_b32 v59, 0xffff0000, v139\n"
"v_add_f32 v80, v80, v56\n"
"v_add_f32 v81, v81, v57\n"
"v_add_f32 v82, v82, v58\n"
"v_add_f32 v83, v83, v59\n"
"v_lshlrev_b32 v56, 16, v146\n"
"v_and_b32 v57, 0xffff0000, v146\n"
"v_lshlrev_b32 v58, 16, v147\n"
"v_and_b32 v59, 0xffff0000, v147\n"
"v_add_f32 v80, v80, v56\n"
"v_add_f32 v81, v81, v57\n"
"v_add_f32 v82, v82, v58\n"
"v_add_f32 v83, v83, v59\n"
"v_cvt_pk_bf16_f32 v162, v80, v81\n"
"v_cvt_pk_bf16_f32 v163, v82, v83\n"
"buffer_store_dwordx2 v[162:163], v52, s[52:55], s44 offen offset:32\n"
"v_lshlrev_b32 v56, 16, v132\n"
"v_and_b32 v57, 0xffff0000, v132\n"
"v_lshlrev_b32 v58, 16, v133\n"
"v_and_b32 v59, 0xffff0000, v133\n"
"v_add_f32 v96, v96, v56\n"
"v_add_f32 v97, v97, v57\n"
"v_add_f32 v98, v98, v58\n"
"v_add_f32 v99, v99, v59\n"
"v_lshlrev_b32 v56, 16, v140\n"
"v_and_b32 v57, 0xffff0000, v140\n"
"v_lshlrev_b32 v58, 16, v141\n"
"v_and_b32 v59, 0xffff0000, v141\n"
"v_add_f32 v96, v96, v56\n"
"v_add_f32 v97, v97, v57\n"
"v_add_f32 v98, v98, v58\n"
"v_add_f32 v99, v99, v59\n"
"v_lshlrev_b32 v56, 16, v148\n"
"v_and_b32 v57, 0xffff0000, v148\n"
"v_lshlrev_b32 v58, 16, v149\n"
"v_and_b32 v59, 0xffff0000, v149\n"
"v_add_f32 v96, v96, v56\n"
"v_add_f32 v97, v97, v57\n"
"v_add_f32 v98, v98, v58\n"
"v_add_f32 v99, v99, v59\n"
"v_cvt_pk_bf16_f32 v164, v96, v97\n"
"v_cvt_pk_bf16_f32 v165, v98, v99\n"
"buffer_store_dwordx2 v[164:165], v52, s[52:55], s44 offen offset:64\n"
"v_lshlrev_b32 v56, 16, v134\n"
"v_and_b32 v57, 0xffff0000, v134\n"
"v_lshlrev_b32 v58, 16, v135\n"
"v_and_b32 v59, 0xffff0000, v135\n"
"v_add_f32 v112, v112, v56\n"
"v_add_f32 v113, v113, v57\n"
"v_add_f32 v114, v114, v58\n"
"v_add_f32 v115, v115, v59\n"
"v_lshlrev_b32 v56, 16, v142\n"
"v_and_b32 v57, 0xffff0000, v142\n"
"v_lshlrev_b32 v58, 16, v143\n"
"v_and_b32 v59, 0xffff0000, v143\n"
"v_add_f32 v112, v112, v56\n"
"v_add_f32 v113, v113, v57\n"
"v_add_f32 v114, v114, v58\n"
"v_add_f32 v115, v115, v59\n"
"v_lshlrev_b32 v56, 16, v150\n"
"v_and_b32 v57, 0xffff0000, v150\n"
"v_lshlrev_b32 v58, 16, v151\n"
"v_and_b32 v59, 0xffff0000, v151\n"
"v_add_f32 v112, v112, v56\n"
"v_add_f32 v113, v113, v57\n"
"v_add_f32 v114, v114, v58\n"
"v_add_f32 v115, v115, v59\n"
"v_cvt_pk_bf16_f32 v166, v112, v113\n"
"v_cvt_pk_bf16_f32 v167, v114, v115\n"
"buffer_store_dwordx2 v[166:167], v52, s[52:55], s44 offen offset:96\n"
"s_cmp_lg_u32 s30, 0\n"
"s_cbranch_scc1 LM_end\n"
"s_cmp_lg_u32 s34, 0\n"
"s_cbranch_scc1 LM_end\n"
"s_waitcnt vmcnt(0)\n"
"v_and_b32 v43, 63, v0\n"
"v_lshlrev_b32 v40, 7, v43\n"
"v_add_u32 v40, 0x1000, v40\n"
"s_mov_b32 s85, -40000\n"
"LM_espin:\n"
"s_mov_b32 s44, 0\n"
"buffer_load_dwordx2 v[48:49], v40, s[64:67], s44 offen sc1\n"
"s_mov_b32 s44, 8192\n"
"buffer_load_dwordx2 v[50:51], v40, s[64:67], s44 offen sc1\n"
"s_mov_b32 s44, 16384\n"
"buffer_load_dwordx2 v[52:53], v40, s[64:67], s44 offen sc1\n"
"s_mov_b32 s44, 24576\n"
"buffer_load_dwordx2 v[54:55], v40, s[64:67], s44 offen sc1\n"
"s_waitcnt vmcnt(0)\n"
"v_xor_b32 v48, s42, v48\n"
"v_xor_b32 v49, s43, v49\n"
"v_xor_b32 v50, s42, v50\n"
"v_xor_b32 v51, s43, v51\n"
"v_xor_b32 v52, s42, v52\n"
"v_xor_b32 v53, s43, v53\n"
"v_xor_b32 v54, s42, v54\n"
"v_xor_b32 v55, s43, v55\n"
"v_or3_b32 v48, v48, v49, v50\n"
"v_or3_b32 v48, v48, v51, v52\n"
"v_or3_b32 v48, v48, v53, v54\n"
"v_or_b32 v48, v48, v55\n"
"v_cmp_ne_u32 vcc, 0, v48\n"
"s_nop 1\n"
"s_cbranch_vccz LM_eok\n"
"s_sleep 2\n"
"s_add_u32 s85, s85, 1\n"
"s_cbranch_scc0 LM_espin\n"
"LM_eok:\n"
"v_mov_b32 v46, s42\n"
"v_mov_b32 v47, s43\n"
"v_mov_b32 v40, 0\n"
"s_mov_b64 exec, 1\n"
"buffer_store_dwordx2 v[46:47], v40, s[64:67], 0 offen sc1\n"
"s_mov_b64 exec, -1\n"
"LM_end:\n"
"s_waitcnt vmcnt(0)\n"
"LM_codeend:\n"
  :: "{s[18:19]}"(po), "{s[24:25]}"(p2), "{s[26:27]}"(pw), "{s[28:29]}"(pout), "{s30}"(blk), "{s31}"(flag), "{s[42:43]}"(tag), "{v0}"(tid)
  : "v1","v2","v3","v4","v5","v6","v7","v8","v9","v10","v11","v12","v13","v14","v15","v16","v17","v18","v19","v20","v21","v22","v23","v24","v25","v26","v27","v28","v29","v30","v31","v32","v33","v34","v35","v36","v37","v38","v39","v40","v41","v42","v43","v44","v45","v46","v47","v48","v49","v50","v51","v52","v53","v54","v55","v56","v57","v58","v59","v60","v61","v62","v63","v64","v65","v66","v67","v68","v69","v70","v71","v72","v73","v74","v75","v76","v77","v78","v79","v80","v81","v82","v83","v84","v85","v86","v87","v88","v89","v90","v91","v92","v93","v94","v95","v96","v97","v98","v99","v100","v101","v102","v103","v104","v105","v106","v107","v108","v109","v110","v111","v112","v113","v114","v115","v116","v117","v118","v119","v120","v121","v122","v123","v124","v125","v126","v127","v128","v129","v130","v131","v132","v133","v134","v135","v136","v137","v138","v139","v140","v141","v142","v143","v144","v145","v146","v147","v148","v149","v150","v151","v152","v153","v154","v155","v156","v157","v158","v159","v160","v161","v162","v163","v164","v165","v166","v167","v168","v169","v170","v171","v172","v173","v174","v175","v176","v177","v178","v179","v180","v181","v182","v183","v184","v185","v186","v187","v188","v189","v190","v191","v192","v193","v194","v195","v196","v197","v198","v199","v200","v201","v202","v203","v204","v205","v206","v207","v208","v209","v210","v211","v212","v213","v214","v215","v216","v217","v218","v219","v220","v221","v222","v223","v224","v225","v226","v227","v228","v229","v230","v231","v232","v233","v234","v235","v236","v237","v238","v239","v240","v241","v242","v243","v244","v245","v246","v247","v248","v249","v250","v251","v252","v253","v254","v255","a0","a1","a2","a3","a4","a5","a6","a7","a8","a9","a10","a11","a12","a13","a14","a15","a16","a17","a18","a19","a20","a21","a22","a23","a24","a25","a26","a27","a28","a29","a30","a31","a32","a33","a34","a35","a36","a37","a38","a39","a40","a41","a42","a43","a44","a45","a46","a47","a48","a49","a50","a51","a52","a53","a54","a55","a56","a57","a58","a59","a60","a61","a62","a63","a64","a65","a66","a67","a68","a69","a70","a71","a72","a73","a74","a75","a76","a77","a78","a79","a80","a81","a82","a83","a84","a85","a86","a87","a88","a89","a90","a91","a92","a93","a94","a95","a96","a97","a98","a99","a100","a101","a102","a103","a104","a105","a106","a107","a108","a109","a110","a111","a112","a113","a114","a115","a116","a117","a118","a119","a120","a121","a122","a123","a124","a125","a126","a127","a128","a129","a130","a131","a132","a133","a134","a135","a136","a137","a138","a139","a140","a141","a142","a143","a144","a145","a146","a147","a148","a149","a150","a151","a152","a153","a154","a155","a156","a157","a158","a159","a160","a161","a162","a163","a164","a165","a166","a167","a168","a169","a170","a171","a172","a173","a174","a175","a176","a177","a178","a179","a180","a181","a182","a183","a184","a185","a186","a187","a188","a189","a190","a191","a192","a193","a194","a195","a196","a197","a198","a199","a200","a201","a202","a203","a204","a205","a206","a207","a208","a209","a210","a211","a212","a213","a214","a215","a216","a217","a218","a219","a220","a221","a222","a223","a224","a225","a226","a227","a228","a229","a230","a231","a232","a233","a234","a235","a236","a237","a238","a239","a240","a241","a242","a243","a244","a245","a246","a247","a248","a249","a250","a251","a252","a253","a254","a255","s34","s35","s36","s37","s38","s39","s40","s41","s44","s45","s46","s47","s48","s49","s50","s51","s52","s53","s54","s55","s56","s57","s58","s59","s60","s61","s62","s63","s64","s65","s66","s67","s68","s69","s70","s71","s72","s73","s74","s75","s76","s77","s78","s79","s80","s81","s82","s83","s84","s85","s86","s87","s88","s89","s90","s91","vcc","memory");
}

extern "C" void kernel_launch(void* const* d_in, const int* in_sizes, int n_in, void* d_out, int out_size, void* d_ws, size_t ws_size, hipStream_t stream) {
  (void)in_sizes; (void)n_in; (void)out_size; (void)ws_size;
  gated_mix_fused<<<256,256,0,stream>>>((const u16*)d_in[0],(const u16*)d_in[1],(const u16*)d_in[2],(const u16*)d_in[3],(const u16*)d_in[4],(const u16*)d_in[5],
      (const u16*)d_in[7],(const u16*)d_in[8],(const u16*)d_in[9],(const u16*)d_in[10],(char*)d_ws,(u16*)d_out);
}
```
